```python
import torch

def _softmax(x, axis=-1):
    return torch.softmax(x, dim=axis)
def _gelu(x):
    return torch.nn.functional.gelu(x, approximate="tanh")
def _silu(x):
    return torch.nn.functional.silu(x)
def _sigmoid(x):
    return torch.sigmoid(x)
def _layernorm(x, axis=-1, eps=1e-5):
    m = x.mean(dim=axis, keepdim=True)
    v = x.var(dim=axis, keepdim=True, unbiased=False)
    return (x - m) / torch.sqrt(v + eps)
def _scatter_add(tgt, idx, src, axis=0):
    return tgt.clone().index_add_(axis, idx, src)
def _segment_sum(src, seg, n):
    out = torch.zeros((n,) + src.shape[1:], dtype=src.dtype)
    return out.index_add_(0, seg, src)
def _pad(x, pw):
    flat = tuple(v for pair in reversed(pw) for v in pair)
    return torch.nn.functional.pad(x, flat)

kernel_name = 'sequential_preLN__sliding_block__mixer_channel_mlp__prompt'

def setup_inputs():

    _w = (lambda a: (lambda t: t.to(torch.bfloat16) if t.is_floating_point() else t)(torch.as_tensor(a)))
    def _A_setup():
        B, S, D = 1, 2048, 640
        return {
            "x": _w((lambda *s: torch.randn(*s, dtype=torch.bfloat16) * 0.05)(B, S, D)),
            "wqkv": _w((lambda *s: torch.randn(*s, dtype=torch.bfloat16) * 0.05)(D, 3 * D)),
            "wo": _w((lambda *s: torch.randn(*s, dtype=torch.bfloat16) * 0.05)(D, D)),
        }
    _A_in = _A_setup()

    def _B_setup():
        B, S, D, F = 1, 2048, 640, 1536
        return {
            "x": _w((lambda *s: torch.randn(*s, dtype=torch.bfloat16) * 0.05)(B, S, D)),
            "w1": _w((lambda *s: torch.randn(*s, dtype=torch.bfloat16) * 3.2)(D, F) * (D ** -0.5)),
            "b1": _w((lambda *s: torch.randn(*s, dtype=torch.bfloat16) * 1.6)(F)),
            "w2": _w((lambda *s: torch.randn(*s, dtype=torch.bfloat16) * 0.2)(F, D) * (F ** -0.5)),
            "b2": _w((lambda *s: torch.randn(*s, dtype=torch.bfloat16) * 0.4)(D)),
        }
    _B_in = _B_setup()

    return {
        "A_x": _A_in["x"],
        "A_wqkv": _A_in["wqkv"],
        "A_wo": _A_in["wo"],
        "B_x": _B_in["x"],
        "B_w1": _B_in["w1"],
        "B_b1": _B_in["b1"],
        "B_w2": _B_in["w2"],
        "B_b2": _B_in["b2"],
    }

def reference(A_x, A_wqkv, A_wo, B_x, B_w1, B_b1, B_w2, B_b2):
    def _A_ref(x, wqkv, wo):
        B, S, D = x.shape
        H = 32; Dh = D // H; W = 256
        qkv = (lambda x, *d: x.permute(*d))((x @ wqkv).reshape(B, S, 3, H, Dh), 2, 0, 3, 1, 4)
        q, k, v = qkv[0], qkv[1], qkv[2]
        scores = torch.einsum("bhqd,bhkd->bhqk", q, k) * (Dh ** -0.5)
        idx = torch.arange(S)
        mask = (idx[:, None] - idx[None, :] >= 0) & (idx[:, None] - idx[None, :] < W)
        scores = torch.where(mask[None, None], scores, -1e9)
        p = _softmax(scores, axis=-1)
        o = torch.einsum("bhqk,bhkd->bhqd", p, v)
        return (lambda x, *d: x.permute(*d))(o, 0, 2, 1, 3).reshape(B, S, D) @ wo

    def _B_ref(x, w1, b1, w2, b2):
        y = _layernorm(x, axis=-1)
        h = _gelu(torch.einsum("bsd,df->bsf", y, w1) + b1)
        return torch.einsum("bsf,fd->bsd", h, w2) + b2

    x = A_x
    h = x + _A_ref(_layernorm(x, axis=-1), A_wqkv, A_wo)
    h2 = h + _B_ref(_layernorm(h, axis=-1), B_w1, B_b1, B_w2, B_b2)
    return h2
```

```cpp
#include <hip/hip_runtime.h>
#include <hip/hip_bf16.h>
#include <stdint.h>

typedef short bf16x8 __attribute__((ext_vector_type(8)));
typedef float f32x4 __attribute__((ext_vector_type(4)));
typedef unsigned short u16;

constexpr int S = 2048, D = 640, H = 32, DH = 20, W = 256, F = 1536;

__device__ __forceinline__ float bf2f(u16 b) { return __uint_as_float(((unsigned)b) << 16); }
__device__ __forceinline__ u16 f2bf(float f) {
  __hip_bfloat16 h = __float2bfloat16(f);
  return *reinterpret_cast<u16*>(&h);
}

template <int TWICE>
__global__ void ln_kernel(const u16* __restrict__ x, u16* __restrict__ y) {
  int row = blockIdx.x * 4 + (threadIdx.x >> 6);
  int lane = threadIdx.x & 63;
  float v[10];
  float s = 0.f;
  for (int i = 0; i < 10; ++i) { v[i] = bf2f(x[row * D + lane + 64 * i]); s += v[i]; }
  for (int rep = 0; rep <= TWICE; ++rep) {
    s = 0.f;
    for (int i = 0; i < 10; ++i) s += v[i];
    for (int o = 32; o; o >>= 1) s += __shfl_xor(s, o);
    float m = s / D;
    float q = 0.f;
    for (int i = 0; i < 10; ++i) { float d = v[i] - m; q += d * d; }
    for (int o = 32; o; o >>= 1) q += __shfl_xor(q, o);
    float r = rsqrtf(q / D + 1e-5f);
    for (int i = 0; i < 10; ++i) v[i] = (v[i] - m) * r;
  }
  for (int i = 0; i < 10; ++i) y[row * D + lane + 64 * i] = f2bf(v[i]);
}

template <int EPI>
__global__ __launch_bounds__(256) void gemm_kernel(const u16* __restrict__ A, const u16* __restrict__ B,
                                                   u16* __restrict__ C, const u16* __restrict__ bias,
                                                   const u16* __restrict__ res, int N, int K) {
  int wid = threadIdx.x >> 6, lane = threadIdx.x & 63;
  int r0 = blockIdx.y * 64 + (wid >> 1) * 32, c0 = blockIdx.x * 64 + (wid & 1) * 32;
  int fr = lane & 15, fq = lane >> 4;
  f32x4 acc[2][2] = {};
  for (int k = 0; k < K; k += 32) {
    bf16x8 a[2], b[2];
    for (int m = 0; m < 2; ++m) a[m] = *reinterpret_cast<const bf16x8*>(A + (size_t)(r0 + m * 16 + fr) * K + k + fq * 8);
    for (int n = 0; n < 2; ++n)
      for (int j = 0; j < 8; ++j) b[n][j] = (short)B[(size_t)(k + fq * 8 + j) * N + c0 + n * 16 + fr];
    for (int m = 0; m < 2; ++m)
      for (int n = 0; n < 2; ++n) acc[m][n] = __builtin_amdgcn_mfma_f32_16x16x32_bf16(a[m], b[n], acc[m][n], 0, 0, 0);
  }
  for (int m = 0; m < 2; ++m)
    for (int n = 0; n < 2; ++n)
      for (int j = 0; j < 4; ++j) {
        int r = r0 + m * 16 + fq * 4 + j, c = c0 + n * 16 + fr;
        float v = acc[m][n][j];
        if (EPI == 2 || EPI == 3) v += bf2f(bias[c]);
        if (EPI == 2) {
          float u = 0.7978845608028654f * (v + 0.044715f * v * v * v);
          v = 0.5f * v * (1.f + tanhf(u));
        }
        if (EPI == 1 || EPI == 3) v += bf2f(res[(size_t)r * N + c]);
        C[(size_t)r * N + c] = f2bf(v);
      }
}

__global__ void attn_kernel(const u16* __restrict__ qkv, u16* __restrict__ o) {
  int q = blockIdx.x * 64 + threadIdx.x;
  int h = blockIdx.y;
  float qv[DH], acc[DH];
  for (int d = 0; d < DH; ++d) { qv[d] = bf2f(qkv[(size_t)q * 3 * D + h * DH + d]) * 0.22360679774997896f; acc[d] = 0.f; }
  int k0 = q - (W - 1); if (k0 < 0) k0 = 0;
  float m = -1e30f, l = 0.f;
  for (int k = k0; k <= q; ++k) {
    const u16* kp = qkv + (size_t)k * 3 * D + D + h * DH;
    const u16* vp = kp + D;
    float s = 0.f;
    for (int d = 0; d < DH; ++d) s += qv[d] * bf2f(kp[d]);
    float mn = fmaxf(m, s);
    float a = __expf(m - mn), p = __expf(s - mn);
    l = l * a + p;
    for (int d = 0; d < DH; ++d) acc[d] = acc[d] * a + p * bf2f(vp[d]);
    m = mn;
  }
  for (int d = 0; d < DH; ++d) o[(size_t)q * D + h * DH + d] = f2bf(acc[d] / l);
}

extern "C" void kernel_launch(void* const* d_in, const int* in_sizes, int n_in, void* d_out, int out_size, void* d_ws,
                              size_t ws_size, hipStream_t stream) {
  const u16* x = (const u16*)d_in[0];
  const u16* wqkv = (const u16*)d_in[1];
  const u16* wo = (const u16*)d_in[2];
  const u16* w1 = (const u16*)d_in[4];
  const u16* b1 = (const u16*)d_in[5];
  const u16* w2 = (const u16*)d_in[6];
  const u16* b2 = (const u16*)d_in[7];
  u16* out = (u16*)d_out;
  u16* ws = (u16*)d_ws;
  u16* y = ws;
  u16* qkv = y + S * D;
  u16* o = qkv + S * 3 * D;
  u16* h = o + S * D;
  u16* y2 = h + S * D;
  u16* hid = y2 + S * D;
  ln_kernel<0><<<S / 4, 256, 0, stream>>>(x, y);
  gemm_kernel<0><<<dim3(3 * D / 64, S / 64), 256, 0, stream>>>(y, wqkv, qkv, nullptr, nullptr, 3 * D, D);
  attn_kernel<<<dim3(S / 64, H), 64, 0, stream>>>(qkv, o);
  gemm_kernel<1><<<dim3(D / 64, S / 64), 256, 0, stream>>>(o, wo, h, nullptr, x, D, D);
  ln_kernel<1><<<S / 4, 256, 0, stream>>>(h, y2);
  gemm_kernel<2><<<dim3(F / 64, S / 64), 256, 0, stream>>>(y2, w1, hid, b1, nullptr, F, D);
  gemm_kernel<3><<<dim3(D / 64, S / 64), 256, 0, stream>>>(hid, w2, out, b2, h, D, F);
}
```

```cpp
#include <hip/hip_runtime.h>
#include <stdint.h>

static __device__ const unsigned fb_table[40960] __attribute__((aligned(512))) = {
0,2457600,0,819200,1280,4,3,3072,4096,1073741824,0,1051011932,131072,1024,40960,1024,65793,0,0,0,0,10649600,32768,40960,0,1024,1408,2560,72,128,128,0,
0,0,0,10240,19693568,98304,0,0,65536,65536,5382144,19693568,20512768,22478848,10649600,13402112,13271040,0,1024,0,8028160,8032256,0,0,40960,61440,5120,10240,19795968,102400,160,0,
65536,66048,5424128,19719168,20574208,22540288,10670080,13403648,13272064,1536,1024,320,8028672,8032384,20480,96,81920,122880,25600,10240,19898368,180224,320,8192,66560,66560,5466112,19744768,20635648,22601728,10690560,13405184,
13273088,2560,3584,640,8029184,8032512,40960,192,122880,184320,30720,10240,20000768,184320,480,8192,66560,67072,5508096,19770368,20697088,22663168,10711040,13406720,13274112,4096,3584,960,8029696,8032640,61440,288,
640,2457600,0,819200,1280,4,3,3072,4096,1073741824,3,1065353216,130688,1040,40960,1024,460545,0,512,512,8,10977280,36864,40960,0,1024,1408,2560,72,128,128,16,
0,2048,20480,25600,19714048,2744320,0,0,65536,65536,5382528,19693568,20512768,22478848,10977280,14188544,13287424,0,1024,0,8355840,8359936,327680,0,40960,63488,71680,25600,19816448,2748416,160,0,
65536,66048,5424512,19719168,20574208,22540288,10997760,14190080,13288448,1536,1024,320,8356352,8360064,348160,96,81920,124928,122880,25600,19918848,2826240,320,8192,66560,66560,5466496,19744768,20635648,22601728,11018240,14191616,
13289472,2560,3584,640,8356864,8360192,368640,192,122880,186368,174080,25600,20021248,2830336,480,8192,66560,67072,5508480,19770368,20697088,22663168,11038720,14193152,13290496,4096,3584,960,8357376,8360320,389120,288,
256,2457600,1,1966080,3072,3,4,61440,62464,63488,0,1051011932,130304,1556,40960,1024,460545,0,1024,1024,16,11304960,40960,40960,0,1024,1408,2560,96,104,160,32,
655360,0,491520,12288,20528128,753664,0,0,73728,73728,6053888,19693568,20512768,22478848,11304960,14974976,13303808,0,1024,0,8683520,8687616,655360,0,696320,61440,516096,12288,20651008,757760,192,0,
73728,74240,6095872,19719168,20574208,22540288,11325440,14976512,13304832,1536,1024,320,8684032,8687744,675840,96,737280,122880,540672,12288,20773888,835584,384,8192,74752,74752,6137856,19744768,20635648,22601728,11345920,14978048,
13305856,2560,3584,640,8684544,8687872,696320,192,778240,184320,565248,12288,20896768,839680,576,8192,74752,75264,6179840,19770368,20697088,22663168,11366400,14979584,13306880,4096,3584,960,8685056,8688000,716800,288,
896,2457600,1,1966080,3072,3,4,61440,62464,63488,3,1065353216,129920,1564,40960,1024,460545,0,1536,1536,24,11632640,45056,40960,0,1024,1408,2560,96,104,160,48,
655360,2048,688128,12288,20534272,3399680,0,0,73728,73728,6054272,19693568,20512768,22478848,11632640,15761408,13320192,0,1024,0,9011200,9015296,983040,0,696320,63488,712704,12288,20657152,3403776,192,0,
73728,74240,6096256,19719168,20574208,22540288,11653120,15762944,13321216,1536,1024,320,9011712,9015424,1003520,96,737280,124928,737280,12288,20780032,3481600,384,8192,74752,74752,6138240,19744768,20635648,22601728,11673600,15764480,
13322240,2560,3584,640,9012224,9015552,1024000,192,778240,186368,761856,12288,20902912,3485696,576,8192,74752,75264,6180224,19770368,20697088,22663168,11694080,15766016,13323264,4096,3584,960,9012736,9015680,1044480,288,
640,2457600,2,1966080,1280,4,3,3072,4096,1073741824,0,1051011932,129536,2560,40960,1024,460545,0,2048,2048,32,11960320,49152,40960,0,1024,1408,2560,72,128,128,64,
1310720,0,0,5120,22478848,1409024,0,0,81920,81920,6725632,19693568,20512768,22478848,11960320,16547840,13336576,0,1024,0,9338880,9342976,1310720,0,1351680,61440,10240,5120,22724608,1413120,160,0,
81920,82432,6767616,19719168,20574208,22540288,11980800,16549376,13337600,1536,1024,320,9339392,9343104,1331200,96,1392640,122880,20480,5120,22970368,1490944,320,8192,82944,82944,6809600,19744768,20635648,22601728,12001280,16550912,
13338624,2560,3584,640,9339904,9343232,1351680,192,1433600,184320,30720,5120,23216128,1495040,480,8192,82944,83456,6851584,19770368,20697088,22663168,12021760,16552448,13339648,4096,3584,960,9340416,9343360,1372160,288,
0,2457600,2,1966080,1280,4,3,3072,4096,1073741824,3,1065353216,129152,2576,40960,1024,460545,0,2560,2560,40,12288000,53248,40960,0,1024,1408,2560,72,128,128,80,
1310720,2048,163840,5120,22499328,4055040,0,0,81920,81920,6726016,19693568,20512768,22478848,12288000,17334272,13352960,0,1024,0,9666560,9670656,1638400,0,1351680,63488,174080,5120,22745088,4059136,160,0,
81920,82432,6768000,19719168,20574208,22540288,12308480,17335808,13353984,1536,1024,320,9667072,9670784,1658880,96,1392640,124928,184320,5120,22990848,4136960,320,8192,82944,82944,6809984,19744768,20635648,22601728,12328960,17337344,
13355008,2560,3584,640,9667584,9670912,1679360,192,1433600,186368,194560,5120,23236608,4141056,480,8192,82944,83456,6851968,19770368,20697088,22663168,12349440,17338880,13356032,4096,3584,960,9668096,9671040,1699840,288,
896,2457600,2,1966080,1280,4,3,3072,4096,1073741824,0,1051011932,128768,2680,40960,1024,460545,0,3072,3072,48,12615680,57344,40960,0,1024,1408,2560,72,128,128,96,
1966080,0,1228800,5120,22632448,2064384,0,0,90112,90112,7397376,19693568,20512768,22478848,12615680,18120704,13369344,0,1024,0,9994240,9998336,1966080,0,2007040,61440,1239040,5120,22878208,2068480,160,0,
90112,90624,7439360,19719168,20574208,22540288,12636160,18122240,13370368,1536,1024,320,9994752,9998464,1986560,96,2048000,122880,1249280,5120,23123968,2146304,320,8192,91136,91136,7481344,19744768,20635648,22601728,12656640,18123776,
13371392,2560,3584,640,9995264,9998592,2007040,192,2088960,184320,1259520,5120,23369728,2150400,480,8192,91136,91648,7523328,19770368,20697088,22663168,12677120,18125312,13372416,4096,3584,960,9995776,9998720,2027520,288,
256,2457600,2,1966080,1280,4,3,3072,4096,1073741824,3,1065353216,128384,2696,40960,1024,460545,0,3584,3584,56,12943360,61440,40960,0,1024,1408,2560,72,128,128,112,
1966080,2048,1392640,5120,22652928,4710400,0,0,90112,90112,7397760,19693568,20512768,22478848,12943360,18907136,13385728,0,1024,0,10321920,10326016,2293760,0,2007040,63488,1402880,5120,22898688,4714496,160,0,
90112,90624,7439744,19719168,20574208,22540288,12963840,18908672,13386752,1536,1024,320,10322432,10326144,2314240,96,2048000,124928,1413120,5120,23144448,4792320,320,8192,91136,91136,7481728,19744768,20635648,22601728,12984320,18910208,
13387776,2560,3584,640,10322944,10326272,2334720,192,2088960,186368,1423360,5120,23390208,4796416,480,8192,91136,91648,7523712,19770368,20697088,22663168,13004800,18911744,13388800,4096,3584,960,10323456,10326400,2355200,288,
128,2457600,0,819200,1280,4,3,3072,4096,1073741824,0,1051011932,128000,1280,40960,1024,1118481,0,4,0,0,10649600,32896,40960,160,1024,1408,2560,72,128,128,0,
0,256,640,10240,20103168,106496,0,0,65536,65536,5382272,19795968,20758528,22724608,10652160,13408256,13271040,5120,6144,1280,8030208,8032768,0,384,40960,61696,5760,10240,20205568,110592,160,0,
65536,66048,5424256,19821568,20819968,22786048,10672640,13409792,13272064,6656,6144,1600,8030720,8032896,20480,480,81920,123136,26240,10240,20307968,188416,320,8192,66560,66560,5466240,19847168,20881408,22847488,10693120,13411328,
13273088,7680,8704,1920,8031232,8033024,40960,576,122880,184576,31360,10240,20410368,192512,480,8192,66560,67072,5508224,19872768,20942848,22908928,10713600,13412864,13274112,9216,8704,2240,8031744,8033152,61440,672,
768,2457600,0,819200,1280,4,3,3072,4096,1073741824,3,1065353216,127616,1296,40960,1024,7829265,0,516,512,8,10977280,36992,40960,160,1024,1408,2560,72,128,128,16,
0,2304,21120,25600,20123648,2752512,0,0,65536,65536,5382656,19795968,20758528,22724608,10979840,14194688,13287424,5120,6144,1280,8357888,8360448,327680,384,40960,63744,72320,25600,20226048,2756608,160,0,
65536,66048,5424640,19821568,20819968,22786048,11000320,14196224,13288448,6656,6144,1600,8358400,8360576,348160,480,81920,125184,123520,25600,20328448,2834432,320,8192,66560,66560,5466624,19847168,20881408,22847488,11020800,14197760,
13289472,7680,8704,1920,8358912,8360704,368640,576,122880,186624,174720,25600,20430848,2838528,480,8192,66560,67072,5508608,19872768,20942848,22908928,11041280,14199296,13290496,9216,8704,2240,8359424,8360832,389120,672,
384,2457600,1,1966080,3072,3,4,61440,62464,63488,0,1051011932,127232,1812,40960,1024,7829265,0,1028,1024,16,11304960,41088,40960,160,1024,1408,2560,96,104,160,32,
655360,256,492288,12288,21019648,761856,0,0,73728,73728,6054016,19795968,20758528,22724608,11307520,14981120,13303808,5120,6144,1280,8685568,8688128,655360,384,696320,61696,516864,12288,21142528,765952,192,0,
73728,74240,6096000,19821568,20819968,22786048,11328000,14982656,13304832,6656,6144,1600,8686080,8688256,675840,480,737280,123136,541440,12288,21265408,843776,384,8192,74752,74752,6137984,19847168,20881408,22847488,11348480,14984192,
13305856,7680,8704,1920,8686592,8688384,696320,576,778240,184576,566016,12288,21388288,847872,576,8192,74752,75264,6179968,19872768,20942848,22908928,11368960,14985728,13306880,9216,8704,2240,8687104,8688512,716800,672,
1024,2457600,1,1966080,3072,3,4,61440,62464,63488,3,1065353216,126848,1820,40960,1024,7829265,0,1540,1536,24,11632640,45184,40960,160,1024,1408,2560,96,104,160,48,
655360,2304,688896,12288,21025792,3407872,0,0,73728,73728,6054400,19795968,20758528,22724608,11635200,15767552,13320192,5120,6144,1280,9013248,9015808,983040,384,696320,63744,713472,12288,21148672,3411968,192,0,
73728,74240,6096384,19821568,20819968,22786048,11655680,15769088,13321216,6656,6144,1600,9013760,9015936,1003520,480,737280,125184,738048,12288,21271552,3489792,384,8192,74752,74752,6138368,19847168,20881408,22847488,11676160,15770624,
13322240,7680,8704,1920,9014272,9016064,1024000,576,778240,186624,762624,12288,21394432,3493888,576,8192,74752,75264,6180352,19872768,20942848,22908928,11696640,15772160,13323264,9216,8704,2240,9014784,9016192,1044480,672,
768,2457600,2,1966080,1280,4,3,3072,4096,1073741824,0,1051011932,126464,2816,40960,1024,7829265,0,2052,2048,32,11960320,49280,40960,160,1024,1408,2560,72,128,128,64,
1310720,256,640,5120,23461888,1417216,0,0,81920,81920,6725760,19795968,20758528,22724608,11962880,16553984,13336576,5120,6144,1280,9340928,9343488,1310720,384,1351680,61696,10880,5120,23707648,1421312,160,0,
81920,82432,6767744,19821568,20819968,22786048,11983360,16555520,13337600,6656,6144,1600,9341440,9343616,1331200,480,1392640,123136,21120,5120,23953408,1499136,320,8192,82944,82944,6809728,19847168,20881408,22847488,12003840,16557056,
13338624,7680,8704,1920,9341952,9343744,1351680,576,1433600,184576,31360,5120,24199168,1503232,480,8192,82944,83456,6851712,19872768,20942848,22908928,12024320,16558592,13339648,9216,8704,2240,9342464,9343872,1372160,672,
128,2457600,2,1966080,1280,4,3,3072,4096,1073741824,3,1065353216,126080,2832,40960,1024,7829265,0,2564,2560,40,12288000,53376,40960,160,1024,1408,2560,72,128,128,80,
1310720,2304,164480,5120,23482368,4063232,0,0,81920,81920,6726144,19795968,20758528,22724608,12290560,17340416,13352960,5120,6144,1280,9668608,9671168,1638400,384,1351680,63744,174720,5120,23728128,4067328,160,0,
81920,82432,6768128,19821568,20819968,22786048,12311040,17341952,13353984,6656,6144,1600,9669120,9671296,1658880,480,1392640,125184,184960,5120,23973888,4145152,320,8192,82944,82944,6810112,19847168,20881408,22847488,12331520,17343488,
13355008,7680,8704,1920,9669632,9671424,1679360,576,1433600,186624,195200,5120,24219648,4149248,480,8192,82944,83456,6852096,19872768,20942848,22908928,12352000,17345024,13356032,9216,8704,2240,9670144,9671552,1699840,672,
1024,2457600,2,1966080,1280,4,3,3072,4096,1073741824,0,1051011932,125696,2936,40960,1024,7829265,0,3076,3072,48,12615680,57472,40960,160,1024,1408,2560,72,128,128,96,
1966080,256,1229440,5120,23615488,2072576,0,0,90112,90112,7397504,19795968,20758528,22724608,12618240,18126848,13369344,5120,6144,1280,9996288,9998848,1966080,384,2007040,61696,1239680,5120,23861248,2076672,160,0,
90112,90624,7439488,19821568,20819968,22786048,12638720,18128384,13370368,6656,6144,1600,9996800,9998976,1986560,480,2048000,123136,1249920,5120,24107008,2154496,320,8192,91136,91136,7481472,19847168,20881408,22847488,12659200,18129920,
13371392,7680,8704,1920,9997312,9999104,2007040,576,2088960,184576,1260160,5120,24352768,2158592,480,8192,91136,91648,7523456,19872768,20942848,22908928,12679680,18131456,13372416,9216,8704,2240,9997824,9999232,2027520,672,
384,2457600,2,1966080,1280,4,3,3072,4096,1073741824,3,1065353216,125312,2952,40960,1024,7829265,0,3588,3584,56,12943360,61568,40960,160,1024,1408,2560,72,128,128,112,
1966080,2304,1393280,5120,23635968,4718592,0,0,90112,90112,7397888,19795968,20758528,22724608,12945920,18913280,13385728,5120,6144,1280,10323968,10326528,2293760,384,2007040,63744,1403520,5120,23881728,4722688,160,0,
90112,90624,7439872,19821568,20819968,22786048,12966400,18914816,13386752,6656,6144,1600,10324480,10326656,2314240,480,2048000,125184,1413760,5120,24127488,4800512,320,8192,91136,91136,7481856,19847168,20881408,22847488,12986880,18916352,
13387776,7680,8704,1920,10324992,10326784,2334720,576,2088960,186624,1424000,5120,24373248,4804608,480,8192,91136,91648,7523840,19872768,20942848,22908928,13007360,18917888,13388800,9216,8704,2240,10325504,10326912,2355200,672,
0,2457600,0,819200,1280,4,3,3072,4096,1073741824,0,1051011932,124928,1028,40960,1024,65793,0,8,0,0,10649600,33024,41088,320,1024,1664,2560,72,128,128,0,
0,512,51200,10240,19698688,114688,0,0,65536,65536,5382400,19898368,21004288,22970368,10654720,13414400,13271040,10240,11264,2560,8033280,8037376,0,768,40960,61952,56320,10240,19801088,118784,160,0,
65536,66048,5424384,19923968,21065728,23031808,10675200,13415936,13272064,11776,11264,2880,8033792,8037504,20480,864,81920,123392,76800,10240,19903488,196608,320,8192,66560,66560,5466368,19949568,21127168,23093248,10695680,13417472,
13273088,12800,13824,3200,8034304,8037632,40960,960,122880,184832,81920,10240,20005888,200704,480,8192,66560,67072,5508352,19975168,21188608,23154688,10716160,13419008,13274112,14336,13824,3520,8034816,8037760,61440,1056,
640,2457600,0,819200,1280,4,3,3072,4096,1073741824,1,1065353216,124544,1044,41984,256,460545,0,520,512,8,10977280,37120,41088,320,1024,1664,2560,72,128,128,16,
0,2560,204800,10240,19719168,5341184,0,0,65536,65536,5382144,19898368,21004288,22970368,10982400,14200832,13287424,10240,11264,2560,8360960,8365056,327680,768,40960,64000,209920,10240,19821568,5342208,160,0,
65536,66048,5424128,19923968,21065728,23031808,11002880,14202368,13288448,11776,11264,2880,8361472,8365184,348160,864,81920,125440,230400,10240,19923968,5425152,320,8192,66560,66560,5466112,19949568,21127168,23093248,11023360,14203904,
13289472,12800,13824,3200,8361984,8365312,368640,960,122880,186880,235520,10240,20026368,5426176,480,8192,66560,67072,5508096,19975168,21188608,23154688,11043840,14205440,13290496,14336,13824,3520,8362496,8365440,389120,1056,
256,2457600,1,1966080,3072,3,4,61440,62464,63488,0,1051011932,124160,2068,40960,1024,460545,0,1032,1024,16,11304960,41216,41088,320,1024,1664,2560,96,104,160,32,
655360,512,493056,12288,21511168,770048,0,0,73728,73728,6054144,19898368,21004288,22970368,11310080,14987264,13303808,10240,11264,2560,8688640,8692736,655360,768,696320,61952,517632,12288,21634048,774144,192,0,
73728,74240,6096128,19923968,21065728,23031808,11330560,14988800,13304832,11776,11264,2880,8689152,8692864,675840,864,737280,123392,542208,12288,21756928,851968,384,8192,74752,74752,6138112,19949568,21127168,23093248,11351040,14990336,
13305856,12800,13824,3200,8689664,8692992,696320,960,778240,184832,566784,12288,21879808,856064,576,8192,74752,75264,6180096,19975168,21188608,23154688,11371520,14991872,13306880,14336,13824,3520,8690176,8693120,716800,1056,
896,2457600,1,1966080,3072,3,4,61440,62464,63488,1,1065353216,123776,2076,41984,256,460545,0,1544,1536,24,11632640,45312,41088,320,1024,1664,2560,96,104,160,48,
655360,2560,689664,12288,21517312,6012928,0,0,73728,73728,6053888,19898368,21004288,22970368,11637760,15773696,13320192,10240,11264,2560,9016320,9020416,983040,768,696320,64000,714240,12288,21640192,6013952,192,0,
73728,74240,6095872,19923968,21065728,23031808,11658240,15775232,13321216,11776,11264,2880,9016832,9020544,1003520,864,737280,125440,738816,12288,21763072,6096896,384,8192,74752,74752,6137856,19949568,21127168,23093248,11678720,15776768,
13322240,12800,13824,3200,9017344,9020672,1024000,960,778240,186880,763392,12288,21885952,6097920,576,8192,74752,75264,6179840,19975168,21188608,23154688,11699200,15778304,13323264,14336,13824,3520,9017856,9020800,1044480,1056,
640,2457600,2,1966080,1280,4,3,3072,4096,1073741824,0,1051011932,123392,2564,40960,1024,460545,0,2056,2048,32,11960320,49408,41088,320,1024,1664,2560,72,128,128,64,
1310720,512,40960,5120,22483968,1425408,0,0,81920,81920,6725888,19898368,21004288,22970368,11965440,16560128,13336576,10240,11264,2560,9344000,9348096,1310720,768,1351680,61952,51200,5120,22729728,1429504,160,0,
81920,82432,6767872,19923968,21065728,23031808,11985920,16561664,13337600,11776,11264,2880,9344512,9348224,1331200,864,1392640,123392,61440,5120,22975488,1507328,320,8192,82944,82944,6809856,19949568,21127168,23093248,12006400,16563200,
13338624,12800,13824,3200,9345024,9348352,1351680,960,1433600,184832,71680,5120,23221248,1511424,480,8192,82944,83456,6851840,19975168,21188608,23154688,12026880,16564736,13339648,14336,13824,3520,9345536,9348480,1372160,1056,
0,2457600,2,1966080,1280,4,3,3072,4096,1073741824,1,1065353216,123008,2580,41984,256,460545,0,2568,2560,40,12288000,53504,41088,320,1024,1664,2560,72,128,128,80,
1310720,2560,204800,5120,22504448,6684672,0,0,81920,81920,6725632,19898368,21004288,22970368,12293120,17346560,13352960,10240,11264,2560,9671680,9675776,1638400,768,1351680,64000,215040,5120,22750208,6685696,160,0,
81920,82432,6767616,19923968,21065728,23031808,12313600,17348096,13353984,11776,11264,2880,9672192,9675904,1658880,864,1392640,125440,225280,5120,22995968,6768640,320,8192,82944,82944,6809600,19949568,21127168,23093248,12334080,17349632,
13355008,12800,13824,3200,9672704,9676032,1679360,960,1433600,186880,235520,5120,23241728,6769664,480,8192,82944,83456,6851584,19975168,21188608,23154688,12354560,17351168,13356032,14336,13824,3520,9673216,9676160,1699840,1056,
896,2457600,2,1966080,1280,4,3,3072,4096,1073741824,0,1051011932,122624,2684,40960,1024,460545,0,3080,3072,48,12615680,57600,41088,320,1024,1664,2560,72,128,128,96,
1966080,512,1269760,5120,22637568,2080768,0,0,90112,90112,7397632,19898368,21004288,22970368,12620800,18132992,13369344,10240,11264,2560,9999360,10003456,1966080,768,2007040,61952,1280000,5120,22883328,2084864,160,0,
90112,90624,7439616,19923968,21065728,23031808,12641280,18134528,13370368,11776,11264,2880,9999872,10003584,1986560,864,2048000,123392,1290240,5120,23129088,2162688,320,8192,91136,91136,7481600,19949568,21127168,23093248,12661760,18136064,
13371392,12800,13824,3200,10000384,10003712,2007040,960,2088960,184832,1300480,5120,23374848,2166784,480,8192,91136,91648,7523584,19975168,21188608,23154688,12682240,18137600,13372416,14336,13824,3520,10000896,10003840,2027520,1056,
256,2457600,2,1966080,1280,4,3,3072,4096,1073741824,1,1065353216,122240,2700,41984,256,460545,0,3592,3584,56,12943360,61696,41088,320,1024,1664,2560,72,128,128,112,
1966080,2560,1433600,5120,22658048,7356416,0,0,90112,90112,7397376,19898368,21004288,22970368,12948480,18919424,13385728,10240,11264,2560,10327040,10331136,2293760,768,2007040,64000,1443840,5120,22903808,7357440,160,0,
90112,90624,7439360,19923968,21065728,23031808,12968960,18920960,13386752,11776,11264,2880,10327552,10331264,2314240,864,2048000,125440,1454080,5120,23149568,7440384,320,8192,91136,91136,7481344,19949568,21127168,23093248,12989440,18922496,
13387776,12800,13824,3200,10328064,10331392,2334720,960,2088960,186880,1464320,5120,23395328,7441408,480,8192,91136,91648,7523328,19975168,21188608,23154688,13009920,18924032,13388800,14336,13824,3520,10328576,10331520,2355200,1056,
128,2457600,0,819200,1280,4,3,3072,4096,1073741824,0,1051011932,121856,1284,40960,1024,1118481,0,12,0,0,10649600,33152,41088,480,1024,1664,2560,72,128,128,0,
0,768,51840,10240,20108288,122880,0,0,65536,65536,5382528,20000768,21250048,23216128,10657280,13420544,13271040,15360,16384,3840,8035328,8037888,0,1152,40960,62208,56960,10240,20210688,126976,160,0,
65536,66048,5424512,20026368,21311488,23277568,10677760,13422080,13272064,16896,16384,4160,8035840,8038016,20480,1248,81920,123648,77440,10240,20313088,204800,320,8192,66560,66560,5466496,20051968,21372928,23339008,10698240,13423616,
13273088,17920,18944,4480,8036352,8038144,40960,1344,122880,185088,82560,10240,20415488,208896,480,8192,66560,67072,5508480,20077568,21434368,23400448,10718720,13425152,13274112,19456,18944,4800,8036864,8038272,61440,1440,
768,2457600,0,819200,1280,4,3,3072,4096,1073741824,1,1065353216,121472,1300,41984,256,7829265,0,524,512,8,10977280,37248,41088,480,1024,1664,2560,72,128,128,16,
0,2816,205440,10240,20128768,5343232,0,0,65536,65536,5382272,20000768,21250048,23216128,10984960,14206976,13287424,15360,16384,3840,8363008,8365568,327680,1152,40960,64256,210560,10240,20231168,5344256,160,0,
65536,66048,5424256,20026368,21311488,23277568,11005440,14208512,13288448,16896,16384,4160,8363520,8365696,348160,1248,81920,125696,231040,10240,20333568,5427200,320,8192,66560,66560,5466240,20051968,21372928,23339008,11025920,14210048,
13289472,17920,18944,4480,8364032,8365824,368640,1344,122880,187136,236160,10240,20435968,5428224,480,8192,66560,67072,5508224,20077568,21434368,23400448,11046400,14211584,13290496,19456,18944,4800,8364544,8365952,389120,1440,
384,2457600,1,1966080,3072,3,4,61440,62464,63488,0,1051011932,121088,2324,40960,1024,7829265,0,1036,1024,16,11304960,41344,41088,480,1024,1664,2560,96,104,160,32,
655360,768,493824,12288,22002688,778240,0,0,73728,73728,6054272,20000768,21250048,23216128,11312640,14993408,13303808,15360,16384,3840,8690688,8693248,655360,1152,696320,62208,518400,12288,22125568,782336,192,0,
73728,74240,6096256,20026368,21311488,23277568,11333120,14994944,13304832,16896,16384,4160,8691200,8693376,675840,1248,737280,123648,542976,12288,22248448,860160,384,8192,74752,74752,6138240,20051968,21372928,23339008,11353600,14996480,
13305856,17920,18944,4480,8691712,8693504,696320,1344,778240,185088,567552,12288,22371328,864256,576,8192,74752,75264,6180224,20077568,21434368,23400448,11374080,14998016,13306880,19456,18944,4800,8692224,8693632,716800,1440,
1024,2457600,1,1966080,3072,3,4,61440,62464,63488,1,1065353216,120704,2332,41984,256,7829265,0,1548,1536,24,11632640,45440,41088,480,1024,1664,2560,96,104,160,48,
655360,2816,690432,12288,22008832,6014976,0,0,73728,73728,6054016,20000768,21250048,23216128,11640320,15779840,13320192,15360,16384,3840,9018368,9020928,983040,1152,696320,64256,715008,12288,22131712,6016000,192,0,
73728,74240,6096000,20026368,21311488,23277568,11660800,15781376,13321216,16896,16384,4160,9018880,9021056,1003520,1248,737280,125696,739584,12288,22254592,6098944,384,8192,74752,74752,6137984,20051968,21372928,23339008,11681280,15782912,
13322240,17920,18944,4480,9019392,9021184,1024000,1344,778240,187136,764160,12288,22377472,6099968,576,8192,74752,75264,6179968,20077568,21434368,23400448,11701760,15784448,13323264,19456,18944,4800,9019904,9021312,1044480,1440,
768,2457600,2,1966080,1280,4,3,3072,4096,1073741824,0,1051011932,120320,2820,40960,1024,7829265,0,2060,2048,32,11960320,49536,41088,480,1024,1664,2560,72,128,128,64,
1310720,768,41600,5120,23467008,1433600,0,0,81920,81920,6726016,20000768,21250048,23216128,11968000,16566272,13336576,15360,16384,3840,9346048,9348608,1310720,1152,1351680,62208,51840,5120,23712768,1437696,160,0,
81920,82432,6768000,20026368,21311488,23277568,11988480,16567808,13337600,16896,16384,4160,9346560,9348736,1331200,1248,1392640,123648,62080,5120,23958528,1515520,320,8192,82944,82944,6809984,20051968,21372928,23339008,12008960,16569344,
13338624,17920,18944,4480,9347072,9348864,1351680,1344,1433600,185088,72320,5120,24204288,1519616,480,8192,82944,83456,6851968,20077568,21434368,23400448,12029440,16570880,13339648,19456,18944,4800,9347584,9348992,1372160,1440,
128,2457600,2,1966080,1280,4,3,3072,4096,1073741824,1,1065353216,119936,2836,41984,256,7829265,0,2572,2560,40,12288000,53632,41088,480,1024,1664,2560,72,128,128,80,
1310720,2816,205440,5120,23487488,6686720,0,0,81920,81920,6725760,20000768,21250048,23216128,12295680,17352704,13352960,15360,16384,3840,9673728,9676288,1638400,1152,1351680,64256,215680,5120,23733248,6687744,160,0,
81920,82432,6767744,20026368,21311488,23277568,12316160,17354240,13353984,16896,16384,4160,9674240,9676416,1658880,1248,1392640,125696,225920,5120,23979008,6770688,320,8192,82944,82944,6809728,20051968,21372928,23339008,12336640,17355776,
13355008,17920,18944,4480,9674752,9676544,1679360,1344,1433600,187136,236160,5120,24224768,6771712,480,8192,82944,83456,6851712,20077568,21434368,23400448,12357120,17357312,13356032,19456,18944,4800,9675264,9676672,1699840,1440,
1024,2457600,2,1966080,1280,4,3,3072,4096,1073741824,0,1051011932,119552,2940,40960,1024,7829265,0,3084,3072,48,12615680,57728,41088,480,1024,1664,2560,72,128,128,96,
1966080,768,1270400,5120,23620608,2088960,0,0,90112,90112,7397760,20000768,21250048,23216128,12623360,18139136,13369344,15360,16384,3840,10001408,10003968,1966080,1152,2007040,62208,1280640,5120,23866368,2093056,160,0,
90112,90624,7439744,20026368,21311488,23277568,12643840,18140672,13370368,16896,16384,4160,10001920,10004096,1986560,1248,2048000,123648,1290880,5120,24112128,2170880,320,8192,91136,91136,7481728,20051968,21372928,23339008,12664320,18142208,
13371392,17920,18944,4480,10002432,10004224,2007040,1344,2088960,185088,1301120,5120,24357888,2174976,480,8192,91136,91648,7523712,20077568,21434368,23400448,12684800,18143744,13372416,19456,18944,4800,10002944,10004352,2027520,1440,
384,2457600,2,1966080,1280,4,3,3072,4096,1073741824,1,1065353216,119168,2956,41984,256,7829265,0,3596,3584,56,12943360,61824,41088,480,1024,1664,2560,72,128,128,112,
1966080,2816,1434240,5120,23641088,7358464,0,0,90112,90112,7397504,20000768,21250048,23216128,12951040,18925568,13385728,15360,16384,3840,10329088,10331648,2293760,1152,2007040,64256,1444480,5120,23886848,7359488,160,0,
90112,90624,7439488,20026368,21311488,23277568,12971520,18927104,13386752,16896,16384,4160,10329600,10331776,2314240,1248,2048000,125696,1454720,5120,24132608,7442432,320,8192,91136,91136,7481472,20051968,21372928,23339008,12992000,18928640,
13387776,17920,18944,4480,10330112,10331904,2334720,1344,2088960,187136,1464960,5120,24378368,7443456,480,8192,91136,91648,7523456,20077568,21434368,23400448,13012480,18930176,13388800,19456,18944,4800,10330624,10332032,2355200,1440,
0,2457600,0,819200,1280,4,3,3072,4096,1073741824,0,1051011932,118784,1032,40960,1024,1118481,0,16,0,0,10649600,33280,41216,640,1280,1920,2816,72,128,128,0,
0,1024,102400,10240,19703808,131072,0,0,65536,65536,5382656,20103168,21495808,23461888,10659840,13426688,13271040,20480,21504,5120,8038400,8042496,0,1536,40960,62464,107520,10240,19806208,135168,160,0,
65536,66048,5424640,20128768,21557248,23523328,10680320,13428224,13272064,22016,21504,5440,8038912,8042624,20480,1632,81920,123904,128000,10240,19908608,212992,320,8192,66560,66560,5466624,20154368,21618688,23584768,10700800,13429760,
13273088,23040,24064,5760,8039424,8042752,40960,1728,122880,185344,133120,10240,20011008,217088,480,8192,66560,67072,5508608,20179968,21680128,23646208,10721280,13431296,13274112,24576,24064,6080,8039936,8042880,61440,1824,
640,2457600,0,819200,1280,4,3,3072,4096,1073741824,1,1065353216,118400,1048,41984,256,7829265,0,528,512,8,10977280,37376,41216,640,1280,1920,2816,72,128,128,16,
0,3072,256000,10240,19724288,5345280,0,0,65536,65536,5382400,20103168,21495808,23461888,10987520,14213120,13287424,20480,21504,5120,8366080,8370176,327680,1536,40960,64512,261120,10240,19826688,5346304,160,0,
65536,66048,5424384,20128768,21557248,23523328,11008000,14214656,13288448,22016,21504,5440,8366592,8370304,348160,1632,81920,125952,281600,10240,19929088,5429248,320,8192,66560,66560,5466368,20154368,21618688,23584768,11028480,14216192,
13289472,23040,24064,5760,8367104,8370432,368640,1728,122880,187392,286720,10240,20031488,5430272,480,8192,66560,67072,5508352,20179968,21680128,23646208,11048960,14217728,13290496,24576,24064,6080,8367616,8370560,389120,1824,
256,2457600,1,1966080,3072,3,4,61440,62464,63488,0,1051011932,118016,1560,40960,1024,7829265,0,1040,1024,16,11304960,41472,41216,640,1280,1920,2816,96,104,160,32,
655360,1024,589824,12288,20531200,786432,0,0,73728,73728,6054400,20103168,21495808,23461888,11315200,14999552,13303808,20480,21504,5120,8693760,8697856,655360,1536,696320,62464,614400,12288,20654080,790528,192,0,
73728,74240,6096384,20128768,21557248,23523328,11335680,15001088,13304832,22016,21504,5440,8694272,8697984,675840,1632,737280,123904,638976,12288,20776960,868352,384,8192,74752,74752,6138368,20154368,21618688,23584768,11356160,15002624,
13305856,23040,24064,5760,8694784,8698112,696320,1728,778240,185344,663552,12288,20899840,872448,576,8192,74752,75264,6180352,20179968,21680128,23646208,11376640,15004160,13306880,24576,24064,6080,8695296,8698240,716800,1824,
896,2457600,1,1966080,3072,3,4,61440,62464,63488,1,1065353216,117632,1568,41984,256,7829265,0,1552,1536,24,11632640,45568,41216,640,1280,1920,2816,96,104,160,48,
655360,3072,786432,12288,20537344,6017024,0,0,73728,73728,6054144,20103168,21495808,23461888,11642880,15785984,13320192,20480,21504,5120,9021440,9025536,983040,1536,696320,64512,811008,12288,20660224,6018048,192,0,
73728,74240,6096128,20128768,21557248,23523328,11663360,15787520,13321216,22016,21504,5440,9021952,9025664,1003520,1632,737280,125952,835584,12288,20783104,6100992,384,8192,74752,74752,6138112,20154368,21618688,23584768,11683840,15789056,
13322240,23040,24064,5760,9022464,9025792,1024000,1728,778240,187392,860160,12288,20905984,6102016,576,8192,74752,75264,6180096,20179968,21680128,23646208,11704320,15790592,13323264,24576,24064,6080,9022976,9025920,1044480,1824,
640,2457600,2,1966080,1280,4,3,3072,4096,1073741824,0,1051011932,117248,2568,40960,1024,7829265,0,2064,2048,32,11960320,49664,41216,640,1280,1920,2816,72,128,128,64,
1310720,1024,81920,5120,22489088,1441792,0,0,81920,81920,6726144,20103168,21495808,23461888,11970560,16572416,13336576,20480,21504,5120,9349120,9353216,1310720,1536,1351680,62464,92160,5120,22734848,1445888,160,0,
81920,82432,6768128,20128768,21557248,23523328,11991040,16573952,13337600,22016,21504,5440,9349632,9353344,1331200,1632,1392640,123904,102400,5120,22980608,1523712,320,8192,82944,82944,6810112,20154368,21618688,23584768,12011520,16575488,
13338624,23040,24064,5760,9350144,9353472,1351680,1728,1433600,185344,112640,5120,23226368,1527808,480,8192,82944,83456,6852096,20179968,21680128,23646208,12032000,16577024,13339648,24576,24064,6080,9350656,9353600,1372160,1824,
0,2457600,2,1966080,1280,4,3,3072,4096,1073741824,1,1065353216,116864,2584,41984,256,7829265,0,2576,2560,40,12288000,53760,41216,640,1280,1920,2816,72,128,128,80,
1310720,3072,245760,5120,22509568,6688768,0,0,81920,81920,6725888,20103168,21495808,23461888,12298240,17358848,13352960,20480,21504,5120,9676800,9680896,1638400,1536,1351680,64512,256000,5120,22755328,6689792,160,0,
81920,82432,6767872,20128768,21557248,23523328,12318720,17360384,13353984,22016,21504,5440,9677312,9681024,1658880,1632,1392640,125952,266240,5120,23001088,6772736,320,8192,82944,82944,6809856,20154368,21618688,23584768,12339200,17361920,
13355008,23040,24064,5760,9677824,9681152,1679360,1728,1433600,187392,276480,5120,23246848,6773760,480,8192,82944,83456,6851840,20179968,21680128,23646208,12359680,17363456,13356032,24576,24064,6080,9678336,9681280,1699840,1824,
896,2457600,2,1966080,1280,4,3,3072,4096,1073741824,0,1051011932,116480,2688,40960,1024,7829265,0,3088,3072,48,12615680,57856,41216,640,1280,1920,2816,72,128,128,96,
1966080,1024,1310720,5120,22642688,2097152,0,0,90112,90112,7397888,20103168,21495808,23461888,12625920,18145280,13369344,20480,21504,5120,10004480,10008576,1966080,1536,2007040,62464,1320960,5120,22888448,2101248,160,0,
90112,90624,7439872,20128768,21557248,23523328,12646400,18146816,13370368,22016,21504,5440,10004992,10008704,1986560,1632,2048000,123904,1331200,5120,23134208,2179072,320,8192,91136,91136,7481856,20154368,21618688,23584768,12666880,18148352,
13371392,23040,24064,5760,10005504,10008832,2007040,1728,2088960,185344,1341440,5120,23379968,2183168,480,8192,91136,91648,7523840,20179968,21680128,23646208,12687360,18149888,13372416,24576,24064,6080,10006016,10008960,2027520,1824,
256,2457600,2,1966080,1280,4,3,3072,4096,1073741824,1,1065353216,116096,2704,41984,256,7829265,0,3600,3584,56,12943360,61952,41216,640,1280,1920,2816,72,128,128,112,
1966080,3072,1474560,5120,22663168,7360512,0,0,90112,90112,7397632,20103168,21495808,23461888,12953600,18931712,13385728,20480,21504,5120,10332160,10336256,2293760,1536,2007040,64512,1484800,5120,22908928,7361536,160,0,
90112,90624,7439616,20128768,21557248,23523328,12974080,18933248,13386752,22016,21504,5440,10332672,10336384,2314240,1632,2048000,125952,1495040,5120,23154688,7444480,320,8192,91136,91136,7481600,20154368,21618688,23584768,12994560,18934784,
13387776,23040,24064,5760,10333184,10336512,2334720,1728,2088960,187392,1505280,5120,23400448,7445504,480,8192,91136,91648,7523584,20179968,21680128,23646208,13015040,18936320,13388800,24576,24064,6080,10333696,10336640,2355200,1824,
128,2457600,0,819200,1280,4,3,3072,4096,1073741824,3,1065353216,115712,1288,40960,1024,65793,0,20,0,0,10649600,33408,41344,800,1280,1920,2816,72,128,128,0,
0,1280,103040,10240,20113408,2719744,0,0,65536,65536,5382144,20205568,21741568,23707648,10662400,13432832,13271040,25600,26624,6400,8040448,8043008,0,1920,40960,62720,108160,10240,20215808,2723840,160,0,
65536,66048,5424128,20231168,21803008,23769088,10682880,13434368,13272064,27136,26624,6720,8040960,8043136,20480,2016,81920,124160,128640,10240,20318208,2801664,320,8192,66560,66560,5466112,20256768,21864448,23830528,10703360,13435904,
13273088,28160,29184,7040,8041472,8043264,40960,2112,122880,185600,133760,10240,20420608,2805760,480,8192,66560,67072,5508096,20282368,21925888,23891968,10723840,13437440,13274112,29696,29184,7360,8041984,8043392,61440,2208,
768,2457600,0,819200,1280,4,3,3072,4096,1073741824,1,1065353216,115328,1304,41984,256,460545,0,532,512,8,10977280,37504,41344,800,1280,1920,2816,72,128,128,16,
0,3328,256640,10240,20133888,5347328,0,0,65536,65536,5382528,20205568,21741568,23707648,10990080,14219264,13287424,25600,26624,6400,8368128,8370688,327680,1920,40960,64768,261760,10240,20236288,5348352,160,0,
65536,66048,5424512,20231168,21803008,23769088,11010560,14220800,13288448,27136,26624,6720,8368640,8370816,348160,2016,81920,126208,282240,10240,20338688,5431296,320,8192,66560,66560,5466496,20256768,21864448,23830528,11031040,14222336,
13289472,28160,29184,7040,8369152,8370944,368640,2112,122880,187648,287360,10240,20441088,5432320,480,8192,66560,67072,5508480,20282368,21925888,23891968,11051520,14223872,13290496,29696,29184,7360,8369664,8371072,389120,2208,
384,2457600,1,1966080,3072,3,4,61440,62464,63488,3,1065353216,114944,1816,40960,1024,460545,0,1044,1024,16,11304960,41600,41344,800,1280,1920,2816,96,104,160,32,
655360,1280,590592,12288,21022720,3375104,0,0,73728,73728,6053888,20205568,21741568,23707648,11317760,15005696,13303808,25600,26624,6400,8695808,8698368,655360,1920,696320,62720,615168,12288,21145600,3379200,192,0,
73728,74240,6095872,20231168,21803008,23769088,11338240,15007232,13304832,27136,26624,6720,8696320,8698496,675840,2016,737280,124160,639744,12288,21268480,3457024,384,8192,74752,74752,6137856,20256768,21864448,23830528,11358720,15008768,
13305856,28160,29184,7040,8696832,8698624,696320,2112,778240,185600,664320,12288,21391360,3461120,576,8192,74752,75264,6179840,20282368,21925888,23891968,11379200,15010304,13306880,29696,29184,7360,8697344,8698752,716800,2208,
1024,2457600,1,1966080,3072,3,4,61440,62464,63488,1,1065353216,114560,1824,41984,256,460545,0,1556,1536,24,11632640,45696,41344,800,1280,1920,2816,96,104,160,48,
655360,3328,787200,12288,21028864,6019072,0,0,73728,73728,6054272,20205568,21741568,23707648,11645440,15792128,13320192,25600,26624,6400,9023488,9026048,983040,1920,696320,64768,811776,12288,21151744,6020096,192,0,
73728,74240,6096256,20231168,21803008,23769088,11665920,15793664,13321216,27136,26624,6720,9024000,9026176,1003520,2016,737280,126208,836352,12288,21274624,6103040,384,8192,74752,74752,6138240,20256768,21864448,23830528,11686400,15795200,
13322240,28160,29184,7040,9024512,9026304,1024000,2112,778240,187648,860928,12288,21397504,6104064,576,8192,74752,75264,6180224,20282368,21925888,23891968,11706880,15796736,13323264,29696,29184,7360,9025024,9026432,1044480,2208,
768,2457600,2,1966080,1280,4,3,3072,4096,1073741824,3,1065353216,114176,2824,40960,1024,460545,0,2068,2048,32,11960320,49792,41344,800,1280,1920,2816,72,128,128,64,
1310720,1280,82560,5120,23472128,4030464,0,0,81920,81920,6725632,20205568,21741568,23707648,11973120,16578560,13336576,25600,26624,6400,9351168,9353728,1310720,1920,1351680,62720,92800,5120,23717888,4034560,160,0,
81920,82432,6767616,20231168,21803008,23769088,11993600,16580096,13337600,27136,26624,6720,9351680,9353856,1331200,2016,1392640,124160,103040,5120,23963648,4112384,320,8192,82944,82944,6809600,20256768,21864448,23830528,12014080,16581632,
13338624,28160,29184,7040,9352192,9353984,1351680,2112,1433600,185600,113280,5120,24209408,4116480,480,8192,82944,83456,6851584,20282368,21925888,23891968,12034560,16583168,13339648,29696,29184,7360,9352704,9354112,1372160,2208,
128,2457600,2,1966080,1280,4,3,3072,4096,1073741824,1,1065353216,113792,2840,41984,256,460545,0,2580,2560,40,12288000,53888,41344,800,1280,1920,2816,72,128,128,80,
1310720,3328,246400,5120,23492608,6690816,0,0,81920,81920,6726016,20205568,21741568,23707648,12300800,17364992,13352960,25600,26624,6400,9678848,9681408,1638400,1920,1351680,64768,256640,5120,23738368,6691840,160,0,
81920,82432,6768000,20231168,21803008,23769088,12321280,17366528,13353984,27136,26624,6720,9679360,9681536,1658880,2016,1392640,126208,266880,5120,23984128,6774784,320,8192,82944,82944,6809984,20256768,21864448,23830528,12341760,17368064,
13355008,28160,29184,7040,9679872,9681664,1679360,2112,1433600,187648,277120,5120,24229888,6775808,480,8192,82944,83456,6851968,20282368,21925888,23891968,12362240,17369600,13356032,29696,29184,7360,9680384,9681792,1699840,2208,
1024,2457600,2,1966080,1280,4,3,3072,4096,1073741824,3,1065353216,113408,2944,40960,1024,460545,0,3092,3072,48,12615680,57984,41344,800,1280,1920,2816,72,128,128,96,
1966080,1280,1311360,5120,23625728,4685824,0,0,90112,90112,7397376,20205568,21741568,23707648,12628480,18151424,13369344,25600,26624,6400,10006528,10009088,1966080,1920,2007040,62720,1321600,5120,23871488,4689920,160,0,
90112,90624,7439360,20231168,21803008,23769088,12648960,18152960,13370368,27136,26624,6720,10007040,10009216,1986560,2016,2048000,124160,1331840,5120,24117248,4767744,320,8192,91136,91136,7481344,20256768,21864448,23830528,12669440,18154496,
13371392,28160,29184,7040,10007552,10009344,2007040,2112,2088960,185600,1342080,5120,24363008,4771840,480,8192,91136,91648,7523328,20282368,21925888,23891968,12689920,18156032,13372416,29696,29184,7360,10008064,10009472,2027520,2208,
384,2457600,2,1966080,1280,4,3,3072,4096,1073741824,1,1065353216,113024,2960,41984,256,460545,0,3604,3584,56,12943360,62080,41344,800,1280,1920,2816,72,128,128,112,
1966080,3328,1475200,5120,23646208,7362560,0,0,90112,90112,7397760,20205568,21741568,23707648,12956160,18937856,13385728,25600,26624,6400,10334208,10336768,2293760,1920,2007040,64768,1485440,5120,23891968,7363584,160,0,
90112,90624,7439744,20231168,21803008,23769088,12976640,18939392,13386752,27136,26624,6720,10334720,10336896,2314240,2016,2048000,126208,1495680,5120,24137728,7446528,320,8192,91136,91136,7481728,20256768,21864448,23830528,12997120,18940928,
13387776,28160,29184,7040,10335232,10337024,2334720,2112,2088960,187648,1505920,5120,24383488,7447552,480,8192,91136,91648,7523712,20282368,21925888,23891968,13017600,18942464,13388800,29696,29184,7360,10335744,10337152,2355200,2208,
0,2457600,0,819200,1280,4,3,3072,4096,1073741824,3,1065353216,112640,1036,40960,1024,1118481,0,24,0,0,10649600,33536,41344,960,1280,2176,2816,72,128,128,0,
0,1536,153600,10240,19708928,2727936,0,0,65536,65536,5382272,20307968,21987328,23953408,10664960,13438976,13271040,30720,31744,7680,8043520,8047616,0,2304,40960,62976,158720,10240,19811328,2732032,160,0,
65536,66048,5424256,20333568,22048768,24014848,10685440,13440512,13272064,32256,31744,8000,8044032,8047744,20480,2400,81920,124416,179200,10240,19913728,2809856,320,8192,66560,66560,5466240,20359168,22110208,24076288,10705920,13442048,
13273088,33280,34304,8320,8044544,8047872,40960,2496,122880,185856,184320,10240,20016128,2813952,480,8192,66560,67072,5508224,20384768,22171648,24137728,10726400,13443584,13274112,34816,34304,8640,8045056,8048000,61440,2592,
640,2457600,0,819200,1280,4,3,3072,4096,1073741824,1,1065353216,112256,1052,41984,256,7829265,0,536,512,8,10977280,37632,41344,960,1280,2176,2816,72,128,128,16,
0,3584,307200,10240,19729408,5349376,0,0,65536,65536,5382656,20307968,21987328,23953408,10992640,14225408,13287424,30720,31744,7680,8371200,8375296,327680,2304,40960,65024,312320,10240,19831808,5350400,160,0,
65536,66048,5424640,20333568,22048768,24014848,11013120,14226944,13288448,32256,31744,8000,8371712,8375424,348160,2400,81920,126464,332800,10240,19934208,5433344,320,8192,66560,66560,5466624,20359168,22110208,24076288,11033600,14228480,
13289472,33280,34304,8320,8372224,8375552,368640,2496,122880,187904,337920,10240,20036608,5434368,480,8192,66560,67072,5508608,20384768,22171648,24137728,11054080,14230016,13290496,34816,34304,8640,8372736,8375680,389120,2592,
256,2457600,1,1966080,3072,3,4,61440,62464,63488,3,1065353216,111872,2072,40960,1024,7829265,0,1048,1024,16,11304960,41728,41344,960,1280,2176,2816,96,104,160,32,
655360,1536,591360,12288,21514240,3383296,0,0,73728,73728,6054016,20307968,21987328,23953408,11320320,15011840,13303808,30720,31744,7680,8698880,8702976,655360,2304,696320,62976,615936,12288,21637120,3387392,192,0,
73728,74240,6096000,20333568,22048768,24014848,11340800,15013376,13304832,32256,31744,8000,8699392,8703104,675840,2400,737280,124416,640512,12288,21760000,3465216,384,8192,74752,74752,6137984,20359168,22110208,24076288,11361280,15014912,
13305856,33280,34304,8320,8699904,8703232,696320,2496,778240,185856,665088,12288,21882880,3469312,576,8192,74752,75264,6179968,20384768,22171648,24137728,11381760,15016448,13306880,34816,34304,8640,8700416,8703360,716800,2592,
896,2457600,1,1966080,3072,3,4,61440,62464,63488,1,1065353216,111488,2080,41984,256,7829265,0,1560,1536,24,11632640,45824,41344,960,1280,2176,2816,96,104,160,48,
655360,3584,787968,12288,21520384,6021120,0,0,73728,73728,6054400,20307968,21987328,23953408,11648000,15798272,13320192,30720,31744,7680,9026560,9030656,983040,2304,696320,65024,812544,12288,21643264,6022144,192,0,
73728,74240,6096384,20333568,22048768,24014848,11668480,15799808,13321216,32256,31744,8000,9027072,9030784,1003520,2400,737280,126464,837120,12288,21766144,6105088,384,8192,74752,74752,6138368,20359168,22110208,24076288,11688960,15801344,
13322240,33280,34304,8320,9027584,9030912,1024000,2496,778240,187904,861696,12288,21889024,6106112,576,8192,74752,75264,6180352,20384768,22171648,24137728,11709440,15802880,13323264,34816,34304,8640,9028096,9031040,1044480,2592,
640,2457600,2,1966080,1280,4,3,3072,4096,1073741824,3,1065353216,111104,2572,40960,1024,7829265,0,2072,2048,32,11960320,49920,41344,960,1280,2176,2816,72,128,128,64,
1310720,1536,122880,5120,22494208,4038656,0,0,81920,81920,6725760,20307968,21987328,23953408,11975680,16584704,13336576,30720,31744,7680,9354240,9358336,1310720,2304,1351680,62976,133120,5120,22739968,4042752,160,0,
81920,82432,6767744,20333568,22048768,24014848,11996160,16586240,13337600,32256,31744,8000,9354752,9358464,1331200,2400,1392640,124416,143360,5120,22985728,4120576,320,8192,82944,82944,6809728,20359168,22110208,24076288,12016640,16587776,
13338624,33280,34304,8320,9355264,9358592,1351680,2496,1433600,185856,153600,5120,23231488,4124672,480,8192,82944,83456,6851712,20384768,22171648,24137728,12037120,16589312,13339648,34816,34304,8640,9355776,9358720,1372160,2592,
0,2457600,2,1966080,1280,4,3,3072,4096,1073741824,1,1065353216,110720,2588,41984,256,7829265,0,2584,2560,40,12288000,54016,41344,960,1280,2176,2816,72,128,128,80,
1310720,3584,286720,5120,22514688,6692864,0,0,81920,81920,6726144,20307968,21987328,23953408,12303360,17371136,13352960,30720,31744,7680,9681920,9686016,1638400,2304,1351680,65024,296960,5120,22760448,6693888,160,0,
81920,82432,6768128,20333568,22048768,24014848,12323840,17372672,13353984,32256,31744,8000,9682432,9686144,1658880,2400,1392640,126464,307200,5120,23006208,6776832,320,8192,82944,82944,6810112,20359168,22110208,24076288,12344320,17374208,
13355008,33280,34304,8320,9682944,9686272,1679360,2496,1433600,187904,317440,5120,23251968,6777856,480,8192,82944,83456,6852096,20384768,22171648,24137728,12364800,17375744,13356032,34816,34304,8640,9683456,9686400,1699840,2592,
896,2457600,2,1966080,1280,4,3,3072,4096,1073741824,3,1065353216,110336,2692,40960,1024,7829265,0,3096,3072,48,12615680,58112,41344,960,1280,2176,2816,72,128,128,96,
1966080,1536,1351680,5120,22647808,4694016,0,0,90112,90112,7397504,20307968,21987328,23953408,12631040,18157568,13369344,30720,31744,7680,10009600,10013696,1966080,2304,2007040,62976,1361920,5120,22893568,4698112,160,0,
90112,90624,7439488,20333568,22048768,24014848,12651520,18159104,13370368,32256,31744,8000,10010112,10013824,1986560,2400,2048000,124416,1372160,5120,23139328,4775936,320,8192,91136,91136,7481472,20359168,22110208,24076288,12672000,18160640,
13371392,33280,34304,8320,10010624,10013952,2007040,2496,2088960,185856,1382400,5120,23385088,4780032,480,8192,91136,91648,7523456,20384768,22171648,24137728,12692480,18162176,13372416,34816,34304,8640,10011136,10014080,2027520,2592,
256,2457600,2,1966080,1280,4,3,3072,4096,1073741824,1,1065353216,109952,2708,41984,256,7829265,0,3608,3584,56,12943360,62208,41344,960,1280,2176,2816,72,128,128,112,
1966080,3584,1515520,5120,22668288,7364608,0,0,90112,90112,7397888,20307968,21987328,23953408,12958720,18944000,13385728,30720,31744,7680,10337280,10341376,2293760,2304,2007040,65024,1525760,5120,22914048,7365632,160,0,
90112,90624,7439872,20333568,22048768,24014848,12979200,18945536,13386752,32256,31744,8000,10337792,10341504,2314240,2400,2048000,126464,1536000,5120,23159808,7448576,320,8192,91136,91136,7481856,20359168,22110208,24076288,12999680,18947072,
13387776,33280,34304,8320,10338304,10341632,2334720,2496,2088960,187904,1546240,5120,23405568,7449600,480,8192,91136,91648,7523840,20384768,22171648,24137728,13020160,18948608,13388800,34816,34304,8640,10338816,10341760,2355200,2592,
128,2457600,0,819200,1280,4,3,3072,4096,1073741824,3,1065353216,109568,1292,40960,1024,65793,0,28,0,0,10649600,33664,41472,1120,1280,2176,2816,72,128,128,0,
0,1792,154240,10240,20118528,2736128,0,0,65536,65536,5382400,20410368,22233088,24199168,10667520,13445120,13271040,35840,36864,8960,8045568,8048128,0,2688,40960,63232,159360,10240,20220928,2740224,160,0,
65536,66048,5424384,20435968,22294528,24260608,10688000,13446656,13272064,37376,36864,9280,8046080,8048256,20480,2784,81920,124672,179840,10240,20323328,2818048,320,8192,66560,66560,5466368,20461568,22355968,24322048,10708480,13448192,
13273088,38400,39424,9600,8046592,8048384,40960,2880,122880,186112,184960,10240,20425728,2822144,480,8192,66560,67072,5508352,20487168,22417408,24383488,10728960,13449728,13274112,39936,39424,9920,8047104,8048512,61440,2976,
768,0,2,0,1280,4,3,3072,4096,1073741824,2,1065353216,109184,1073741824,40960,1024,460545,0,540,512,8,10977280,37760,41472,1120,1280,2176,2816,72,128,128,16,
0,3840,17982080,5120,1073741824,2719744,0,0,65536,65536,5382144,20410368,22233088,24199168,10995200,14231552,13287424,35840,36864,8960,8373248,8375808,327680,2688,40960,65280,17992320,5120,1073741824,2723840,160,4096,
65536,66048,5424128,20435968,22294528,24260608,11015680,14233088,13288448,37376,36864,9280,8373760,8375936,348160,2784,81920,126720,18002560,5120,1073741824,2801664,320,8192,66560,66560,5466112,20461568,22355968,24322048,11036160,14234624,
13289472,38400,39424,9600,8374272,8376064,368640,2880,122880,188160,18012800,5120,1073741824,2805760,480,12288,66560,67072,5508096,20487168,22417408,24383488,11056640,14236160,13290496,39936,39424,9920,8374784,8376192,389120,2976,
384,2457600,1,1966080,3072,3,4,61440,62464,63488,3,1065353216,108800,2328,40960,1024,460545,0,1052,1024,16,11304960,41856,41472,1120,1280,2176,2816,96,104,160,32,
655360,1792,592128,12288,22005760,3391488,0,0,73728,73728,6054144,20410368,22233088,24199168,11322880,15017984,13303808,35840,36864,8960,8700928,8703488,655360,2688,696320,63232,616704,12288,22128640,3395584,192,0,
73728,74240,6096128,20435968,22294528,24260608,11343360,15019520,13304832,37376,36864,9280,8701440,8703616,675840,2784,737280,124672,641280,12288,22251520,3473408,384,8192,74752,74752,6138112,20461568,22355968,24322048,11363840,15021056,
13305856,38400,39424,9600,8701952,8703744,696320,2880,778240,186112,665856,12288,22374400,3477504,576,8192,74752,75264,6180096,20487168,22417408,24383488,11384320,15022592,13306880,39936,39424,9920,8702464,8703872,716800,2976,
1024,0,2,0,1280,4,3,3072,4096,1073741824,2,1065353216,108416,1073741824,40960,1024,460545,0,1564,1536,24,11632640,45952,41472,1120,1280,2176,2816,72,128,128,48,
655360,3840,17982080,5120,1073741824,3375104,0,0,73728,73728,6053888,20410368,22233088,24199168,11650560,15804416,13320192,35840,36864,8960,9028608,9031168,983040,2688,696320,65280,17992320,5120,1073741824,3379200,160,4096,
73728,74240,6095872,20435968,22294528,24260608,11671040,15805952,13321216,37376,36864,9280,9029120,9031296,1003520,2784,737280,126720,18002560,5120,1073741824,3457024,320,8192,74752,74752,6137856,20461568,22355968,24322048,11691520,15807488,
13322240,38400,39424,9600,9029632,9031424,1024000,2880,778240,188160,18012800,5120,1073741824,3461120,480,12288,74752,75264,6179840,20487168,22417408,24383488,11712000,15809024,13323264,39936,39424,9920,9030144,9031552,1044480,2976,
768,2457600,2,1966080,1280,4,3,3072,4096,1073741824,3,1065353216,108032,2828,40960,1024,460545,0,2076,2048,32,11960320,50048,41472,1120,1280,2176,2816,72,128,128,64,
1310720,1792,123520,5120,23477248,4046848,0,0,81920,81920,6725888,20410368,22233088,24199168,11978240,16590848,13336576,35840,36864,8960,9356288,9358848,1310720,2688,1351680,63232,133760,5120,23723008,4050944,160,0,
81920,82432,6767872,20435968,22294528,24260608,11998720,16592384,13337600,37376,36864,9280,9356800,9358976,1331200,2784,1392640,124672,144000,5120,23968768,4128768,320,8192,82944,82944,6809856,20461568,22355968,24322048,12019200,16593920,
13338624,38400,39424,9600,9357312,9359104,1351680,2880,1433600,186112,154240,5120,24214528,4132864,480,8192,82944,83456,6851840,20487168,22417408,24383488,12039680,16595456,13339648,39936,39424,9920,9357824,9359232,1372160,2976,
128,0,2,0,1280,4,3,3072,4096,1073741824,2,1065353216,107648,1073741824,40960,1024,460545,0,2588,2560,40,12288000,54144,41472,1120,1280,2176,2816,72,128,128,80,
1310720,3840,17982080,5120,1073741824,4030464,0,0,81920,81920,6725632,20410368,22233088,24199168,12305920,17377280,13352960,35840,36864,8960,9683968,9686528,1638400,2688,1351680,65280,17992320,5120,1073741824,4034560,160,4096,
81920,82432,6767616,20435968,22294528,24260608,12326400,17378816,13353984,37376,36864,9280,9684480,9686656,1658880,2784,1392640,126720,18002560,5120,1073741824,4112384,320,8192,82944,82944,6809600,20461568,22355968,24322048,12346880,17380352,
13355008,38400,39424,9600,9684992,9686784,1679360,2880,1433600,188160,18012800,5120,1073741824,4116480,480,12288,82944,83456,6851584,20487168,22417408,24383488,12367360,17381888,13356032,39936,39424,9920,9685504,9686912,1699840,2976,
1024,2457600,2,1966080,1280,4,3,3072,4096,1073741824,3,1065353216,107264,2948,40960,1024,460545,0,3100,3072,48,12615680,58240,41472,1120,1280,2176,2816,72,128,128,96,
1966080,1792,1352320,5120,23630848,4702208,0,0,90112,90112,7397632,20410368,22233088,24199168,12633600,18163712,13369344,35840,36864,8960,10011648,10014208,1966080,2688,2007040,63232,1362560,5120,23876608,4706304,160,0,
90112,90624,7439616,20435968,22294528,24260608,12654080,18165248,13370368,37376,36864,9280,10012160,10014336,1986560,2784,2048000,124672,1372800,5120,24122368,4784128,320,8192,91136,91136,7481600,20461568,22355968,24322048,12674560,18166784,
13371392,38400,39424,9600,10012672,10014464,2007040,2880,2088960,186112,1383040,5120,24368128,4788224,480,8192,91136,91648,7523584,20487168,22417408,24383488,12695040,18168320,13372416,39936,39424,9920,10013184,10014592,2027520,2976,
384,0,2,0,1280,4,3,3072,4096,1073741824,2,1065353216,106880,1073741824,40960,1024,460545,0,3612,3584,56,12943360,62336,41472,1120,1280,2176,2816,72,128,128,112,
1966080,3840,17982080,5120,1073741824,4685824,0,0,90112,90112,7397376,20410368,22233088,24199168,12961280,18950144,13385728,35840,36864,8960,10339328,10341888,2293760,2688,2007040,65280,17992320,5120,1073741824,4689920,160,4096,
90112,90624,7439360,20435968,22294528,24260608,12981760,18951680,13386752,37376,36864,9280,10339840,10342016,2314240,2784,2048000,126720,18002560,5120,1073741824,4767744,320,8192,91136,91136,7481344,20461568,22355968,24322048,13002240,18953216,
13387776,38400,39424,9600,10340352,10342144,2334720,2880,2088960,188160,18012800,5120,1073741824,4771840,480,12288,91136,91648,7523328,20487168,22417408,24383488,13022720,18954752,13388800,39936,39424,9920,10340864,10342272,2355200,2976,
128,2457600,0,819200,1280,4,3,3072,4096,1073741824,0,1051011932,106496,1308,40960,1024,65793,0,128,128,2,10731520,33792,40960,0,1024,1408,2560,72,128,128,4,
163840,0,307840,10240,20139008,262144,0,0,67584,67584,5550080,19693568,20512768,22478848,10731520,13598720,13275136,0,1024,0,8110080,8114176,81920,0,204800,61440,312960,10240,20241408,266240,160,0,
67584,68096,5592064,19719168,20574208,22540288,10752000,13600256,13276160,1536,1024,320,8110592,8114304,102400,96,245760,122880,333440,10240,20343808,344064,320,8192,68608,68608,5634048,19744768,20635648,22601728,10772480,13601792,
13277184,2560,3584,640,8111104,8114432,122880,192,286720,184320,338560,10240,20446208,348160,480,8192,68608,69120,5676032,19770368,20697088,22663168,10792960,13603328,13278208,4096,3584,960,8111616,8114560,143360,288,
768,2457600,0,819200,1280,4,3,3072,4096,1073741824,3,1065353216,106112,1324,40960,1024,460545,0,640,640,10,11059200,37888,40960,0,1024,1408,2560,72,128,128,20,
163840,2048,461440,10240,20159488,2908160,0,0,67584,67584,5550464,19693568,20512768,22478848,11059200,14385152,13291520,0,1024,0,8437760,8441856,409600,0,204800,63488,466560,10240,20261888,2912256,160,0,
67584,68096,5592448,19719168,20574208,22540288,11079680,14386688,13292544,1536,1024,320,8438272,8441984,430080,96,245760,124928,487040,10240,20364288,2990080,320,8192,68608,68608,5634432,19744768,20635648,22601728,11100160,14388224,
13293568,2560,3584,640,8438784,8442112,450560,192,286720,186368,492160,10240,20466688,2994176,480,8192,68608,69120,5676416,19770368,20697088,22663168,11120640,14389760,13294592,4096,3584,960,8439296,8442240,471040,288,
384,2457600,1,1966080,3072,3,4,61440,62464,63488,0,1051011932,105728,2336,40960,1024,460545,0,1152,1152,18,11386880,41984,40960,0,1024,1408,2560,96,104,160,36,
819200,0,788736,12288,22011904,917504,0,0,75776,75776,6221824,19693568,20512768,22478848,11386880,15171584,13307904,0,1024,0,8765440,8769536,737280,0,860160,61440,813312,12288,22134784,921600,192,0,
75776,76288,6263808,19719168,20574208,22540288,11407360,15173120,13308928,1536,1024,320,8765952,8769664,757760,96,901120,122880,837888,12288,22257664,999424,384,8192,76800,76800,6305792,19744768,20635648,22601728,11427840,15174656,
13309952,2560,3584,640,8766464,8769792,778240,192,942080,184320,862464,12288,22380544,1003520,576,8192,76800,77312,6347776,19770368,20697088,22663168,11448320,15176192,13310976,4096,3584,960,8766976,8769920,798720,288,
1024,2457600,1,1966080,3072,3,4,61440,62464,63488,3,1065353216,105344,2344,40960,1024,460545,0,1664,1664,26,11714560,46080,40960,0,1024,1408,2560,96,104,160,52,
819200,2048,985344,12288,22018048,3563520,0,0,75776,75776,6222208,19693568,20512768,22478848,11714560,15958016,13324288,0,1024,0,9093120,9097216,1064960,0,860160,63488,1009920,12288,22140928,3567616,192,0,
75776,76288,6264192,19719168,20574208,22540288,11735040,15959552,13325312,1536,1024,320,9093632,9097344,1085440,96,901120,124928,1034496,12288,22263808,3645440,384,8192,76800,76800,6306176,19744768,20635648,22601728,11755520,15961088,
13326336,2560,3584,640,9094144,9097472,1105920,192,942080,186368,1059072,12288,22386688,3649536,576,8192,76800,77312,6348160,19770368,20697088,22663168,11776000,15962624,13327360,4096,3584,960,9094656,9097600,1126400,288,
768,2457600,2,1966080,1280,4,3,3072,4096,1073741824,0,1051011932,104960,2844,40960,1024,460545,0,2176,2176,34,12042240,50176,40960,0,1024,1408,2560,72,128,128,68,
1474560,0,287360,5120,23497728,1572864,0,0,83968,83968,6893568,19693568,20512768,22478848,12042240,16744448,13340672,0,1024,0,9420800,9424896,1392640,0,1515520,61440,297600,5120,23743488,1576960,160,0,
83968,84480,6935552,19719168,20574208,22540288,12062720,16745984,13341696,1536,1024,320,9421312,9425024,1413120,96,1556480,122880,307840,5120,23989248,1654784,320,8192,84992,84992,6977536,19744768,20635648,22601728,12083200,16747520,
13342720,2560,3584,640,9421824,9425152,1433600,192,1597440,184320,318080,5120,24235008,1658880,480,8192,84992,85504,7019520,19770368,20697088,22663168,12103680,16749056,13343744,4096,3584,960,9422336,9425280,1454080,288,
128,2457600,2,1966080,1280,4,3,3072,4096,1073741824,3,1065353216,104576,2860,40960,1024,460545,0,2688,2688,42,12369920,54272,40960,0,1024,1408,2560,72,128,128,84,
1474560,2048,451200,5120,23518208,4218880,0,0,83968,83968,6893952,19693568,20512768,22478848,12369920,17530880,13357056,0,1024,0,9748480,9752576,1720320,0,1515520,63488,461440,5120,23763968,4222976,160,0,
83968,84480,6935936,19719168,20574208,22540288,12390400,17532416,13358080,1536,1024,320,9748992,9752704,1740800,96,1556480,124928,471680,5120,24009728,4300800,320,8192,84992,84992,6977920,19744768,20635648,22601728,12410880,17533952,
13359104,2560,3584,640,9749504,9752832,1761280,192,1597440,186368,481920,5120,24255488,4304896,480,8192,84992,85504,7019904,19770368,20697088,22663168,12431360,17535488,13360128,4096,3584,960,9750016,9752960,1781760,288,
1024,2457600,2,1966080,1280,4,3,3072,4096,1073741824,0,1051011932,104192,2964,40960,1024,460545,0,3200,3200,50,12697600,58368,40960,0,1024,1408,2560,72,128,128,100,
2129920,0,1516160,5120,23651328,2228224,0,0,92160,92160,7565312,19693568,20512768,22478848,12697600,18317312,13373440,0,1024,0,10076160,10080256,2048000,0,2170880,61440,1526400,5120,23897088,2232320,160,0,
92160,92672,7607296,19719168,20574208,22540288,12718080,18318848,13374464,1536,1024,320,10076672,10080384,2068480,96,2211840,122880,1536640,5120,24142848,2310144,320,8192,93184,93184,7649280,19744768,20635648,22601728,12738560,18320384,
13375488,2560,3584,640,10077184,10080512,2088960,192,2252800,184320,1546880,5120,24388608,2314240,480,8192,93184,93696,7691264,19770368,20697088,22663168,12759040,18321920,13376512,4096,3584,960,10077696,10080640,2109440,288,
384,2457600,2,1966080,1280,4,3,3072,4096,1073741824,3,1065353216,103808,2980,40960,1024,460545,0,3712,3712,58,13025280,62464,40960,0,1024,1408,2560,72,128,128,116,
2129920,2048,1680000,5120,23671808,4874240,0,0,92160,92160,7565696,19693568,20512768,22478848,13025280,19103744,13389824,0,1024,0,10403840,10407936,2375680,0,2170880,63488,1690240,5120,23917568,4878336,160,0,
92160,92672,7607680,19719168,20574208,22540288,13045760,19105280,13390848,1536,1024,320,10404352,10408064,2396160,96,2211840,124928,1700480,5120,24163328,4956160,320,8192,93184,93184,7649664,19744768,20635648,22601728,13066240,19106816,
13391872,2560,3584,640,10404864,10408192,2416640,192,2252800,186368,1710720,5120,24409088,4960256,480,8192,93184,93696,7691648,19770368,20697088,22663168,13086720,19108352,13392896,4096,3584,960,10405376,10408320,2437120,288,
256,2457600,0,819200,1280,4,3,3072,4096,1073741824,0,1051011932,103424,1056,40960,1024,1118481,0,132,128,2,10731520,33920,40960,160,1024,1408,2560,72,128,128,4,
163840,256,358400,10240,19734528,270336,0,0,67584,67584,5550208,19795968,20758528,22724608,10734080,13604864,13275136,5120,6144,1280,8112128,8114688,81920,384,204800,61696,363520,10240,19836928,274432,160,0,
67584,68096,5592192,19821568,20819968,22786048,10754560,13606400,13276160,6656,6144,1600,8112640,8114816,102400,480,245760,123136,384000,10240,19939328,352256,320,8192,68608,68608,5634176,19847168,20881408,22847488,10775040,13607936,
13277184,7680,8704,1920,8113152,8114944,122880,576,286720,184576,389120,10240,20041728,356352,480,8192,68608,69120,5676160,19872768,20942848,22908928,10795520,13609472,13278208,9216,8704,2240,8113664,8115072,143360,672,
896,2457600,0,819200,1280,4,3,3072,4096,1073741824,3,1065353216,103040,1072,40960,1024,7829265,0,644,640,10,11059200,38016,40960,160,1024,1408,2560,72,128,128,20,
163840,2304,512000,10240,19755008,2916352,0,0,67584,67584,5550592,19795968,20758528,22724608,11061760,14391296,13291520,5120,6144,1280,8439808,8442368,409600,384,204800,63744,517120,10240,19857408,2920448,160,0,
67584,68096,5592576,19821568,20819968,22786048,11082240,14392832,13292544,6656,6144,1600,8440320,8442496,430080,480,245760,125184,537600,10240,19959808,2998272,320,8192,68608,68608,5634560,19847168,20881408,22847488,11102720,14394368,
13293568,7680,8704,1920,8440832,8442624,450560,576,286720,186624,542720,10240,20062208,3002368,480,8192,68608,69120,5676544,19872768,20942848,22908928,11123200,14395904,13294592,9216,8704,2240,8441344,8442752,471040,672,
512,2457600,1,1966080,3072,3,4,61440,62464,63488,0,1051011932,102656,1572,40960,1024,7829265,0,1156,1152,18,11386880,42112,40960,160,1024,1408,2560,96,104,160,36,
819200,256,884736,12288,20540416,925696,0,0,75776,75776,6221952,19795968,20758528,22724608,11389440,15177728,13307904,5120,6144,1280,8767488,8770048,737280,384,860160,61696,909312,12288,20663296,929792,192,0,
75776,76288,6263936,19821568,20819968,22786048,11409920,15179264,13308928,6656,6144,1600,8768000,8770176,757760,480,901120,123136,933888,12288,20786176,1007616,384,8192,76800,76800,6305920,19847168,20881408,22847488,11430400,15180800,
13309952,7680,8704,1920,8768512,8770304,778240,576,942080,184576,958464,12288,20909056,1011712,576,8192,76800,77312,6347904,19872768,20942848,22908928,11450880,15182336,13310976,9216,8704,2240,8769024,8770432,798720,672,
1152,2457600,1,1966080,3072,3,4,61440,62464,63488,3,1065353216,102272,1580,40960,1024,7829265,0,1668,1664,26,11714560,46208,40960,160,1024,1408,2560,96,104,160,52,
819200,2304,1081344,12288,20546560,3571712,0,0,75776,75776,6222336,19795968,20758528,22724608,11717120,15964160,13324288,5120,6144,1280,9095168,9097728,1064960,384,860160,63744,1105920,12288,20669440,3575808,192,0,
75776,76288,6264320,19821568,20819968,22786048,11737600,15965696,13325312,6656,6144,1600,9095680,9097856,1085440,480,901120,125184,1130496,12288,20792320,3653632,384,8192,76800,76800,6306304,19847168,20881408,22847488,11758080,15967232,
13326336,7680,8704,1920,9096192,9097984,1105920,576,942080,186624,1155072,12288,20915200,3657728,576,8192,76800,77312,6348288,19872768,20942848,22908928,11778560,15968768,13327360,9216,8704,2240,9096704,9098112,1126400,672,
896,2457600,2,1966080,1280,4,3,3072,4096,1073741824,0,1051011932,101888,2592,40960,1024,7829265,0,2180,2176,34,12042240,50304,40960,160,1024,1408,2560,72,128,128,68,
1474560,256,327680,5120,22519808,1581056,0,0,83968,83968,6893696,19795968,20758528,22724608,12044800,16750592,13340672,5120,6144,1280,9422848,9425408,1392640,384,1515520,61696,337920,5120,22765568,1585152,160,0,
83968,84480,6935680,19821568,20819968,22786048,12065280,16752128,13341696,6656,6144,1600,9423360,9425536,1413120,480,1556480,123136,348160,5120,23011328,1662976,320,8192,84992,84992,6977664,19847168,20881408,22847488,12085760,16753664,
13342720,7680,8704,1920,9423872,9425664,1433600,576,1597440,184576,358400,5120,23257088,1667072,480,8192,84992,85504,7019648,19872768,20942848,22908928,12106240,16755200,13343744,9216,8704,2240,9424384,9425792,1454080,672,
256,2457600,2,1966080,1280,4,3,3072,4096,1073741824,3,1065353216,101504,2608,40960,1024,7829265,0,2692,2688,42,12369920,54400,40960,160,1024,1408,2560,72,128,128,84,
1474560,2304,491520,5120,22540288,4227072,0,0,83968,83968,6894080,19795968,20758528,22724608,12372480,17537024,13357056,5120,6144,1280,9750528,9753088,1720320,384,1515520,63744,501760,5120,22786048,4231168,160,0,
83968,84480,6936064,19821568,20819968,22786048,12392960,17538560,13358080,6656,6144,1600,9751040,9753216,1740800,480,1556480,125184,512000,5120,23031808,4308992,320,8192,84992,84992,6978048,19847168,20881408,22847488,12413440,17540096,
13359104,7680,8704,1920,9751552,9753344,1761280,576,1597440,186624,522240,5120,23277568,4313088,480,8192,84992,85504,7020032,19872768,20942848,22908928,12433920,17541632,13360128,9216,8704,2240,9752064,9753472,1781760,672,
1152,2457600,2,1966080,1280,4,3,3072,4096,1073741824,0,1051011932,101120,2712,40960,1024,7829265,0,3204,3200,50,12697600,58496,40960,160,1024,1408,2560,72,128,128,100,
2129920,256,1556480,5120,22673408,2236416,0,0,92160,92160,7565440,19795968,20758528,22724608,12700160,18323456,13373440,5120,6144,1280,10078208,10080768,2048000,384,2170880,61696,1566720,5120,22919168,2240512,160,0,
92160,92672,7607424,19821568,20819968,22786048,12720640,18324992,13374464,6656,6144,1600,10078720,10080896,2068480,480,2211840,123136,1576960,5120,23164928,2318336,320,8192,93184,93184,7649408,19847168,20881408,22847488,12741120,18326528,
13375488,7680,8704,1920,10079232,10081024,2088960,576,2252800,184576,1587200,5120,23410688,2322432,480,8192,93184,93696,7691392,19872768,20942848,22908928,12761600,18328064,13376512,9216,8704,2240,10079744,10081152,2109440,672,
512,2457600,2,1966080,1280,4,3,3072,4096,1073741824,3,1065353216,100736,2728,40960,1024,7829265,0,3716,3712,58,13025280,62592,40960,160,1024,1408,2560,72,128,128,116,
2129920,2304,1720320,5120,22693888,4882432,0,0,92160,92160,7565824,19795968,20758528,22724608,13027840,19109888,13389824,5120,6144,1280,10405888,10408448,2375680,384,2170880,63744,1730560,5120,22939648,4886528,160,0,
92160,92672,7607808,19821568,20819968,22786048,13048320,19111424,13390848,6656,6144,1600,10406400,10408576,2396160,480,2211840,125184,1740800,5120,23185408,4964352,320,8192,93184,93184,7649792,19847168,20881408,22847488,13068800,19112960,
13391872,7680,8704,1920,10406912,10408704,2416640,576,2252800,186624,1751040,5120,23431168,4968448,480,8192,93184,93696,7691776,19872768,20942848,22908928,13089280,19114496,13392896,9216,8704,2240,10407424,10408832,2437120,672,
128,2457600,0,819200,1280,4,3,3072,4096,1073741824,0,1051011932,100352,1312,40960,1024,65793,0,136,128,2,10731520,34048,41088,320,1024,1664,2560,72,128,128,4,
163840,512,359040,10240,20144128,278528,0,0,67584,67584,5550336,19898368,21004288,22970368,10736640,13611008,13275136,10240,11264,2560,8115200,8119296,81920,768,204800,61952,364160,10240,20246528,282624,160,0,
67584,68096,5592320,19923968,21065728,23031808,10757120,13612544,13276160,11776,11264,2880,8115712,8119424,102400,864,245760,123392,384640,10240,20348928,360448,320,8192,68608,68608,5634304,19949568,21127168,23093248,10777600,13614080,
13277184,12800,13824,3200,8116224,8119552,122880,960,286720,184832,389760,10240,20451328,364544,480,8192,68608,69120,5676288,19975168,21188608,23154688,10798080,13615616,13278208,14336,13824,3520,8116736,8119680,143360,1056,
768,2457600,0,819200,1280,4,3,3072,4096,1073741824,1,1065353216,99968,1328,41984,256,460545,0,648,640,10,11059200,38144,41088,320,1024,1664,2560,72,128,128,20,
163840,2560,512640,10240,20164608,5509120,0,0,67584,67584,5550080,19898368,21004288,22970368,11064320,14397440,13291520,10240,11264,2560,8442880,8446976,409600,768,204800,64000,517760,10240,20267008,5510144,160,0,
67584,68096,5592064,19923968,21065728,23031808,11084800,14398976,13292544,11776,11264,2880,8443392,8447104,430080,864,245760,125440,538240,10240,20369408,5593088,320,8192,68608,68608,5634048,19949568,21127168,23093248,11105280,14400512,
13293568,12800,13824,3200,8443904,8447232,450560,960,286720,186880,543360,10240,20471808,5594112,480,8192,68608,69120,5676032,19975168,21188608,23154688,11125760,14402048,13294592,14336,13824,3520,8444416,8447360,471040,1056,
384,2457600,1,1966080,3072,3,4,61440,62464,63488,0,1051011932,99584,1828,40960,1024,460545,0,1160,1152,18,11386880,42240,41088,320,1024,1664,2560,96,104,160,36,
819200,512,885504,12288,21031936,933888,0,0,75776,75776,6222080,19898368,21004288,22970368,11392000,15183872,13307904,10240,11264,2560,8770560,8774656,737280,768,860160,61952,910080,12288,21154816,937984,192,0,
75776,76288,6264064,19923968,21065728,23031808,11412480,15185408,13308928,11776,11264,2880,8771072,8774784,757760,864,901120,123392,934656,12288,21277696,1015808,384,8192,76800,76800,6306048,19949568,21127168,23093248,11432960,15186944,
13309952,12800,13824,3200,8771584,8774912,778240,960,942080,184832,959232,12288,21400576,1019904,576,8192,76800,77312,6348032,19975168,21188608,23154688,11453440,15188480,13310976,14336,13824,3520,8772096,8775040,798720,1056,
1024,2457600,1,1966080,3072,3,4,61440,62464,63488,1,1065353216,99200,1836,41984,256,460545,0,1672,1664,26,11714560,46336,41088,320,1024,1664,2560,96,104,160,52,
819200,2560,1082112,12288,21038080,6180864,0,0,75776,75776,6221824,19898368,21004288,22970368,11719680,15970304,13324288,10240,11264,2560,9098240,9102336,1064960,768,860160,64000,1106688,12288,21160960,6181888,192,0,
75776,76288,6263808,19923968,21065728,23031808,11740160,15971840,13325312,11776,11264,2880,9098752,9102464,1085440,864,901120,125440,1131264,12288,21283840,6264832,384,8192,76800,76800,6305792,19949568,21127168,23093248,11760640,15973376,
13326336,12800,13824,3200,9099264,9102592,1105920,960,942080,186880,1155840,12288,21406720,6265856,576,8192,76800,77312,6347776,19975168,21188608,23154688,11781120,15974912,13327360,14336,13824,3520,9099776,9102720,1126400,1056,
768,2457600,2,1966080,1280,4,3,3072,4096,1073741824,0,1051011932,98816,2848,40960,1024,460545,0,2184,2176,34,12042240,50432,41088,320,1024,1664,2560,72,128,128,68,
1474560,512,328320,5120,23502848,1589248,0,0,83968,83968,6893824,19898368,21004288,22970368,12047360,16756736,13340672,10240,11264,2560,9425920,9430016,1392640,768,1515520,61952,338560,5120,23748608,1593344,160,0,
83968,84480,6935808,19923968,21065728,23031808,12067840,16758272,13341696,11776,11264,2880,9426432,9430144,1413120,864,1556480,123392,348800,5120,23994368,1671168,320,8192,84992,84992,6977792,19949568,21127168,23093248,12088320,16759808,
13342720,12800,13824,3200,9426944,9430272,1433600,960,1597440,184832,359040,5120,24240128,1675264,480,8192,84992,85504,7019776,19975168,21188608,23154688,12108800,16761344,13343744,14336,13824,3520,9427456,9430400,1454080,1056,
128,2457600,2,1966080,1280,4,3,3072,4096,1073741824,1,1065353216,98432,2864,41984,256,460545,0,2696,2688,42,12369920,54528,41088,320,1024,1664,2560,72,128,128,84,
1474560,2560,492160,5120,23523328,6852608,0,0,83968,83968,6893568,19898368,21004288,22970368,12375040,17543168,13357056,10240,11264,2560,9753600,9757696,1720320,768,1515520,64000,502400,5120,23769088,6853632,160,0,
83968,84480,6935552,19923968,21065728,23031808,12395520,17544704,13358080,11776,11264,2880,9754112,9757824,1740800,864,1556480,125440,512640,5120,24014848,6936576,320,8192,84992,84992,6977536,19949568,21127168,23093248,12416000,17546240,
13359104,12800,13824,3200,9754624,9757952,1761280,960,1597440,186880,522880,5120,24260608,6937600,480,8192,84992,85504,7019520,19975168,21188608,23154688,12436480,17547776,13360128,14336,13824,3520,9755136,9758080,1781760,1056,
1024,2457600,2,1966080,1280,4,3,3072,4096,1073741824,0,1051011932,98048,2968,40960,1024,460545,0,3208,3200,50,12697600,58624,41088,320,1024,1664,2560,72,128,128,100,
2129920,512,1557120,5120,23656448,2244608,0,0,92160,92160,7565568,19898368,21004288,22970368,12702720,18329600,13373440,10240,11264,2560,10081280,10085376,2048000,768,2170880,61952,1567360,5120,23902208,2248704,160,0,
92160,92672,7607552,19923968,21065728,23031808,12723200,18331136,13374464,11776,11264,2880,10081792,10085504,2068480,864,2211840,123392,1577600,5120,24147968,2326528,320,8192,93184,93184,7649536,19949568,21127168,23093248,12743680,18332672,
13375488,12800,13824,3200,10082304,10085632,2088960,960,2252800,184832,1587840,5120,24393728,2330624,480,8192,93184,93696,7691520,19975168,21188608,23154688,12764160,18334208,13376512,14336,13824,3520,10082816,10085760,2109440,1056,
384,2457600,2,1966080,1280,4,3,3072,4096,1073741824,1,1065353216,97664,2984,41984,256,460545,0,3720,3712,58,13025280,62720,41088,320,1024,1664,2560,72,128,128,116,
2129920,2560,1720960,5120,23676928,7524352,0,0,92160,92160,7565312,19898368,21004288,22970368,13030400,19116032,13389824,10240,11264,2560,10408960,10413056,2375680,768,2170880,64000,1731200,5120,23922688,7525376,160,0,
92160,92672,7607296,19923968,21065728,23031808,13050880,19117568,13390848,11776,11264,2880,10409472,10413184,2396160,864,2211840,125440,1741440,5120,24168448,7608320,320,8192,93184,93184,7649280,19949568,21127168,23093248,13071360,19119104,
13391872,12800,13824,3200,10409984,10413312,2416640,960,2252800,186880,1751680,5120,24414208,7609344,480,8192,93184,93696,7691264,19975168,21188608,23154688,13091840,19120640,13392896,14336,13824,3520,10410496,10413440,2437120,1056,
256,2457600,0,819200,1280,4,3,3072,4096,1073741824,0,1051011932,97280,1060,40960,1024,1118481,0,140,128,2,10731520,34176,41088,480,1024,1664,2560,72,128,128,4,
163840,768,225280,25600,19739648,286720,0,0,67584,67584,5550464,20000768,21250048,23216128,10739200,13617152,13275136,15360,16384,3840,8117248,8119808,81920,1152,204800,62208,276480,25600,19842048,290816,160,0,
67584,68096,5592448,20026368,21311488,23277568,10759680,13618688,13276160,16896,16384,4160,8117760,8119936,102400,1248,245760,123648,327680,25600,19944448,368640,320,8192,68608,68608,5634432,20051968,21372928,23339008,10780160,13620224,
13277184,17920,18944,4480,8118272,8120064,122880,1344,286720,185088,378880,25600,20046848,372736,480,8192,68608,69120,5676416,20077568,21434368,23400448,10800640,13621760,13278208,19456,18944,4800,8118784,8120192,143360,1440,
896,2457600,0,819200,1280,4,3,3072,4096,1073741824,1,1065353216,96896,1076,41984,256,7829265,0,652,640,10,11059200,38272,41088,480,1024,1664,2560,72,128,128,20,
163840,2816,563200,10240,19760128,5511168,0,0,67584,67584,5550208,20000768,21250048,23216128,11066880,14403584,13291520,15360,16384,3840,8444928,8447488,409600,1152,204800,64256,568320,10240,19862528,5512192,160,0,
67584,68096,5592192,20026368,21311488,23277568,11087360,14405120,13292544,16896,16384,4160,8445440,8447616,430080,1248,245760,125696,588800,10240,19964928,5595136,320,8192,68608,68608,5634176,20051968,21372928,23339008,11107840,14406656,
13293568,17920,18944,4480,8445952,8447744,450560,1344,286720,187136,593920,10240,20067328,5596160,480,8192,68608,69120,5676160,20077568,21434368,23400448,11128320,14408192,13294592,19456,18944,4800,8446464,8447872,471040,1440,
512,2457600,1,1966080,3072,3,4,61440,62464,63488,0,1051011932,96512,2084,40960,1024,7829265,0,1164,1152,18,11386880,42368,41088,480,1024,1664,2560,96,104,160,36,
819200,768,886272,12288,21523456,942080,0,0,75776,75776,6222208,20000768,21250048,23216128,11394560,15190016,13307904,15360,16384,3840,8772608,8775168,737280,1152,860160,62208,910848,12288,21646336,946176,192,0,
75776,76288,6264192,20026368,21311488,23277568,11415040,15191552,13308928,16896,16384,4160,8773120,8775296,757760,1248,901120,123648,935424,12288,21769216,1024000,384,8192,76800,76800,6306176,20051968,21372928,23339008,11435520,15193088,
13309952,17920,18944,4480,8773632,8775424,778240,1344,942080,185088,960000,12288,21892096,1028096,576,8192,76800,77312,6348160,20077568,21434368,23400448,11456000,15194624,13310976,19456,18944,4800,8774144,8775552,798720,1440,
1152,2457600,1,1966080,3072,3,4,61440,62464,63488,1,1065353216,96128,2092,41984,256,7829265,0,1676,1664,26,11714560,46464,41088,480,1024,1664,2560,96,104,160,52,
819200,2816,1082880,12288,21529600,6182912,0,0,75776,75776,6221952,20000768,21250048,23216128,11722240,15976448,13324288,15360,16384,3840,9100288,9102848,1064960,1152,860160,64256,1107456,12288,21652480,6183936,192,0,
75776,76288,6263936,20026368,21311488,23277568,11742720,15977984,13325312,16896,16384,4160,9100800,9102976,1085440,1248,901120,125696,1132032,12288,21775360,6266880,384,8192,76800,76800,6305920,20051968,21372928,23339008,11763200,15979520,
13326336,17920,18944,4480,9101312,9103104,1105920,1344,942080,187136,1156608,12288,21898240,6267904,576,8192,76800,77312,6347904,20077568,21434368,23400448,11783680,15981056,13327360,19456,18944,4800,9101824,9103232,1126400,1440,
896,2457600,2,1966080,1280,4,3,3072,4096,1073741824,0,1051011932,95744,2596,40960,1024,7829265,0,2188,2176,34,12042240,50560,41088,480,1024,1664,2560,72,128,128,68,
1474560,768,368640,5120,22524928,1597440,0,0,83968,83968,6893952,20000768,21250048,23216128,12049920,16762880,13340672,15360,16384,3840,9427968,9430528,1392640,1152,1515520,62208,378880,5120,22770688,1601536,160,0,
83968,84480,6935936,20026368,21311488,23277568,12070400,16764416,13341696,16896,16384,4160,9428480,9430656,1413120,1248,1556480,123648,389120,5120,23016448,1679360,320,8192,84992,84992,6977920,20051968,21372928,23339008,12090880,16765952,
13342720,17920,18944,4480,9428992,9430784,1433600,1344,1597440,185088,399360,5120,23262208,1683456,480,8192,84992,85504,7019904,20077568,21434368,23400448,12111360,16767488,13343744,19456,18944,4800,9429504,9430912,1454080,1440,
256,2457600,2,1966080,1280,4,3,3072,4096,1073741824,1,1065353216,95360,2612,41984,256,7829265,0,2700,2688,42,12369920,54656,41088,480,1024,1664,2560,72,128,128,84,
1474560,2816,532480,5120,22545408,6854656,0,0,83968,83968,6893696,20000768,21250048,23216128,12377600,17549312,13357056,15360,16384,3840,9755648,9758208,1720320,1152,1515520,64256,542720,5120,22791168,6855680,160,0,
83968,84480,6935680,20026368,21311488,23277568,12398080,17550848,13358080,16896,16384,4160,9756160,9758336,1740800,1248,1556480,125696,552960,5120,23036928,6938624,320,8192,84992,84992,6977664,20051968,21372928,23339008,12418560,17552384,
13359104,17920,18944,4480,9756672,9758464,1761280,1344,1597440,187136,563200,5120,23282688,6939648,480,8192,84992,85504,7019648,20077568,21434368,23400448,12439040,17553920,13360128,19456,18944,4800,9757184,9758592,1781760,1440,
1152,2457600,2,1966080,1280,4,3,3072,4096,1073741824,0,1051011932,94976,2716,40960,1024,7829265,0,3212,3200,50,12697600,58752,41088,480,1024,1664,2560,72,128,128,100,
2129920,768,1597440,5120,22678528,2252800,0,0,92160,92160,7565696,20000768,21250048,23216128,12705280,18335744,13373440,15360,16384,3840,10083328,10085888,2048000,1152,2170880,62208,1607680,5120,22924288,2256896,160,0,
92160,92672,7607680,20026368,21311488,23277568,12725760,18337280,13374464,16896,16384,4160,10083840,10086016,2068480,1248,2211840,123648,1617920,5120,23170048,2334720,320,8192,93184,93184,7649664,20051968,21372928,23339008,12746240,18338816,
13375488,17920,18944,4480,10084352,10086144,2088960,1344,2252800,185088,1628160,5120,23415808,2338816,480,8192,93184,93696,7691648,20077568,21434368,23400448,12766720,18340352,13376512,19456,18944,4800,10084864,10086272,2109440,1440,
512,2457600,2,1966080,1280,4,3,3072,4096,1073741824,1,1065353216,94592,2732,41984,256,7829265,0,3724,3712,58,13025280,62848,41088,480,1024,1664,2560,72,128,128,116,
2129920,2816,1761280,5120,22699008,7526400,0,0,92160,92160,7565440,20000768,21250048,23216128,13032960,19122176,13389824,15360,16384,3840,10411008,10413568,2375680,1152,2170880,64256,1771520,5120,22944768,7527424,160,0,
92160,92672,7607424,20026368,21311488,23277568,13053440,19123712,13390848,16896,16384,4160,10411520,10413696,2396160,1248,2211840,125696,1781760,5120,23190528,7610368,320,8192,93184,93184,7649408,20051968,21372928,23339008,13073920,19125248,
13391872,17920,18944,4480,10412032,10413824,2416640,1344,2252800,187136,1792000,5120,23436288,7611392,480,8192,93184,93696,7691392,20077568,21434368,23400448,13094400,19126784,13392896,19456,18944,4800,10412544,10413952,2437120,1440,
128,2457600,0,819200,1280,4,3,3072,4096,1073741824,0,1051011932,94208,1316,40960,1024,1118481,0,144,128,2,10731520,34304,41216,640,1280,1920,2816,72,128,128,4,
163840,1024,225920,25600,20149248,294912,0,0,67584,67584,5550592,20103168,21495808,23461888,10741760,13623296,13275136,20480,21504,5120,8120320,8124416,81920,1536,204800,62464,277120,25600,20251648,299008,160,0,
67584,68096,5592576,20128768,21557248,23523328,10762240,13624832,13276160,22016,21504,5440,8120832,8124544,102400,1632,245760,123904,328320,25600,20354048,376832,320,8192,68608,68608,5634560,20154368,21618688,23584768,10782720,13626368,
13277184,23040,24064,5760,8121344,8124672,122880,1728,286720,185344,379520,25600,20456448,380928,480,8192,68608,69120,5676544,20179968,21680128,23646208,10803200,13627904,13278208,24576,24064,6080,8121856,8124800,143360,1824,
768,2457600,0,819200,1280,4,3,3072,4096,1073741824,1,1065353216,93824,1332,41984,256,7829265,0,656,640,10,11059200,38400,41216,640,1280,1920,2816,72,128,128,20,
163840,3072,563840,10240,20169728,5513216,0,0,67584,67584,5550336,20103168,21495808,23461888,11069440,14409728,13291520,20480,21504,5120,8448000,8452096,409600,1536,204800,64512,568960,10240,20272128,5514240,160,0,
67584,68096,5592320,20128768,21557248,23523328,11089920,14411264,13292544,22016,21504,5440,8448512,8452224,430080,1632,245760,125952,589440,10240,20374528,5597184,320,8192,68608,68608,5634304,20154368,21618688,23584768,11110400,14412800,
13293568,23040,24064,5760,8449024,8452352,450560,1728,286720,187392,594560,10240,20476928,5598208,480,8192,68608,69120,5676288,20179968,21680128,23646208,11130880,14414336,13294592,24576,24064,6080,8449536,8452480,471040,1824,
384,2457600,1,1966080,3072,3,4,61440,62464,63488,0,1051011932,93440,2340,40960,1024,7829265,0,1168,1152,18,11386880,42496,41216,640,1280,1920,2816,96,104,160,36,
819200,1024,887040,12288,22014976,950272,0,0,75776,75776,6222336,20103168,21495808,23461888,11397120,15196160,13307904,20480,21504,5120,8775680,8779776,737280,1536,860160,62464,911616,12288,22137856,954368,192,0,
75776,76288,6264320,20128768,21557248,23523328,11417600,15197696,13308928,22016,21504,5440,8776192,8779904,757760,1632,901120,123904,936192,12288,22260736,1032192,384,8192,76800,76800,6306304,20154368,21618688,23584768,11438080,15199232,
13309952,23040,24064,5760,8776704,8780032,778240,1728,942080,185344,960768,12288,22383616,1036288,576,8192,76800,77312,6348288,20179968,21680128,23646208,11458560,15200768,13310976,24576,24064,6080,8777216,8780160,798720,1824,
1024,2457600,1,1966080,3072,3,4,61440,62464,63488,1,1065353216,93056,2348,41984,256,7829265,0,1680,1664,26,11714560,46592,41216,640,1280,1920,2816,96,104,160,52,
819200,3072,1083648,12288,22021120,6184960,0,0,75776,75776,6222080,20103168,21495808,23461888,11724800,15982592,13324288,20480,21504,5120,9103360,9107456,1064960,1536,860160,64512,1108224,12288,22144000,6185984,192,0,
75776,76288,6264064,20128768,21557248,23523328,11745280,15984128,13325312,22016,21504,5440,9103872,9107584,1085440,1632,901120,125952,1132800,12288,22266880,6268928,384,8192,76800,76800,6306048,20154368,21618688,23584768,11765760,15985664,
13326336,23040,24064,5760,9104384,9107712,1105920,1728,942080,187392,1157376,12288,22389760,6269952,576,8192,76800,77312,6348032,20179968,21680128,23646208,11786240,15987200,13327360,24576,24064,6080,9104896,9107840,1126400,1824,
768,2457600,2,1966080,1280,4,3,3072,4096,1073741824,0,1051011932,92672,2852,40960,1024,7829265,0,2192,2176,34,12042240,50688,41216,640,1280,1920,2816,72,128,128,68,
1474560,1024,369280,5120,23507968,1605632,0,0,83968,83968,6894080,20103168,21495808,23461888,12052480,16769024,13340672,20480,21504,5120,9431040,9435136,1392640,1536,1515520,62464,379520,5120,23753728,1609728,160,0,
83968,84480,6936064,20128768,21557248,23523328,12072960,16770560,13341696,22016,21504,5440,9431552,9435264,1413120,1632,1556480,123904,389760,5120,23999488,1687552,320,8192,84992,84992,6978048,20154368,21618688,23584768,12093440,16772096,
13342720,23040,24064,5760,9432064,9435392,1433600,1728,1597440,185344,400000,5120,24245248,1691648,480,8192,84992,85504,7020032,20179968,21680128,23646208,12113920,16773632,13343744,24576,24064,6080,9432576,9435520,1454080,1824,
128,2457600,2,1966080,1280,4,3,3072,4096,1073741824,1,1065353216,92288,2868,41984,256,7829265,0,2704,2688,42,12369920,54784,41216,640,1280,1920,2816,72,128,128,84,
1474560,3072,533120,5120,23528448,6856704,0,0,83968,83968,6893824,20103168,21495808,23461888,12380160,17555456,13357056,20480,21504,5120,9758720,9762816,1720320,1536,1515520,64512,543360,5120,23774208,6857728,160,0,
83968,84480,6935808,20128768,21557248,23523328,12400640,17556992,13358080,22016,21504,5440,9759232,9762944,1740800,1632,1556480,125952,553600,5120,24019968,6940672,320,8192,84992,84992,6977792,20154368,21618688,23584768,12421120,17558528,
13359104,23040,24064,5760,9759744,9763072,1761280,1728,1597440,187392,563840,5120,24265728,6941696,480,8192,84992,85504,7019776,20179968,21680128,23646208,12441600,17560064,13360128,24576,24064,6080,9760256,9763200,1781760,1824,
1024,2457600,2,1966080,1280,4,3,3072,4096,1073741824,0,1051011932,91904,2972,40960,1024,7829265,0,3216,3200,50,12697600,58880,41216,640,1280,1920,2816,72,128,128,100,
2129920,1024,1598080,5120,23661568,2260992,0,0,92160,92160,7565824,20103168,21495808,23461888,12707840,18341888,13373440,20480,21504,5120,10086400,10090496,2048000,1536,2170880,62464,1608320,5120,23907328,2265088,160,0,
92160,92672,7607808,20128768,21557248,23523328,12728320,18343424,13374464,22016,21504,5440,10086912,10090624,2068480,1632,2211840,123904,1618560,5120,24153088,2342912,320,8192,93184,93184,7649792,20154368,21618688,23584768,12748800,18344960,
13375488,23040,24064,5760,10087424,10090752,2088960,1728,2252800,185344,1628800,5120,24398848,2347008,480,8192,93184,93696,7691776,20179968,21680128,23646208,12769280,18346496,13376512,24576,24064,6080,10087936,10090880,2109440,1824,
384,2457600,2,1966080,1280,4,3,3072,4096,1073741824,1,1065353216,91520,2988,41984,256,7829265,0,3728,3712,58,13025280,62976,41216,640,1280,1920,2816,72,128,128,116,
2129920,3072,1761920,5120,23682048,7528448,0,0,92160,92160,7565568,20103168,21495808,23461888,13035520,19128320,13389824,20480,21504,5120,10414080,10418176,2375680,1536,2170880,64512,1772160,5120,23927808,7529472,160,0,
92160,92672,7607552,20128768,21557248,23523328,13056000,19129856,13390848,22016,21504,5440,10414592,10418304,2396160,1632,2211840,125952,1782400,5120,24173568,7612416,320,8192,93184,93184,7649536,20154368,21618688,23584768,13076480,19131392,
13391872,23040,24064,5760,10415104,10418432,2416640,1728,2252800,187392,1792640,5120,24419328,7613440,480,8192,93184,93696,7691520,20179968,21680128,23646208,13096960,19132928,13392896,24576,24064,6080,10415616,10418560,2437120,1824,
256,2457600,0,819200,1280,4,3,3072,4096,1073741824,3,1065353216,91136,1064,40960,1024,65793,0,148,128,2,10731520,34432,41344,800,1280,1920,2816,72,128,128,4,
163840,1280,409600,10240,19744768,2883584,0,0,67584,67584,5550080,20205568,21741568,23707648,10744320,13629440,13275136,25600,26624,6400,8122368,8124928,81920,1920,204800,62720,414720,10240,19847168,2887680,160,0,
67584,68096,5592064,20231168,21803008,23769088,10764800,13630976,13276160,27136,26624,6720,8122880,8125056,102400,2016,245760,124160,435200,10240,19949568,2965504,320,8192,68608,68608,5634048,20256768,21864448,23830528,10785280,13632512,
13277184,28160,29184,7040,8123392,8125184,122880,2112,286720,185600,440320,10240,20051968,2969600,480,8192,68608,69120,5676032,20282368,21925888,23891968,10805760,13634048,13278208,29696,29184,7360,8123904,8125312,143360,2208,
896,2457600,0,819200,1280,4,3,3072,4096,1073741824,1,1065353216,90752,1080,41984,256,460545,0,660,640,10,11059200,38528,41344,800,1280,1920,2816,72,128,128,20,
163840,3328,430080,25600,19765248,5515264,0,0,67584,67584,5550464,20205568,21741568,23707648,11072000,14415872,13291520,25600,26624,6400,8450048,8452608,409600,1920,204800,64768,481280,25600,19867648,5516288,160,0,
67584,68096,5592448,20231168,21803008,23769088,11092480,14417408,13292544,27136,26624,6720,8450560,8452736,430080,2016,245760,126208,532480,25600,19970048,5599232,320,8192,68608,68608,5634432,20256768,21864448,23830528,11112960,14418944,
13293568,28160,29184,7040,8451072,8452864,450560,2112,286720,187648,583680,25600,20072448,5600256,480,8192,68608,69120,5676416,20282368,21925888,23891968,11133440,14420480,13294592,29696,29184,7360,8451584,8452992,471040,2208,
512,2457600,1,1966080,3072,3,4,61440,62464,63488,3,1065353216,90368,1576,40960,1024,460545,0,1172,1152,18,11386880,42624,41344,800,1280,1920,2816,96,104,160,36,
819200,1280,983040,12288,20543488,3538944,0,0,75776,75776,6221824,20205568,21741568,23707648,11399680,15202304,13307904,25600,26624,6400,8777728,8780288,737280,1920,860160,62720,1007616,12288,20666368,3543040,192,0,
75776,76288,6263808,20231168,21803008,23769088,11420160,15203840,13308928,27136,26624,6720,8778240,8780416,757760,2016,901120,124160,1032192,12288,20789248,3620864,384,8192,76800,76800,6305792,20256768,21864448,23830528,11440640,15205376,
13309952,28160,29184,7040,8778752,8780544,778240,2112,942080,185600,1056768,12288,20912128,3624960,576,8192,76800,77312,6347776,20282368,21925888,23891968,11461120,15206912,13310976,29696,29184,7360,8779264,8780672,798720,2208,
1152,2457600,1,1966080,3072,3,4,61440,62464,63488,1,1065353216,89984,1584,41984,256,460545,0,1684,1664,26,11714560,46720,41344,800,1280,1920,2816,96,104,160,52,
819200,3328,1179648,12288,20549632,6187008,0,0,75776,75776,6222208,20205568,21741568,23707648,11727360,15988736,13324288,25600,26624,6400,9105408,9107968,1064960,1920,860160,64768,1204224,12288,20672512,6188032,192,0,
75776,76288,6264192,20231168,21803008,23769088,11747840,15990272,13325312,27136,26624,6720,9105920,9108096,1085440,2016,901120,126208,1228800,12288,20795392,6270976,384,8192,76800,76800,6306176,20256768,21864448,23830528,11768320,15991808,
13326336,28160,29184,7040,9106432,9108224,1105920,2112,942080,187648,1253376,12288,20918272,6272000,576,8192,76800,77312,6348160,20282368,21925888,23891968,11788800,15993344,13327360,29696,29184,7360,9106944,9108352,1126400,2208,
896,2457600,2,1966080,1280,4,3,3072,4096,1073741824,3,1065353216,89600,2600,40960,1024,460545,0,2196,2176,34,12042240,50816,41344,800,1280,1920,2816,72,128,128,68,
1474560,1280,409600,5120,22530048,4194304,0,0,83968,83968,6893568,20205568,21741568,23707648,12055040,16775168,13340672,25600,26624,6400,9433088,9435648,1392640,1920,1515520,62720,419840,5120,22775808,4198400,160,0,
83968,84480,6935552,20231168,21803008,23769088,12075520,16776704,13341696,27136,26624,6720,9433600,9435776,1413120,2016,1556480,124160,430080,5120,23021568,4276224,320,8192,84992,84992,6977536,20256768,21864448,23830528,12096000,16778240,
13342720,28160,29184,7040,9434112,9435904,1433600,2112,1597440,185600,440320,5120,23267328,4280320,480,8192,84992,85504,7019520,20282368,21925888,23891968,12116480,16779776,13343744,29696,29184,7360,9434624,9436032,1454080,2208,
256,2457600,2,1966080,1280,4,3,3072,4096,1073741824,1,1065353216,89216,2616,41984,256,460545,0,2708,2688,42,12369920,54912,41344,800,1280,1920,2816,72,128,128,84,
1474560,3328,573440,5120,22550528,6858752,0,0,83968,83968,6893952,20205568,21741568,23707648,12382720,17561600,13357056,25600,26624,6400,9760768,9763328,1720320,1920,1515520,64768,583680,5120,22796288,6859776,160,0,
83968,84480,6935936,20231168,21803008,23769088,12403200,17563136,13358080,27136,26624,6720,9761280,9763456,1740800,2016,1556480,126208,593920,5120,23042048,6942720,320,8192,84992,84992,6977920,20256768,21864448,23830528,12423680,17564672,
13359104,28160,29184,7040,9761792,9763584,1761280,2112,1597440,187648,604160,5120,23287808,6943744,480,8192,84992,85504,7019904,20282368,21925888,23891968,12444160,17566208,13360128,29696,29184,7360,9762304,9763712,1781760,2208,
1152,2457600,2,1966080,1280,4,3,3072,4096,1073741824,3,1065353216,88832,2720,40960,1024,460545,0,3220,3200,50,12697600,59008,41344,800,1280,1920,2816,72,128,128,100,
2129920,1280,1638400,5120,22683648,4849664,0,0,92160,92160,7565312,20205568,21741568,23707648,12710400,18348032,13373440,25600,26624,6400,10088448,10091008,2048000,1920,2170880,62720,1648640,5120,22929408,4853760,160,0,
92160,92672,7607296,20231168,21803008,23769088,12730880,18349568,13374464,27136,26624,6720,10088960,10091136,2068480,2016,2211840,124160,1658880,5120,23175168,4931584,320,8192,93184,93184,7649280,20256768,21864448,23830528,12751360,18351104,
13375488,28160,29184,7040,10089472,10091264,2088960,2112,2252800,185600,1669120,5120,23420928,4935680,480,8192,93184,93696,7691264,20282368,21925888,23891968,12771840,18352640,13376512,29696,29184,7360,10089984,10091392,2109440,2208,
512,2457600,2,1966080,1280,4,3,3072,4096,1073741824,1,1065353216,88448,2736,41984,256,460545,0,3732,3712,58,13025280,63104,41344,800,1280,1920,2816,72,128,128,116,
2129920,3328,1802240,5120,22704128,7530496,0,0,92160,92160,7565696,20205568,21741568,23707648,13038080,19134464,13389824,25600,26624,6400,10416128,10418688,2375680,1920,2170880,64768,1812480,5120,22949888,7531520,160,0,
92160,92672,7607680,20231168,21803008,23769088,13058560,19136000,13390848,27136,26624,6720,10416640,10418816,2396160,2016,2211840,126208,1822720,5120,23195648,7614464,320,8192,93184,93184,7649664,20256768,21864448,23830528,13079040,19137536,
13391872,28160,29184,7040,10417152,10418944,2416640,2112,2252800,187648,1832960,5120,23441408,7615488,480,8192,93184,93696,7691648,20282368,21925888,23891968,13099520,19139072,13392896,29696,29184,7360,10417664,10419072,2437120,2208,
128,2457600,0,819200,1280,4,3,3072,4096,1073741824,3,1065353216,88064,1320,40960,1024,1118481,0,152,128,2,10731520,34560,41344,960,1280,2176,2816,72,128,128,4,
163840,1536,410240,10240,20154368,2891776,0,0,67584,67584,5550208,20307968,21987328,23953408,10746880,13635584,13275136,30720,31744,7680,8125440,8129536,81920,2304,204800,62976,415360,10240,20256768,2895872,160,0,
67584,68096,5592192,20333568,22048768,24014848,10767360,13637120,13276160,32256,31744,8000,8125952,8129664,102400,2400,245760,124416,435840,10240,20359168,2973696,320,8192,68608,68608,5634176,20359168,22110208,24076288,10787840,13638656,
13277184,33280,34304,8320,8126464,8129792,122880,2496,286720,185856,440960,10240,20461568,2977792,480,8192,68608,69120,5676160,20384768,22171648,24137728,10808320,13640192,13278208,34816,34304,8640,8126976,8129920,143360,2592,
768,2457600,0,819200,1280,4,3,3072,4096,1073741824,1,1065353216,87680,1336,41984,256,7829265,0,664,640,10,11059200,38656,41344,960,1280,2176,2816,72,128,128,20,
163840,3584,430720,25600,20174848,5517312,0,0,67584,67584,5550592,20307968,21987328,23953408,11074560,14422016,13291520,30720,31744,7680,8453120,8457216,409600,2304,204800,65024,481920,25600,20277248,5518336,160,0,
67584,68096,5592576,20333568,22048768,24014848,11095040,14423552,13292544,32256,31744,8000,8453632,8457344,430080,2400,245760,126464,533120,25600,20379648,5601280,320,8192,68608,68608,5634560,20359168,22110208,24076288,11115520,14425088,
13293568,33280,34304,8320,8454144,8457472,450560,2496,286720,187904,584320,25600,20482048,5602304,480,8192,68608,69120,5676544,20384768,22171648,24137728,11136000,14426624,13294592,34816,34304,8640,8454656,8457600,471040,2592,
384,2457600,1,1966080,3072,3,4,61440,62464,63488,3,1065353216,87296,1832,40960,1024,7829265,0,1176,1152,18,11386880,42752,41344,960,1280,2176,2816,96,104,160,36,
819200,1536,983808,12288,21035008,3547136,0,0,75776,75776,6221952,20307968,21987328,23953408,11402240,15208448,13307904,30720,31744,7680,8780800,8784896,737280,2304,860160,62976,1008384,12288,21157888,3551232,192,0,
75776,76288,6263936,20333568,22048768,24014848,11422720,15209984,13308928,32256,31744,8000,8781312,8785024,757760,2400,901120,124416,1032960,12288,21280768,3629056,384,8192,76800,76800,6305920,20359168,22110208,24076288,11443200,15211520,
13309952,33280,34304,8320,8781824,8785152,778240,2496,942080,185856,1057536,12288,21403648,3633152,576,8192,76800,77312,6347904,20384768,22171648,24137728,11463680,15213056,13310976,34816,34304,8640,8782336,8785280,798720,2592,
1024,2457600,1,1966080,3072,3,4,61440,62464,63488,1,1065353216,86912,1840,41984,256,7829265,0,1688,1664,26,11714560,46848,41344,960,1280,2176,2816,96,104,160,52,
819200,3584,1180416,12288,21041152,6189056,0,0,75776,75776,6222336,20307968,21987328,23953408,11729920,15994880,13324288,30720,31744,7680,9108480,9112576,1064960,2304,860160,65024,1204992,12288,21164032,6190080,192,0,
75776,76288,6264320,20333568,22048768,24014848,11750400,15996416,13325312,32256,31744,8000,9108992,9112704,1085440,2400,901120,126464,1229568,12288,21286912,6273024,384,8192,76800,76800,6306304,20359168,22110208,24076288,11770880,15997952,
13326336,33280,34304,8320,9109504,9112832,1105920,2496,942080,187904,1254144,12288,21409792,6274048,576,8192,76800,77312,6348288,20384768,22171648,24137728,11791360,15999488,13327360,34816,34304,8640,9110016,9112960,1126400,2592,
768,2457600,2,1966080,1280,4,3,3072,4096,1073741824,3,1065353216,86528,2856,40960,1024,7829265,0,2200,2176,34,12042240,50944,41344,960,1280,2176,2816,72,128,128,68,
1474560,1536,410240,5120,23513088,4202496,0,0,83968,83968,6893696,20307968,21987328,23953408,12057600,16781312,13340672,30720,31744,7680,9436160,9440256,1392640,2304,1515520,62976,420480,5120,23758848,4206592,160,0,
83968,84480,6935680,20333568,22048768,24014848,12078080,16782848,13341696,32256,31744,8000,9436672,9440384,1413120,2400,1556480,124416,430720,5120,24004608,4284416,320,8192,84992,84992,6977664,20359168,22110208,24076288,12098560,16784384,
13342720,33280,34304,8320,9437184,9440512,1433600,2496,1597440,185856,440960,5120,24250368,4288512,480,8192,84992,85504,7019648,20384768,22171648,24137728,12119040,16785920,13343744,34816,34304,8640,9437696,9440640,1454080,2592,
128,2457600,2,1966080,1280,4,3,3072,4096,1073741824,1,1065353216,86144,2872,41984,256,7829265,0,2712,2688,42,12369920,55040,41344,960,1280,2176,2816,72,128,128,84,
1474560,3584,574080,5120,23533568,6860800,0,0,83968,83968,6894080,20307968,21987328,23953408,12385280,17567744,13357056,30720,31744,7680,9763840,9767936,1720320,2304,1515520,65024,584320,5120,23779328,6861824,160,0,
83968,84480,6936064,20333568,22048768,24014848,12405760,17569280,13358080,32256,31744,8000,9764352,9768064,1740800,2400,1556480,126464,594560,5120,24025088,6944768,320,8192,84992,84992,6978048,20359168,22110208,24076288,12426240,17570816,
13359104,33280,34304,8320,9764864,9768192,1761280,2496,1597440,187904,604800,5120,24270848,6945792,480,8192,84992,85504,7020032,20384768,22171648,24137728,12446720,17572352,13360128,34816,34304,8640,9765376,9768320,1781760,2592,
1024,2457600,2,1966080,1280,4,3,3072,4096,1073741824,3,1065353216,85760,2976,40960,1024,7829265,0,3224,3200,50,12697600,59136,41344,960,1280,2176,2816,72,128,128,100,
2129920,1536,1639040,5120,23666688,4857856,0,0,92160,92160,7565440,20307968,21987328,23953408,12712960,18354176,13373440,30720,31744,7680,10091520,10095616,2048000,2304,2170880,62976,1649280,5120,23912448,4861952,160,0,
92160,92672,7607424,20333568,22048768,24014848,12733440,18355712,13374464,32256,31744,8000,10092032,10095744,2068480,2400,2211840,124416,1659520,5120,24158208,4939776,320,8192,93184,93184,7649408,20359168,22110208,24076288,12753920,18357248,
13375488,33280,34304,8320,10092544,10095872,2088960,2496,2252800,185856,1669760,5120,24403968,4943872,480,8192,93184,93696,7691392,20384768,22171648,24137728,12774400,18358784,13376512,34816,34304,8640,10093056,10096000,2109440,2592,
384,2457600,2,1966080,1280,4,3,3072,4096,1073741824,1,1065353216,85376,2992,41984,256,7829265,0,3736,3712,58,13025280,63232,41344,960,1280,2176,2816,72,128,128,116,
2129920,3584,1802880,5120,23687168,7532544,0,0,92160,92160,7565824,20307968,21987328,23953408,13040640,19140608,13389824,30720,31744,7680,10419200,10423296,2375680,2304,2170880,65024,1813120,5120,23932928,7533568,160,0,
92160,92672,7607808,20333568,22048768,24014848,13061120,19142144,13390848,32256,31744,8000,10419712,10423424,2396160,2400,2211840,126464,1823360,5120,24178688,7616512,320,8192,93184,93184,7649792,20359168,22110208,24076288,13081600,19143680,
13391872,33280,34304,8320,10420224,10423552,2416640,2496,2252800,187904,1833600,5120,24424448,7617536,480,8192,93184,93696,7691776,20384768,22171648,24137728,13102080,19145216,13392896,34816,34304,8640,10420736,10423680,2437120,2592,
256,2457600,0,819200,1280,4,3,3072,4096,1073741824,3,1065353216,84992,1068,40960,1024,65793,0,156,128,2,10731520,34688,41472,1120,1280,2176,2816,72,128,128,4,
163840,1792,460800,10240,19749888,2899968,0,0,67584,67584,5550336,20410368,22233088,24199168,10749440,13641728,13275136,35840,36864,8960,8127488,8130048,81920,2688,204800,63232,465920,10240,19852288,2904064,160,0,
67584,68096,5592320,20435968,22294528,24260608,10769920,13643264,13276160,37376,36864,9280,8128000,8130176,102400,2784,245760,124672,486400,10240,19954688,2981888,320,8192,68608,68608,5634304,20461568,22355968,24322048,10790400,13644800,
13277184,38400,39424,9600,8128512,8130304,122880,2880,286720,186112,491520,10240,20057088,2985984,480,8192,68608,69120,5676288,20487168,22417408,24383488,10810880,13646336,13278208,39936,39424,9920,8129024,8130432,143360,2976,
896,0,2,0,1280,4,3,3072,4096,1073741824,2,1065353216,84608,1073741824,40960,1024,460545,0,668,640,10,11059200,38784,41472,1120,1280,2176,2816,72,128,128,20,
163840,3840,17982080,5120,1073741824,2883584,0,0,67584,67584,5550080,20410368,22233088,24199168,11077120,14428160,13291520,35840,36864,8960,8455168,8457728,409600,2688,204800,65280,17992320,5120,1073741824,2887680,160,4096,
67584,68096,5592064,20435968,22294528,24260608,11097600,14429696,13292544,37376,36864,9280,8455680,8457856,430080,2784,245760,126720,18002560,5120,1073741824,2965504,320,8192,68608,68608,5634048,20461568,22355968,24322048,11118080,14431232,
13293568,38400,39424,9600,8456192,8457984,450560,2880,286720,188160,18012800,5120,1073741824,2969600,480,12288,68608,69120,5676032,20487168,22417408,24383488,11138560,14432768,13294592,39936,39424,9920,8456704,8458112,471040,2976,
512,2457600,1,1966080,3072,3,4,61440,62464,63488,3,1065353216,84224,2088,40960,1024,460545,0,1180,1152,18,11386880,42880,41472,1120,1280,2176,2816,96,104,160,36,
819200,1792,984576,12288,21526528,3555328,0,0,75776,75776,6222080,20410368,22233088,24199168,11404800,15214592,13307904,35840,36864,8960,8782848,8785408,737280,2688,860160,63232,1009152,12288,21649408,3559424,192,0,
75776,76288,6264064,20435968,22294528,24260608,11425280,15216128,13308928,37376,36864,9280,8783360,8785536,757760,2784,901120,124672,1033728,12288,21772288,3637248,384,8192,76800,76800,6306048,20461568,22355968,24322048,11445760,15217664,
13309952,38400,39424,9600,8783872,8785664,778240,2880,942080,186112,1058304,12288,21895168,3641344,576,8192,76800,77312,6348032,20487168,22417408,24383488,11466240,15219200,13310976,39936,39424,9920,8784384,8785792,798720,2976,
1152,0,2,0,1280,4,3,3072,4096,1073741824,2,1065353216,83840,1073741824,40960,1024,460545,0,1692,1664,26,11714560,46976,41472,1120,1280,2176,2816,72,128,128,52,
819200,3840,17982080,5120,1073741824,3538944,0,0,75776,75776,6221824,20410368,22233088,24199168,11732480,16001024,13324288,35840,36864,8960,9110528,9113088,1064960,2688,860160,65280,17992320,5120,1073741824,3543040,160,4096,
75776,76288,6263808,20435968,22294528,24260608,11752960,16002560,13325312,37376,36864,9280,9111040,9113216,1085440,2784,901120,126720,18002560,5120,1073741824,3620864,320,8192,76800,76800,6305792,20461568,22355968,24322048,11773440,16004096,
13326336,38400,39424,9600,9111552,9113344,1105920,2880,942080,188160,18012800,5120,1073741824,3624960,480,12288,76800,77312,6347776,20487168,22417408,24383488,11793920,16005632,13327360,39936,39424,9920,9112064,9113472,1126400,2976,
896,2457600,2,1966080,1280,4,3,3072,4096,1073741824,3,1065353216,83456,2604,40960,1024,460545,0,2204,2176,34,12042240,51072,41472,1120,1280,2176,2816,72,128,128,68,
1474560,1792,450560,5120,22535168,4210688,0,0,83968,83968,6893824,20410368,22233088,24199168,12060160,16787456,13340672,35840,36864,8960,9438208,9440768,1392640,2688,1515520,63232,460800,5120,22780928,4214784,160,0,
83968,84480,6935808,20435968,22294528,24260608,12080640,16788992,13341696,37376,36864,9280,9438720,9440896,1413120,2784,1556480,124672,471040,5120,23026688,4292608,320,8192,84992,84992,6977792,20461568,22355968,24322048,12101120,16790528,
13342720,38400,39424,9600,9439232,9441024,1433600,2880,1597440,186112,481280,5120,23272448,4296704,480,8192,84992,85504,7019776,20487168,22417408,24383488,12121600,16792064,13343744,39936,39424,9920,9439744,9441152,1454080,2976,
256,0,2,0,1280,4,3,3072,4096,1073741824,2,1065353216,83072,1073741824,40960,1024,460545,0,2716,2688,42,12369920,55168,41472,1120,1280,2176,2816,72,128,128,84,
1474560,3840,17982080,5120,1073741824,4194304,0,0,83968,83968,6893568,20410368,22233088,24199168,12387840,17573888,13357056,35840,36864,8960,9765888,9768448,1720320,2688,1515520,65280,17992320,5120,1073741824,4198400,160,4096,
83968,84480,6935552,20435968,22294528,24260608,12408320,17575424,13358080,37376,36864,9280,9766400,9768576,1740800,2784,1556480,126720,18002560,5120,1073741824,4276224,320,8192,84992,84992,6977536,20461568,22355968,24322048,12428800,17576960,
13359104,38400,39424,9600,9766912,9768704,1761280,2880,1597440,188160,18012800,5120,1073741824,4280320,480,12288,84992,85504,7019520,20487168,22417408,24383488,12449280,17578496,13360128,39936,39424,9920,9767424,9768832,1781760,2976,
1152,2457600,2,1966080,1280,4,3,3072,4096,1073741824,3,1065353216,82688,2724,40960,1024,460545,0,3228,3200,50,12697600,59264,41472,1120,1280,2176,2816,72,128,128,100,
2129920,1792,1679360,5120,22688768,4866048,0,0,92160,92160,7565568,20410368,22233088,24199168,12715520,18360320,13373440,35840,36864,8960,10093568,10096128,2048000,2688,2170880,63232,1689600,5120,22934528,4870144,160,0,
92160,92672,7607552,20435968,22294528,24260608,12736000,18361856,13374464,37376,36864,9280,10094080,10096256,2068480,2784,2211840,124672,1699840,5120,23180288,4947968,320,8192,93184,93184,7649536,20461568,22355968,24322048,12756480,18363392,
13375488,38400,39424,9600,10094592,10096384,2088960,2880,2252800,186112,1710080,5120,23426048,4952064,480,8192,93184,93696,7691520,20487168,22417408,24383488,12776960,18364928,13376512,39936,39424,9920,10095104,10096512,2109440,2976,
512,0,2,0,1280,4,3,3072,4096,1073741824,2,1065353216,82304,1073741824,40960,1024,460545,0,3740,3712,58,13025280,63360,41472,1120,1280,2176,2816,72,128,128,116,
2129920,3840,17982080,5120,1073741824,4849664,0,0,92160,92160,7565312,20410368,22233088,24199168,13043200,19146752,13389824,35840,36864,8960,10421248,10423808,2375680,2688,2170880,65280,17992320,5120,1073741824,4853760,160,4096,
92160,92672,7607296,20435968,22294528,24260608,13063680,19148288,13390848,37376,36864,9280,10421760,10423936,2396160,2784,2211840,126720,18002560,5120,1073741824,4931584,320,8192,93184,93184,7649280,20461568,22355968,24322048,13084160,19149824,
13391872,38400,39424,9600,10422272,10424064,2416640,2880,2252800,188160,18012800,5120,1073741824,4935680,480,12288,93184,93696,7691264,20487168,22417408,24383488,13104640,19151360,13392896,39936,39424,9920,10422784,10424192,2437120,2976,
0,2457600,0,819200,1280,4,3,3072,4096,1073741824,0,1051011932,81920,1084,40960,1024,197377,0,256,256,4,10813440,34816,40960,0,1024,1408,2560,72,128,128,8,
327680,0,614400,10240,19770368,425984,0,0,69632,69632,5718016,19693568,20512768,22478848,10813440,13795328,13279232,0,1024,0,8192000,8196096,163840,0,368640,61440,619520,10240,19872768,430080,160,0,
69632,70144,5760000,19719168,20574208,22540288,10833920,13796864,13280256,1536,1024,320,8192512,8196224,184320,96,409600,122880,640000,10240,19975168,507904,320,8192,70656,70656,5801984,19744768,20635648,22601728,10854400,13798400,
13281280,2560,3584,640,8193024,8196352,204800,192,450560,184320,645120,10240,20077568,512000,480,8192,70656,71168,5843968,19770368,20697088,22663168,10874880,13799936,13282304,4096,3584,960,8193536,8196480,225280,288,
640,2457600,0,819200,1280,4,3,3072,4096,1073741824,3,1065353216,81536,1100,40960,1024,460545,0,768,768,12,11141120,38912,40960,0,1024,1408,2560,72,128,128,24,
327680,2048,634880,25600,19790848,3072000,0,0,69632,69632,5718400,19693568,20512768,22478848,11141120,14581760,13295616,0,1024,0,8519680,8523776,491520,0,368640,63488,686080,25600,19893248,3076096,160,0,
69632,70144,5760384,19719168,20574208,22540288,11161600,14583296,13296640,1536,1024,320,8520192,8523904,512000,96,409600,124928,737280,25600,19995648,3153920,320,8192,70656,70656,5802368,19744768,20635648,22601728,11182080,14584832,
13297664,2560,3584,640,8520704,8524032,532480,192,450560,186368,788480,25600,20098048,3158016,480,8192,70656,71168,5844352,19770368,20697088,22663168,11202560,14586368,13298688,4096,3584,960,8521216,8524160,552960,288,
256,2457600,1,1966080,3072,3,4,61440,62464,63488,0,1051011932,81152,2096,40960,1024,460545,0,1280,1280,20,11468800,43008,40960,0,1024,1408,2560,96,104,160,40,
983040,0,1181184,12288,21532672,1081344,0,0,77824,77824,6389760,19693568,20512768,22478848,11468800,15368192,13312000,0,1024,0,8847360,8851456,819200,0,1024000,61440,1205760,12288,21655552,1085440,192,0,
77824,78336,6431744,19719168,20574208,22540288,11489280,15369728,13313024,1536,1024,320,8847872,8851584,839680,96,1064960,122880,1230336,12288,21778432,1163264,384,8192,78848,78848,6473728,19744768,20635648,22601728,11509760,15371264,
13314048,2560,3584,640,8848384,8851712,860160,192,1105920,184320,1254912,12288,21901312,1167360,576,8192,78848,79360,6515712,19770368,20697088,22663168,11530240,15372800,13315072,4096,3584,960,8848896,8851840,880640,288,
896,2457600,1,1966080,3072,3,4,61440,62464,63488,3,1065353216,80768,2104,40960,1024,460545,0,1792,1792,28,11796480,47104,40960,0,1024,1408,2560,96,104,160,56,
983040,2048,1377792,12288,21538816,3727360,0,0,77824,77824,6390144,19693568,20512768,22478848,11796480,16154624,13328384,0,1024,0,9175040,9179136,1146880,0,1024000,63488,1402368,12288,21661696,3731456,192,0,
77824,78336,6432128,19719168,20574208,22540288,11816960,16156160,13329408,1536,1024,320,9175552,9179264,1167360,96,1064960,124928,1426944,12288,21784576,3809280,384,8192,78848,78848,6474112,19744768,20635648,22601728,11837440,16157696,
13330432,2560,3584,640,9176064,9179392,1187840,192,1105920,186368,1451520,12288,21907456,3813376,576,8192,78848,79360,6516096,19770368,20697088,22663168,11857920,16159232,13331456,4096,3584,960,9176576,9179520,1208320,288,
640,2457600,2,1966080,1280,4,3,3072,4096,1073741824,0,1051011932,80384,2620,40960,1024,460545,0,2304,2304,36,12124160,51200,40960,0,1024,1408,2560,72,128,128,72,
1638400,0,614400,5120,22555648,1736704,0,0,86016,86016,7061504,19693568,20512768,22478848,12124160,16941056,13344768,0,1024,0,9502720,9506816,1474560,0,1679360,61440,624640,5120,22801408,1740800,160,0,
86016,86528,7103488,19719168,20574208,22540288,12144640,16942592,13345792,1536,1024,320,9503232,9506944,1495040,96,1720320,122880,634880,5120,23047168,1818624,320,8192,87040,87040,7145472,19744768,20635648,22601728,12165120,16944128,
13346816,2560,3584,640,9503744,9507072,1515520,192,1761280,184320,645120,5120,23292928,1822720,480,8192,87040,87552,7187456,19770368,20697088,22663168,12185600,16945664,13347840,4096,3584,960,9504256,9507200,1536000,288,
0,2457600,2,1966080,1280,4,3,3072,4096,1073741824,3,1065353216,80000,2636,40960,1024,460545,0,2816,2816,44,12451840,55296,40960,0,1024,1408,2560,72,128,128,88,
1638400,2048,778240,5120,22576128,4382720,0,0,86016,86016,7061888,19693568,20512768,22478848,12451840,17727488,13361152,0,1024,0,9830400,9834496,1802240,0,1679360,63488,788480,5120,22821888,4386816,160,0,
86016,86528,7103872,19719168,20574208,22540288,12472320,17729024,13362176,1536,1024,320,9830912,9834624,1822720,96,1720320,124928,798720,5120,23067648,4464640,320,8192,87040,87040,7145856,19744768,20635648,22601728,12492800,17730560,
13363200,2560,3584,640,9831424,9834752,1843200,192,1761280,186368,808960,5120,23313408,4468736,480,8192,87040,87552,7187840,19770368,20697088,22663168,12513280,17732096,13364224,4096,3584,960,9831936,9834880,1863680,288,
896,2457600,2,1966080,1280,4,3,3072,4096,1073741824,0,1051011932,79616,2740,40960,1024,460545,0,3328,3328,52,12779520,59392,40960,0,1024,1408,2560,72,128,128,104,
2293760,0,1843200,5120,22709248,2392064,0,0,94208,94208,7733248,19693568,20512768,22478848,12779520,18513920,13377536,0,1024,0,10158080,10162176,2129920,0,2334720,61440,1853440,5120,22955008,2396160,160,0,
94208,94720,7775232,19719168,20574208,22540288,12800000,18515456,13378560,1536,1024,320,10158592,10162304,2150400,96,2375680,122880,1863680,5120,23200768,2473984,320,8192,95232,95232,7817216,19744768,20635648,22601728,12820480,18516992,
13379584,2560,3584,640,10159104,10162432,2170880,192,2416640,184320,1873920,5120,23446528,2478080,480,8192,95232,95744,7859200,19770368,20697088,22663168,12840960,18518528,13380608,4096,3584,960,10159616,10162560,2191360,288,
256,2457600,2,0,1280,4,3,3072,4096,1073741824,3,1065353216,79232,1073741824,40960,1024,460545,0,3840,3840,60,13107200,63488,40960,0,1024,1408,2560,72,128,128,120,
2293760,2048,2007040,5120,1073741824,5038080,0,0,94208,94208,7733632,19693568,20512768,22478848,13107200,19300352,13393920,0,1024,0,10485760,10489856,2457600,0,2334720,63488,2017280,5120,1073741824,5042176,160,0,
94208,94720,7775616,19719168,20574208,22540288,13127680,19301888,13394944,1536,1024,320,10486272,10489984,2478080,96,2375680,124928,2027520,5120,1073741824,5120000,320,8192,95232,95232,7817600,19744768,20635648,22601728,13148160,19303424,
13395968,2560,3584,640,10486784,10490112,2498560,192,2416640,186368,2037760,5120,1073741824,5124096,480,8192,95232,95744,7859584,19770368,20697088,22663168,13168640,19304960,13396992,4096,3584,960,10487296,10490240,2519040,288,
128,2457600,0,819200,1280,4,3,3072,4096,1073741824,0,1051011932,78848,1340,40960,1024,3355409,0,260,256,4,10813440,34944,40960,160,1024,1408,2560,72,128,128,8,
327680,256,615040,10240,20179968,434176,0,0,69632,69632,5718144,19795968,20758528,22724608,10816000,13801472,13279232,5120,6144,1280,8194048,8196608,163840,384,368640,61696,620160,10240,20282368,438272,160,0,
69632,70144,5760128,19821568,20819968,22786048,10836480,13803008,13280256,6656,6144,1600,8194560,8196736,184320,480,409600,123136,640640,10240,20384768,516096,320,8192,70656,70656,5802112,19847168,20881408,22847488,10856960,13804544,
13281280,7680,8704,1920,8195072,8196864,204800,576,450560,184576,645760,10240,20487168,520192,480,8192,70656,71168,5844096,19872768,20942848,22908928,10877440,13806080,13282304,9216,8704,2240,8195584,8196992,225280,672,
768,2457600,0,819200,1280,4,3,3072,4096,1073741824,3,1065353216,78464,1356,40960,1024,7829265,0,772,768,12,11141120,39040,40960,160,1024,1408,2560,72,128,128,24,
327680,2304,635520,25600,20200448,3080192,0,0,69632,69632,5718528,19795968,20758528,22724608,11143680,14587904,13295616,5120,6144,1280,8521728,8524288,491520,384,368640,63744,686720,25600,20302848,3084288,160,0,
69632,70144,5760512,19821568,20819968,22786048,11164160,14589440,13296640,6656,6144,1600,8522240,8524416,512000,480,409600,125184,737920,25600,20405248,3162112,320,8192,70656,70656,5802496,19847168,20881408,22847488,11184640,14590976,
13297664,7680,8704,1920,8522752,8524544,532480,576,450560,186624,789120,25600,20507648,3166208,480,8192,70656,71168,5844480,19872768,20942848,22908928,11205120,14592512,13298688,9216,8704,2240,8523264,8524672,552960,672,
384,2457600,1,1966080,3072,3,4,61440,62464,63488,0,1051011932,78080,2352,40960,1024,7829265,0,1284,1280,20,11468800,43136,40960,160,1024,1408,2560,96,104,160,40,
983040,256,1181952,12288,22024192,1089536,0,0,77824,77824,6389888,19795968,20758528,22724608,11471360,15374336,13312000,5120,6144,1280,8849408,8851968,819200,384,1024000,61696,1206528,12288,22147072,1093632,192,0,
77824,78336,6431872,19821568,20819968,22786048,11491840,15375872,13313024,6656,6144,1600,8849920,8852096,839680,480,1064960,123136,1231104,12288,22269952,1171456,384,8192,78848,78848,6473856,19847168,20881408,22847488,11512320,15377408,
13314048,7680,8704,1920,8850432,8852224,860160,576,1105920,184576,1255680,12288,22392832,1175552,576,8192,78848,79360,6515840,19872768,20942848,22908928,11532800,15378944,13315072,9216,8704,2240,8850944,8852352,880640,672,
1024,2457600,1,1966080,3072,3,4,61440,62464,63488,3,1065353216,77696,2360,40960,1024,7829265,0,1796,1792,28,11796480,47232,40960,160,1024,1408,2560,96,104,160,56,
983040,2304,1378560,12288,22030336,3735552,0,0,77824,77824,6390272,19795968,20758528,22724608,11799040,16160768,13328384,5120,6144,1280,9177088,9179648,1146880,384,1024000,63744,1403136,12288,22153216,3739648,192,0,
77824,78336,6432256,19821568,20819968,22786048,11819520,16162304,13329408,6656,6144,1600,9177600,9179776,1167360,480,1064960,125184,1427712,12288,22276096,3817472,384,8192,78848,78848,6474240,19847168,20881408,22847488,11840000,16163840,
13330432,7680,8704,1920,9178112,9179904,1187840,576,1105920,186624,1452288,12288,22398976,3821568,576,8192,78848,79360,6516224,19872768,20942848,22908928,11860480,16165376,13331456,9216,8704,2240,9178624,9180032,1208320,672,
768,2457600,2,1966080,1280,4,3,3072,4096,1073741824,0,1051011932,77312,2876,40960,1024,7829265,0,2308,2304,36,12124160,51328,40960,160,1024,1408,2560,72,128,128,72,
1638400,256,615040,5120,23538688,1744896,0,0,86016,86016,7061632,19795968,20758528,22724608,12126720,16947200,13344768,5120,6144,1280,9504768,9507328,1474560,384,1679360,61696,625280,5120,23784448,1748992,160,0,
86016,86528,7103616,19821568,20819968,22786048,12147200,16948736,13345792,6656,6144,1600,9505280,9507456,1495040,480,1720320,123136,635520,5120,24030208,1826816,320,8192,87040,87040,7145600,19847168,20881408,22847488,12167680,16950272,
13346816,7680,8704,1920,9505792,9507584,1515520,576,1761280,184576,645760,5120,24275968,1830912,480,8192,87040,87552,7187584,19872768,20942848,22908928,12188160,16951808,13347840,9216,8704,2240,9506304,9507712,1536000,672,
128,2457600,2,1966080,1280,4,3,3072,4096,1073741824,3,1065353216,76928,2892,40960,1024,7829265,0,2820,2816,44,12451840,55424,40960,160,1024,1408,2560,72,128,128,88,
1638400,2304,778880,5120,23559168,4390912,0,0,86016,86016,7062016,19795968,20758528,22724608,12454400,17733632,13361152,5120,6144,1280,9832448,9835008,1802240,384,1679360,63744,789120,5120,23804928,4395008,160,0,
86016,86528,7104000,19821568,20819968,22786048,12474880,17735168,13362176,6656,6144,1600,9832960,9835136,1822720,480,1720320,125184,799360,5120,24050688,4472832,320,8192,87040,87040,7145984,19847168,20881408,22847488,12495360,17736704,
13363200,7680,8704,1920,9833472,9835264,1843200,576,1761280,186624,809600,5120,24296448,4476928,480,8192,87040,87552,7187968,19872768,20942848,22908928,12515840,17738240,13364224,9216,8704,2240,9833984,9835392,1863680,672,
1024,2457600,2,1966080,1280,4,3,3072,4096,1073741824,0,1051011932,76544,2996,40960,1024,7829265,0,3332,3328,52,12779520,59520,40960,160,1024,1408,2560,72,128,128,104,
2293760,256,1843840,5120,23692288,2400256,0,0,94208,94208,7733376,19795968,20758528,22724608,12782080,18520064,13377536,5120,6144,1280,10160128,10162688,2129920,384,2334720,61696,1854080,5120,23938048,2404352,160,0,
94208,94720,7775360,19821568,20819968,22786048,12802560,18521600,13378560,6656,6144,1600,10160640,10162816,2150400,480,2375680,123136,1864320,5120,24183808,2482176,320,8192,95232,95232,7817344,19847168,20881408,22847488,12823040,18523136,
13379584,7680,8704,1920,10161152,10162944,2170880,576,2416640,184576,1874560,5120,24429568,2486272,480,8192,95232,95744,7859328,19872768,20942848,22908928,12843520,18524672,13380608,9216,8704,2240,10161664,10163072,2191360,672,
384,2457600,2,0,1280,4,3,3072,4096,1073741824,3,1065353216,76160,1073741824,40960,1024,7829265,0,3844,3840,60,13107200,63616,40960,160,1024,1408,2560,72,128,128,120,
2293760,2304,2007680,5120,1073741824,5046272,0,0,94208,94208,7733760,19795968,20758528,22724608,13109760,19306496,13393920,5120,6144,1280,10487808,10490368,2457600,384,2334720,63744,2017920,5120,1073741824,5050368,160,0,
94208,94720,7775744,19821568,20819968,22786048,13130240,19308032,13394944,6656,6144,1600,10488320,10490496,2478080,480,2375680,125184,2028160,5120,1073741824,5128192,320,8192,95232,95232,7817728,19847168,20881408,22847488,13150720,19309568,
13395968,7680,8704,1920,10488832,10490624,2498560,576,2416640,186624,2038400,5120,1073741824,5132288,480,8192,95232,95744,7859712,19872768,20942848,22908928,13171200,19311104,13396992,9216,8704,2240,10489344,10490752,2519040,672,
0,2457600,0,819200,1280,4,3,3072,4096,1073741824,0,1051011932,75776,1088,40960,1024,197377,0,264,256,4,10813440,35072,41088,320,1024,1664,2560,72,128,128,8,
327680,512,665600,10240,19775488,442368,0,0,69632,69632,5718272,19898368,21004288,22970368,10818560,13807616,13279232,10240,11264,2560,8197120,8201216,163840,768,368640,61952,670720,10240,19877888,446464,160,0,
69632,70144,5760256,19923968,21065728,23031808,10839040,13809152,13280256,11776,11264,2880,8197632,8201344,184320,864,409600,123392,691200,10240,19980288,524288,320,8192,70656,70656,5802240,19949568,21127168,23093248,10859520,13810688,
13281280,12800,13824,3200,8198144,8201472,204800,960,450560,184832,696320,10240,20082688,528384,480,8192,70656,71168,5844224,19975168,21188608,23154688,10880000,13812224,13282304,14336,13824,3520,8198656,8201600,225280,1056,
640,2457600,1,1966080,3072,3,4,61440,62464,63488,1,1065353216,75392,1536,41984,256,460545,0,776,768,12,11141120,39168,41088,320,1024,1664,2560,96,104,160,24,
327680,2560,0,12288,20512768,5677056,0,0,69632,69632,5718016,19898368,21004288,22970368,11146240,14594048,13295616,10240,11264,2560,8524800,8528896,491520,768,368640,64000,24576,12288,20635648,5678080,192,0,
69632,70144,5760000,19923968,21065728,23031808,11166720,14595584,13296640,11776,11264,2880,8525312,8529024,512000,864,409600,125440,49152,12288,20758528,5761024,384,8192,70656,70656,5801984,19949568,21127168,23093248,11187200,14597120,
13297664,12800,13824,3200,8525824,8529152,532480,960,450560,186880,73728,12288,20881408,5762048,576,8192,70656,71168,5843968,19975168,21188608,23154688,11207680,14598656,13298688,14336,13824,3520,8526336,8529280,552960,1056,
256,2457600,1,1966080,3072,3,4,61440,62464,63488,0,1051011932,75008,1588,40960,1024,460545,0,1288,1280,20,11468800,43264,41088,320,1024,1664,2560,96,104,160,40,
983040,512,1277952,12288,20552704,1097728,0,0,77824,77824,6390016,19898368,21004288,22970368,11473920,15380480,13312000,10240,11264,2560,8852480,8856576,819200,768,1024000,61952,1302528,12288,20675584,1101824,192,0,
77824,78336,6432000,19923968,21065728,23031808,11494400,15382016,13313024,11776,11264,2880,8852992,8856704,839680,864,1064960,123392,1327104,12288,20798464,1179648,384,8192,78848,78848,6473984,19949568,21127168,23093248,11514880,15383552,
13314048,12800,13824,3200,8853504,8856832,860160,960,1105920,184832,1351680,12288,20921344,1183744,576,8192,78848,79360,6515968,19975168,21188608,23154688,11535360,15385088,13315072,14336,13824,3520,8854016,8856960,880640,1056,
896,2457600,1,1966080,3072,3,4,61440,62464,63488,1,1065353216,74624,1596,41984,256,460545,0,1800,1792,28,11796480,47360,41088,320,1024,1664,2560,96,104,160,56,
983040,2560,1474560,12288,20558848,6348800,0,0,77824,77824,6389760,19898368,21004288,22970368,11801600,16166912,13328384,10240,11264,2560,9180160,9184256,1146880,768,1024000,64000,1499136,12288,20681728,6349824,192,0,
77824,78336,6431744,19923968,21065728,23031808,11822080,16168448,13329408,11776,11264,2880,9180672,9184384,1167360,864,1064960,125440,1523712,12288,20804608,6432768,384,8192,78848,78848,6473728,19949568,21127168,23093248,11842560,16169984,
13330432,12800,13824,3200,9181184,9184512,1187840,960,1105920,186880,1548288,12288,20927488,6433792,576,8192,78848,79360,6515712,19975168,21188608,23154688,11863040,16171520,13331456,14336,13824,3520,9181696,9184640,1208320,1056,
640,2457600,2,1966080,1280,4,3,3072,4096,1073741824,0,1051011932,74240,2624,40960,1024,460545,0,2312,2304,36,12124160,51456,41088,320,1024,1664,2560,72,128,128,72,
1638400,512,655360,5120,22560768,1753088,0,0,86016,86016,7061760,19898368,21004288,22970368,12129280,16953344,13344768,10240,11264,2560,9507840,9511936,1474560,768,1679360,61952,665600,5120,22806528,1757184,160,0,
86016,86528,7103744,19923968,21065728,23031808,12149760,16954880,13345792,11776,11264,2880,9508352,9512064,1495040,864,1720320,123392,675840,5120,23052288,1835008,320,8192,87040,87040,7145728,19949568,21127168,23093248,12170240,16956416,
13346816,12800,13824,3200,9508864,9512192,1515520,960,1761280,184832,686080,5120,23298048,1839104,480,8192,87040,87552,7187712,19975168,21188608,23154688,12190720,16957952,13347840,14336,13824,3520,9509376,9512320,1536000,1056,
0,2457600,2,1966080,1280,4,3,3072,4096,1073741824,1,1065353216,73856,2640,41984,256,460545,0,2824,2816,44,12451840,55552,41088,320,1024,1664,2560,72,128,128,88,
1638400,2560,819200,5120,22581248,7020544,0,0,86016,86016,7061504,19898368,21004288,22970368,12456960,17739776,13361152,10240,11264,2560,9835520,9839616,1802240,768,1679360,64000,829440,5120,22827008,7021568,160,0,
86016,86528,7103488,19923968,21065728,23031808,12477440,17741312,13362176,11776,11264,2880,9836032,9839744,1822720,864,1720320,125440,839680,5120,23072768,7104512,320,8192,87040,87040,7145472,19949568,21127168,23093248,12497920,17742848,
13363200,12800,13824,3200,9836544,9839872,1843200,960,1761280,186880,849920,5120,23318528,7105536,480,8192,87040,87552,7187456,19975168,21188608,23154688,12518400,17744384,13364224,14336,13824,3520,9837056,9840000,1863680,1056,
896,2457600,2,1966080,1280,4,3,3072,4096,1073741824,0,1051011932,73472,2744,40960,1024,460545,0,3336,3328,52,12779520,59648,41088,320,1024,1664,2560,72,128,128,104,
2293760,512,1884160,5120,22714368,2408448,0,0,94208,94208,7733504,19898368,21004288,22970368,12784640,18526208,13377536,10240,11264,2560,10163200,10167296,2129920,768,2334720,61952,1894400,5120,22960128,2412544,160,0,
94208,94720,7775488,19923968,21065728,23031808,12805120,18527744,13378560,11776,11264,2880,10163712,10167424,2150400,864,2375680,123392,1904640,5120,23205888,2490368,320,8192,95232,95232,7817472,19949568,21127168,23093248,12825600,18529280,
13379584,12800,13824,3200,10164224,10167552,2170880,960,2416640,184832,1914880,5120,23451648,2494464,480,8192,95232,95744,7859456,19975168,21188608,23154688,12846080,18530816,13380608,14336,13824,3520,10164736,10167680,2191360,1056,
256,2457600,2,0,1280,4,3,3072,4096,1073741824,1,1065353216,73088,1073741824,41984,256,460545,0,3848,3840,60,13107200,63744,41088,320,1024,1664,2560,72,128,128,120,
2293760,2560,2048000,5120,1073741824,7692288,0,0,94208,94208,7733248,19898368,21004288,22970368,13112320,19312640,13393920,10240,11264,2560,10490880,10494976,2457600,768,2334720,64000,2058240,5120,1073741824,7693312,160,0,
94208,94720,7775232,19923968,21065728,23031808,13132800,19314176,13394944,11776,11264,2880,10491392,10495104,2478080,864,2375680,125440,2068480,5120,1073741824,7776256,320,8192,95232,95232,7817216,19949568,21127168,23093248,13153280,19315712,
13395968,12800,13824,3200,10491904,10495232,2498560,960,2416640,186880,2078720,5120,1073741824,7777280,480,8192,95232,95744,7859200,19975168,21188608,23154688,13173760,19317248,13396992,14336,13824,3520,10492416,10495360,2519040,1056,
128,2457600,0,819200,1280,4,3,3072,4096,1073741824,0,1051011932,72704,1344,40960,1024,3355409,0,268,256,4,10813440,35200,41088,480,1024,1664,2560,72,128,128,8,
327680,768,666240,10240,20185088,450560,0,0,69632,69632,5718400,20000768,21250048,23216128,10821120,13813760,13279232,15360,16384,3840,8199168,8201728,163840,1152,368640,62208,671360,10240,20287488,454656,160,0,
69632,70144,5760384,20026368,21311488,23277568,10841600,13815296,13280256,16896,16384,4160,8199680,8201856,184320,1248,409600,123648,691840,10240,20389888,532480,320,8192,70656,70656,5802368,20051968,21372928,23339008,10862080,13816832,
13281280,17920,18944,4480,8200192,8201984,204800,1344,450560,185088,696960,10240,20492288,536576,480,8192,70656,71168,5844352,20077568,21434368,23400448,10882560,13818368,13282304,19456,18944,4800,8200704,8202112,225280,1440,
768,2457600,1,1966080,3072,3,4,61440,62464,63488,1,1065353216,72320,1792,41984,256,7829265,0,780,768,12,11141120,39296,41088,480,1024,1664,2560,96,104,160,24,
327680,2816,768,12288,21004288,5679104,0,0,69632,69632,5718144,20000768,21250048,23216128,11148800,14600192,13295616,15360,16384,3840,8526848,8529408,491520,1152,368640,64256,25344,12288,21127168,5680128,192,0,
69632,70144,5760128,20026368,21311488,23277568,11169280,14601728,13296640,16896,16384,4160,8527360,8529536,512000,1248,409600,125696,49920,12288,21250048,5763072,384,8192,70656,70656,5802112,20051968,21372928,23339008,11189760,14603264,
13297664,17920,18944,4480,8527872,8529664,532480,1344,450560,187136,74496,12288,21372928,5764096,576,8192,70656,71168,5844096,20077568,21434368,23400448,11210240,14604800,13298688,19456,18944,4800,8528384,8529792,552960,1440,
384,2457600,1,1966080,3072,3,4,61440,62464,63488,0,1051011932,71936,1844,40960,1024,7829265,0,1292,1280,20,11468800,43392,41088,480,1024,1664,2560,96,104,160,40,
983040,768,1278720,12288,21044224,1105920,0,0,77824,77824,6390144,20000768,21250048,23216128,11476480,15386624,13312000,15360,16384,3840,8854528,8857088,819200,1152,1024000,62208,1303296,12288,21167104,1110016,192,0,
77824,78336,6432128,20026368,21311488,23277568,11496960,15388160,13313024,16896,16384,4160,8855040,8857216,839680,1248,1064960,123648,1327872,12288,21289984,1187840,384,8192,78848,78848,6474112,20051968,21372928,23339008,11517440,15389696,
13314048,17920,18944,4480,8855552,8857344,860160,1344,1105920,185088,1352448,12288,21412864,1191936,576,8192,78848,79360,6516096,20077568,21434368,23400448,11537920,15391232,13315072,19456,18944,4800,8856064,8857472,880640,1440,
1024,2457600,1,1966080,3072,3,4,61440,62464,63488,1,1065353216,71552,1852,41984,256,7829265,0,1804,1792,28,11796480,47488,41088,480,1024,1664,2560,96,104,160,56,
983040,2816,1475328,12288,21050368,6350848,0,0,77824,77824,6389888,20000768,21250048,23216128,11804160,16173056,13328384,15360,16384,3840,9182208,9184768,1146880,1152,1024000,64256,1499904,12288,21173248,6351872,192,0,
77824,78336,6431872,20026368,21311488,23277568,11824640,16174592,13329408,16896,16384,4160,9182720,9184896,1167360,1248,1064960,125696,1524480,12288,21296128,6434816,384,8192,78848,78848,6473856,20051968,21372928,23339008,11845120,16176128,
13330432,17920,18944,4480,9183232,9185024,1187840,1344,1105920,187136,1549056,12288,21419008,6435840,576,8192,78848,79360,6515840,20077568,21434368,23400448,11865600,16177664,13331456,19456,18944,4800,9183744,9185152,1208320,1440,
768,2457600,2,1966080,1280,4,3,3072,4096,1073741824,0,1051011932,71168,2880,40960,1024,7829265,0,2316,2304,36,12124160,51584,41088,480,1024,1664,2560,72,128,128,72,
1638400,768,656000,5120,23543808,1761280,0,0,86016,86016,7061888,20000768,21250048,23216128,12131840,16959488,13344768,15360,16384,3840,9509888,9512448,1474560,1152,1679360,62208,666240,5120,23789568,1765376,160,0,
86016,86528,7103872,20026368,21311488,23277568,12152320,16961024,13345792,16896,16384,4160,9510400,9512576,1495040,1248,1720320,123648,676480,5120,24035328,1843200,320,8192,87040,87040,7145856,20051968,21372928,23339008,12172800,16962560,
13346816,17920,18944,4480,9510912,9512704,1515520,1344,1761280,185088,686720,5120,24281088,1847296,480,8192,87040,87552,7187840,20077568,21434368,23400448,12193280,16964096,13347840,19456,18944,4800,9511424,9512832,1536000,1440,
128,2457600,2,1966080,1280,4,3,3072,4096,1073741824,1,1065353216,70784,2896,41984,256,7829265,0,2828,2816,44,12451840,55680,41088,480,1024,1664,2560,72,128,128,88,
1638400,2816,819840,5120,23564288,7022592,0,0,86016,86016,7061632,20000768,21250048,23216128,12459520,17745920,13361152,15360,16384,3840,9837568,9840128,1802240,1152,1679360,64256,830080,5120,23810048,7023616,160,0,
86016,86528,7103616,20026368,21311488,23277568,12480000,17747456,13362176,16896,16384,4160,9838080,9840256,1822720,1248,1720320,125696,840320,5120,24055808,7106560,320,8192,87040,87040,7145600,20051968,21372928,23339008,12500480,17748992,
13363200,17920,18944,4480,9838592,9840384,1843200,1344,1761280,187136,850560,5120,24301568,7107584,480,8192,87040,87552,7187584,20077568,21434368,23400448,12520960,17750528,13364224,19456,18944,4800,9839104,9840512,1863680,1440,
1024,2457600,2,1966080,1280,4,3,3072,4096,1073741824,0,1051011932,70400,3000,40960,1024,7829265,0,3340,3328,52,12779520,59776,41088,480,1024,1664,2560,72,128,128,104,
2293760,768,1884800,5120,23697408,2416640,0,0,94208,94208,7733632,20000768,21250048,23216128,12787200,18532352,13377536,15360,16384,3840,10165248,10167808,2129920,1152,2334720,62208,1895040,5120,23943168,2420736,160,0,
94208,94720,7775616,20026368,21311488,23277568,12807680,18533888,13378560,16896,16384,4160,10165760,10167936,2150400,1248,2375680,123648,1905280,5120,24188928,2498560,320,8192,95232,95232,7817600,20051968,21372928,23339008,12828160,18535424,
13379584,17920,18944,4480,10166272,10168064,2170880,1344,2416640,185088,1915520,5120,24434688,2502656,480,8192,95232,95744,7859584,20077568,21434368,23400448,12848640,18536960,13380608,19456,18944,4800,10166784,10168192,2191360,1440,
384,2457600,2,0,1280,4,3,3072,4096,1073741824,1,1065353216,70016,1073741824,41984,256,7829265,0,3852,3840,60,13107200,63872,41088,480,1024,1664,2560,72,128,128,120,
2293760,2816,2048640,5120,1073741824,7694336,0,0,94208,94208,7733376,20000768,21250048,23216128,13114880,19318784,13393920,15360,16384,3840,10492928,10495488,2457600,1152,2334720,64256,2058880,5120,1073741824,7695360,160,0,
94208,94720,7775360,20026368,21311488,23277568,13135360,19320320,13394944,16896,16384,4160,10493440,10495616,2478080,1248,2375680,125696,2069120,5120,1073741824,7778304,320,8192,95232,95232,7817344,20051968,21372928,23339008,13155840,19321856,
13395968,17920,18944,4480,10493952,10495744,2498560,1344,2416640,187136,2079360,5120,1073741824,7779328,480,8192,95232,95744,7859328,20077568,21434368,23400448,13176320,19323392,13396992,19456,18944,4800,10494464,10495872,2519040,1440,
0,2457600,0,819200,1280,4,3,3072,4096,1073741824,0,1051011932,69632,1092,40960,1024,3355409,0,272,256,4,10813440,35328,41216,640,1280,1920,2816,72,128,128,8,
327680,1024,716800,10240,19780608,458752,0,0,69632,69632,5718528,20103168,21495808,23461888,10823680,13819904,13279232,20480,21504,5120,8202240,8206336,163840,1536,368640,62464,721920,10240,19883008,462848,160,0,
69632,70144,5760512,20128768,21557248,23523328,10844160,13821440,13280256,22016,21504,5440,8202752,8206464,184320,1632,409600,123904,742400,10240,19985408,540672,320,8192,70656,70656,5802496,20154368,21618688,23584768,10864640,13822976,
13281280,23040,24064,5760,8203264,8206592,204800,1728,450560,185344,747520,10240,20087808,544768,480,8192,70656,71168,5844480,20179968,21680128,23646208,10885120,13824512,13282304,24576,24064,6080,8203776,8206720,225280,1824,
640,2457600,1,1966080,3072,3,4,61440,62464,63488,1,1065353216,69248,2048,41984,256,7829265,0,784,768,12,11141120,39424,41216,640,1280,1920,2816,96,104,160,24,
327680,3072,1536,12288,21495808,5681152,0,0,69632,69632,5718272,20103168,21495808,23461888,11151360,14606336,13295616,20480,21504,5120,8529920,8534016,491520,1536,368640,64512,26112,12288,21618688,5682176,192,0,
69632,70144,5760256,20128768,21557248,23523328,11171840,14607872,13296640,22016,21504,5440,8530432,8534144,512000,1632,409600,125952,50688,12288,21741568,5765120,384,8192,70656,70656,5802240,20154368,21618688,23584768,11192320,14609408,
13297664,23040,24064,5760,8530944,8534272,532480,1728,450560,187392,75264,12288,21864448,5766144,576,8192,70656,71168,5844224,20179968,21680128,23646208,11212800,14610944,13298688,24576,24064,6080,8531456,8534400,552960,1824,
256,2457600,1,1966080,3072,3,4,61440,62464,63488,0,1051011932,68864,2100,40960,1024,7829265,0,1296,1280,20,11468800,43520,41216,640,1280,1920,2816,96,104,160,40,
983040,1024,1279488,12288,21535744,1114112,0,0,77824,77824,6390272,20103168,21495808,23461888,11479040,15392768,13312000,20480,21504,5120,8857600,8861696,819200,1536,1024000,62464,1304064,12288,21658624,1118208,192,0,
77824,78336,6432256,20128768,21557248,23523328,11499520,15394304,13313024,22016,21504,5440,8858112,8861824,839680,1632,1064960,123904,1328640,12288,21781504,1196032,384,8192,78848,78848,6474240,20154368,21618688,23584768,11520000,15395840,
13314048,23040,24064,5760,8858624,8861952,860160,1728,1105920,185344,1353216,12288,21904384,1200128,576,8192,78848,79360,6516224,20179968,21680128,23646208,11540480,15397376,13315072,24576,24064,6080,8859136,8862080,880640,1824,
896,2457600,1,1966080,3072,3,4,61440,62464,63488,1,1065353216,68480,2108,41984,256,7829265,0,1808,1792,28,11796480,47616,41216,640,1280,1920,2816,96,104,160,56,
983040,3072,1476096,12288,21541888,6352896,0,0,77824,77824,6390016,20103168,21495808,23461888,11806720,16179200,13328384,20480,21504,5120,9185280,9189376,1146880,1536,1024000,64512,1500672,12288,21664768,6353920,192,0,
77824,78336,6432000,20128768,21557248,23523328,11827200,16180736,13329408,22016,21504,5440,9185792,9189504,1167360,1632,1064960,125952,1525248,12288,21787648,6436864,384,8192,78848,78848,6473984,20154368,21618688,23584768,11847680,16182272,
13330432,23040,24064,5760,9186304,9189632,1187840,1728,1105920,187392,1549824,12288,21910528,6437888,576,8192,78848,79360,6515968,20179968,21680128,23646208,11868160,16183808,13331456,24576,24064,6080,9186816,9189760,1208320,1824,
640,2457600,2,1966080,1280,4,3,3072,4096,1073741824,0,1051011932,68096,2628,40960,1024,7829265,0,2320,2304,36,12124160,51712,41216,640,1280,1920,2816,72,128,128,72,
1638400,1024,696320,5120,22565888,1769472,0,0,86016,86016,7062016,20103168,21495808,23461888,12134400,16965632,13344768,20480,21504,5120,9512960,9517056,1474560,1536,1679360,62464,706560,5120,22811648,1773568,160,0,
86016,86528,7104000,20128768,21557248,23523328,12154880,16967168,13345792,22016,21504,5440,9513472,9517184,1495040,1632,1720320,123904,716800,5120,23057408,1851392,320,8192,87040,87040,7145984,20154368,21618688,23584768,12175360,16968704,
13346816,23040,24064,5760,9513984,9517312,1515520,1728,1761280,185344,727040,5120,23303168,1855488,480,8192,87040,87552,7187968,20179968,21680128,23646208,12195840,16970240,13347840,24576,24064,6080,9514496,9517440,1536000,1824,
0,2457600,2,1966080,1280,4,3,3072,4096,1073741824,1,1065353216,67712,2644,41984,256,7829265,0,2832,2816,44,12451840,55808,41216,640,1280,1920,2816,72,128,128,88,
1638400,3072,860160,5120,22586368,7024640,0,0,86016,86016,7061760,20103168,21495808,23461888,12462080,17752064,13361152,20480,21504,5120,9840640,9844736,1802240,1536,1679360,64512,870400,5120,22832128,7025664,160,0,
86016,86528,7103744,20128768,21557248,23523328,12482560,17753600,13362176,22016,21504,5440,9841152,9844864,1822720,1632,1720320,125952,880640,5120,23077888,7108608,320,8192,87040,87040,7145728,20154368,21618688,23584768,12503040,17755136,
13363200,23040,24064,5760,9841664,9844992,1843200,1728,1761280,187392,890880,5120,23323648,7109632,480,8192,87040,87552,7187712,20179968,21680128,23646208,12523520,17756672,13364224,24576,24064,6080,9842176,9845120,1863680,1824,
896,2457600,2,1966080,1280,4,3,3072,4096,1073741824,0,1051011932,67328,2748,40960,1024,7829265,0,3344,3328,52,12779520,59904,41216,640,1280,1920,2816,72,128,128,104,
2293760,1024,1925120,5120,22719488,2424832,0,0,94208,94208,7733760,20103168,21495808,23461888,12789760,18538496,13377536,20480,21504,5120,10168320,10172416,2129920,1536,2334720,62464,1935360,5120,22965248,2428928,160,0,
94208,94720,7775744,20128768,21557248,23523328,12810240,18540032,13378560,22016,21504,5440,10168832,10172544,2150400,1632,2375680,123904,1945600,5120,23211008,2506752,320,8192,95232,95232,7817728,20154368,21618688,23584768,12830720,18541568,
13379584,23040,24064,5760,10169344,10172672,2170880,1728,2416640,185344,1955840,5120,23456768,2510848,480,8192,95232,95744,7859712,20179968,21680128,23646208,12851200,18543104,13380608,24576,24064,6080,10169856,10172800,2191360,1824,
256,2457600,2,0,1280,4,3,3072,4096,1073741824,1,1065353216,66944,1073741824,41984,256,7829265,0,3856,3840,60,13107200,64000,41216,640,1280,1920,2816,72,128,128,120,
2293760,3072,2088960,5120,1073741824,7696384,0,0,94208,94208,7733504,20103168,21495808,23461888,13117440,19324928,13393920,20480,21504,5120,10496000,10500096,2457600,1536,2334720,64512,2099200,5120,1073741824,7697408,160,0,
94208,94720,7775488,20128768,21557248,23523328,13137920,19326464,13394944,22016,21504,5440,10496512,10500224,2478080,1632,2375680,125952,2109440,5120,1073741824,7780352,320,8192,95232,95232,7817472,20154368,21618688,23584768,13158400,19328000,
13395968,23040,24064,5760,10497024,10500352,2498560,1728,2416640,187392,2119680,5120,1073741824,7781376,480,8192,95232,95744,7859456,20179968,21680128,23646208,13178880,19329536,13396992,24576,24064,6080,10497536,10500480,2519040,1824,
128,2457600,0,819200,1280,4,3,3072,4096,1073741824,3,1065353216,66560,1348,40960,1024,197377,0,276,256,4,10813440,35456,41344,800,1280,1920,2816,72,128,128,8,
327680,1280,717440,10240,20190208,3047424,0,0,69632,69632,5718016,20205568,21741568,23707648,10826240,13826048,13279232,25600,26624,6400,8204288,8206848,163840,1920,368640,62720,722560,10240,20292608,3051520,160,0,
69632,70144,5760000,20231168,21803008,23769088,10846720,13827584,13280256,27136,26624,6720,8204800,8206976,184320,2016,409600,124160,743040,10240,20395008,3129344,320,8192,70656,70656,5801984,20256768,21864448,23830528,10867200,13829120,
13281280,28160,29184,7040,8205312,8207104,204800,2112,450560,185600,748160,10240,20497408,3133440,480,8192,70656,71168,5843968,20282368,21925888,23891968,10887680,13830656,13282304,29696,29184,7360,8205824,8207232,225280,2208,
768,2457600,1,1966080,3072,3,4,61440,62464,63488,1,1065353216,66176,2304,41984,256,460545,0,788,768,12,11141120,39552,41344,800,1280,1920,2816,96,104,160,24,
327680,3328,2304,12288,21987328,5683200,0,0,69632,69632,5718400,20205568,21741568,23707648,11153920,14612480,13295616,25600,26624,6400,8531968,8534528,491520,1920,368640,64768,26880,12288,22110208,5684224,192,0,
69632,70144,5760384,20231168,21803008,23769088,11174400,14614016,13296640,27136,26624,6720,8532480,8534656,512000,2016,409600,126208,51456,12288,22233088,5767168,384,8192,70656,70656,5802368,20256768,21864448,23830528,11194880,14615552,
13297664,28160,29184,7040,8532992,8534784,532480,2112,450560,187648,76032,12288,22355968,5768192,576,8192,70656,71168,5844352,20282368,21925888,23891968,11215360,14617088,13298688,29696,29184,7360,8533504,8534912,552960,2208,
384,2457600,1,1966080,3072,3,4,61440,62464,63488,3,1065353216,65792,2356,40960,1024,460545,0,1300,1280,20,11468800,43648,41344,800,1280,1920,2816,96,104,160,40,
983040,1280,1280256,12288,22027264,3702784,0,0,77824,77824,6389760,20205568,21741568,23707648,11481600,15398912,13312000,25600,26624,6400,8859648,8862208,819200,1920,1024000,62720,1304832,12288,22150144,3706880,192,0,
77824,78336,6431744,20231168,21803008,23769088,11502080,15400448,13313024,27136,26624,6720,8860160,8862336,839680,2016,1064960,124160,1329408,12288,22273024,3784704,384,8192,78848,78848,6473728,20256768,21864448,23830528,11522560,15401984,
13314048,28160,29184,7040,8860672,8862464,860160,2112,1105920,185600,1353984,12288,22395904,3788800,576,8192,78848,79360,6515712,20282368,21925888,23891968,11543040,15403520,13315072,29696,29184,7360,8861184,8862592,880640,2208,
1024,2457600,1,1966080,3072,3,4,61440,62464,63488,1,1065353216,65408,2364,41984,256,460545,0,1812,1792,28,11796480,47744,41344,800,1280,1920,2816,96,104,160,56,
983040,3328,1476864,12288,22033408,6354944,0,0,77824,77824,6390144,20205568,21741568,23707648,11809280,16185344,13328384,25600,26624,6400,9187328,9189888,1146880,1920,1024000,64768,1501440,12288,22156288,6355968,192,0,
77824,78336,6432128,20231168,21803008,23769088,11829760,16186880,13329408,27136,26624,6720,9187840,9190016,1167360,2016,1064960,126208,1526016,12288,22279168,6438912,384,8192,78848,78848,6474112,20256768,21864448,23830528,11850240,16188416,
13330432,28160,29184,7040,9188352,9190144,1187840,2112,1105920,187648,1550592,12288,22402048,6439936,576,8192,78848,79360,6516096,20282368,21925888,23891968,11870720,16189952,13331456,29696,29184,7360,9188864,9190272,1208320,2208,
768,2457600,2,1966080,1280,4,3,3072,4096,1073741824,3,1065353216,65024,2884,40960,1024,460545,0,2324,2304,36,12124160,51840,41344,800,1280,1920,2816,72,128,128,72,
1638400,1280,696960,5120,23548928,4358144,0,0,86016,86016,7061504,20205568,21741568,23707648,12136960,16971776,13344768,25600,26624,6400,9515008,9517568,1474560,1920,1679360,62720,707200,5120,23794688,4362240,160,0,
86016,86528,7103488,20231168,21803008,23769088,12157440,16973312,13345792,27136,26624,6720,9515520,9517696,1495040,2016,1720320,124160,717440,5120,24040448,4440064,320,8192,87040,87040,7145472,20256768,21864448,23830528,12177920,16974848,
13346816,28160,29184,7040,9516032,9517824,1515520,2112,1761280,185600,727680,5120,24286208,4444160,480,8192,87040,87552,7187456,20282368,21925888,23891968,12198400,16976384,13347840,29696,29184,7360,9516544,9517952,1536000,2208,
128,2457600,2,1966080,1280,4,3,3072,4096,1073741824,1,1065353216,64640,2900,41984,256,460545,0,2836,2816,44,12451840,55936,41344,800,1280,1920,2816,72,128,128,88,
1638400,3328,860800,5120,23569408,7026688,0,0,86016,86016,7061888,20205568,21741568,23707648,12464640,17758208,13361152,25600,26624,6400,9842688,9845248,1802240,1920,1679360,64768,871040,5120,23815168,7027712,160,0,
86016,86528,7103872,20231168,21803008,23769088,12485120,17759744,13362176,27136,26624,6720,9843200,9845376,1822720,2016,1720320,126208,881280,5120,24060928,7110656,320,8192,87040,87040,7145856,20256768,21864448,23830528,12505600,17761280,
13363200,28160,29184,7040,9843712,9845504,1843200,2112,1761280,187648,891520,5120,24306688,7111680,480,8192,87040,87552,7187840,20282368,21925888,23891968,12526080,17762816,13364224,29696,29184,7360,9844224,9845632,1863680,2208,
1024,2457600,2,1966080,1280,4,3,3072,4096,1073741824,3,1065353216,64256,3004,40960,1024,460545,0,3348,3328,52,12779520,60032,41344,800,1280,1920,2816,72,128,128,104,
2293760,1280,1925760,5120,23702528,5013504,0,0,94208,94208,7733248,20205568,21741568,23707648,12792320,18544640,13377536,25600,26624,6400,10170368,10172928,2129920,1920,2334720,62720,1936000,5120,23948288,5017600,160,0,
94208,94720,7775232,20231168,21803008,23769088,12812800,18546176,13378560,27136,26624,6720,10170880,10173056,2150400,2016,2375680,124160,1946240,5120,24194048,5095424,320,8192,95232,95232,7817216,20256768,21864448,23830528,12833280,18547712,
13379584,28160,29184,7040,10171392,10173184,2170880,2112,2416640,185600,1956480,5120,24439808,5099520,480,8192,95232,95744,7859200,20282368,21925888,23891968,12853760,18549248,13380608,29696,29184,7360,10171904,10173312,2191360,2208,
384,2457600,2,0,1280,4,3,3072,4096,1073741824,1,1065353216,63872,1073741824,41984,256,460545,0,3860,3840,60,13107200,64128,41344,800,1280,1920,2816,72,128,128,120,
2293760,3328,2089600,5120,1073741824,7698432,0,0,94208,94208,7733632,20205568,21741568,23707648,13120000,19331072,13393920,25600,26624,6400,10498048,10500608,2457600,1920,2334720,64768,2099840,5120,1073741824,7699456,160,0,
94208,94720,7775616,20231168,21803008,23769088,13140480,19332608,13394944,27136,26624,6720,10498560,10500736,2478080,2016,2375680,126208,2110080,5120,1073741824,7782400,320,8192,95232,95232,7817600,20256768,21864448,23830528,13160960,19334144,
13395968,28160,29184,7040,10499072,10500864,2498560,2112,2416640,187648,2120320,5120,1073741824,7783424,480,8192,95232,95744,7859584,20282368,21925888,23891968,13181440,19335680,13396992,29696,29184,7360,10499584,10500992,2519040,2208,
0,2457600,0,819200,1280,4,3,3072,4096,1073741824,3,1065353216,63488,1096,40960,1024,3355409,0,280,256,4,10813440,35584,41344,960,1280,2176,2816,72,128,128,8,
327680,1536,768000,10240,19785728,3055616,0,0,69632,69632,5718144,20307968,21987328,23953408,10828800,13832192,13279232,30720,31744,7680,8207360,8211456,163840,2304,368640,62976,773120,10240,19888128,3059712,160,0,
69632,70144,5760128,20333568,22048768,24014848,10849280,13833728,13280256,32256,31744,8000,8207872,8211584,184320,2400,409600,124416,793600,10240,19990528,3137536,320,8192,70656,70656,5802112,20359168,22110208,24076288,10869760,13835264,
13281280,33280,34304,8320,8208384,8211712,204800,2496,450560,185856,798720,10240,20092928,3141632,480,8192,70656,71168,5844096,20384768,22171648,24137728,10890240,13836800,13282304,34816,34304,8640,8208896,8211840,225280,2592,
640,2457600,1,1966080,3072,3,4,61440,62464,63488,1,1065353216,63104,1540,41984,256,7829265,0,792,768,12,11141120,39680,41344,960,1280,2176,2816,96,104,160,24,
327680,3584,98304,12288,20515840,5685248,0,0,69632,69632,5718528,20307968,21987328,23953408,11156480,14618624,13295616,30720,31744,7680,8535040,8539136,491520,2304,368640,65024,122880,12288,20638720,5686272,192,0,
69632,70144,5760512,20333568,22048768,24014848,11176960,14620160,13296640,32256,31744,8000,8535552,8539264,512000,2400,409600,126464,147456,12288,20761600,5769216,384,8192,70656,70656,5802496,20359168,22110208,24076288,11197440,14621696,
13297664,33280,34304,8320,8536064,8539392,532480,2496,450560,187904,172032,12288,20884480,5770240,576,8192,70656,71168,5844480,20384768,22171648,24137728,11217920,14623232,13298688,34816,34304,8640,8536576,8539520,552960,2592,
256,2457600,1,1966080,3072,3,4,61440,62464,63488,3,1065353216,62720,1592,40960,1024,7829265,0,1304,1280,20,11468800,43776,41344,960,1280,2176,2816,96,104,160,40,
983040,1536,1376256,12288,20555776,3710976,0,0,77824,77824,6389888,20307968,21987328,23953408,11484160,15405056,13312000,30720,31744,7680,8862720,8866816,819200,2304,1024000,62976,1400832,12288,20678656,3715072,192,0,
77824,78336,6431872,20333568,22048768,24014848,11504640,15406592,13313024,32256,31744,8000,8863232,8866944,839680,2400,1064960,124416,1425408,12288,20801536,3792896,384,8192,78848,78848,6473856,20359168,22110208,24076288,11525120,15408128,
13314048,33280,34304,8320,8863744,8867072,860160,2496,1105920,185856,1449984,12288,20924416,3796992,576,8192,78848,79360,6515840,20384768,22171648,24137728,11545600,15409664,13315072,34816,34304,8640,8864256,8867200,880640,2592,
896,2457600,1,1966080,3072,3,4,61440,62464,63488,1,1065353216,62336,1600,41984,256,7829265,0,1816,1792,28,11796480,47872,41344,960,1280,2176,2816,96,104,160,56,
983040,3584,1572864,12288,20561920,6356992,0,0,77824,77824,6390272,20307968,21987328,23953408,11811840,16191488,13328384,30720,31744,7680,9190400,9194496,1146880,2304,1024000,65024,1597440,12288,20684800,6358016,192,0,
77824,78336,6432256,20333568,22048768,24014848,11832320,16193024,13329408,32256,31744,8000,9190912,9194624,1167360,2400,1064960,126464,1622016,12288,20807680,6440960,384,8192,78848,78848,6474240,20359168,22110208,24076288,11852800,16194560,
13330432,33280,34304,8320,9191424,9194752,1187840,2496,1105920,187904,1646592,12288,20930560,6441984,576,8192,78848,79360,6516224,20384768,22171648,24137728,11873280,16196096,13331456,34816,34304,8640,9191936,9194880,1208320,2592,
640,2457600,2,1966080,1280,4,3,3072,4096,1073741824,3,1065353216,61952,2632,40960,1024,7829265,0,2328,2304,36,12124160,51968,41344,960,1280,2176,2816,72,128,128,72,
1638400,1536,737280,5120,22571008,4366336,0,0,86016,86016,7061632,20307968,21987328,23953408,12139520,16977920,13344768,30720,31744,7680,9518080,9522176,1474560,2304,1679360,62976,747520,5120,22816768,4370432,160,0,
86016,86528,7103616,20333568,22048768,24014848,12160000,16979456,13345792,32256,31744,8000,9518592,9522304,1495040,2400,1720320,124416,757760,5120,23062528,4448256,320,8192,87040,87040,7145600,20359168,22110208,24076288,12180480,16980992,
13346816,33280,34304,8320,9519104,9522432,1515520,2496,1761280,185856,768000,5120,23308288,4452352,480,8192,87040,87552,7187584,20384768,22171648,24137728,12200960,16982528,13347840,34816,34304,8640,9519616,9522560,1536000,2592,
0,2457600,2,1966080,1280,4,3,3072,4096,1073741824,1,1065353216,61568,2648,41984,256,7829265,0,2840,2816,44,12451840,56064,41344,960,1280,2176,2816,72,128,128,88,
1638400,3584,901120,5120,22591488,7028736,0,0,86016,86016,7062016,20307968,21987328,23953408,12467200,17764352,13361152,30720,31744,7680,9845760,9849856,1802240,2304,1679360,65024,911360,5120,22837248,7029760,160,0,
86016,86528,7104000,20333568,22048768,24014848,12487680,17765888,13362176,32256,31744,8000,9846272,9849984,1822720,2400,1720320,126464,921600,5120,23083008,7112704,320,8192,87040,87040,7145984,20359168,22110208,24076288,12508160,17767424,
13363200,33280,34304,8320,9846784,9850112,1843200,2496,1761280,187904,931840,5120,23328768,7113728,480,8192,87040,87552,7187968,20384768,22171648,24137728,12528640,17768960,13364224,34816,34304,8640,9847296,9850240,1863680,2592,
896,2457600,2,0,1280,4,3,3072,4096,1073741824,3,1065353216,61184,1073741824,40960,1024,7829265,0,3352,3328,52,12779520,60160,41344,960,1280,2176,2816,72,128,128,104,
2293760,1536,1966080,5120,1073741824,5021696,0,0,94208,94208,7733376,20307968,21987328,23953408,12794880,18550784,13377536,30720,31744,7680,10173440,10177536,2129920,2304,2334720,62976,1976320,5120,1073741824,5025792,160,0,
94208,94720,7775360,20333568,22048768,24014848,12815360,18552320,13378560,32256,31744,8000,10173952,10177664,2150400,2400,2375680,124416,1986560,5120,1073741824,5103616,320,8192,95232,95232,7817344,20359168,22110208,24076288,12835840,18553856,
13379584,33280,34304,8320,10174464,10177792,2170880,2496,2416640,185856,1996800,5120,1073741824,5107712,480,8192,95232,95744,7859328,20384768,22171648,24137728,12856320,18555392,13380608,34816,34304,8640,10174976,10177920,2191360,2592,
256,2457600,2,0,1280,4,3,3072,4096,1073741824,1,1065353216,60800,1073741824,41984,256,7829265,0,3864,3840,60,13107200,64256,41344,960,1280,2176,2816,72,128,128,120,
2293760,3584,2129920,5120,1073741824,7700480,0,0,94208,94208,7733760,20307968,21987328,23953408,13122560,19337216,13393920,30720,31744,7680,10501120,10505216,2457600,2304,2334720,65024,2140160,5120,1073741824,7701504,160,0,
94208,94720,7775744,20333568,22048768,24014848,13143040,19338752,13394944,32256,31744,8000,10501632,10505344,2478080,2400,2375680,126464,2150400,5120,1073741824,7784448,320,8192,95232,95232,7817728,20359168,22110208,24076288,13163520,19340288,
13395968,33280,34304,8320,10502144,10505472,2498560,2496,2416640,187904,2160640,5120,1073741824,7785472,480,8192,95232,95744,7859712,20384768,22171648,24137728,13184000,19341824,13396992,34816,34304,8640,10502656,10505600,2519040,2592,
128,2457600,0,819200,1280,4,3,3072,4096,1073741824,3,1065353216,60416,1352,40960,1024,197377,0,284,256,4,10813440,35712,41472,1120,1280,2176,2816,72,128,128,8,
327680,1792,768640,10240,20195328,3063808,0,0,69632,69632,5718272,20410368,22233088,24199168,10831360,13838336,13279232,35840,36864,8960,8209408,8211968,163840,2688,368640,63232,773760,10240,20297728,3067904,160,0,
69632,70144,5760256,20435968,22294528,24260608,10851840,13839872,13280256,37376,36864,9280,8209920,8212096,184320,2784,409600,124672,794240,10240,20400128,3145728,320,8192,70656,70656,5802240,20461568,22355968,24322048,10872320,13841408,
13281280,38400,39424,9600,8210432,8212224,204800,2880,450560,186112,799360,10240,20502528,3149824,480,8192,70656,71168,5844224,20487168,22417408,24383488,10892800,13842944,13282304,39936,39424,9920,8210944,8212352,225280,2976,
768,0,2,0,1280,4,3,3072,4096,1073741824,2,1065353216,60032,1073741824,40960,1024,460545,0,796,768,12,11141120,39808,41472,1120,1280,2176,2816,72,128,128,24,
327680,3840,17982080,5120,1073741824,3047424,0,0,69632,69632,5718016,20410368,22233088,24199168,11159040,14624768,13295616,35840,36864,8960,8537088,8539648,491520,2688,368640,65280,17992320,5120,1073741824,3051520,160,4096,
69632,70144,5760000,20435968,22294528,24260608,11179520,14626304,13296640,37376,36864,9280,8537600,8539776,512000,2784,409600,126720,18002560,5120,1073741824,3129344,320,8192,70656,70656,5801984,20461568,22355968,24322048,11200000,14627840,
13297664,38400,39424,9600,8538112,8539904,532480,2880,450560,188160,18012800,5120,1073741824,3133440,480,12288,70656,71168,5843968,20487168,22417408,24383488,11220480,14629376,13298688,39936,39424,9920,8538624,8540032,552960,2976,
384,2457600,1,1966080,3072,3,4,61440,62464,63488,3,1065353216,59648,1848,40960,1024,460545,0,1308,1280,20,11468800,43904,41472,1120,1280,2176,2816,96,104,160,40,
983040,1792,1377024,12288,21047296,3719168,0,0,77824,77824,6390016,20410368,22233088,24199168,11486720,15411200,13312000,35840,36864,8960,8864768,8867328,819200,2688,1024000,63232,1401600,12288,21170176,3723264,192,0,
77824,78336,6432000,20435968,22294528,24260608,11507200,15412736,13313024,37376,36864,9280,8865280,8867456,839680,2784,1064960,124672,1426176,12288,21293056,3801088,384,8192,78848,78848,6473984,20461568,22355968,24322048,11527680,15414272,
13314048,38400,39424,9600,8865792,8867584,860160,2880,1105920,186112,1450752,12288,21415936,3805184,576,8192,78848,79360,6515968,20487168,22417408,24383488,11548160,15415808,13315072,39936,39424,9920,8866304,8867712,880640,2976,
1024,0,2,0,1280,4,3,3072,4096,1073741824,2,1065353216,59264,1073741824,40960,1024,460545,0,1820,1792,28,11796480,48000,41472,1120,1280,2176,2816,72,128,128,56,
983040,3840,17982080,5120,1073741824,3702784,0,0,77824,77824,6389760,20410368,22233088,24199168,11814400,16197632,13328384,35840,36864,8960,9192448,9195008,1146880,2688,1024000,65280,17992320,5120,1073741824,3706880,160,4096,
77824,78336,6431744,20435968,22294528,24260608,11834880,16199168,13329408,37376,36864,9280,9192960,9195136,1167360,2784,1064960,126720,18002560,5120,1073741824,3784704,320,8192,78848,78848,6473728,20461568,22355968,24322048,11855360,16200704,
13330432,38400,39424,9600,9193472,9195264,1187840,2880,1105920,188160,18012800,5120,1073741824,3788800,480,12288,78848,79360,6515712,20487168,22417408,24383488,11875840,16202240,13331456,39936,39424,9920,9193984,9195392,1208320,2976,
768,2457600,2,1966080,1280,4,3,3072,4096,1073741824,3,1065353216,58880,2888,40960,1024,460545,0,2332,2304,36,12124160,52096,41472,1120,1280,2176,2816,72,128,128,72,
1638400,1792,737920,5120,23554048,4374528,0,0,86016,86016,7061760,20410368,22233088,24199168,12142080,16984064,13344768,35840,36864,8960,9520128,9522688,1474560,2688,1679360,63232,748160,5120,23799808,4378624,160,0,
86016,86528,7103744,20435968,22294528,24260608,12162560,16985600,13345792,37376,36864,9280,9520640,9522816,1495040,2784,1720320,124672,758400,5120,24045568,4456448,320,8192,87040,87040,7145728,20461568,22355968,24322048,12183040,16987136,
13346816,38400,39424,9600,9521152,9522944,1515520,2880,1761280,186112,768640,5120,24291328,4460544,480,8192,87040,87552,7187712,20487168,22417408,24383488,12203520,16988672,13347840,39936,39424,9920,9521664,9523072,1536000,2976,
128,0,2,0,1280,4,3,3072,4096,1073741824,2,1065353216,58496,1073741824,40960,1024,460545,0,2844,2816,44,12451840,56192,41472,1120,1280,2176,2816,72,128,128,88,
1638400,3840,17982080,5120,1073741824,4358144,0,0,86016,86016,7061504,20410368,22233088,24199168,12469760,17770496,13361152,35840,36864,8960,9847808,9850368,1802240,2688,1679360,65280,17992320,5120,1073741824,4362240,160,4096,
86016,86528,7103488,20435968,22294528,24260608,12490240,17772032,13362176,37376,36864,9280,9848320,9850496,1822720,2784,1720320,126720,18002560,5120,1073741824,4440064,320,8192,87040,87040,7145472,20461568,22355968,24322048,12510720,17773568,
13363200,38400,39424,9600,9848832,9850624,1843200,2880,1761280,188160,18012800,5120,1073741824,4444160,480,12288,87040,87552,7187456,20487168,22417408,24383488,12531200,17775104,13364224,39936,39424,9920,9849344,9850752,1863680,2976,
1024,2457600,2,0,1280,4,3,3072,4096,1073741824,3,1065353216,58112,1073741824,40960,1024,460545,0,3356,3328,52,12779520,60288,41472,1120,1280,2176,2816,72,128,128,104,
2293760,1792,1966720,5120,1073741824,5029888,0,0,94208,94208,7733504,20410368,22233088,24199168,12797440,18556928,13377536,35840,36864,8960,10175488,10178048,2129920,2688,2334720,63232,1976960,5120,1073741824,5033984,160,0,
94208,94720,7775488,20435968,22294528,24260608,12817920,18558464,13378560,37376,36864,9280,10176000,10178176,2150400,2784,2375680,124672,1987200,5120,1073741824,5111808,320,8192,95232,95232,7817472,20461568,22355968,24322048,12838400,18560000,
13379584,38400,39424,9600,10176512,10178304,2170880,2880,2416640,186112,1997440,5120,1073741824,5115904,480,8192,95232,95744,7859456,20487168,22417408,24383488,12858880,18561536,13380608,39936,39424,9920,10177024,10178432,2191360,2976,
384,0,2,0,1280,4,3,3072,4096,1073741824,2,1065353216,57728,1073741824,40960,1024,460545,0,3868,3840,60,13107200,64384,41472,1120,1280,2176,2816,72,128,128,120,
2293760,3840,17982080,5120,1073741824,5013504,0,0,94208,94208,7733248,20410368,22233088,24199168,13125120,19343360,13393920,35840,36864,8960,10503168,10505728,2457600,2688,2334720,65280,17992320,5120,1073741824,5017600,160,4096,
94208,94720,7775232,20435968,22294528,24260608,13145600,19344896,13394944,37376,36864,9280,10503680,10505856,2478080,2784,2375680,126720,18002560,5120,1073741824,5095424,320,8192,95232,95232,7817216,20461568,22355968,24322048,13166080,19346432,
13395968,38400,39424,9600,10504192,10505984,2498560,2880,2416640,188160,18012800,5120,1073741824,5099520,480,12288,95232,95744,7859200,20487168,22417408,24383488,13186560,19347968,13396992,39936,39424,9920,10504704,10506112,2519040,2976,
128,2457600,1,1966080,3072,3,4,61440,62464,63488,0,1051011932,57344,1796,40960,1024,197377,0,384,384,6,10895360,35840,40960,0,1024,1408,2560,96,104,160,12,
491520,0,99072,12288,21007360,589824,0,0,71680,71680,5885952,19693568,20512768,22478848,10895360,13991936,13283328,0,1024,0,8273920,8278016,245760,0,532480,61440,123648,12288,21130240,593920,192,0,
71680,72192,5927936,19719168,20574208,22540288,10915840,13993472,13284352,1536,1024,320,8274432,8278144,266240,96,573440,122880,148224,12288,21253120,671744,384,8192,72704,72704,5969920,19744768,20635648,22601728,10936320,13995008,
13285376,2560,3584,640,8274944,8278272,286720,192,614400,184320,172800,12288,21376000,675840,576,8192,72704,73216,6011904,19770368,20697088,22663168,10956800,13996544,13286400,4096,3584,960,8275456,8278400,307200,288,
768,2457600,1,1966080,3072,3,4,61440,62464,63488,3,1065353216,56960,1804,40960,1024,460545,0,896,896,14,11223040,39936,40960,0,1024,1408,2560,96,104,160,28,
491520,2048,295680,12288,21013504,3235840,0,0,71680,71680,5886336,19693568,20512768,22478848,11223040,14778368,13299712,0,1024,0,8601600,8605696,573440,0,532480,63488,320256,12288,21136384,3239936,192,0,
71680,72192,5928320,19719168,20574208,22540288,11243520,14779904,13300736,1536,1024,320,8602112,8605824,593920,96,573440,124928,344832,12288,21259264,3317760,384,8192,72704,72704,5970304,19744768,20635648,22601728,11264000,14781440,
13301760,2560,3584,640,8602624,8605952,614400,192,614400,186368,369408,12288,21382144,3321856,576,8192,72704,73216,6012288,19770368,20697088,22663168,11284480,14782976,13302784,4096,3584,960,8603136,8606080,634880,288,
384,2457600,1,1966080,3072,3,4,61440,62464,63488,0,1051011932,56576,1856,40960,1024,460545,0,1408,1408,22,11550720,44032,40960,0,1024,1408,2560,96,104,160,44,
1146880,0,1573632,12288,21053440,1245184,0,0,79872,79872,6557696,19693568,20512768,22478848,11550720,15564800,13316096,0,1024,0,8929280,8933376,901120,0,1187840,61440,1598208,12288,21176320,1249280,192,0,
79872,80384,6599680,19719168,20574208,22540288,11571200,15566336,13317120,1536,1024,320,8929792,8933504,921600,96,1228800,122880,1622784,12288,21299200,1327104,384,8192,80896,80896,6641664,19744768,20635648,22601728,11591680,15567872,
13318144,2560,3584,640,8930304,8933632,942080,192,1269760,184320,1647360,12288,21422080,1331200,576,8192,80896,81408,6683648,19770368,20697088,22663168,11612160,15569408,13319168,4096,3584,960,8930816,8933760,962560,288,
1024,2457600,1,1966080,3072,3,4,61440,62464,63488,3,1065353216,56192,1864,40960,1024,460545,0,1920,1920,30,11878400,48128,40960,0,1024,1408,2560,96,104,160,60,
1146880,2048,1770240,12288,21059584,3891200,0,0,79872,79872,6558080,19693568,20512768,22478848,11878400,16351232,13332480,0,1024,0,9256960,9261056,1228800,0,1187840,63488,1794816,12288,21182464,3895296,192,0,
79872,80384,6600064,19719168,20574208,22540288,11898880,16352768,13333504,1536,1024,320,9257472,9261184,1249280,96,1228800,124928,1819392,12288,21305344,3973120,384,8192,80896,80896,6642048,19744768,20635648,22601728,11919360,16354304,
13334528,2560,3584,640,9257984,9261312,1269760,192,1269760,186368,1843968,12288,21428224,3977216,576,8192,80896,81408,6684032,19770368,20697088,22663168,11939840,16355840,13335552,4096,3584,960,9258496,9261440,1290240,288,
768,2457600,2,1966080,1280,4,3,3072,4096,1073741824,0,1051011932,55808,2904,40960,1024,460545,0,2432,2432,38,12206080,52224,40960,0,1024,1408,2560,72,128,128,76,
1802240,0,901760,5120,23574528,1900544,0,0,88064,88064,7229440,19693568,20512768,22478848,12206080,17137664,13348864,0,1024,0,9584640,9588736,1556480,0,1843200,61440,912000,5120,23820288,1904640,160,0,
88064,88576,7271424,19719168,20574208,22540288,12226560,17139200,13349888,1536,1024,320,9585152,9588864,1576960,96,1884160,122880,922240,5120,24066048,1982464,320,8192,89088,89088,7313408,19744768,20635648,22601728,12247040,17140736,
13350912,2560,3584,640,9585664,9588992,1597440,192,1925120,184320,932480,5120,24311808,1986560,480,8192,89088,89600,7355392,19770368,20697088,22663168,12267520,17142272,13351936,4096,3584,960,9586176,9589120,1617920,288,
128,2457600,2,1966080,1280,4,3,3072,4096,1073741824,3,1065353216,55424,2920,40960,1024,460545,0,2944,2944,46,12533760,56320,40960,0,1024,1408,2560,72,128,128,92,
1802240,2048,1065600,5120,23595008,4546560,0,0,88064,88064,7229824,19693568,20512768,22478848,12533760,17924096,13365248,0,1024,0,9912320,9916416,1884160,0,1843200,63488,1075840,5120,23840768,4550656,160,0,
88064,88576,7271808,19719168,20574208,22540288,12554240,17925632,13366272,1536,1024,320,9912832,9916544,1904640,96,1884160,124928,1086080,5120,24086528,4628480,320,8192,89088,89088,7313792,19744768,20635648,22601728,12574720,17927168,
13367296,2560,3584,640,9913344,9916672,1925120,192,1925120,186368,1096320,5120,24332288,4632576,480,8192,89088,89600,7355776,19770368,20697088,22663168,12595200,17928704,13368320,4096,3584,960,9913856,9916800,1945600,288,
1024,2457600,2,0,1280,4,3,3072,4096,1073741824,0,1051011932,55040,1073741824,40960,1024,460545,0,3456,3456,54,12861440,60416,40960,0,1024,1408,2560,72,128,128,108,
2457600,0,2130560,5120,1073741824,2555904,0,0,96256,96256,7901184,19693568,20512768,22478848,12861440,18710528,13381632,0,1024,0,10240000,10244096,2211840,0,2498560,61440,2140800,5120,1073741824,2560000,160,0,
96256,96768,7943168,19719168,20574208,22540288,12881920,18712064,13382656,1536,1024,320,10240512,10244224,2232320,96,2539520,122880,2151040,5120,1073741824,2637824,320,8192,97280,97280,7985152,19744768,20635648,22601728,12902400,18713600,
13383680,2560,3584,640,10241024,10244352,2252800,192,2580480,184320,2161280,5120,1073741824,2641920,480,8192,97280,97792,8027136,19770368,20697088,22663168,12922880,18715136,13384704,4096,3584,960,10241536,10244480,2273280,288,
384,2457600,2,0,1280,4,3,3072,4096,1073741824,3,1065353216,54656,1073741824,40960,1024,460545,0,3968,3968,62,13189120,64512,40960,0,1024,1408,2560,72,128,128,124,
2457600,2048,2294400,5120,1073741824,5201920,0,0,96256,96256,7901568,19693568,20512768,22478848,13189120,19496960,13398016,0,1024,0,10567680,10571776,2539520,0,2498560,63488,2304640,5120,1073741824,5206016,160,0,
96256,96768,7943552,19719168,20574208,22540288,13209600,19498496,13399040,1536,1024,320,10568192,10571904,2560000,96,2539520,124928,2314880,5120,1073741824,5283840,320,8192,97280,97280,7985536,19744768,20635648,22601728,13230080,19500032,
13400064,2560,3584,640,10568704,10572032,2580480,192,2580480,186368,2325120,5120,1073741824,5287936,480,8192,97280,97792,8027520,19770368,20697088,22663168,13250560,19501568,13401088,4096,3584,960,10569216,10572160,2600960,288,
256,2457600,1,1966080,3072,3,4,61440,62464,63488,0,1051011932,54272,2052,40960,1024,3355409,0,388,384,6,10895360,35968,40960,160,1024,1408,2560,96,104,160,12,
491520,256,99840,12288,21498880,598016,0,0,71680,71680,5886080,19795968,20758528,22724608,10897920,13998080,13283328,5120,6144,1280,8275968,8278528,245760,384,532480,61696,124416,12288,21621760,602112,192,0,
71680,72192,5928064,19821568,20819968,22786048,10918400,13999616,13284352,6656,6144,1600,8276480,8278656,266240,480,573440,123136,148992,12288,21744640,679936,384,8192,72704,72704,5970048,19847168,20881408,22847488,10938880,14001152,
13285376,7680,8704,1920,8276992,8278784,286720,576,614400,184576,173568,12288,21867520,684032,576,8192,72704,73216,6012032,19872768,20942848,22908928,10959360,14002688,13286400,9216,8704,2240,8277504,8278912,307200,672,
896,2457600,1,1966080,3072,3,4,61440,62464,63488,3,1065353216,53888,2060,40960,1024,7829265,0,900,896,14,11223040,40064,40960,160,1024,1408,2560,96,104,160,28,
491520,2304,296448,12288,21505024,3244032,0,0,71680,71680,5886464,19795968,20758528,22724608,11225600,14784512,13299712,5120,6144,1280,8603648,8606208,573440,384,532480,63744,321024,12288,21627904,3248128,192,0,
71680,72192,5928448,19821568,20819968,22786048,11246080,14786048,13300736,6656,6144,1600,8604160,8606336,593920,480,573440,125184,345600,12288,21750784,3325952,384,8192,72704,72704,5970432,19847168,20881408,22847488,11266560,14787584,
13301760,7680,8704,1920,8604672,8606464,614400,576,614400,186624,370176,12288,21873664,3330048,576,8192,72704,73216,6012416,19872768,20942848,22908928,11287040,14789120,13302784,9216,8704,2240,8605184,8606592,634880,672,
512,2457600,1,1966080,3072,3,4,61440,62464,63488,0,1051011932,53504,2112,40960,1024,7829265,0,1412,1408,22,11550720,44160,40960,160,1024,1408,2560,96,104,160,44,
1146880,256,1574400,12288,21544960,1253376,0,0,79872,79872,6557824,19795968,20758528,22724608,11553280,15570944,13316096,5120,6144,1280,8931328,8933888,901120,384,1187840,61696,1598976,12288,21667840,1257472,192,0,
79872,80384,6599808,19821568,20819968,22786048,11573760,15572480,13317120,6656,6144,1600,8931840,8934016,921600,480,1228800,123136,1623552,12288,21790720,1335296,384,8192,80896,80896,6641792,19847168,20881408,22847488,11594240,15574016,
13318144,7680,8704,1920,8932352,8934144,942080,576,1269760,184576,1648128,12288,21913600,1339392,576,8192,80896,81408,6683776,19872768,20942848,22908928,11614720,15575552,13319168,9216,8704,2240,8932864,8934272,962560,672,
1152,2457600,1,1966080,3072,3,4,61440,62464,63488,3,1065353216,53120,2120,40960,1024,7829265,0,1924,1920,30,11878400,48256,40960,160,1024,1408,2560,96,104,160,60,
1146880,2304,1771008,12288,21551104,3899392,0,0,79872,79872,6558208,19795968,20758528,22724608,11880960,16357376,13332480,5120,6144,1280,9259008,9261568,1228800,384,1187840,63744,1795584,12288,21673984,3903488,192,0,
79872,80384,6600192,19821568,20819968,22786048,11901440,16358912,13333504,6656,6144,1600,9259520,9261696,1249280,480,1228800,125184,1820160,12288,21796864,3981312,384,8192,80896,80896,6642176,19847168,20881408,22847488,11921920,16360448,
13334528,7680,8704,1920,9260032,9261824,1269760,576,1269760,186624,1844736,12288,21919744,3985408,576,8192,80896,81408,6684160,19872768,20942848,22908928,11942400,16361984,13335552,9216,8704,2240,9260544,9261952,1290240,672,
896,2457600,2,1966080,1280,4,3,3072,4096,1073741824,0,1051011932,52736,2652,40960,1024,7829265,0,2436,2432,38,12206080,52352,40960,160,1024,1408,2560,72,128,128,76,
1802240,256,942080,5120,22596608,1908736,0,0,88064,88064,7229568,19795968,20758528,22724608,12208640,17143808,13348864,5120,6144,1280,9586688,9589248,1556480,384,1843200,61696,952320,5120,22842368,1912832,160,0,
88064,88576,7271552,19821568,20819968,22786048,12229120,17145344,13349888,6656,6144,1600,9587200,9589376,1576960,480,1884160,123136,962560,5120,23088128,1990656,320,8192,89088,89088,7313536,19847168,20881408,22847488,12249600,17146880,
13350912,7680,8704,1920,9587712,9589504,1597440,576,1925120,184576,972800,5120,23333888,1994752,480,8192,89088,89600,7355520,19872768,20942848,22908928,12270080,17148416,13351936,9216,8704,2240,9588224,9589632,1617920,672,
256,2457600,2,1966080,1280,4,3,3072,4096,1073741824,3,1065353216,52352,2668,40960,1024,7829265,0,2948,2944,46,12533760,56448,40960,160,1024,1408,2560,72,128,128,92,
1802240,2304,1105920,5120,22617088,4554752,0,0,88064,88064,7229952,19795968,20758528,22724608,12536320,17930240,13365248,5120,6144,1280,9914368,9916928,1884160,384,1843200,63744,1116160,5120,22862848,4558848,160,0,
88064,88576,7271936,19821568,20819968,22786048,12556800,17931776,13366272,6656,6144,1600,9914880,9917056,1904640,480,1884160,125184,1126400,5120,23108608,4636672,320,8192,89088,89088,7313920,19847168,20881408,22847488,12577280,17933312,
13367296,7680,8704,1920,9915392,9917184,1925120,576,1925120,186624,1136640,5120,23354368,4640768,480,8192,89088,89600,7355904,19872768,20942848,22908928,12597760,17934848,13368320,9216,8704,2240,9915904,9917312,1945600,672,
1152,2457600,2,0,1280,4,3,3072,4096,1073741824,0,1051011932,51968,1073741824,40960,1024,7829265,0,3460,3456,54,12861440,60544,40960,160,1024,1408,2560,72,128,128,108,
2457600,256,2170880,5120,1073741824,2564096,0,0,96256,96256,7901312,19795968,20758528,22724608,12864000,18716672,13381632,5120,6144,1280,10242048,10244608,2211840,384,2498560,61696,2181120,5120,1073741824,2568192,160,0,
96256,96768,7943296,19821568,20819968,22786048,12884480,18718208,13382656,6656,6144,1600,10242560,10244736,2232320,480,2539520,123136,2191360,5120,1073741824,2646016,320,8192,97280,97280,7985280,19847168,20881408,22847488,12904960,18719744,
13383680,7680,8704,1920,10243072,10244864,2252800,576,2580480,184576,2201600,5120,1073741824,2650112,480,8192,97280,97792,8027264,19872768,20942848,22908928,12925440,18721280,13384704,9216,8704,2240,10243584,10244992,2273280,672,
512,2457600,2,0,1280,4,3,3072,4096,1073741824,3,1065353216,51584,1073741824,40960,1024,7829265,0,3972,3968,62,13189120,64640,40960,160,1024,1408,2560,72,128,128,124,
2457600,2304,2334720,5120,1073741824,5210112,0,0,96256,96256,7901696,19795968,20758528,22724608,13191680,19503104,13398016,5120,6144,1280,10569728,10572288,2539520,384,2498560,63744,2344960,5120,1073741824,5214208,160,0,
96256,96768,7943680,19821568,20819968,22786048,13212160,19504640,13399040,6656,6144,1600,10570240,10572416,2560000,480,2539520,125184,2355200,5120,1073741824,5292032,320,8192,97280,97280,7985664,19847168,20881408,22847488,13232640,19506176,
13400064,7680,8704,1920,10570752,10572544,2580480,576,2580480,186624,2365440,5120,1073741824,5296128,480,8192,97280,97792,8027648,19872768,20942848,22908928,13253120,19507712,13401088,9216,8704,2240,10571264,10572672,2600960,672,
128,2457600,1,1966080,3072,3,4,61440,62464,63488,0,1051011932,51200,2308,40960,1024,197377,0,392,384,6,10895360,36096,41088,320,1024,1664,2560,96,104,160,12,
491520,512,100608,12288,21990400,606208,0,0,71680,71680,5886208,19898368,21004288,22970368,10900480,14004224,13283328,10240,11264,2560,8279040,8283136,245760,768,532480,61952,125184,12288,22113280,610304,192,0,
71680,72192,5928192,19923968,21065728,23031808,10920960,14005760,13284352,11776,11264,2880,8279552,8283264,266240,864,573440,123392,149760,12288,22236160,688128,384,8192,72704,72704,5970176,19949568,21127168,23093248,10941440,14007296,
13285376,12800,13824,3200,8280064,8283392,286720,960,614400,184832,174336,12288,22359040,692224,576,8192,72704,73216,6012160,19975168,21188608,23154688,10961920,14008832,13286400,14336,13824,3520,8280576,8283520,307200,1056,
768,2457600,1,1966080,3072,3,4,61440,62464,63488,1,1065353216,50816,2316,41984,256,460545,0,904,896,14,11223040,40192,41088,320,1024,1664,2560,96,104,160,28,
491520,2560,297216,12288,21996544,5844992,0,0,71680,71680,5885952,19898368,21004288,22970368,11228160,14790656,13299712,10240,11264,2560,8606720,8610816,573440,768,532480,64000,321792,12288,22119424,5846016,192,0,
71680,72192,5927936,19923968,21065728,23031808,11248640,14792192,13300736,11776,11264,2880,8607232,8610944,593920,864,573440,125440,346368,12288,22242304,5928960,384,8192,72704,72704,5969920,19949568,21127168,23093248,11269120,14793728,
13301760,12800,13824,3200,8607744,8611072,614400,960,614400,186880,370944,12288,22365184,5929984,576,8192,72704,73216,6011904,19975168,21188608,23154688,11289600,14795264,13302784,14336,13824,3520,8608256,8611200,634880,1056,
384,2457600,1,1966080,3072,3,4,61440,62464,63488,0,1051011932,50432,2368,40960,1024,460545,0,1416,1408,22,11550720,44288,41088,320,1024,1664,2560,96,104,160,44,
1146880,512,1575168,12288,22036480,1261568,0,0,79872,79872,6557952,19898368,21004288,22970368,11555840,15577088,13316096,10240,11264,2560,8934400,8938496,901120,768,1187840,61952,1599744,12288,22159360,1265664,192,0,
79872,80384,6599936,19923968,21065728,23031808,11576320,15578624,13317120,11776,11264,2880,8934912,8938624,921600,864,1228800,123392,1624320,12288,22282240,1343488,384,8192,80896,80896,6641920,19949568,21127168,23093248,11596800,15580160,
13318144,12800,13824,3200,8935424,8938752,942080,960,1269760,184832,1648896,12288,22405120,1347584,576,8192,80896,81408,6683904,19975168,21188608,23154688,11617280,15581696,13319168,14336,13824,3520,8935936,8938880,962560,1056,
1024,2457600,1,1966080,3072,3,4,61440,62464,63488,1,1065353216,50048,2376,41984,256,460545,0,1928,1920,30,11878400,48384,41088,320,1024,1664,2560,96,104,160,60,
1146880,2560,1771776,12288,22042624,6516736,0,0,79872,79872,6557696,19898368,21004288,22970368,11883520,16363520,13332480,10240,11264,2560,9262080,9266176,1228800,768,1187840,64000,1796352,12288,22165504,6517760,192,0,
79872,80384,6599680,19923968,21065728,23031808,11904000,16365056,13333504,11776,11264,2880,9262592,9266304,1249280,864,1228800,125440,1820928,12288,22288384,6600704,384,8192,80896,80896,6641664,19949568,21127168,23093248,11924480,16366592,
13334528,12800,13824,3200,9263104,9266432,1269760,960,1269760,186880,1845504,12288,22411264,6601728,576,8192,80896,81408,6683648,19975168,21188608,23154688,11944960,16368128,13335552,14336,13824,3520,9263616,9266560,1290240,1056,
768,2457600,2,1966080,1280,4,3,3072,4096,1073741824,0,1051011932,49664,2908,40960,1024,460545,0,2440,2432,38,12206080,52480,41088,320,1024,1664,2560,72,128,128,76,
1802240,512,942720,5120,23579648,1916928,0,0,88064,88064,7229696,19898368,21004288,22970368,12211200,17149952,13348864,10240,11264,2560,9589760,9593856,1556480,768,1843200,61952,952960,5120,23825408,1921024,160,0,
88064,88576,7271680,19923968,21065728,23031808,12231680,17151488,13349888,11776,11264,2880,9590272,9593984,1576960,864,1884160,123392,963200,5120,24071168,1998848,320,8192,89088,89088,7313664,19949568,21127168,23093248,12252160,17153024,
13350912,12800,13824,3200,9590784,9594112,1597440,960,1925120,184832,973440,5120,24316928,2002944,480,8192,89088,89600,7355648,19975168,21188608,23154688,12272640,17154560,13351936,14336,13824,3520,9591296,9594240,1617920,1056,
128,2457600,2,1966080,1280,4,3,3072,4096,1073741824,1,1065353216,49280,2924,41984,256,460545,0,2952,2944,46,12533760,56576,41088,320,1024,1664,2560,72,128,128,92,
1802240,2560,1106560,5120,23600128,7188480,0,0,88064,88064,7229440,19898368,21004288,22970368,12538880,17936384,13365248,10240,11264,2560,9917440,9921536,1884160,768,1843200,64000,1116800,5120,23845888,7189504,160,0,
88064,88576,7271424,19923968,21065728,23031808,12559360,17937920,13366272,11776,11264,2880,9917952,9921664,1904640,864,1884160,125440,1127040,5120,24091648,7272448,320,8192,89088,89088,7313408,19949568,21127168,23093248,12579840,17939456,
13367296,12800,13824,3200,9918464,9921792,1925120,960,1925120,186880,1137280,5120,24337408,7273472,480,8192,89088,89600,7355392,19975168,21188608,23154688,12600320,17940992,13368320,14336,13824,3520,9918976,9921920,1945600,1056,
1024,2457600,2,0,1280,4,3,3072,4096,1073741824,0,1051011932,48896,1073741824,40960,1024,460545,0,3464,3456,54,12861440,60672,41088,320,1024,1664,2560,72,128,128,108,
2457600,512,2171520,5120,1073741824,2572288,0,0,96256,96256,7901440,19898368,21004288,22970368,12866560,18722816,13381632,10240,11264,2560,10245120,10249216,2211840,768,2498560,61952,2181760,5120,1073741824,2576384,160,0,
96256,96768,7943424,19923968,21065728,23031808,12887040,18724352,13382656,11776,11264,2880,10245632,10249344,2232320,864,2539520,123392,2192000,5120,1073741824,2654208,320,8192,97280,97280,7985408,19949568,21127168,23093248,12907520,18725888,
13383680,12800,13824,3200,10246144,10249472,2252800,960,2580480,184832,2202240,5120,1073741824,2658304,480,8192,97280,97792,8027392,19975168,21188608,23154688,12928000,18727424,13384704,14336,13824,3520,10246656,10249600,2273280,1056,
384,2457600,2,0,1280,4,3,3072,4096,1073741824,1,1065353216,48512,1073741824,41984,256,460545,0,3976,3968,62,13189120,64768,41088,320,1024,1664,2560,72,128,128,124,
2457600,2560,2335360,5120,1073741824,7860224,0,0,96256,96256,7901184,19898368,21004288,22970368,13194240,19509248,13398016,10240,11264,2560,10572800,10576896,2539520,768,2498560,64000,2345600,5120,1073741824,7861248,160,0,
96256,96768,7943168,19923968,21065728,23031808,13214720,19510784,13399040,11776,11264,2880,10573312,10577024,2560000,864,2539520,125440,2355840,5120,1073741824,7944192,320,8192,97280,97280,7985152,19949568,21127168,23093248,13235200,19512320,
13400064,12800,13824,3200,10573824,10577152,2580480,960,2580480,186880,2366080,5120,1073741824,7945216,480,8192,97280,97792,8027136,19975168,21188608,23154688,13255680,19513856,13401088,14336,13824,3520,10574336,10577280,2600960,1056,
256,2457600,1,1966080,3072,3,4,61440,62464,63488,0,1051011932,48128,1544,40960,1024,3355409,0,396,384,6,10895360,36224,41088,480,1024,1664,2560,96,104,160,12,
491520,768,196608,12288,20518912,614400,0,0,71680,71680,5886336,20000768,21250048,23216128,10903040,14010368,13283328,15360,16384,3840,8281088,8283648,245760,1152,532480,62208,221184,12288,20641792,618496,192,0,
71680,72192,5928320,20026368,21311488,23277568,10923520,14011904,13284352,16896,16384,4160,8281600,8283776,266240,1248,573440,123648,245760,12288,20764672,696320,384,8192,72704,72704,5970304,20051968,21372928,23339008,10944000,14013440,
13285376,17920,18944,4480,8282112,8283904,286720,1344,614400,185088,270336,12288,20887552,700416,576,8192,72704,73216,6012288,20077568,21434368,23400448,10964480,14014976,13286400,19456,18944,4800,8282624,8284032,307200,1440,
896,2457600,1,1966080,3072,3,4,61440,62464,63488,1,1065353216,47744,1552,41984,256,7829265,0,908,896,14,11223040,40320,41088,480,1024,1664,2560,96,104,160,28,
491520,2816,393216,12288,20525056,5847040,0,0,71680,71680,5886080,20000768,21250048,23216128,11230720,14796800,13299712,15360,16384,3840,8608768,8611328,573440,1152,532480,64256,417792,12288,20647936,5848064,192,0,
71680,72192,5928064,20026368,21311488,23277568,11251200,14798336,13300736,16896,16384,4160,8609280,8611456,593920,1248,573440,125696,442368,12288,20770816,5931008,384,8192,72704,72704,5970048,20051968,21372928,23339008,11271680,14799872,
13301760,17920,18944,4480,8609792,8611584,614400,1344,614400,187136,466944,12288,20893696,5932032,576,8192,72704,73216,6012032,20077568,21434368,23400448,11292160,14801408,13302784,19456,18944,4800,8610304,8611712,634880,1440,
512,2457600,1,1966080,3072,3,4,61440,62464,63488,0,1051011932,47360,1604,40960,1024,7829265,0,1420,1408,22,11550720,44416,41088,480,1024,1664,2560,96,104,160,44,
1146880,768,1671168,12288,20564992,1269760,0,0,79872,79872,6558080,20000768,21250048,23216128,11558400,15583232,13316096,15360,16384,3840,8936448,8939008,901120,1152,1187840,62208,1695744,12288,20687872,1273856,192,0,
79872,80384,6600064,20026368,21311488,23277568,11578880,15584768,13317120,16896,16384,4160,8936960,8939136,921600,1248,1228800,123648,1720320,12288,20810752,1351680,384,8192,80896,80896,6642048,20051968,21372928,23339008,11599360,15586304,
13318144,17920,18944,4480,8937472,8939264,942080,1344,1269760,185088,1744896,12288,20933632,1355776,576,8192,80896,81408,6684032,20077568,21434368,23400448,11619840,15587840,13319168,19456,18944,4800,8937984,8939392,962560,1440,
1152,2457600,1,1966080,3072,3,4,61440,62464,63488,1,1065353216,46976,1612,41984,256,7829265,0,1932,1920,30,11878400,48512,41088,480,1024,1664,2560,96,104,160,60,
1146880,2816,1867776,12288,20571136,6518784,0,0,79872,79872,6557824,20000768,21250048,23216128,11886080,16369664,13332480,15360,16384,3840,9264128,9266688,1228800,1152,1187840,64256,1892352,12288,20694016,6519808,192,0,
79872,80384,6599808,20026368,21311488,23277568,11906560,16371200,13333504,16896,16384,4160,9264640,9266816,1249280,1248,1228800,125696,1916928,12288,20816896,6602752,384,8192,80896,80896,6641792,20051968,21372928,23339008,11927040,16372736,
13334528,17920,18944,4480,9265152,9266944,1269760,1344,1269760,187136,1941504,12288,20939776,6603776,576,8192,80896,81408,6683776,20077568,21434368,23400448,11947520,16374272,13335552,19456,18944,4800,9265664,9267072,1290240,1440,
896,2457600,2,1966080,1280,4,3,3072,4096,1073741824,0,1051011932,46592,2656,40960,1024,7829265,0,2444,2432,38,12206080,52608,41088,480,1024,1664,2560,72,128,128,76,
1802240,768,983040,5120,22601728,1925120,0,0,88064,88064,7229824,20000768,21250048,23216128,12213760,17156096,13348864,15360,16384,3840,9591808,9594368,1556480,1152,1843200,62208,993280,5120,22847488,1929216,160,0,
88064,88576,7271808,20026368,21311488,23277568,12234240,17157632,13349888,16896,16384,4160,9592320,9594496,1576960,1248,1884160,123648,1003520,5120,23093248,2007040,320,8192,89088,89088,7313792,20051968,21372928,23339008,12254720,17159168,
13350912,17920,18944,4480,9592832,9594624,1597440,1344,1925120,185088,1013760,5120,23339008,2011136,480,8192,89088,89600,7355776,20077568,21434368,23400448,12275200,17160704,13351936,19456,18944,4800,9593344,9594752,1617920,1440,
256,2457600,2,1966080,1280,4,3,3072,4096,1073741824,1,1065353216,46208,2672,41984,256,7829265,0,2956,2944,46,12533760,56704,41088,480,1024,1664,2560,72,128,128,92,
1802240,2816,1146880,5120,22622208,7190528,0,0,88064,88064,7229568,20000768,21250048,23216128,12541440,17942528,13365248,15360,16384,3840,9919488,9922048,1884160,1152,1843200,64256,1157120,5120,22867968,7191552,160,0,
88064,88576,7271552,20026368,21311488,23277568,12561920,17944064,13366272,16896,16384,4160,9920000,9922176,1904640,1248,1884160,125696,1167360,5120,23113728,7274496,320,8192,89088,89088,7313536,20051968,21372928,23339008,12582400,17945600,
13367296,17920,18944,4480,9920512,9922304,1925120,1344,1925120,187136,1177600,5120,23359488,7275520,480,8192,89088,89600,7355520,20077568,21434368,23400448,12602880,17947136,13368320,19456,18944,4800,9921024,9922432,1945600,1440,
1152,2457600,2,0,1280,4,3,3072,4096,1073741824,0,1051011932,45824,1073741824,40960,1024,7829265,0,3468,3456,54,12861440,60800,41088,480,1024,1664,2560,72,128,128,108,
2457600,768,2211840,5120,1073741824,2580480,0,0,96256,96256,7901568,20000768,21250048,23216128,12869120,18728960,13381632,15360,16384,3840,10247168,10249728,2211840,1152,2498560,62208,2222080,5120,1073741824,2584576,160,0,
96256,96768,7943552,20026368,21311488,23277568,12889600,18730496,13382656,16896,16384,4160,10247680,10249856,2232320,1248,2539520,123648,2232320,5120,1073741824,2662400,320,8192,97280,97280,7985536,20051968,21372928,23339008,12910080,18732032,
13383680,17920,18944,4480,10248192,10249984,2252800,1344,2580480,185088,2242560,5120,1073741824,2666496,480,8192,97280,97792,8027520,20077568,21434368,23400448,12930560,18733568,13384704,19456,18944,4800,10248704,10250112,2273280,1440,
512,2457600,2,0,1280,4,3,3072,4096,1073741824,1,1065353216,45440,1073741824,41984,256,7829265,0,3980,3968,62,13189120,64896,41088,480,1024,1664,2560,72,128,128,124,
2457600,2816,2375680,5120,1073741824,7862272,0,0,96256,96256,7901312,20000768,21250048,23216128,13196800,19515392,13398016,15360,16384,3840,10574848,10577408,2539520,1152,2498560,64256,2385920,5120,1073741824,7863296,160,0,
96256,96768,7943296,20026368,21311488,23277568,13217280,19516928,13399040,16896,16384,4160,10575360,10577536,2560000,1248,2539520,125696,2396160,5120,1073741824,7946240,320,8192,97280,97280,7985280,20051968,21372928,23339008,13237760,19518464,
13400064,17920,18944,4480,10575872,10577664,2580480,1344,2580480,187136,2406400,5120,1073741824,7947264,480,8192,97280,97792,8027264,20077568,21434368,23400448,13258240,19520000,13401088,19456,18944,4800,10576384,10577792,2600960,1440,
128,2457600,1,1966080,3072,3,4,61440,62464,63488,0,1051011932,45056,1800,40960,1024,3355409,0,400,384,6,10895360,36352,41216,640,1280,1920,2816,96,104,160,12,
491520,1024,197376,12288,21010432,622592,0,0,71680,71680,5886464,20103168,21495808,23461888,10905600,14016512,13283328,20480,21504,5120,8284160,8288256,245760,1536,532480,62464,221952,12288,21133312,626688,192,0,
71680,72192,5928448,20128768,21557248,23523328,10926080,14018048,13284352,22016,21504,5440,8284672,8288384,266240,1632,573440,123904,246528,12288,21256192,704512,384,8192,72704,72704,5970432,20154368,21618688,23584768,10946560,14019584,
13285376,23040,24064,5760,8285184,8288512,286720,1728,614400,185344,271104,12288,21379072,708608,576,8192,72704,73216,6012416,20179968,21680128,23646208,10967040,14021120,13286400,24576,24064,6080,8285696,8288640,307200,1824,
768,2457600,1,1966080,3072,3,4,61440,62464,63488,1,1065353216,44672,1808,41984,256,7829265,0,912,896,14,11223040,40448,41216,640,1280,1920,2816,96,104,160,28,
491520,3072,393984,12288,21016576,5849088,0,0,71680,71680,5886208,20103168,21495808,23461888,11233280,14802944,13299712,20480,21504,5120,8611840,8615936,573440,1536,532480,64512,418560,12288,21139456,5850112,192,0,
71680,72192,5928192,20128768,21557248,23523328,11253760,14804480,13300736,22016,21504,5440,8612352,8616064,593920,1632,573440,125952,443136,12288,21262336,5933056,384,8192,72704,72704,5970176,20154368,21618688,23584768,11274240,14806016,
13301760,23040,24064,5760,8612864,8616192,614400,1728,614400,187392,467712,12288,21385216,5934080,576,8192,72704,73216,6012160,20179968,21680128,23646208,11294720,14807552,13302784,24576,24064,6080,8613376,8616320,634880,1824,
384,2457600,1,1966080,3072,3,4,61440,62464,63488,0,1051011932,44288,1860,40960,1024,7829265,0,1424,1408,22,11550720,44544,41216,640,1280,1920,2816,96,104,160,44,
1146880,1024,1671936,12288,21056512,1277952,0,0,79872,79872,6558208,20103168,21495808,23461888,11560960,15589376,13316096,20480,21504,5120,8939520,8943616,901120,1536,1187840,62464,1696512,12288,21179392,1282048,192,0,
79872,80384,6600192,20128768,21557248,23523328,11581440,15590912,13317120,22016,21504,5440,8940032,8943744,921600,1632,1228800,123904,1721088,12288,21302272,1359872,384,8192,80896,80896,6642176,20154368,21618688,23584768,11601920,15592448,
13318144,23040,24064,5760,8940544,8943872,942080,1728,1269760,185344,1745664,12288,21425152,1363968,576,8192,80896,81408,6684160,20179968,21680128,23646208,11622400,15593984,13319168,24576,24064,6080,8941056,8944000,962560,1824,
1024,2457600,1,1966080,3072,3,4,61440,62464,63488,1,1065353216,43904,1868,41984,256,7829265,0,1936,1920,30,11878400,48640,41216,640,1280,1920,2816,96,104,160,60,
1146880,3072,1868544,12288,21062656,6520832,0,0,79872,79872,6557952,20103168,21495808,23461888,11888640,16375808,13332480,20480,21504,5120,9267200,9271296,1228800,1536,1187840,64512,1893120,12288,21185536,6521856,192,0,
79872,80384,6599936,20128768,21557248,23523328,11909120,16377344,13333504,22016,21504,5440,9267712,9271424,1249280,1632,1228800,125952,1917696,12288,21308416,6604800,384,8192,80896,80896,6641920,20154368,21618688,23584768,11929600,16378880,
13334528,23040,24064,5760,9268224,9271552,1269760,1728,1269760,187392,1942272,12288,21431296,6605824,576,8192,80896,81408,6683904,20179968,21680128,23646208,11950080,16380416,13335552,24576,24064,6080,9268736,9271680,1290240,1824,
768,2457600,2,1966080,1280,4,3,3072,4096,1073741824,0,1051011932,43520,2912,40960,1024,7829265,0,2448,2432,38,12206080,52736,41216,640,1280,1920,2816,72,128,128,76,
1802240,1024,983680,5120,23584768,1933312,0,0,88064,88064,7229952,20103168,21495808,23461888,12216320,17162240,13348864,20480,21504,5120,9594880,9598976,1556480,1536,1843200,62464,993920,5120,23830528,1937408,160,0,
88064,88576,7271936,20128768,21557248,23523328,12236800,17163776,13349888,22016,21504,5440,9595392,9599104,1576960,1632,1884160,123904,1004160,5120,24076288,2015232,320,8192,89088,89088,7313920,20154368,21618688,23584768,12257280,17165312,
13350912,23040,24064,5760,9595904,9599232,1597440,1728,1925120,185344,1014400,5120,24322048,2019328,480,8192,89088,89600,7355904,20179968,21680128,23646208,12277760,17166848,13351936,24576,24064,6080,9596416,9599360,1617920,1824,
128,2457600,2,1966080,1280,4,3,3072,4096,1073741824,1,1065353216,43136,2928,41984,256,7829265,0,2960,2944,46,12533760,56832,41216,640,1280,1920,2816,72,128,128,92,
1802240,3072,1147520,5120,23605248,7192576,0,0,88064,88064,7229696,20103168,21495808,23461888,12544000,17948672,13365248,20480,21504,5120,9922560,9926656,1884160,1536,1843200,64512,1157760,5120,23851008,7193600,160,0,
88064,88576,7271680,20128768,21557248,23523328,12564480,17950208,13366272,22016,21504,5440,9923072,9926784,1904640,1632,1884160,125952,1168000,5120,24096768,7276544,320,8192,89088,89088,7313664,20154368,21618688,23584768,12584960,17951744,
13367296,23040,24064,5760,9923584,9926912,1925120,1728,1925120,187392,1178240,5120,24342528,7277568,480,8192,89088,89600,7355648,20179968,21680128,23646208,12605440,17953280,13368320,24576,24064,6080,9924096,9927040,1945600,1824,
1024,2457600,2,0,1280,4,3,3072,4096,1073741824,0,1051011932,42752,1073741824,40960,1024,7829265,0,3472,3456,54,12861440,60928,41216,640,1280,1920,2816,72,128,128,108,
2457600,1024,2212480,5120,1073741824,2588672,0,0,96256,96256,7901696,20103168,21495808,23461888,12871680,18735104,13381632,20480,21504,5120,10250240,10254336,2211840,1536,2498560,62464,2222720,5120,1073741824,2592768,160,0,
96256,96768,7943680,20128768,21557248,23523328,12892160,18736640,13382656,22016,21504,5440,10250752,10254464,2232320,1632,2539520,123904,2232960,5120,1073741824,2670592,320,8192,97280,97280,7985664,20154368,21618688,23584768,12912640,18738176,
13383680,23040,24064,5760,10251264,10254592,2252800,1728,2580480,185344,2243200,5120,1073741824,2674688,480,8192,97280,97792,8027648,20179968,21680128,23646208,12933120,18739712,13384704,24576,24064,6080,10251776,10254720,2273280,1824,
384,2457600,2,0,1280,4,3,3072,4096,1073741824,1,1065353216,42368,1073741824,41984,256,7829265,0,3984,3968,62,13189120,65024,41216,640,1280,1920,2816,72,128,128,124,
2457600,3072,2376320,5120,1073741824,7864320,0,0,96256,96256,7901440,20103168,21495808,23461888,13199360,19521536,13398016,20480,21504,5120,10577920,10582016,2539520,1536,2498560,64512,2386560,5120,1073741824,7865344,160,0,
96256,96768,7943424,20128768,21557248,23523328,13219840,19523072,13399040,22016,21504,5440,10578432,10582144,2560000,1632,2539520,125952,2396800,5120,1073741824,7948288,320,8192,97280,97280,7985408,20154368,21618688,23584768,13240320,19524608,
13400064,23040,24064,5760,10578944,10582272,2580480,1728,2580480,187392,2407040,5120,1073741824,7949312,480,8192,97280,97792,8027392,20179968,21680128,23646208,13260800,19526144,13401088,24576,24064,6080,10579456,10582400,2600960,1824,
256,2457600,1,1966080,3072,3,4,61440,62464,63488,3,1065353216,41984,2056,40960,1024,197377,0,404,384,6,10895360,36480,41344,800,1280,1920,2816,96,104,160,12,
491520,1280,198144,12288,21501952,3211264,0,0,71680,71680,5885952,20205568,21741568,23707648,10908160,14022656,13283328,25600,26624,6400,8286208,8288768,245760,1920,532480,62720,222720,12288,21624832,3215360,192,0,
71680,72192,5927936,20231168,21803008,23769088,10928640,14024192,13284352,27136,26624,6720,8286720,8288896,266240,2016,573440,124160,247296,12288,21747712,3293184,384,8192,72704,72704,5969920,20256768,21864448,23830528,10949120,14025728,
13285376,28160,29184,7040,8287232,8289024,286720,2112,614400,185600,271872,12288,21870592,3297280,576,8192,72704,73216,6011904,20282368,21925888,23891968,10969600,14027264,13286400,29696,29184,7360,8287744,8289152,307200,2208,
896,2457600,1,1966080,3072,3,4,61440,62464,63488,1,1065353216,41600,2064,41984,256,460545,0,916,896,14,11223040,40576,41344,800,1280,1920,2816,96,104,160,28,
491520,3328,394752,12288,21508096,5851136,0,0,71680,71680,5886336,20205568,21741568,23707648,11235840,14809088,13299712,25600,26624,6400,8613888,8616448,573440,1920,532480,64768,419328,12288,21630976,5852160,192,0,
71680,72192,5928320,20231168,21803008,23769088,11256320,14810624,13300736,27136,26624,6720,8614400,8616576,593920,2016,573440,126208,443904,12288,21753856,5935104,384,8192,72704,72704,5970304,20256768,21864448,23830528,11276800,14812160,
13301760,28160,29184,7040,8614912,8616704,614400,2112,614400,187648,468480,12288,21876736,5936128,576,8192,72704,73216,6012288,20282368,21925888,23891968,11297280,14813696,13302784,29696,29184,7360,8615424,8616832,634880,2208,
512,2457600,1,1966080,3072,3,4,61440,62464,63488,3,1065353216,41216,2116,40960,1024,460545,0,1428,1408,22,11550720,44672,41344,800,1280,1920,2816,96,104,160,44,
1146880,1280,1672704,12288,21548032,3866624,0,0,79872,79872,6557696,20205568,21741568,23707648,11563520,15595520,13316096,25600,26624,6400,8941568,8944128,901120,1920,1187840,62720,1697280,12288,21670912,3870720,192,0,
79872,80384,6599680,20231168,21803008,23769088,11584000,15597056,13317120,27136,26624,6720,8942080,8944256,921600,2016,1228800,124160,1721856,12288,21793792,3948544,384,8192,80896,80896,6641664,20256768,21864448,23830528,11604480,15598592,
13318144,28160,29184,7040,8942592,8944384,942080,2112,1269760,185600,1746432,12288,21916672,3952640,576,8192,80896,81408,6683648,20282368,21925888,23891968,11624960,15600128,13319168,29696,29184,7360,8943104,8944512,962560,2208,
1152,2457600,1,1966080,3072,3,4,61440,62464,63488,1,1065353216,40832,2124,41984,256,460545,0,1940,1920,30,11878400,48768,41344,800,1280,1920,2816,96,104,160,60,
1146880,3328,1869312,12288,21554176,6522880,0,0,79872,79872,6558080,20205568,21741568,23707648,11891200,16381952,13332480,25600,26624,6400,9269248,9271808,1228800,1920,1187840,64768,1893888,12288,21677056,6523904,192,0,
79872,80384,6600064,20231168,21803008,23769088,11911680,16383488,13333504,27136,26624,6720,9269760,9271936,1249280,2016,1228800,126208,1918464,12288,21799936,6606848,384,8192,80896,80896,6642048,20256768,21864448,23830528,11932160,16385024,
13334528,28160,29184,7040,9270272,9272064,1269760,2112,1269760,187648,1943040,12288,21922816,6607872,576,8192,80896,81408,6684032,20282368,21925888,23891968,11952640,16386560,13335552,29696,29184,7360,9270784,9272192,1290240,2208,
896,2457600,2,1966080,1280,4,3,3072,4096,1073741824,3,1065353216,40448,2660,40960,1024,460545,0,2452,2432,38,12206080,52864,41344,800,1280,1920,2816,72,128,128,76,
1802240,1280,1024000,5120,22606848,4521984,0,0,88064,88064,7229440,20205568,21741568,23707648,12218880,17168384,13348864,25600,26624,6400,9596928,9599488,1556480,1920,1843200,62720,1034240,5120,22852608,4526080,160,0,
88064,88576,7271424,20231168,21803008,23769088,12239360,17169920,13349888,27136,26624,6720,9597440,9599616,1576960,2016,1884160,124160,1044480,5120,23098368,4603904,320,8192,89088,89088,7313408,20256768,21864448,23830528,12259840,17171456,
13350912,28160,29184,7040,9597952,9599744,1597440,2112,1925120,185600,1054720,5120,23344128,4608000,480,8192,89088,89600,7355392,20282368,21925888,23891968,12280320,17172992,13351936,29696,29184,7360,9598464,9599872,1617920,2208,
256,2457600,2,1966080,1280,4,3,3072,4096,1073741824,1,1065353216,40064,2676,41984,256,460545,0,2964,2944,46,12533760,56960,41344,800,1280,1920,2816,72,128,128,92,
1802240,3328,1187840,5120,22627328,7194624,0,0,88064,88064,7229824,20205568,21741568,23707648,12546560,17954816,13365248,25600,26624,6400,9924608,9927168,1884160,1920,1843200,64768,1198080,5120,22873088,7195648,160,0,
88064,88576,7271808,20231168,21803008,23769088,12567040,17956352,13366272,27136,26624,6720,9925120,9927296,1904640,2016,1884160,126208,1208320,5120,23118848,7278592,320,8192,89088,89088,7313792,20256768,21864448,23830528,12587520,17957888,
13367296,28160,29184,7040,9925632,9927424,1925120,2112,1925120,187648,1218560,5120,23364608,7279616,480,8192,89088,89600,7355776,20282368,21925888,23891968,12608000,17959424,13368320,29696,29184,7360,9926144,9927552,1945600,2208,
1152,2457600,2,0,1280,4,3,3072,4096,1073741824,3,1065353216,39680,1073741824,40960,1024,460545,0,3476,3456,54,12861440,61056,41344,800,1280,1920,2816,72,128,128,108,
2457600,1280,2252800,5120,1073741824,5177344,0,0,96256,96256,7901184,20205568,21741568,23707648,12874240,18741248,13381632,25600,26624,6400,10252288,10254848,2211840,1920,2498560,62720,2263040,5120,1073741824,5181440,160,0,
96256,96768,7943168,20231168,21803008,23769088,12894720,18742784,13382656,27136,26624,6720,10252800,10254976,2232320,2016,2539520,124160,2273280,5120,1073741824,5259264,320,8192,97280,97280,7985152,20256768,21864448,23830528,12915200,18744320,
13383680,28160,29184,7040,10253312,10255104,2252800,2112,2580480,185600,2283520,5120,1073741824,5263360,480,8192,97280,97792,8027136,20282368,21925888,23891968,12935680,18745856,13384704,29696,29184,7360,10253824,10255232,2273280,2208,
512,2457600,2,0,1280,4,3,3072,4096,1073741824,1,1065353216,39296,1073741824,41984,256,460545,0,3988,3968,62,13189120,65152,41344,800,1280,1920,2816,72,128,128,124,
2457600,3328,2416640,5120,1073741824,7866368,0,0,96256,96256,7901568,20205568,21741568,23707648,13201920,19527680,13398016,25600,26624,6400,10579968,10582528,2539520,1920,2498560,64768,2426880,5120,1073741824,7867392,160,0,
96256,96768,7943552,20231168,21803008,23769088,13222400,19529216,13399040,27136,26624,6720,10580480,10582656,2560000,2016,2539520,126208,2437120,5120,1073741824,7950336,320,8192,97280,97280,7985536,20256768,21864448,23830528,13242880,19530752,
13400064,28160,29184,7040,10580992,10582784,2580480,2112,2580480,187648,2447360,5120,1073741824,7951360,480,8192,97280,97792,8027520,20282368,21925888,23891968,13263360,19532288,13401088,29696,29184,7360,10581504,10582912,2600960,2208,
128,2457600,1,1966080,3072,3,4,61440,62464,63488,3,1065353216,38912,2312,40960,1024,3355409,0,408,384,6,10895360,36608,41344,960,1280,2176,2816,96,104,160,12,
491520,1536,198912,12288,21993472,3219456,0,0,71680,71680,5886080,20307968,21987328,23953408,10910720,14028800,13283328,30720,31744,7680,8289280,8293376,245760,2304,532480,62976,223488,12288,22116352,3223552,192,0,
71680,72192,5928064,20333568,22048768,24014848,10931200,14030336,13284352,32256,31744,8000,8289792,8293504,266240,2400,573440,124416,248064,12288,22239232,3301376,384,8192,72704,72704,5970048,20359168,22110208,24076288,10951680,14031872,
13285376,33280,34304,8320,8290304,8293632,286720,2496,614400,185856,272640,12288,22362112,3305472,576,8192,72704,73216,6012032,20384768,22171648,24137728,10972160,14033408,13286400,34816,34304,8640,8290816,8293760,307200,2592,
768,2457600,1,1966080,3072,3,4,61440,62464,63488,1,1065353216,38528,2320,41984,256,7829265,0,920,896,14,11223040,40704,41344,960,1280,2176,2816,96,104,160,28,
491520,3584,395520,12288,21999616,5853184,0,0,71680,71680,5886464,20307968,21987328,23953408,11238400,14815232,13299712,30720,31744,7680,8616960,8621056,573440,2304,532480,65024,420096,12288,22122496,5854208,192,0,
71680,72192,5928448,20333568,22048768,24014848,11258880,14816768,13300736,32256,31744,8000,8617472,8621184,593920,2400,573440,126464,444672,12288,22245376,5937152,384,8192,72704,72704,5970432,20359168,22110208,24076288,11279360,14818304,
13301760,33280,34304,8320,8617984,8621312,614400,2496,614400,187904,469248,12288,22368256,5938176,576,8192,72704,73216,6012416,20384768,22171648,24137728,11299840,14819840,13302784,34816,34304,8640,8618496,8621440,634880,2592,
384,2457600,1,1966080,3072,3,4,61440,62464,63488,3,1065353216,38144,2372,40960,1024,7829265,0,1432,1408,22,11550720,44800,41344,960,1280,2176,2816,96,104,160,44,
1146880,1536,1673472,12288,22039552,3874816,0,0,79872,79872,6557824,20307968,21987328,23953408,11566080,15601664,13316096,30720,31744,7680,8944640,8948736,901120,2304,1187840,62976,1698048,12288,22162432,3878912,192,0,
79872,80384,6599808,20333568,22048768,24014848,11586560,15603200,13317120,32256,31744,8000,8945152,8948864,921600,2400,1228800,124416,1722624,12288,22285312,3956736,384,8192,80896,80896,6641792,20359168,22110208,24076288,11607040,15604736,
13318144,33280,34304,8320,8945664,8948992,942080,2496,1269760,185856,1747200,12288,22408192,3960832,576,8192,80896,81408,6683776,20384768,22171648,24137728,11627520,15606272,13319168,34816,34304,8640,8946176,8949120,962560,2592,
1024,2457600,1,1966080,3072,3,4,61440,62464,63488,1,1065353216,37760,2380,41984,256,7829265,0,1944,1920,30,11878400,48896,41344,960,1280,2176,2816,96,104,160,60,
1146880,3584,1870080,12288,22045696,6524928,0,0,79872,79872,6558208,20307968,21987328,23953408,11893760,16388096,13332480,30720,31744,7680,9272320,9276416,1228800,2304,1187840,65024,1894656,12288,22168576,6525952,192,0,
79872,80384,6600192,20333568,22048768,24014848,11914240,16389632,13333504,32256,31744,8000,9272832,9276544,1249280,2400,1228800,126464,1919232,12288,22291456,6608896,384,8192,80896,80896,6642176,20359168,22110208,24076288,11934720,16391168,
13334528,33280,34304,8320,9273344,9276672,1269760,2496,1269760,187904,1943808,12288,22414336,6609920,576,8192,80896,81408,6684160,20384768,22171648,24137728,11955200,16392704,13335552,34816,34304,8640,9273856,9276800,1290240,2592,
768,2457600,2,1966080,1280,4,3,3072,4096,1073741824,3,1065353216,37376,2916,40960,1024,7829265,0,2456,2432,38,12206080,52992,41344,960,1280,2176,2816,72,128,128,76,
1802240,1536,1024640,5120,23589888,4530176,0,0,88064,88064,7229568,20307968,21987328,23953408,12221440,17174528,13348864,30720,31744,7680,9600000,9604096,1556480,2304,1843200,62976,1034880,5120,23835648,4534272,160,0,
88064,88576,7271552,20333568,22048768,24014848,12241920,17176064,13349888,32256,31744,8000,9600512,9604224,1576960,2400,1884160,124416,1045120,5120,24081408,4612096,320,8192,89088,89088,7313536,20359168,22110208,24076288,12262400,17177600,
13350912,33280,34304,8320,9601024,9604352,1597440,2496,1925120,185856,1055360,5120,24327168,4616192,480,8192,89088,89600,7355520,20384768,22171648,24137728,12282880,17179136,13351936,34816,34304,8640,9601536,9604480,1617920,2592,
128,2457600,2,1966080,1280,4,3,3072,4096,1073741824,1,1065353216,36992,2932,41984,256,7829265,0,2968,2944,46,12533760,57088,41344,960,1280,2176,2816,72,128,128,92,
1802240,3584,1188480,5120,23610368,7196672,0,0,88064,88064,7229952,20307968,21987328,23953408,12549120,17960960,13365248,30720,31744,7680,9927680,9931776,1884160,2304,1843200,65024,1198720,5120,23856128,7197696,160,0,
88064,88576,7271936,20333568,22048768,24014848,12569600,17962496,13366272,32256,31744,8000,9928192,9931904,1904640,2400,1884160,126464,1208960,5120,24101888,7280640,320,8192,89088,89088,7313920,20359168,22110208,24076288,12590080,17964032,
13367296,33280,34304,8320,9928704,9932032,1925120,2496,1925120,187904,1219200,5120,24347648,7281664,480,8192,89088,89600,7355904,20384768,22171648,24137728,12610560,17965568,13368320,34816,34304,8640,9929216,9932160,1945600,2592,
1024,2457600,2,0,1280,4,3,3072,4096,1073741824,3,1065353216,36608,1073741824,40960,1024,7829265,0,3480,3456,54,12861440,61184,41344,960,1280,2176,2816,72,128,128,108,
2457600,1536,2253440,5120,1073741824,5185536,0,0,96256,96256,7901312,20307968,21987328,23953408,12876800,18747392,13381632,30720,31744,7680,10255360,10259456,2211840,2304,2498560,62976,2263680,5120,1073741824,5189632,160,0,
96256,96768,7943296,20333568,22048768,24014848,12897280,18748928,13382656,32256,31744,8000,10255872,10259584,2232320,2400,2539520,124416,2273920,5120,1073741824,5267456,320,8192,97280,97280,7985280,20359168,22110208,24076288,12917760,18750464,
13383680,33280,34304,8320,10256384,10259712,2252800,2496,2580480,185856,2284160,5120,1073741824,5271552,480,8192,97280,97792,8027264,20384768,22171648,24137728,12938240,18752000,13384704,34816,34304,8640,10256896,10259840,2273280,2592,
384,2457600,2,0,1280,4,3,3072,4096,1073741824,1,1065353216,36224,1073741824,41984,256,7829265,0,3992,3968,62,13189120,65280,41344,960,1280,2176,2816,72,128,128,124,
2457600,3584,2417280,5120,1073741824,7868416,0,0,96256,96256,7901696,20307968,21987328,23953408,13204480,19533824,13398016,30720,31744,7680,10583040,10587136,2539520,2304,2498560,65024,2427520,5120,1073741824,7869440,160,0,
96256,96768,7943680,20333568,22048768,24014848,13224960,19535360,13399040,32256,31744,8000,10583552,10587264,2560000,2400,2539520,126464,2437760,5120,1073741824,7952384,320,8192,97280,97280,7985664,20359168,22110208,24076288,13245440,19536896,
13400064,33280,34304,8320,10584064,10587392,2580480,2496,2580480,187904,2448000,5120,1073741824,7953408,480,8192,97280,97792,8027648,20384768,22171648,24137728,13265920,19538432,13401088,34816,34304,8640,10584576,10587520,2600960,2592,
256,2457600,1,1966080,3072,3,4,61440,62464,63488,3,1065353216,35840,1548,40960,1024,197377,0,412,384,6,10895360,36736,41472,1120,1280,2176,2816,96,104,160,12,
491520,1792,294912,12288,20521984,3227648,0,0,71680,71680,5886208,20410368,22233088,24199168,10913280,14034944,13283328,35840,36864,8960,8291328,8293888,245760,2688,532480,63232,319488,12288,20644864,3231744,192,0,
71680,72192,5928192,20435968,22294528,24260608,10933760,14036480,13284352,37376,36864,9280,8291840,8294016,266240,2784,573440,124672,344064,12288,20767744,3309568,384,8192,72704,72704,5970176,20461568,22355968,24322048,10954240,14038016,
13285376,38400,39424,9600,8292352,8294144,286720,2880,614400,186112,368640,12288,20890624,3313664,576,8192,72704,73216,6012160,20487168,22417408,24383488,10974720,14039552,13286400,39936,39424,9920,8292864,8294272,307200,2976,
896,0,2,0,1280,4,3,3072,4096,1073741824,2,1065353216,35456,1073741824,40960,1024,460545,0,924,896,14,11223040,40832,41472,1120,1280,2176,2816,72,128,128,28,
491520,3840,17982080,5120,1073741824,3211264,0,0,71680,71680,5885952,20410368,22233088,24199168,11240960,14821376,13299712,35840,36864,8960,8619008,8621568,573440,2688,532480,65280,17992320,5120,1073741824,3215360,160,4096,
71680,72192,5927936,20435968,22294528,24260608,11261440,14822912,13300736,37376,36864,9280,8619520,8621696,593920,2784,573440,126720,18002560,5120,1073741824,3293184,320,8192,72704,72704,5969920,20461568,22355968,24322048,11281920,14824448,
13301760,38400,39424,9600,8620032,8621824,614400,2880,614400,188160,18012800,5120,1073741824,3297280,480,12288,72704,73216,6011904,20487168,22417408,24383488,11302400,14825984,13302784,39936,39424,9920,8620544,8621952,634880,2976,
512,2457600,1,1966080,3072,3,4,61440,62464,63488,3,1065353216,35072,1608,40960,1024,460545,0,1436,1408,22,11550720,44928,41472,1120,1280,2176,2816,96,104,160,44,
1146880,1792,1769472,12288,20568064,3883008,0,0,79872,79872,6557952,20410368,22233088,24199168,11568640,15607808,13316096,35840,36864,8960,8946688,8949248,901120,2688,1187840,63232,1794048,12288,20690944,3887104,192,0,
79872,80384,6599936,20435968,22294528,24260608,11589120,15609344,13317120,37376,36864,9280,8947200,8949376,921600,2784,1228800,124672,1818624,12288,20813824,3964928,384,8192,80896,80896,6641920,20461568,22355968,24322048,11609600,15610880,
13318144,38400,39424,9600,8947712,8949504,942080,2880,1269760,186112,1843200,12288,20936704,3969024,576,8192,80896,81408,6683904,20487168,22417408,24383488,11630080,15612416,13319168,39936,39424,9920,8948224,8949632,962560,2976,
1152,0,2,0,1280,4,3,3072,4096,1073741824,2,1065353216,34688,1073741824,40960,1024,460545,0,1948,1920,30,11878400,49024,41472,1120,1280,2176,2816,72,128,128,60,
1146880,3840,17982080,5120,1073741824,3866624,0,0,79872,79872,6557696,20410368,22233088,24199168,11896320,16394240,13332480,35840,36864,8960,9274368,9276928,1228800,2688,1187840,65280,17992320,5120,1073741824,3870720,160,4096,
79872,80384,6599680,20435968,22294528,24260608,11916800,16395776,13333504,37376,36864,9280,9274880,9277056,1249280,2784,1228800,126720,18002560,5120,1073741824,3948544,320,8192,80896,80896,6641664,20461568,22355968,24322048,11937280,16397312,
13334528,38400,39424,9600,9275392,9277184,1269760,2880,1269760,188160,18012800,5120,1073741824,3952640,480,12288,80896,81408,6683648,20487168,22417408,24383488,11957760,16398848,13335552,39936,39424,9920,9275904,9277312,1290240,2976,
896,2457600,2,1966080,1280,4,3,3072,4096,1073741824,3,1065353216,34304,2664,40960,1024,460545,0,2460,2432,38,12206080,53120,41472,1120,1280,2176,2816,72,128,128,76,
1802240,1792,1064960,5120,22611968,4538368,0,0,88064,88064,7229696,20410368,22233088,24199168,12224000,17180672,13348864,35840,36864,8960,9602048,9604608,1556480,2688,1843200,63232,1075200,5120,22857728,4542464,160,0,
88064,88576,7271680,20435968,22294528,24260608,12244480,17182208,13349888,37376,36864,9280,9602560,9604736,1576960,2784,1884160,124672,1085440,5120,23103488,4620288,320,8192,89088,89088,7313664,20461568,22355968,24322048,12264960,17183744,
13350912,38400,39424,9600,9603072,9604864,1597440,2880,1925120,186112,1095680,5120,23349248,4624384,480,8192,89088,89600,7355648,20487168,22417408,24383488,12285440,17185280,13351936,39936,39424,9920,9603584,9604992,1617920,2976,
256,0,2,0,1280,4,3,3072,4096,1073741824,2,1065353216,33920,1073741824,40960,1024,460545,0,2972,2944,46,12533760,57216,41472,1120,1280,2176,2816,72,128,128,92,
1802240,3840,17982080,5120,1073741824,4521984,0,0,88064,88064,7229440,20410368,22233088,24199168,12551680,17967104,13365248,35840,36864,8960,9929728,9932288,1884160,2688,1843200,65280,17992320,5120,1073741824,4526080,160,4096,
88064,88576,7271424,20435968,22294528,24260608,12572160,17968640,13366272,37376,36864,9280,9930240,9932416,1904640,2784,1884160,126720,18002560,5120,1073741824,4603904,320,8192,89088,89088,7313408,20461568,22355968,24322048,12592640,17970176,
13367296,38400,39424,9600,9930752,9932544,1925120,2880,1925120,188160,18012800,5120,1073741824,4608000,480,12288,89088,89600,7355392,20487168,22417408,24383488,12613120,17971712,13368320,39936,39424,9920,9931264,9932672,1945600,2976,
1152,2457600,2,0,1280,4,3,3072,4096,1073741824,3,1065353216,33536,1073741824,40960,1024,460545,0,3484,3456,54,12861440,61312,41472,1120,1280,2176,2816,72,128,128,108,
2457600,1792,2293760,5120,1073741824,5193728,0,0,96256,96256,7901440,20410368,22233088,24199168,12879360,18753536,13381632,35840,36864,8960,10257408,10259968,2211840,2688,2498560,63232,2304000,5120,1073741824,5197824,160,0,
96256,96768,7943424,20435968,22294528,24260608,12899840,18755072,13382656,37376,36864,9280,10257920,10260096,2232320,2784,2539520,124672,2314240,5120,1073741824,5275648,320,8192,97280,97280,7985408,20461568,22355968,24322048,12920320,18756608,
13383680,38400,39424,9600,10258432,10260224,2252800,2880,2580480,186112,2324480,5120,1073741824,5279744,480,8192,97280,97792,8027392,20487168,22417408,24383488,12940800,18758144,13384704,39936,39424,9920,10258944,10260352,2273280,2976,
512,0,2,0,1280,4,3,3072,4096,1073741824,2,1065353216,33152,1073741824,40960,1024,460545,0,3996,3968,62,13189120,65408,41472,1120,1280,2176,2816,72,128,128,124,
2457600,3840,17982080,5120,1073741824,5177344,0,0,96256,96256,7901184,20410368,22233088,24199168,13207040,19539968,13398016,35840,36864,8960,10585088,10587648,2539520,2688,2498560,65280,17992320,5120,1073741824,5181440,160,4096,
96256,96768,7943168,20435968,22294528,24260608,13227520,19541504,13399040,37376,36864,9280,10585600,10587776,2560000,2784,2539520,126720,18002560,5120,1073741824,5259264,320,8192,97280,97280,7985152,20461568,22355968,24322048,13248000,19543040,
13400064,38400,39424,9600,10586112,10587904,2580480,2880,2580480,188160,18012800,5120,1073741824,5263360,480,12288,97280,97792,8027136,20487168,22417408,24383488,13268480,19544576,13401088,39936,39424,9920,10586624,10588032,2600960,2976,
32768,32896,33792,33920,1073741824,1073741824,1073741824,1073741824,1073741824,1073741824,1073741824,1073741824,1073741824,1073741824,1073741824,1073741824,1073741824,1073741824,1073741824,1073741824,1073741824,1073741824,1073741824,1073741824,1073741824,1073741824,1073741824,1073741824,1073741824,1073741824,1073741824,1073741824,
33328,33440,33568,34352,34464,34592,35380,35492,35620,36404,36516,36644,37432,37544,37672,38456,38568,38696,1073741824,1073741824,1073741824,1073741824,1073741824,1073741824,1073741824,1073741824,1073741824,1073741824,1073741824,1073741824,1073741824,1073741824,
40960,41088,41984,42112,1073741824,1073741824,1073741824,1073741824,1073741824,1073741824,1073741824,1073741824,1073741824,1073741824,1073741824,1073741824,1073741824,1073741824,1073741824,1073741824,1073741824,1073741824,1073741824,1073741824,1073741824,1073741824,1073741824,1073741824,1073741824,1073741824,1073741824,1073741824,
41520,41632,41760,42544,42656,42784,43572,43684,43812,44596,44708,44836,45624,45736,45864,46648,46760,46888,1073741824,1073741824,1073741824,1073741824,1073741824,1073741824,1073741824,1073741824,1073741824,1073741824,1073741824,1073741824,1073741824,1073741824,
49152,49280,50176,50304,1073741824,1073741824,1073741824,1073741824,1073741824,1073741824,1073741824,1073741824,1073741824,1073741824,1073741824,1073741824,1073741824,1073741824,1073741824,1073741824,1073741824,1073741824,1073741824,1073741824,1073741824,1073741824,1073741824,1073741824,1073741824,1073741824,1073741824,1073741824,
49712,49824,49952,50736,50848,50976,51764,51876,52004,52788,52900,53028,53816,53928,54056,54840,54952,55080,1073741824,1073741824,1073741824,1073741824,1073741824,1073741824,1073741824,1073741824,1073741824,1073741824,1073741824,1073741824,1073741824,1073741824,
57344,57472,58368,58496,1073741824,1073741824,1073741824,1073741824,1073741824,1073741824,1073741824,1073741824,1073741824,1073741824,1073741824,1073741824,1073741824,1073741824,1073741824,1073741824,1073741824,1073741824,1073741824,1073741824,1073741824,1073741824,1073741824,1073741824,1073741824,1073741824,1073741824,1073741824,
57904,58016,58144,58928,59040,59168,59956,60068,60196,60980,61092,61220,62008,62120,62248,63032,63144,63272,1073741824,1073741824,1073741824,1073741824,1073741824,1073741824,1073741824,1073741824,1073741824,1073741824,1073741824,1073741824,1073741824,1073741824,
32912,33024,33152,33936,34048,34176,1073741824,1073741824,1073741824,1073741824,1073741824,1073741824,1073741824,1073741824,1073741824,1073741824,1073741824,1073741824,1073741824,1073741824,1073741824,1073741824,1073741824,1073741824,1073741824,1073741824,1073741824,1073741824,1073741824,1073741824,1073741824,1073741824,
33584,33696,34608,34720,35636,35748,36660,36772,37688,37800,38712,38824,1073741824,1073741824,1073741824,1073741824,1073741824,1073741824,1073741824,1073741824,1073741824,1073741824,1073741824,1073741824,1073741824,1073741824,1073741824,1073741824,1073741824,1073741824,1073741824,1073741824,
41104,41216,41344,42128,42240,42368,1073741824,1073741824,1073741824,1073741824,1073741824,1073741824,1073741824,1073741824,1073741824,1073741824,1073741824,1073741824,1073741824,1073741824,1073741824,1073741824,1073741824,1073741824,1073741824,1073741824,1073741824,1073741824,1073741824,1073741824,1073741824,1073741824,
41776,41888,42800,42912,43828,43940,44852,44964,45880,45992,46904,47016,1073741824,1073741824,1073741824,1073741824,1073741824,1073741824,1073741824,1073741824,1073741824,1073741824,1073741824,1073741824,1073741824,1073741824,1073741824,1073741824,1073741824,1073741824,1073741824,1073741824,
49296,49408,49536,50320,50432,50560,1073741824,1073741824,1073741824,1073741824,1073741824,1073741824,1073741824,1073741824,1073741824,1073741824,1073741824,1073741824,1073741824,1073741824,1073741824,1073741824,1073741824,1073741824,1073741824,1073741824,1073741824,1073741824,1073741824,1073741824,1073741824,1073741824,
49968,50080,50992,51104,52020,52132,53044,53156,54072,54184,55096,55208,1073741824,1073741824,1073741824,1073741824,1073741824,1073741824,1073741824,1073741824,1073741824,1073741824,1073741824,1073741824,1073741824,1073741824,1073741824,1073741824,1073741824,1073741824,1073741824,1073741824,
57488,57600,57728,58512,58624,58752,1073741824,1073741824,1073741824,1073741824,1073741824,1073741824,1073741824,1073741824,1073741824,1073741824,1073741824,1073741824,1073741824,1073741824,1073741824,1073741824,1073741824,1073741824,1073741824,1073741824,1073741824,1073741824,1073741824,1073741824,1073741824,1073741824,
58160,58272,59184,59296,60212,60324,61236,61348,62264,62376,63288,63400,1073741824,1073741824,1073741824,1073741824,1073741824,1073741824,1073741824,1073741824,1073741824,1073741824,1073741824,1073741824,1073741824,1073741824,1073741824,1073741824,1073741824,1073741824,1073741824,1073741824,
33168,33280,34192,34304,1073741824,1073741824,1073741824,1073741824,1073741824,1073741824,1073741824,1073741824,1073741824,1073741824,1073741824,1073741824,1073741824,1073741824,1073741824,1073741824,1073741824,1073741824,1073741824,1073741824,1073741824,1073741824,1073741824,1073741824,1073741824,1073741824,1073741824,1073741824,
32832,32960,33856,33984,34884,35012,35908,36036,36936,37064,37960,38088,1073741824,1073741824,1073741824,1073741824,1073741824,1073741824,1073741824,1073741824,1073741824,1073741824,1073741824,1073741824,1073741824,1073741824,1073741824,1073741824,1073741824,1073741824,1073741824,1073741824,
41360,41472,42384,42496,1073741824,1073741824,1073741824,1073741824,1073741824,1073741824,1073741824,1073741824,1073741824,1073741824,1073741824,1073741824,1073741824,1073741824,1073741824,1073741824,1073741824,1073741824,1073741824,1073741824,1073741824,1073741824,1073741824,1073741824,1073741824,1073741824,1073741824,1073741824,
41024,41152,42048,42176,43076,43204,44100,44228,45128,45256,46152,46280,1073741824,1073741824,1073741824,1073741824,1073741824,1073741824,1073741824,1073741824,1073741824,1073741824,1073741824,1073741824,1073741824,1073741824,1073741824,1073741824,1073741824,1073741824,1073741824,1073741824,
49552,49664,50576,50688,1073741824,1073741824,1073741824,1073741824,1073741824,1073741824,1073741824,1073741824,1073741824,1073741824,1073741824,1073741824,1073741824,1073741824,1073741824,1073741824,1073741824,1073741824,1073741824,1073741824,1073741824,1073741824,1073741824,1073741824,1073741824,1073741824,1073741824,1073741824,
49216,49344,50240,50368,51268,51396,52292,52420,53320,53448,54344,54472,1073741824,1073741824,1073741824,1073741824,1073741824,1073741824,1073741824,1073741824,1073741824,1073741824,1073741824,1073741824,1073741824,1073741824,1073741824,1073741824,1073741824,1073741824,1073741824,1073741824,
57744,57856,58768,58880,1073741824,1073741824,1073741824,1073741824,1073741824,1073741824,1073741824,1073741824,1073741824,1073741824,1073741824,1073741824,1073741824,1073741824,1073741824,1073741824,1073741824,1073741824,1073741824,1073741824,1073741824,1073741824,1073741824,1073741824,1073741824,1073741824,1073741824,1073741824,
57408,57536,58432,58560,59460,59588,60484,60612,61512,61640,62536,62664,1073741824,1073741824,1073741824,1073741824,1073741824,1073741824,1073741824,1073741824,1073741824,1073741824,1073741824,1073741824,1073741824,1073741824,1073741824,1073741824,1073741824,1073741824,1073741824,1073741824,
33296,33408,33536,34320,34432,34560,1073741824,1073741824,1073741824,1073741824,1073741824,1073741824,1073741824,1073741824,1073741824,1073741824,1073741824,1073741824,1073741824,1073741824,1073741824,1073741824,1073741824,1073741824,1073741824,1073741824,1073741824,1073741824,1073741824,1073741824,1073741824,1073741824,
32976,33088,33216,34000,34112,34240,35028,35140,35268,36052,36164,36292,37080,37192,37320,38104,38216,38344,1073741824,1073741824,1073741824,1073741824,1073741824,1073741824,1073741824,1073741824,1073741824,1073741824,1073741824,1073741824,1073741824,1073741824,
41488,41600,41728,42512,42624,42752,1073741824,1073741824,1073741824,1073741824,1073741824,1073741824,1073741824,1073741824,1073741824,1073741824,1073741824,1073741824,1073741824,1073741824,1073741824,1073741824,1073741824,1073741824,1073741824,1073741824,1073741824,1073741824,1073741824,1073741824,1073741824,1073741824,
41168,41280,41408,42192,42304,42432,43220,43332,43460,44244,44356,44484,45272,45384,45512,46296,46408,46536,1073741824,1073741824,1073741824,1073741824,1073741824,1073741824,1073741824,1073741824,1073741824,1073741824,1073741824,1073741824,1073741824,1073741824,
49680,49792,49920,50704,50816,50944,1073741824,1073741824,1073741824,1073741824,1073741824,1073741824,1073741824,1073741824,1073741824,1073741824,1073741824,1073741824,1073741824,1073741824,1073741824,1073741824,1073741824,1073741824,1073741824,1073741824,1073741824,1073741824,1073741824,1073741824,1073741824,1073741824,
49360,49472,49600,50384,50496,50624,51412,51524,51652,52436,52548,52676,53464,53576,53704,54488,54600,54728,1073741824,1073741824,1073741824,1073741824,1073741824,1073741824,1073741824,1073741824,1073741824,1073741824,1073741824,1073741824,1073741824,1073741824,
57872,57984,58112,58896,59008,59136,1073741824,1073741824,1073741824,1073741824,1073741824,1073741824,1073741824,1073741824,1073741824,1073741824,1073741824,1073741824,1073741824,1073741824,1073741824,1073741824,1073741824,1073741824,1073741824,1073741824,1073741824,1073741824,1073741824,1073741824,1073741824,1073741824,
57552,57664,57792,58576,58688,58816,59604,59716,59844,60628,60740,60868,61656,61768,61896,62680,62792,62920,1073741824,1073741824,1073741824,1073741824,1073741824,1073741824,1073741824,1073741824,1073741824,1073741824,1073741824,1073741824,1073741824,1073741824,
33552,33664,34576,34688,1073741824,1073741824,1073741824,1073741824,1073741824,1073741824,1073741824,1073741824,1073741824,1073741824,1073741824,1073741824,1073741824,1073741824,1073741824,1073741824,1073741824,1073741824,1073741824,1073741824,1073741824,1073741824,1073741824,1073741824,1073741824,1073741824,1073741824,1073741824,
33232,33344,34256,34368,35284,35396,36308,36420,37336,37448,38360,38472,1073741824,1073741824,1073741824,1073741824,1073741824,1073741824,1073741824,1073741824,1073741824,1073741824,1073741824,1073741824,1073741824,1073741824,1073741824,1073741824,1073741824,1073741824,1073741824,1073741824,
41744,41856,42768,42880,1073741824,1073741824,1073741824,1073741824,1073741824,1073741824,1073741824,1073741824,1073741824,1073741824,1073741824,1073741824,1073741824,1073741824,1073741824,1073741824,1073741824,1073741824,1073741824,1073741824,1073741824,1073741824,1073741824,1073741824,1073741824,1073741824,1073741824,1073741824,
41424,41536,42448,42560,43476,43588,44500,44612,45528,45640,46552,46664,1073741824,1073741824,1073741824,1073741824,1073741824,1073741824,1073741824,1073741824,1073741824,1073741824,1073741824,1073741824,1073741824,1073741824,1073741824,1073741824,1073741824,1073741824,1073741824,1073741824,
49936,50048,50960,51072,1073741824,1073741824,1073741824,1073741824,1073741824,1073741824,1073741824,1073741824,1073741824,1073741824,1073741824,1073741824,1073741824,1073741824,1073741824,1073741824,1073741824,1073741824,1073741824,1073741824,1073741824,1073741824,1073741824,1073741824,1073741824,1073741824,1073741824,1073741824,
49616,49728,50640,50752,51668,51780,52692,52804,53720,53832,54744,54856,1073741824,1073741824,1073741824,1073741824,1073741824,1073741824,1073741824,1073741824,1073741824,1073741824,1073741824,1073741824,1073741824,1073741824,1073741824,1073741824,1073741824,1073741824,1073741824,1073741824,
58128,58240,59152,59264,1073741824,1073741824,1073741824,1073741824,1073741824,1073741824,1073741824,1073741824,1073741824,1073741824,1073741824,1073741824,1073741824,1073741824,1073741824,1073741824,1073741824,1073741824,1073741824,1073741824,1073741824,1073741824,1073741824,1073741824,1073741824,1073741824,1073741824,1073741824,
57808,57920,58832,58944,59860,59972,60884,60996,61912,62024,62936,63048,1073741824,1073741824,1073741824,1073741824,1073741824,1073741824,1073741824,1073741824,1073741824,1073741824,1073741824,1073741824,1073741824,1073741824,1073741824,1073741824,1073741824,1073741824,1073741824,1073741824,
32800,32928,33824,33952,34852,34980,35876,36004,36904,37032,37928,38056,1073741824,1073741824,1073741824,1073741824,1073741824,1073741824,1073741824,1073741824,1073741824,1073741824,1073741824,1073741824,1073741824,1073741824,1073741824,1073741824,1073741824,1073741824,1073741824,1073741824,
33360,33472,33600,34384,34496,34624,35412,35524,35652,36436,36548,36676,37464,37576,37704,38488,38600,38728,1073741824,1073741824,1073741824,1073741824,1073741824,1073741824,1073741824,1073741824,1073741824,1073741824,1073741824,1073741824,1073741824,1073741824,
40992,41120,42016,42144,43044,43172,44068,44196,45096,45224,46120,46248,1073741824,1073741824,1073741824,1073741824,1073741824,1073741824,1073741824,1073741824,1073741824,1073741824,1073741824,1073741824,1073741824,1073741824,1073741824,1073741824,1073741824,1073741824,1073741824,1073741824,
41552,41664,41792,42576,42688,42816,43604,43716,43844,44628,44740,44868,45656,45768,45896,46680,46792,46920,1073741824,1073741824,1073741824,1073741824,1073741824,1073741824,1073741824,1073741824,1073741824,1073741824,1073741824,1073741824,1073741824,1073741824,
49184,49312,50208,50336,51236,51364,52260,52388,53288,53416,54312,54440,1073741824,1073741824,1073741824,1073741824,1073741824,1073741824,1073741824,1073741824,1073741824,1073741824,1073741824,1073741824,1073741824,1073741824,1073741824,1073741824,1073741824,1073741824,1073741824,1073741824,
49744,49856,49984,50768,50880,51008,51796,51908,52036,52820,52932,53060,53848,53960,54088,54872,54984,55112,1073741824,1073741824,1073741824,1073741824,1073741824,1073741824,1073741824,1073741824,1073741824,1073741824,1073741824,1073741824,1073741824,1073741824,
57376,57504,58400,58528,59428,59556,60452,60580,61480,61608,62504,62632,1073741824,1073741824,1073741824,1073741824,1073741824,1073741824,1073741824,1073741824,1073741824,1073741824,1073741824,1073741824,1073741824,1073741824,1073741824,1073741824,1073741824,1073741824,1073741824,1073741824,
57936,58048,58176,58960,59072,59200,59988,60100,60228,61012,61124,61252,62040,62152,62280,63064,63176,63304,1073741824,1073741824,1073741824,1073741824,1073741824,1073741824,1073741824,1073741824,1073741824,1073741824,1073741824,1073741824,1073741824,1073741824,
32944,33056,33184,33968,34080,34208,34996,35108,35236,36020,36132,36260,37048,37160,37288,38072,38184,38312,1073741824,1073741824,1073741824,1073741824,1073741824,1073741824,1073741824,1073741824,1073741824,1073741824,1073741824,1073741824,1073741824,1073741824,
33616,33728,34640,34752,35668,35780,36692,36804,37720,37832,38744,38856,1073741824,1073741824,1073741824,1073741824,1073741824,1073741824,1073741824,1073741824,1073741824,1073741824,1073741824,1073741824,1073741824,1073741824,1073741824,1073741824,1073741824,1073741824,1073741824,1073741824,
41136,41248,41376,42160,42272,42400,43188,43300,43428,44212,44324,44452,45240,45352,45480,46264,46376,46504,1073741824,1073741824,1073741824,1073741824,1073741824,1073741824,1073741824,1073741824,1073741824,1073741824,1073741824,1073741824,1073741824,1073741824,
41808,41920,42832,42944,43860,43972,44884,44996,45912,46024,46936,47048,1073741824,1073741824,1073741824,1073741824,1073741824,1073741824,1073741824,1073741824,1073741824,1073741824,1073741824,1073741824,1073741824,1073741824,1073741824,1073741824,1073741824,1073741824,1073741824,1073741824,
49328,49440,49568,50352,50464,50592,51380,51492,51620,52404,52516,52644,53432,53544,53672,54456,54568,54696,1073741824,1073741824,1073741824,1073741824,1073741824,1073741824,1073741824,1073741824,1073741824,1073741824,1073741824,1073741824,1073741824,1073741824,
50000,50112,51024,51136,52052,52164,53076,53188,54104,54216,55128,55240,1073741824,1073741824,1073741824,1073741824,1073741824,1073741824,1073741824,1073741824,1073741824,1073741824,1073741824,1073741824,1073741824,1073741824,1073741824,1073741824,1073741824,1073741824,1073741824,1073741824,
57520,57632,57760,58544,58656,58784,59572,59684,59812,60596,60708,60836,61624,61736,61864,62648,62760,62888,1073741824,1073741824,1073741824,1073741824,1073741824,1073741824,1073741824,1073741824,1073741824,1073741824,1073741824,1073741824,1073741824,1073741824,
58192,58304,59216,59328,60244,60356,61268,61380,62296,62408,63320,63432,1073741824,1073741824,1073741824,1073741824,1073741824,1073741824,1073741824,1073741824,1073741824,1073741824,1073741824,1073741824,1073741824,1073741824,1073741824,1073741824,1073741824,1073741824,1073741824,1073741824,
33200,33312,34224,34336,35252,35364,36276,36388,37304,37416,38328,38440,1073741824,1073741824,1073741824,1073741824,1073741824,1073741824,1073741824,1073741824,1073741824,1073741824,1073741824,1073741824,1073741824,1073741824,1073741824,1073741824,1073741824,1073741824,1073741824,1073741824,
1073741824,1073741824,1073741824,1073741824,1073741824,1073741824,1073741824,1073741824,1073741824,1073741824,1073741824,1073741824,1073741824,1073741824,1073741824,1073741824,1073741824,1073741824,1073741824,1073741824,1073741824,1073741824,1073741824,1073741824,1073741824,1073741824,1073741824,1073741824,1073741824,1073741824,1073741824,1073741824,
41392,41504,42416,42528,43444,43556,44468,44580,45496,45608,46520,46632,1073741824,1073741824,1073741824,1073741824,1073741824,1073741824,1073741824,1073741824,1073741824,1073741824,1073741824,1073741824,1073741824,1073741824,1073741824,1073741824,1073741824,1073741824,1073741824,1073741824,
1073741824,1073741824,1073741824,1073741824,1073741824,1073741824,1073741824,1073741824,1073741824,1073741824,1073741824,1073741824,1073741824,1073741824,1073741824,1073741824,1073741824,1073741824,1073741824,1073741824,1073741824,1073741824,1073741824,1073741824,1073741824,1073741824,1073741824,1073741824,1073741824,1073741824,1073741824,1073741824,
49584,49696,50608,50720,51636,51748,52660,52772,53688,53800,54712,54824,1073741824,1073741824,1073741824,1073741824,1073741824,1073741824,1073741824,1073741824,1073741824,1073741824,1073741824,1073741824,1073741824,1073741824,1073741824,1073741824,1073741824,1073741824,1073741824,1073741824,
1073741824,1073741824,1073741824,1073741824,1073741824,1073741824,1073741824,1073741824,1073741824,1073741824,1073741824,1073741824,1073741824,1073741824,1073741824,1073741824,1073741824,1073741824,1073741824,1073741824,1073741824,1073741824,1073741824,1073741824,1073741824,1073741824,1073741824,1073741824,1073741824,1073741824,1073741824,1073741824,
57776,57888,58800,58912,59828,59940,60852,60964,61880,61992,62904,63016,1073741824,1073741824,1073741824,1073741824,1073741824,1073741824,1073741824,1073741824,1073741824,1073741824,1073741824,1073741824,1073741824,1073741824,1073741824,1073741824,1073741824,1073741824,1073741824,1073741824,
1073741824,1073741824,1073741824,1073741824,1073741824,1073741824,1073741824,1073741824,1073741824,1073741824,1073741824,1073741824,1073741824,1073741824,1073741824,1073741824,1073741824,1073741824,1073741824,1073741824,1073741824,1073741824,1073741824,1073741824,1073741824,1073741824,1073741824,1073741824,1073741824,1073741824,1073741824,1073741824,
34816,34944,35840,35968,1073741824,1073741824,1073741824,1073741824,1073741824,1073741824,1073741824,1073741824,1073741824,1073741824,1073741824,1073741824,1073741824,1073741824,1073741824,1073741824,1073741824,1073741824,1073741824,1073741824,1073741824,1073741824,1073741824,1073741824,1073741824,1073741824,1073741824,1073741824,
35376,35488,35616,36400,36512,36640,37428,37540,37668,38452,38564,38692,39480,39592,39720,40504,40616,40744,1073741824,1073741824,1073741824,1073741824,1073741824,1073741824,1073741824,1073741824,1073741824,1073741824,1073741824,1073741824,1073741824,1073741824,
43008,43136,44032,44160,1073741824,1073741824,1073741824,1073741824,1073741824,1073741824,1073741824,1073741824,1073741824,1073741824,1073741824,1073741824,1073741824,1073741824,1073741824,1073741824,1073741824,1073741824,1073741824,1073741824,1073741824,1073741824,1073741824,1073741824,1073741824,1073741824,1073741824,1073741824,
43568,43680,43808,44592,44704,44832,45620,45732,45860,46644,46756,46884,47672,47784,47912,48696,48808,48936,1073741824,1073741824,1073741824,1073741824,1073741824,1073741824,1073741824,1073741824,1073741824,1073741824,1073741824,1073741824,1073741824,1073741824,
51200,51328,52224,52352,1073741824,1073741824,1073741824,1073741824,1073741824,1073741824,1073741824,1073741824,1073741824,1073741824,1073741824,1073741824,1073741824,1073741824,1073741824,1073741824,1073741824,1073741824,1073741824,1073741824,1073741824,1073741824,1073741824,1073741824,1073741824,1073741824,1073741824,1073741824,
51760,51872,52000,52784,52896,53024,53812,53924,54052,54836,54948,55076,55864,55976,56104,56888,57000,57128,1073741824,1073741824,1073741824,1073741824,1073741824,1073741824,1073741824,1073741824,1073741824,1073741824,1073741824,1073741824,1073741824,1073741824,
59392,59520,60416,60544,1073741824,1073741824,1073741824,1073741824,1073741824,1073741824,1073741824,1073741824,1073741824,1073741824,1073741824,1073741824,1073741824,1073741824,1073741824,1073741824,1073741824,1073741824,1073741824,1073741824,1073741824,1073741824,1073741824,1073741824,1073741824,1073741824,1073741824,1073741824,
59952,60064,60192,60976,61088,61216,62004,62116,62244,63028,63140,63268,64056,64168,64296,65080,65192,65320,1073741824,1073741824,1073741824,1073741824,1073741824,1073741824,1073741824,1073741824,1073741824,1073741824,1073741824,1073741824,1073741824,1073741824,
34960,35072,35200,35984,36096,36224,1073741824,1073741824,1073741824,1073741824,1073741824,1073741824,1073741824,1073741824,1073741824,1073741824,1073741824,1073741824,1073741824,1073741824,1073741824,1073741824,1073741824,1073741824,1073741824,1073741824,1073741824,1073741824,1073741824,1073741824,1073741824,1073741824,
35632,35744,36656,36768,37684,37796,38708,38820,39736,39848,40760,40872,1073741824,1073741824,1073741824,1073741824,1073741824,1073741824,1073741824,1073741824,1073741824,1073741824,1073741824,1073741824,1073741824,1073741824,1073741824,1073741824,1073741824,1073741824,1073741824,1073741824,
43152,43264,43392,44176,44288,44416,1073741824,1073741824,1073741824,1073741824,1073741824,1073741824,1073741824,1073741824,1073741824,1073741824,1073741824,1073741824,1073741824,1073741824,1073741824,1073741824,1073741824,1073741824,1073741824,1073741824,1073741824,1073741824,1073741824,1073741824,1073741824,1073741824,
43824,43936,44848,44960,45876,45988,46900,47012,47928,48040,48952,49064,1073741824,1073741824,1073741824,1073741824,1073741824,1073741824,1073741824,1073741824,1073741824,1073741824,1073741824,1073741824,1073741824,1073741824,1073741824,1073741824,1073741824,1073741824,1073741824,1073741824,
51344,51456,51584,52368,52480,52608,1073741824,1073741824,1073741824,1073741824,1073741824,1073741824,1073741824,1073741824,1073741824,1073741824,1073741824,1073741824,1073741824,1073741824,1073741824,1073741824,1073741824,1073741824,1073741824,1073741824,1073741824,1073741824,1073741824,1073741824,1073741824,1073741824,
52016,52128,53040,53152,54068,54180,55092,55204,56120,56232,57144,57256,1073741824,1073741824,1073741824,1073741824,1073741824,1073741824,1073741824,1073741824,1073741824,1073741824,1073741824,1073741824,1073741824,1073741824,1073741824,1073741824,1073741824,1073741824,1073741824,1073741824,
59536,59648,59776,60560,60672,60800,1073741824,1073741824,1073741824,1073741824,1073741824,1073741824,1073741824,1073741824,1073741824,1073741824,1073741824,1073741824,1073741824,1073741824,1073741824,1073741824,1073741824,1073741824,1073741824,1073741824,1073741824,1073741824,1073741824,1073741824,1073741824,1073741824,
60208,60320,61232,61344,62260,62372,63284,63396,64312,64424,65336,65448,1073741824,1073741824,1073741824,1073741824,1073741824,1073741824,1073741824,1073741824,1073741824,1073741824,1073741824,1073741824,1073741824,1073741824,1073741824,1073741824,1073741824,1073741824,1073741824,1073741824,
35216,35328,36240,36352,1073741824,1073741824,1073741824,1073741824,1073741824,1073741824,1073741824,1073741824,1073741824,1073741824,1073741824,1073741824,1073741824,1073741824,1073741824,1073741824,1073741824,1073741824,1073741824,1073741824,1073741824,1073741824,1073741824,1073741824,1073741824,1073741824,1073741824,1073741824,
34880,35008,35904,36032,36932,37060,37956,38084,38984,39112,40008,40136,1073741824,1073741824,1073741824,1073741824,1073741824,1073741824,1073741824,1073741824,1073741824,1073741824,1073741824,1073741824,1073741824,1073741824,1073741824,1073741824,1073741824,1073741824,1073741824,1073741824,
43408,43520,44432,44544,1073741824,1073741824,1073741824,1073741824,1073741824,1073741824,1073741824,1073741824,1073741824,1073741824,1073741824,1073741824,1073741824,1073741824,1073741824,1073741824,1073741824,1073741824,1073741824,1073741824,1073741824,1073741824,1073741824,1073741824,1073741824,1073741824,1073741824,1073741824,
43072,43200,44096,44224,45124,45252,46148,46276,47176,47304,48200,48328,1073741824,1073741824,1073741824,1073741824,1073741824,1073741824,1073741824,1073741824,1073741824,1073741824,1073741824,1073741824,1073741824,1073741824,1073741824,1073741824,1073741824,1073741824,1073741824,1073741824,
51600,51712,52624,52736,1073741824,1073741824,1073741824,1073741824,1073741824,1073741824,1073741824,1073741824,1073741824,1073741824,1073741824,1073741824,1073741824,1073741824,1073741824,1073741824,1073741824,1073741824,1073741824,1073741824,1073741824,1073741824,1073741824,1073741824,1073741824,1073741824,1073741824,1073741824,
51264,51392,52288,52416,53316,53444,54340,54468,55368,55496,56392,56520,1073741824,1073741824,1073741824,1073741824,1073741824,1073741824,1073741824,1073741824,1073741824,1073741824,1073741824,1073741824,1073741824,1073741824,1073741824,1073741824,1073741824,1073741824,1073741824,1073741824,
59792,59904,60816,60928,1073741824,1073741824,1073741824,1073741824,1073741824,1073741824,1073741824,1073741824,1073741824,1073741824,1073741824,1073741824,1073741824,1073741824,1073741824,1073741824,1073741824,1073741824,1073741824,1073741824,1073741824,1073741824,1073741824,1073741824,1073741824,1073741824,1073741824,1073741824,
59456,59584,60480,60608,61508,61636,62532,62660,63560,63688,64584,64712,1073741824,1073741824,1073741824,1073741824,1073741824,1073741824,1073741824,1073741824,1073741824,1073741824,1073741824,1073741824,1073741824,1073741824,1073741824,1073741824,1073741824,1073741824,1073741824,1073741824,
35344,35456,35584,36368,36480,36608,1073741824,1073741824,1073741824,1073741824,1073741824,1073741824,1073741824,1073741824,1073741824,1073741824,1073741824,1073741824,1073741824,1073741824,1073741824,1073741824,1073741824,1073741824,1073741824,1073741824,1073741824,1073741824,1073741824,1073741824,1073741824,1073741824,
35024,35136,35264,36048,36160,36288,37076,37188,37316,38100,38212,38340,39128,39240,39368,40152,40264,40392,1073741824,1073741824,1073741824,1073741824,1073741824,1073741824,1073741824,1073741824,1073741824,1073741824,1073741824,1073741824,1073741824,1073741824,
43536,43648,43776,44560,44672,44800,1073741824,1073741824,1073741824,1073741824,1073741824,1073741824,1073741824,1073741824,1073741824,1073741824,1073741824,1073741824,1073741824,1073741824,1073741824,1073741824,1073741824,1073741824,1073741824,1073741824,1073741824,1073741824,1073741824,1073741824,1073741824,1073741824,
43216,43328,43456,44240,44352,44480,45268,45380,45508,46292,46404,46532,47320,47432,47560,48344,48456,48584,1073741824,1073741824,1073741824,1073741824,1073741824,1073741824,1073741824,1073741824,1073741824,1073741824,1073741824,1073741824,1073741824,1073741824,
51728,51840,51968,52752,52864,52992,1073741824,1073741824,1073741824,1073741824,1073741824,1073741824,1073741824,1073741824,1073741824,1073741824,1073741824,1073741824,1073741824,1073741824,1073741824,1073741824,1073741824,1073741824,1073741824,1073741824,1073741824,1073741824,1073741824,1073741824,1073741824,1073741824,
51408,51520,51648,52432,52544,52672,53460,53572,53700,54484,54596,54724,55512,55624,55752,56536,56648,56776,1073741824,1073741824,1073741824,1073741824,1073741824,1073741824,1073741824,1073741824,1073741824,1073741824,1073741824,1073741824,1073741824,1073741824,
59920,60032,60160,60944,61056,61184,1073741824,1073741824,1073741824,1073741824,1073741824,1073741824,1073741824,1073741824,1073741824,1073741824,1073741824,1073741824,1073741824,1073741824,1073741824,1073741824,1073741824,1073741824,1073741824,1073741824,1073741824,1073741824,1073741824,1073741824,1073741824,1073741824,
59600,59712,59840,60624,60736,60864,61652,61764,61892,62676,62788,62916,63704,63816,63944,64728,64840,64968,1073741824,1073741824,1073741824,1073741824,1073741824,1073741824,1073741824,1073741824,1073741824,1073741824,1073741824,1073741824,1073741824,1073741824,
35600,35712,36624,36736,1073741824,1073741824,1073741824,1073741824,1073741824,1073741824,1073741824,1073741824,1073741824,1073741824,1073741824,1073741824,1073741824,1073741824,1073741824,1073741824,1073741824,1073741824,1073741824,1073741824,1073741824,1073741824,1073741824,1073741824,1073741824,1073741824,1073741824,1073741824,
35280,35392,36304,36416,37332,37444,38356,38468,39384,39496,40408,40520,1073741824,1073741824,1073741824,1073741824,1073741824,1073741824,1073741824,1073741824,1073741824,1073741824,1073741824,1073741824,1073741824,1073741824,1073741824,1073741824,1073741824,1073741824,1073741824,1073741824,
43792,43904,44816,44928,1073741824,1073741824,1073741824,1073741824,1073741824,1073741824,1073741824,1073741824,1073741824,1073741824,1073741824,1073741824,1073741824,1073741824,1073741824,1073741824,1073741824,1073741824,1073741824,1073741824,1073741824,1073741824,1073741824,1073741824,1073741824,1073741824,1073741824,1073741824,
43472,43584,44496,44608,45524,45636,46548,46660,47576,47688,48600,48712,1073741824,1073741824,1073741824,1073741824,1073741824,1073741824,1073741824,1073741824,1073741824,1073741824,1073741824,1073741824,1073741824,1073741824,1073741824,1073741824,1073741824,1073741824,1073741824,1073741824,
51984,52096,53008,53120,1073741824,1073741824,1073741824,1073741824,1073741824,1073741824,1073741824,1073741824,1073741824,1073741824,1073741824,1073741824,1073741824,1073741824,1073741824,1073741824,1073741824,1073741824,1073741824,1073741824,1073741824,1073741824,1073741824,1073741824,1073741824,1073741824,1073741824,1073741824,
51664,51776,52688,52800,53716,53828,54740,54852,55768,55880,56792,56904,1073741824,1073741824,1073741824,1073741824,1073741824,1073741824,1073741824,1073741824,1073741824,1073741824,1073741824,1073741824,1073741824,1073741824,1073741824,1073741824,1073741824,1073741824,1073741824,1073741824,
60176,60288,61200,61312,1073741824,1073741824,1073741824,1073741824,1073741824,1073741824,1073741824,1073741824,1073741824,1073741824,1073741824,1073741824,1073741824,1073741824,1073741824,1073741824,1073741824,1073741824,1073741824,1073741824,1073741824,1073741824,1073741824,1073741824,1073741824,1073741824,1073741824,1073741824,
59856,59968,60880,60992,61908,62020,62932,63044,63960,64072,64984,65096,1073741824,1073741824,1073741824,1073741824,1073741824,1073741824,1073741824,1073741824,1073741824,1073741824,1073741824,1073741824,1073741824,1073741824,1073741824,1073741824,1073741824,1073741824,1073741824,1073741824,
34848,34976,35872,36000,36900,37028,37924,38052,38952,39080,39976,40104,1073741824,1073741824,1073741824,1073741824,1073741824,1073741824,1073741824,1073741824,1073741824,1073741824,1073741824,1073741824,1073741824,1073741824,1073741824,1073741824,1073741824,1073741824,1073741824,1073741824,
35408,35520,35648,36432,36544,36672,37460,37572,37700,38484,38596,38724,39512,39624,39752,40536,40648,40776,1073741824,1073741824,1073741824,1073741824,1073741824,1073741824,1073741824,1073741824,1073741824,1073741824,1073741824,1073741824,1073741824,1073741824,
43040,43168,44064,44192,45092,45220,46116,46244,47144,47272,48168,48296,1073741824,1073741824,1073741824,1073741824,1073741824,1073741824,1073741824,1073741824,1073741824,1073741824,1073741824,1073741824,1073741824,1073741824,1073741824,1073741824,1073741824,1073741824,1073741824,1073741824,
43600,43712,43840,44624,44736,44864,45652,45764,45892,46676,46788,46916,47704,47816,47944,48728,48840,48968,1073741824,1073741824,1073741824,1073741824,1073741824,1073741824,1073741824,1073741824,1073741824,1073741824,1073741824,1073741824,1073741824,1073741824,
51232,51360,52256,52384,53284,53412,54308,54436,55336,55464,56360,56488,1073741824,1073741824,1073741824,1073741824,1073741824,1073741824,1073741824,1073741824,1073741824,1073741824,1073741824,1073741824,1073741824,1073741824,1073741824,1073741824,1073741824,1073741824,1073741824,1073741824,
51792,51904,52032,52816,52928,53056,53844,53956,54084,54868,54980,55108,55896,56008,56136,56920,57032,57160,1073741824,1073741824,1073741824,1073741824,1073741824,1073741824,1073741824,1073741824,1073741824,1073741824,1073741824,1073741824,1073741824,1073741824,
59424,59552,60448,60576,61476,61604,62500,62628,63528,63656,64552,64680,1073741824,1073741824,1073741824,1073741824,1073741824,1073741824,1073741824,1073741824,1073741824,1073741824,1073741824,1073741824,1073741824,1073741824,1073741824,1073741824,1073741824,1073741824,1073741824,1073741824,
59984,60096,60224,61008,61120,61248,62036,62148,62276,63060,63172,63300,64088,64200,64328,65112,65224,65352,1073741824,1073741824,1073741824,1073741824,1073741824,1073741824,1073741824,1073741824,1073741824,1073741824,1073741824,1073741824,1073741824,1073741824,
34992,35104,35232,36016,36128,36256,37044,37156,37284,38068,38180,38308,39096,39208,39336,40120,40232,40360,1073741824,1073741824,1073741824,1073741824,1073741824,1073741824,1073741824,1073741824,1073741824,1073741824,1073741824,1073741824,1073741824,1073741824,
35664,35776,36688,36800,37716,37828,38740,38852,39768,39880,40792,40904,1073741824,1073741824,1073741824,1073741824,1073741824,1073741824,1073741824,1073741824,1073741824,1073741824,1073741824,1073741824,1073741824,1073741824,1073741824,1073741824,1073741824,1073741824,1073741824,1073741824,
43184,43296,43424,44208,44320,44448,45236,45348,45476,46260,46372,46500,47288,47400,47528,48312,48424,48552,1073741824,1073741824,1073741824,1073741824,1073741824,1073741824,1073741824,1073741824,1073741824,1073741824,1073741824,1073741824,1073741824,1073741824,
43856,43968,44880,44992,45908,46020,46932,47044,47960,48072,48984,49096,1073741824,1073741824,1073741824,1073741824,1073741824,1073741824,1073741824,1073741824,1073741824,1073741824,1073741824,1073741824,1073741824,1073741824,1073741824,1073741824,1073741824,1073741824,1073741824,1073741824,
51376,51488,51616,52400,52512,52640,53428,53540,53668,54452,54564,54692,55480,55592,55720,56504,56616,56744,1073741824,1073741824,1073741824,1073741824,1073741824,1073741824,1073741824,1073741824,1073741824,1073741824,1073741824,1073741824,1073741824,1073741824,
52048,52160,53072,53184,54100,54212,55124,55236,56152,56264,57176,57288,1073741824,1073741824,1073741824,1073741824,1073741824,1073741824,1073741824,1073741824,1073741824,1073741824,1073741824,1073741824,1073741824,1073741824,1073741824,1073741824,1073741824,1073741824,1073741824,1073741824,
59568,59680,59808,60592,60704,60832,61620,61732,61860,62644,62756,62884,63672,63784,63912,64696,64808,64936,1073741824,1073741824,1073741824,1073741824,1073741824,1073741824,1073741824,1073741824,1073741824,1073741824,1073741824,1073741824,1073741824,1073741824,
60240,60352,61264,61376,62292,62404,63316,63428,64344,64456,65368,65480,1073741824,1073741824,1073741824,1073741824,1073741824,1073741824,1073741824,1073741824,1073741824,1073741824,1073741824,1073741824,1073741824,1073741824,1073741824,1073741824,1073741824,1073741824,1073741824,1073741824,
35248,35360,36272,36384,37300,37412,38324,38436,39352,39464,40376,40488,1073741824,1073741824,1073741824,1073741824,1073741824,1073741824,1073741824,1073741824,1073741824,1073741824,1073741824,1073741824,1073741824,1073741824,1073741824,1073741824,1073741824,1073741824,1073741824,1073741824,
1073741824,1073741824,1073741824,1073741824,1073741824,1073741824,1073741824,1073741824,1073741824,1073741824,1073741824,1073741824,1073741824,1073741824,1073741824,1073741824,1073741824,1073741824,1073741824,1073741824,1073741824,1073741824,1073741824,1073741824,1073741824,1073741824,1073741824,1073741824,1073741824,1073741824,1073741824,1073741824,
43440,43552,44464,44576,45492,45604,46516,46628,47544,47656,48568,48680,1073741824,1073741824,1073741824,1073741824,1073741824,1073741824,1073741824,1073741824,1073741824,1073741824,1073741824,1073741824,1073741824,1073741824,1073741824,1073741824,1073741824,1073741824,1073741824,1073741824,
1073741824,1073741824,1073741824,1073741824,1073741824,1073741824,1073741824,1073741824,1073741824,1073741824,1073741824,1073741824,1073741824,1073741824,1073741824,1073741824,1073741824,1073741824,1073741824,1073741824,1073741824,1073741824,1073741824,1073741824,1073741824,1073741824,1073741824,1073741824,1073741824,1073741824,1073741824,1073741824,
51632,51744,52656,52768,53684,53796,54708,54820,55736,55848,56760,56872,1073741824,1073741824,1073741824,1073741824,1073741824,1073741824,1073741824,1073741824,1073741824,1073741824,1073741824,1073741824,1073741824,1073741824,1073741824,1073741824,1073741824,1073741824,1073741824,1073741824,
1073741824,1073741824,1073741824,1073741824,1073741824,1073741824,1073741824,1073741824,1073741824,1073741824,1073741824,1073741824,1073741824,1073741824,1073741824,1073741824,1073741824,1073741824,1073741824,1073741824,1073741824,1073741824,1073741824,1073741824,1073741824,1073741824,1073741824,1073741824,1073741824,1073741824,1073741824,1073741824,
59824,59936,60848,60960,61876,61988,62900,63012,63928,64040,64952,65064,1073741824,1073741824,1073741824,1073741824,1073741824,1073741824,1073741824,1073741824,1073741824,1073741824,1073741824,1073741824,1073741824,1073741824,1073741824,1073741824,1073741824,1073741824,1073741824,1073741824,
1073741824,1073741824,1073741824,1073741824,1073741824,1073741824,1073741824,1073741824,1073741824,1073741824,1073741824,1073741824,1073741824,1073741824,1073741824,1073741824,1073741824,1073741824,1073741824,1073741824,1073741824,1073741824,1073741824,1073741824,1073741824,1073741824,1073741824,1073741824,1073741824,1073741824,1073741824,1073741824,
36864,36992,37888,38016,1073741824,1073741824,1073741824,1073741824,1073741824,1073741824,1073741824,1073741824,1073741824,1073741824,1073741824,1073741824,1073741824,1073741824,1073741824,1073741824,1073741824,1073741824,1073741824,1073741824,1073741824,1073741824,1073741824,1073741824,1073741824,1073741824,1073741824,1073741824,
37424,37536,37664,38448,38560,38688,39476,39588,39716,40500,40612,40740,41528,41640,41768,42552,42664,42792,1073741824,1073741824,1073741824,1073741824,1073741824,1073741824,1073741824,1073741824,1073741824,1073741824,1073741824,1073741824,1073741824,1073741824,
45056,45184,46080,46208,1073741824,1073741824,1073741824,1073741824,1073741824,1073741824,1073741824,1073741824,1073741824,1073741824,1073741824,1073741824,1073741824,1073741824,1073741824,1073741824,1073741824,1073741824,1073741824,1073741824,1073741824,1073741824,1073741824,1073741824,1073741824,1073741824,1073741824,1073741824,
45616,45728,45856,46640,46752,46880,47668,47780,47908,48692,48804,48932,49720,49832,49960,50744,50856,50984,1073741824,1073741824,1073741824,1073741824,1073741824,1073741824,1073741824,1073741824,1073741824,1073741824,1073741824,1073741824,1073741824,1073741824,
53248,53376,54272,54400,1073741824,1073741824,1073741824,1073741824,1073741824,1073741824,1073741824,1073741824,1073741824,1073741824,1073741824,1073741824,1073741824,1073741824,1073741824,1073741824,1073741824,1073741824,1073741824,1073741824,1073741824,1073741824,1073741824,1073741824,1073741824,1073741824,1073741824,1073741824,
53808,53920,54048,54832,54944,55072,55860,55972,56100,56884,56996,57124,57912,58024,58152,58936,59048,59176,1073741824,1073741824,1073741824,1073741824,1073741824,1073741824,1073741824,1073741824,1073741824,1073741824,1073741824,1073741824,1073741824,1073741824,
61440,61568,62464,62592,1073741824,1073741824,1073741824,1073741824,1073741824,1073741824,1073741824,1073741824,1073741824,1073741824,1073741824,1073741824,1073741824,1073741824,1073741824,1073741824,1073741824,1073741824,1073741824,1073741824,1073741824,1073741824,1073741824,1073741824,1073741824,1073741824,1073741824,1073741824,
62000,62112,62240,63024,63136,63264,64052,64164,64292,65076,65188,65316,1073741824,1073741824,1073741824,1073741824,1073741824,1073741824,1073741824,1073741824,1073741824,1073741824,1073741824,1073741824,1073741824,1073741824,1073741824,1073741824,1073741824,1073741824,1073741824,1073741824,
37008,37120,37248,38032,38144,38272,1073741824,1073741824,1073741824,1073741824,1073741824,1073741824,1073741824,1073741824,1073741824,1073741824,1073741824,1073741824,1073741824,1073741824,1073741824,1073741824,1073741824,1073741824,1073741824,1073741824,1073741824,1073741824,1073741824,1073741824,1073741824,1073741824,
37680,37792,38704,38816,39732,39844,40756,40868,41784,41896,42808,42920,1073741824,1073741824,1073741824,1073741824,1073741824,1073741824,1073741824,1073741824,1073741824,1073741824,1073741824,1073741824,1073741824,1073741824,1073741824,1073741824,1073741824,1073741824,1073741824,1073741824,
45200,45312,45440,46224,46336,46464,1073741824,1073741824,1073741824,1073741824,1073741824,1073741824,1073741824,1073741824,1073741824,1073741824,1073741824,1073741824,1073741824,1073741824,1073741824,1073741824,1073741824,1073741824,1073741824,1073741824,1073741824,1073741824,1073741824,1073741824,1073741824,1073741824,
45872,45984,46896,47008,47924,48036,48948,49060,49976,50088,51000,51112,1073741824,1073741824,1073741824,1073741824,1073741824,1073741824,1073741824,1073741824,1073741824,1073741824,1073741824,1073741824,1073741824,1073741824,1073741824,1073741824,1073741824,1073741824,1073741824,1073741824,
53392,53504,53632,54416,54528,54656,1073741824,1073741824,1073741824,1073741824,1073741824,1073741824,1073741824,1073741824,1073741824,1073741824,1073741824,1073741824,1073741824,1073741824,1073741824,1073741824,1073741824,1073741824,1073741824,1073741824,1073741824,1073741824,1073741824,1073741824,1073741824,1073741824,
54064,54176,55088,55200,56116,56228,57140,57252,58168,58280,59192,59304,1073741824,1073741824,1073741824,1073741824,1073741824,1073741824,1073741824,1073741824,1073741824,1073741824,1073741824,1073741824,1073741824,1073741824,1073741824,1073741824,1073741824,1073741824,1073741824,1073741824,
61584,61696,61824,62608,62720,62848,1073741824,1073741824,1073741824,1073741824,1073741824,1073741824,1073741824,1073741824,1073741824,1073741824,1073741824,1073741824,1073741824,1073741824,1073741824,1073741824,1073741824,1073741824,1073741824,1073741824,1073741824,1073741824,1073741824,1073741824,1073741824,1073741824,
62256,62368,63280,63392,64308,64420,65332,65444,1073741824,1073741824,1073741824,1073741824,1073741824,1073741824,1073741824,1073741824,1073741824,1073741824,1073741824,1073741824,1073741824,1073741824,1073741824,1073741824,1073741824,1073741824,1073741824,1073741824,1073741824,1073741824,1073741824,1073741824,
37264,37376,38288,38400,1073741824,1073741824,1073741824,1073741824,1073741824,1073741824,1073741824,1073741824,1073741824,1073741824,1073741824,1073741824,1073741824,1073741824,1073741824,1073741824,1073741824,1073741824,1073741824,1073741824,1073741824,1073741824,1073741824,1073741824,1073741824,1073741824,1073741824,1073741824,
36928,37056,37952,38080,38980,39108,40004,40132,41032,41160,42056,42184,1073741824,1073741824,1073741824,1073741824,1073741824,1073741824,1073741824,1073741824,1073741824,1073741824,1073741824,1073741824,1073741824,1073741824,1073741824,1073741824,1073741824,1073741824,1073741824,1073741824,
45456,45568,46480,46592,1073741824,1073741824,1073741824,1073741824,1073741824,1073741824,1073741824,1073741824,1073741824,1073741824,1073741824,1073741824,1073741824,1073741824,1073741824,1073741824,1073741824,1073741824,1073741824,1073741824,1073741824,1073741824,1073741824,1073741824,1073741824,1073741824,1073741824,1073741824,
45120,45248,46144,46272,47172,47300,48196,48324,49224,49352,50248,50376,1073741824,1073741824,1073741824,1073741824,1073741824,1073741824,1073741824,1073741824,1073741824,1073741824,1073741824,1073741824,1073741824,1073741824,1073741824,1073741824,1073741824,1073741824,1073741824,1073741824,
53648,53760,54672,54784,1073741824,1073741824,1073741824,1073741824,1073741824,1073741824,1073741824,1073741824,1073741824,1073741824,1073741824,1073741824,1073741824,1073741824,1073741824,1073741824,1073741824,1073741824,1073741824,1073741824,1073741824,1073741824,1073741824,1073741824,1073741824,1073741824,1073741824,1073741824,
53312,53440,54336,54464,55364,55492,56388,56516,57416,57544,58440,58568,1073741824,1073741824,1073741824,1073741824,1073741824,1073741824,1073741824,1073741824,1073741824,1073741824,1073741824,1073741824,1073741824,1073741824,1073741824,1073741824,1073741824,1073741824,1073741824,1073741824,
61840,61952,62864,62976,1073741824,1073741824,1073741824,1073741824,1073741824,1073741824,1073741824,1073741824,1073741824,1073741824,1073741824,1073741824,1073741824,1073741824,1073741824,1073741824,1073741824,1073741824,1073741824,1073741824,1073741824,1073741824,1073741824,1073741824,1073741824,1073741824,1073741824,1073741824,
61504,61632,62528,62656,63556,63684,64580,64708,1073741824,1073741824,1073741824,1073741824,1073741824,1073741824,1073741824,1073741824,1073741824,1073741824,1073741824,1073741824,1073741824,1073741824,1073741824,1073741824,1073741824,1073741824,1073741824,1073741824,1073741824,1073741824,1073741824,1073741824,
37392,37504,37632,38416,38528,38656,1073741824,1073741824,1073741824,1073741824,1073741824,1073741824,1073741824,1073741824,1073741824,1073741824,1073741824,1073741824,1073741824,1073741824,1073741824,1073741824,1073741824,1073741824,1073741824,1073741824,1073741824,1073741824,1073741824,1073741824,1073741824,1073741824,
37072,37184,37312,38096,38208,38336,39124,39236,39364,40148,40260,40388,41176,41288,41416,42200,42312,42440,1073741824,1073741824,1073741824,1073741824,1073741824,1073741824,1073741824,1073741824,1073741824,1073741824,1073741824,1073741824,1073741824,1073741824,
45584,45696,45824,46608,46720,46848,1073741824,1073741824,1073741824,1073741824,1073741824,1073741824,1073741824,1073741824,1073741824,1073741824,1073741824,1073741824,1073741824,1073741824,1073741824,1073741824,1073741824,1073741824,1073741824,1073741824,1073741824,1073741824,1073741824,1073741824,1073741824,1073741824,
45264,45376,45504,46288,46400,46528,47316,47428,47556,48340,48452,48580,49368,49480,49608,50392,50504,50632,1073741824,1073741824,1073741824,1073741824,1073741824,1073741824,1073741824,1073741824,1073741824,1073741824,1073741824,1073741824,1073741824,1073741824,
53776,53888,54016,54800,54912,55040,1073741824,1073741824,1073741824,1073741824,1073741824,1073741824,1073741824,1073741824,1073741824,1073741824,1073741824,1073741824,1073741824,1073741824,1073741824,1073741824,1073741824,1073741824,1073741824,1073741824,1073741824,1073741824,1073741824,1073741824,1073741824,1073741824,
53456,53568,53696,54480,54592,54720,55508,55620,55748,56532,56644,56772,57560,57672,57800,58584,58696,58824,1073741824,1073741824,1073741824,1073741824,1073741824,1073741824,1073741824,1073741824,1073741824,1073741824,1073741824,1073741824,1073741824,1073741824,
61968,62080,62208,62992,63104,63232,1073741824,1073741824,1073741824,1073741824,1073741824,1073741824,1073741824,1073741824,1073741824,1073741824,1073741824,1073741824,1073741824,1073741824,1073741824,1073741824,1073741824,1073741824,1073741824,1073741824,1073741824,1073741824,1073741824,1073741824,1073741824,1073741824,
61648,61760,61888,62672,62784,62912,63700,63812,63940,64724,64836,64964,1073741824,1073741824,1073741824,1073741824,1073741824,1073741824,1073741824,1073741824,1073741824,1073741824,1073741824,1073741824,1073741824,1073741824,1073741824,1073741824,1073741824,1073741824,1073741824,1073741824,
37648,37760,38672,38784,1073741824,1073741824,1073741824,1073741824,1073741824,1073741824,1073741824,1073741824,1073741824,1073741824,1073741824,1073741824,1073741824,1073741824,1073741824,1073741824,1073741824,1073741824,1073741824,1073741824,1073741824,1073741824,1073741824,1073741824,1073741824,1073741824,1073741824,1073741824,
37328,37440,38352,38464,39380,39492,40404,40516,41432,41544,42456,42568,1073741824,1073741824,1073741824,1073741824,1073741824,1073741824,1073741824,1073741824,1073741824,1073741824,1073741824,1073741824,1073741824,1073741824,1073741824,1073741824,1073741824,1073741824,1073741824,1073741824,
45840,45952,46864,46976,1073741824,1073741824,1073741824,1073741824,1073741824,1073741824,1073741824,1073741824,1073741824,1073741824,1073741824,1073741824,1073741824,1073741824,1073741824,1073741824,1073741824,1073741824,1073741824,1073741824,1073741824,1073741824,1073741824,1073741824,1073741824,1073741824,1073741824,1073741824,
45520,45632,46544,46656,47572,47684,48596,48708,49624,49736,50648,50760,1073741824,1073741824,1073741824,1073741824,1073741824,1073741824,1073741824,1073741824,1073741824,1073741824,1073741824,1073741824,1073741824,1073741824,1073741824,1073741824,1073741824,1073741824,1073741824,1073741824,
54032,54144,55056,55168,1073741824,1073741824,1073741824,1073741824,1073741824,1073741824,1073741824,1073741824,1073741824,1073741824,1073741824,1073741824,1073741824,1073741824,1073741824,1073741824,1073741824,1073741824,1073741824,1073741824,1073741824,1073741824,1073741824,1073741824,1073741824,1073741824,1073741824,1073741824,
53712,53824,54736,54848,55764,55876,56788,56900,57816,57928,58840,58952,1073741824,1073741824,1073741824,1073741824,1073741824,1073741824,1073741824,1073741824,1073741824,1073741824,1073741824,1073741824,1073741824,1073741824,1073741824,1073741824,1073741824,1073741824,1073741824,1073741824,
62224,62336,63248,63360,1073741824,1073741824,1073741824,1073741824,1073741824,1073741824,1073741824,1073741824,1073741824,1073741824,1073741824,1073741824,1073741824,1073741824,1073741824,1073741824,1073741824,1073741824,1073741824,1073741824,1073741824,1073741824,1073741824,1073741824,1073741824,1073741824,1073741824,1073741824,
61904,62016,62928,63040,63956,64068,64980,65092,1073741824,1073741824,1073741824,1073741824,1073741824,1073741824,1073741824,1073741824,1073741824,1073741824,1073741824,1073741824,1073741824,1073741824,1073741824,1073741824,1073741824,1073741824,1073741824,1073741824,1073741824,1073741824,1073741824,1073741824,
36896,37024,37920,38048,38948,39076,39972,40100,41000,41128,42024,42152,1073741824,1073741824,1073741824,1073741824,1073741824,1073741824,1073741824,1073741824,1073741824,1073741824,1073741824,1073741824,1073741824,1073741824,1073741824,1073741824,1073741824,1073741824,1073741824,1073741824,
37456,37568,37696,38480,38592,38720,39508,39620,39748,40532,40644,40772,41560,41672,41800,42584,42696,42824,1073741824,1073741824,1073741824,1073741824,1073741824,1073741824,1073741824,1073741824,1073741824,1073741824,1073741824,1073741824,1073741824,1073741824,
45088,45216,46112,46240,47140,47268,48164,48292,49192,49320,50216,50344,1073741824,1073741824,1073741824,1073741824,1073741824,1073741824,1073741824,1073741824,1073741824,1073741824,1073741824,1073741824,1073741824,1073741824,1073741824,1073741824,1073741824,1073741824,1073741824,1073741824,
45648,45760,45888,46672,46784,46912,47700,47812,47940,48724,48836,48964,49752,49864,49992,50776,50888,51016,1073741824,1073741824,1073741824,1073741824,1073741824,1073741824,1073741824,1073741824,1073741824,1073741824,1073741824,1073741824,1073741824,1073741824,
53280,53408,54304,54432,55332,55460,56356,56484,57384,57512,58408,58536,1073741824,1073741824,1073741824,1073741824,1073741824,1073741824,1073741824,1073741824,1073741824,1073741824,1073741824,1073741824,1073741824,1073741824,1073741824,1073741824,1073741824,1073741824,1073741824,1073741824,
53840,53952,54080,54864,54976,55104,55892,56004,56132,56916,57028,57156,57944,58056,58184,58968,59080,59208,1073741824,1073741824,1073741824,1073741824,1073741824,1073741824,1073741824,1073741824,1073741824,1073741824,1073741824,1073741824,1073741824,1073741824,
61472,61600,62496,62624,63524,63652,64548,64676,1073741824,1073741824,1073741824,1073741824,1073741824,1073741824,1073741824,1073741824,1073741824,1073741824,1073741824,1073741824,1073741824,1073741824,1073741824,1073741824,1073741824,1073741824,1073741824,1073741824,1073741824,1073741824,1073741824,1073741824,
62032,62144,62272,63056,63168,63296,64084,64196,64324,65108,65220,65348,1073741824,1073741824,1073741824,1073741824,1073741824,1073741824,1073741824,1073741824,1073741824,1073741824,1073741824,1073741824,1073741824,1073741824,1073741824,1073741824,1073741824,1073741824,1073741824,1073741824,
37040,37152,37280,38064,38176,38304,39092,39204,39332,40116,40228,40356,41144,41256,41384,42168,42280,42408,1073741824,1073741824,1073741824,1073741824,1073741824,1073741824,1073741824,1073741824,1073741824,1073741824,1073741824,1073741824,1073741824,1073741824,
37712,37824,38736,38848,39764,39876,40788,40900,41816,41928,42840,42952,1073741824,1073741824,1073741824,1073741824,1073741824,1073741824,1073741824,1073741824,1073741824,1073741824,1073741824,1073741824,1073741824,1073741824,1073741824,1073741824,1073741824,1073741824,1073741824,1073741824,
45232,45344,45472,46256,46368,46496,47284,47396,47524,48308,48420,48548,49336,49448,49576,50360,50472,50600,1073741824,1073741824,1073741824,1073741824,1073741824,1073741824,1073741824,1073741824,1073741824,1073741824,1073741824,1073741824,1073741824,1073741824,
45904,46016,46928,47040,47956,48068,48980,49092,50008,50120,51032,51144,1073741824,1073741824,1073741824,1073741824,1073741824,1073741824,1073741824,1073741824,1073741824,1073741824,1073741824,1073741824,1073741824,1073741824,1073741824,1073741824,1073741824,1073741824,1073741824,1073741824,
53424,53536,53664,54448,54560,54688,55476,55588,55716,56500,56612,56740,57528,57640,57768,58552,58664,58792,1073741824,1073741824,1073741824,1073741824,1073741824,1073741824,1073741824,1073741824,1073741824,1073741824,1073741824,1073741824,1073741824,1073741824,
54096,54208,55120,55232,56148,56260,57172,57284,58200,58312,59224,59336,1073741824,1073741824,1073741824,1073741824,1073741824,1073741824,1073741824,1073741824,1073741824,1073741824,1073741824,1073741824,1073741824,1073741824,1073741824,1073741824,1073741824,1073741824,1073741824,1073741824,
61616,61728,61856,62640,62752,62880,63668,63780,63908,64692,64804,64932,1073741824,1073741824,1073741824,1073741824,1073741824,1073741824,1073741824,1073741824,1073741824,1073741824,1073741824,1073741824,1073741824,1073741824,1073741824,1073741824,1073741824,1073741824,1073741824,1073741824,
62288,62400,63312,63424,64340,64452,65364,65476,1073741824,1073741824,1073741824,1073741824,1073741824,1073741824,1073741824,1073741824,1073741824,1073741824,1073741824,1073741824,1073741824,1073741824,1073741824,1073741824,1073741824,1073741824,1073741824,1073741824,1073741824,1073741824,1073741824,1073741824,
37296,37408,38320,38432,39348,39460,40372,40484,41400,41512,42424,42536,1073741824,1073741824,1073741824,1073741824,1073741824,1073741824,1073741824,1073741824,1073741824,1073741824,1073741824,1073741824,1073741824,1073741824,1073741824,1073741824,1073741824,1073741824,1073741824,1073741824,
1073741824,1073741824,1073741824,1073741824,1073741824,1073741824,1073741824,1073741824,1073741824,1073741824,1073741824,1073741824,1073741824,1073741824,1073741824,1073741824,1073741824,1073741824,1073741824,1073741824,1073741824,1073741824,1073741824,1073741824,1073741824,1073741824,1073741824,1073741824,1073741824,1073741824,1073741824,1073741824,
45488,45600,46512,46624,47540,47652,48564,48676,49592,49704,50616,50728,1073741824,1073741824,1073741824,1073741824,1073741824,1073741824,1073741824,1073741824,1073741824,1073741824,1073741824,1073741824,1073741824,1073741824,1073741824,1073741824,1073741824,1073741824,1073741824,1073741824,
1073741824,1073741824,1073741824,1073741824,1073741824,1073741824,1073741824,1073741824,1073741824,1073741824,1073741824,1073741824,1073741824,1073741824,1073741824,1073741824,1073741824,1073741824,1073741824,1073741824,1073741824,1073741824,1073741824,1073741824,1073741824,1073741824,1073741824,1073741824,1073741824,1073741824,1073741824,1073741824,
53680,53792,54704,54816,55732,55844,56756,56868,57784,57896,58808,58920,1073741824,1073741824,1073741824,1073741824,1073741824,1073741824,1073741824,1073741824,1073741824,1073741824,1073741824,1073741824,1073741824,1073741824,1073741824,1073741824,1073741824,1073741824,1073741824,1073741824,
1073741824,1073741824,1073741824,1073741824,1073741824,1073741824,1073741824,1073741824,1073741824,1073741824,1073741824,1073741824,1073741824,1073741824,1073741824,1073741824,1073741824,1073741824,1073741824,1073741824,1073741824,1073741824,1073741824,1073741824,1073741824,1073741824,1073741824,1073741824,1073741824,1073741824,1073741824,1073741824,
61872,61984,62896,63008,63924,64036,64948,65060,1073741824,1073741824,1073741824,1073741824,1073741824,1073741824,1073741824,1073741824,1073741824,1073741824,1073741824,1073741824,1073741824,1073741824,1073741824,1073741824,1073741824,1073741824,1073741824,1073741824,1073741824,1073741824,1073741824,1073741824,
1073741824,1073741824,1073741824,1073741824,1073741824,1073741824,1073741824,1073741824,1073741824,1073741824,1073741824,1073741824,1073741824,1073741824,1073741824,1073741824,1073741824,1073741824,1073741824,1073741824,1073741824,1073741824,1073741824,1073741824,1073741824,1073741824,1073741824,1073741824,1073741824,1073741824,1073741824,1073741824,
38912,39040,39936,40064,1073741824,1073741824,1073741824,1073741824,1073741824,1073741824,1073741824,1073741824,1073741824,1073741824,1073741824,1073741824,1073741824,1073741824,1073741824,1073741824,1073741824,1073741824,1073741824,1073741824,1073741824,1073741824,1073741824,1073741824,1073741824,1073741824,1073741824,1073741824,
39472,39584,39712,40496,40608,40736,41524,41636,41764,42548,42660,42788,43576,43688,43816,44600,44712,44840,1073741824,1073741824,1073741824,1073741824,1073741824,1073741824,1073741824,1073741824,1073741824,1073741824,1073741824,1073741824,1073741824,1073741824,
47104,47232,48128,48256,1073741824,1073741824,1073741824,1073741824,1073741824,1073741824,1073741824,1073741824,1073741824,1073741824,1073741824,1073741824,1073741824,1073741824,1073741824,1073741824,1073741824,1073741824,1073741824,1073741824,1073741824,1073741824,1073741824,1073741824,1073741824,1073741824,1073741824,1073741824,
47664,47776,47904,48688,48800,48928,49716,49828,49956,50740,50852,50980,51768,51880,52008,52792,52904,53032,1073741824,1073741824,1073741824,1073741824,1073741824,1073741824,1073741824,1073741824,1073741824,1073741824,1073741824,1073741824,1073741824,1073741824,
55296,55424,56320,56448,1073741824,1073741824,1073741824,1073741824,1073741824,1073741824,1073741824,1073741824,1073741824,1073741824,1073741824,1073741824,1073741824,1073741824,1073741824,1073741824,1073741824,1073741824,1073741824,1073741824,1073741824,1073741824,1073741824,1073741824,1073741824,1073741824,1073741824,1073741824,
55856,55968,56096,56880,56992,57120,57908,58020,58148,58932,59044,59172,59960,60072,60200,60984,61096,61224,1073741824,1073741824,1073741824,1073741824,1073741824,1073741824,1073741824,1073741824,1073741824,1073741824,1073741824,1073741824,1073741824,1073741824,
63488,63616,64512,64640,1073741824,1073741824,1073741824,1073741824,1073741824,1073741824,1073741824,1073741824,1073741824,1073741824,1073741824,1073741824,1073741824,1073741824,1073741824,1073741824,1073741824,1073741824,1073741824,1073741824,1073741824,1073741824,1073741824,1073741824,1073741824,1073741824,1073741824,1073741824,
64048,64160,64288,65072,65184,65312,1073741824,1073741824,1073741824,1073741824,1073741824,1073741824,1073741824,1073741824,1073741824,1073741824,1073741824,1073741824,1073741824,1073741824,1073741824,1073741824,1073741824,1073741824,1073741824,1073741824,1073741824,1073741824,1073741824,1073741824,1073741824,1073741824,
39056,39168,39296,40080,40192,40320,1073741824,1073741824,1073741824,1073741824,1073741824,1073741824,1073741824,1073741824,1073741824,1073741824,1073741824,1073741824,1073741824,1073741824,1073741824,1073741824,1073741824,1073741824,1073741824,1073741824,1073741824,1073741824,1073741824,1073741824,1073741824,1073741824,
39728,39840,40752,40864,41780,41892,42804,42916,43832,43944,44856,44968,1073741824,1073741824,1073741824,1073741824,1073741824,1073741824,1073741824,1073741824,1073741824,1073741824,1073741824,1073741824,1073741824,1073741824,1073741824,1073741824,1073741824,1073741824,1073741824,1073741824,
47248,47360,47488,48272,48384,48512,1073741824,1073741824,1073741824,1073741824,1073741824,1073741824,1073741824,1073741824,1073741824,1073741824,1073741824,1073741824,1073741824,1073741824,1073741824,1073741824,1073741824,1073741824,1073741824,1073741824,1073741824,1073741824,1073741824,1073741824,1073741824,1073741824,
47920,48032,48944,49056,49972,50084,50996,51108,52024,52136,53048,53160,1073741824,1073741824,1073741824,1073741824,1073741824,1073741824,1073741824,1073741824,1073741824,1073741824,1073741824,1073741824,1073741824,1073741824,1073741824,1073741824,1073741824,1073741824,1073741824,1073741824,
55440,55552,55680,56464,56576,56704,1073741824,1073741824,1073741824,1073741824,1073741824,1073741824,1073741824,1073741824,1073741824,1073741824,1073741824,1073741824,1073741824,1073741824,1073741824,1073741824,1073741824,1073741824,1073741824,1073741824,1073741824,1073741824,1073741824,1073741824,1073741824,1073741824,
56112,56224,57136,57248,58164,58276,59188,59300,60216,60328,61240,61352,1073741824,1073741824,1073741824,1073741824,1073741824,1073741824,1073741824,1073741824,1073741824,1073741824,1073741824,1073741824,1073741824,1073741824,1073741824,1073741824,1073741824,1073741824,1073741824,1073741824,
63632,63744,63872,64656,64768,64896,1073741824,1073741824,1073741824,1073741824,1073741824,1073741824,1073741824,1073741824,1073741824,1073741824,1073741824,1073741824,1073741824,1073741824,1073741824,1073741824,1073741824,1073741824,1073741824,1073741824,1073741824,1073741824,1073741824,1073741824,1073741824,1073741824,
64304,64416,65328,65440,1073741824,1073741824,1073741824,1073741824,1073741824,1073741824,1073741824,1073741824,1073741824,1073741824,1073741824,1073741824,1073741824,1073741824,1073741824,1073741824,1073741824,1073741824,1073741824,1073741824,1073741824,1073741824,1073741824,1073741824,1073741824,1073741824,1073741824,1073741824,
39312,39424,40336,40448,1073741824,1073741824,1073741824,1073741824,1073741824,1073741824,1073741824,1073741824,1073741824,1073741824,1073741824,1073741824,1073741824,1073741824,1073741824,1073741824,1073741824,1073741824,1073741824,1073741824,1073741824,1073741824,1073741824,1073741824,1073741824,1073741824,1073741824,1073741824,
38976,39104,40000,40128,41028,41156,42052,42180,43080,43208,44104,44232,1073741824,1073741824,1073741824,1073741824,1073741824,1073741824,1073741824,1073741824,1073741824,1073741824,1073741824,1073741824,1073741824,1073741824,1073741824,1073741824,1073741824,1073741824,1073741824,1073741824,
47504,47616,48528,48640,1073741824,1073741824,1073741824,1073741824,1073741824,1073741824,1073741824,1073741824,1073741824,1073741824,1073741824,1073741824,1073741824,1073741824,1073741824,1073741824,1073741824,1073741824,1073741824,1073741824,1073741824,1073741824,1073741824,1073741824,1073741824,1073741824,1073741824,1073741824,
47168,47296,48192,48320,49220,49348,50244,50372,51272,51400,52296,52424,1073741824,1073741824,1073741824,1073741824,1073741824,1073741824,1073741824,1073741824,1073741824,1073741824,1073741824,1073741824,1073741824,1073741824,1073741824,1073741824,1073741824,1073741824,1073741824,1073741824,
55696,55808,56720,56832,1073741824,1073741824,1073741824,1073741824,1073741824,1073741824,1073741824,1073741824,1073741824,1073741824,1073741824,1073741824,1073741824,1073741824,1073741824,1073741824,1073741824,1073741824,1073741824,1073741824,1073741824,1073741824,1073741824,1073741824,1073741824,1073741824,1073741824,1073741824,
55360,55488,56384,56512,57412,57540,58436,58564,59464,59592,60488,60616,1073741824,1073741824,1073741824,1073741824,1073741824,1073741824,1073741824,1073741824,1073741824,1073741824,1073741824,1073741824,1073741824,1073741824,1073741824,1073741824,1073741824,1073741824,1073741824,1073741824,
63888,64000,64912,65024,1073741824,1073741824,1073741824,1073741824,1073741824,1073741824,1073741824,1073741824,1073741824,1073741824,1073741824,1073741824,1073741824,1073741824,1073741824,1073741824,1073741824,1073741824,1073741824,1073741824,1073741824,1073741824,1073741824,1073741824,1073741824,1073741824,1073741824,1073741824,
63552,63680,64576,64704,1073741824,1073741824,1073741824,1073741824,1073741824,1073741824,1073741824,1073741824,1073741824,1073741824,1073741824,1073741824,1073741824,1073741824,1073741824,1073741824,1073741824,1073741824,1073741824,1073741824,1073741824,1073741824,1073741824,1073741824,1073741824,1073741824,1073741824,1073741824,
39440,39552,39680,40464,40576,40704,1073741824,1073741824,1073741824,1073741824,1073741824,1073741824,1073741824,1073741824,1073741824,1073741824,1073741824,1073741824,1073741824,1073741824,1073741824,1073741824,1073741824,1073741824,1073741824,1073741824,1073741824,1073741824,1073741824,1073741824,1073741824,1073741824,
39120,39232,39360,40144,40256,40384,41172,41284,41412,42196,42308,42436,43224,43336,43464,44248,44360,44488,1073741824,1073741824,1073741824,1073741824,1073741824,1073741824,1073741824,1073741824,1073741824,1073741824,1073741824,1073741824,1073741824,1073741824,
47632,47744,47872,48656,48768,48896,1073741824,1073741824,1073741824,1073741824,1073741824,1073741824,1073741824,1073741824,1073741824,1073741824,1073741824,1073741824,1073741824,1073741824,1073741824,1073741824,1073741824,1073741824,1073741824,1073741824,1073741824,1073741824,1073741824,1073741824,1073741824,1073741824,
47312,47424,47552,48336,48448,48576,49364,49476,49604,50388,50500,50628,51416,51528,51656,52440,52552,52680,1073741824,1073741824,1073741824,1073741824,1073741824,1073741824,1073741824,1073741824,1073741824,1073741824,1073741824,1073741824,1073741824,1073741824,
55824,55936,56064,56848,56960,57088,1073741824,1073741824,1073741824,1073741824,1073741824,1073741824,1073741824,1073741824,1073741824,1073741824,1073741824,1073741824,1073741824,1073741824,1073741824,1073741824,1073741824,1073741824,1073741824,1073741824,1073741824,1073741824,1073741824,1073741824,1073741824,1073741824,
55504,55616,55744,56528,56640,56768,57556,57668,57796,58580,58692,58820,59608,59720,59848,60632,60744,60872,1073741824,1073741824,1073741824,1073741824,1073741824,1073741824,1073741824,1073741824,1073741824,1073741824,1073741824,1073741824,1073741824,1073741824,
64016,64128,64256,65040,65152,65280,1073741824,1073741824,1073741824,1073741824,1073741824,1073741824,1073741824,1073741824,1073741824,1073741824,1073741824,1073741824,1073741824,1073741824,1073741824,1073741824,1073741824,1073741824,1073741824,1073741824,1073741824,1073741824,1073741824,1073741824,1073741824,1073741824,
63696,63808,63936,64720,64832,64960,1073741824,1073741824,1073741824,1073741824,1073741824,1073741824,1073741824,1073741824,1073741824,1073741824,1073741824,1073741824,1073741824,1073741824,1073741824,1073741824,1073741824,1073741824,1073741824,1073741824,1073741824,1073741824,1073741824,1073741824,1073741824,1073741824,
39696,39808,40720,40832,1073741824,1073741824,1073741824,1073741824,1073741824,1073741824,1073741824,1073741824,1073741824,1073741824,1073741824,1073741824,1073741824,1073741824,1073741824,1073741824,1073741824,1073741824,1073741824,1073741824,1073741824,1073741824,1073741824,1073741824,1073741824,1073741824,1073741824,1073741824,
39376,39488,40400,40512,41428,41540,42452,42564,43480,43592,44504,44616,1073741824,1073741824,1073741824,1073741824,1073741824,1073741824,1073741824,1073741824,1073741824,1073741824,1073741824,1073741824,1073741824,1073741824,1073741824,1073741824,1073741824,1073741824,1073741824,1073741824,
47888,48000,48912,49024,1073741824,1073741824,1073741824,1073741824,1073741824,1073741824,1073741824,1073741824,1073741824,1073741824,1073741824,1073741824,1073741824,1073741824,1073741824,1073741824,1073741824,1073741824,1073741824,1073741824,1073741824,1073741824,1073741824,1073741824,1073741824,1073741824,1073741824,1073741824,
47568,47680,48592,48704,49620,49732,50644,50756,51672,51784,52696,52808,1073741824,1073741824,1073741824,1073741824,1073741824,1073741824,1073741824,1073741824,1073741824,1073741824,1073741824,1073741824,1073741824,1073741824,1073741824,1073741824,1073741824,1073741824,1073741824,1073741824,
56080,56192,57104,57216,1073741824,1073741824,1073741824,1073741824,1073741824,1073741824,1073741824,1073741824,1073741824,1073741824,1073741824,1073741824,1073741824,1073741824,1073741824,1073741824,1073741824,1073741824,1073741824,1073741824,1073741824,1073741824,1073741824,1073741824,1073741824,1073741824,1073741824,1073741824,
55760,55872,56784,56896,57812,57924,58836,58948,59864,59976,60888,61000,1073741824,1073741824,1073741824,1073741824,1073741824,1073741824,1073741824,1073741824,1073741824,1073741824,1073741824,1073741824,1073741824,1073741824,1073741824,1073741824,1073741824,1073741824,1073741824,1073741824,
64272,64384,65296,65408,1073741824,1073741824,1073741824,1073741824,1073741824,1073741824,1073741824,1073741824,1073741824,1073741824,1073741824,1073741824,1073741824,1073741824,1073741824,1073741824,1073741824,1073741824,1073741824,1073741824,1073741824,1073741824,1073741824,1073741824,1073741824,1073741824,1073741824,1073741824,
63952,64064,64976,65088,1073741824,1073741824,1073741824,1073741824,1073741824,1073741824,1073741824,1073741824,1073741824,1073741824,1073741824,1073741824,1073741824,1073741824,1073741824,1073741824,1073741824,1073741824,1073741824,1073741824,1073741824,1073741824,1073741824,1073741824,1073741824,1073741824,1073741824,1073741824,
38944,39072,39968,40096,40996,41124,42020,42148,43048,43176,44072,44200,1073741824,1073741824,1073741824,1073741824,1073741824,1073741824,1073741824,1073741824,1073741824,1073741824,1073741824,1073741824,1073741824,1073741824,1073741824,1073741824,1073741824,1073741824,1073741824,1073741824,
39504,39616,39744,40528,40640,40768,41556,41668,41796,42580,42692,42820,43608,43720,43848,44632,44744,44872,1073741824,1073741824,1073741824,1073741824,1073741824,1073741824,1073741824,1073741824,1073741824,1073741824,1073741824,1073741824,1073741824,1073741824,
47136,47264,48160,48288,49188,49316,50212,50340,51240,51368,52264,52392,1073741824,1073741824,1073741824,1073741824,1073741824,1073741824,1073741824,1073741824,1073741824,1073741824,1073741824,1073741824,1073741824,1073741824,1073741824,1073741824,1073741824,1073741824,1073741824,1073741824,
47696,47808,47936,48720,48832,48960,49748,49860,49988,50772,50884,51012,51800,51912,52040,52824,52936,53064,1073741824,1073741824,1073741824,1073741824,1073741824,1073741824,1073741824,1073741824,1073741824,1073741824,1073741824,1073741824,1073741824,1073741824,
55328,55456,56352,56480,57380,57508,58404,58532,59432,59560,60456,60584,1073741824,1073741824,1073741824,1073741824,1073741824,1073741824,1073741824,1073741824,1073741824,1073741824,1073741824,1073741824,1073741824,1073741824,1073741824,1073741824,1073741824,1073741824,1073741824,1073741824,
55888,56000,56128,56912,57024,57152,57940,58052,58180,58964,59076,59204,59992,60104,60232,61016,61128,61256,1073741824,1073741824,1073741824,1073741824,1073741824,1073741824,1073741824,1073741824,1073741824,1073741824,1073741824,1073741824,1073741824,1073741824,
63520,63648,64544,64672,1073741824,1073741824,1073741824,1073741824,1073741824,1073741824,1073741824,1073741824,1073741824,1073741824,1073741824,1073741824,1073741824,1073741824,1073741824,1073741824,1073741824,1073741824,1073741824,1073741824,1073741824,1073741824,1073741824,1073741824,1073741824,1073741824,1073741824,1073741824,
64080,64192,64320,65104,65216,65344,1073741824,1073741824,1073741824,1073741824,1073741824,1073741824,1073741824,1073741824,1073741824,1073741824,1073741824,1073741824,1073741824,1073741824,1073741824,1073741824,1073741824,1073741824,1073741824,1073741824,1073741824,1073741824,1073741824,1073741824,1073741824,1073741824,
39088,39200,39328,40112,40224,40352,41140,41252,41380,42164,42276,42404,43192,43304,43432,44216,44328,44456,1073741824,1073741824,1073741824,1073741824,1073741824,1073741824,1073741824,1073741824,1073741824,1073741824,1073741824,1073741824,1073741824,1073741824,
39760,39872,40784,40896,41812,41924,42836,42948,43864,43976,44888,45000,1073741824,1073741824,1073741824,1073741824,1073741824,1073741824,1073741824,1073741824,1073741824,1073741824,1073741824,1073741824,1073741824,1073741824,1073741824,1073741824,1073741824,1073741824,1073741824,1073741824,
47280,47392,47520,48304,48416,48544,49332,49444,49572,50356,50468,50596,51384,51496,51624,52408,52520,52648,1073741824,1073741824,1073741824,1073741824,1073741824,1073741824,1073741824,1073741824,1073741824,1073741824,1073741824,1073741824,1073741824,1073741824,
47952,48064,48976,49088,50004,50116,51028,51140,52056,52168,53080,53192,1073741824,1073741824,1073741824,1073741824,1073741824,1073741824,1073741824,1073741824,1073741824,1073741824,1073741824,1073741824,1073741824,1073741824,1073741824,1073741824,1073741824,1073741824,1073741824,1073741824,
55472,55584,55712,56496,56608,56736,57524,57636,57764,58548,58660,58788,59576,59688,59816,60600,60712,60840,1073741824,1073741824,1073741824,1073741824,1073741824,1073741824,1073741824,1073741824,1073741824,1073741824,1073741824,1073741824,1073741824,1073741824,
56144,56256,57168,57280,58196,58308,59220,59332,60248,60360,61272,61384,1073741824,1073741824,1073741824,1073741824,1073741824,1073741824,1073741824,1073741824,1073741824,1073741824,1073741824,1073741824,1073741824,1073741824,1073741824,1073741824,1073741824,1073741824,1073741824,1073741824,
63664,63776,63904,64688,64800,64928,1073741824,1073741824,1073741824,1073741824,1073741824,1073741824,1073741824,1073741824,1073741824,1073741824,1073741824,1073741824,1073741824,1073741824,1073741824,1073741824,1073741824,1073741824,1073741824,1073741824,1073741824,1073741824,1073741824,1073741824,1073741824,1073741824,
64336,64448,65360,65472,1073741824,1073741824,1073741824,1073741824,1073741824,1073741824,1073741824,1073741824,1073741824,1073741824,1073741824,1073741824,1073741824,1073741824,1073741824,1073741824,1073741824,1073741824,1073741824,1073741824,1073741824,1073741824,1073741824,1073741824,1073741824,1073741824,1073741824,1073741824,
39344,39456,40368,40480,41396,41508,42420,42532,43448,43560,44472,44584,1073741824,1073741824,1073741824,1073741824,1073741824,1073741824,1073741824,1073741824,1073741824,1073741824,1073741824,1073741824,1073741824,1073741824,1073741824,1073741824,1073741824,1073741824,1073741824,1073741824,
1073741824,1073741824,1073741824,1073741824,1073741824,1073741824,1073741824,1073741824,1073741824,1073741824,1073741824,1073741824,1073741824,1073741824,1073741824,1073741824,1073741824,1073741824,1073741824,1073741824,1073741824,1073741824,1073741824,1073741824,1073741824,1073741824,1073741824,1073741824,1073741824,1073741824,1073741824,1073741824,
47536,47648,48560,48672,49588,49700,50612,50724,51640,51752,52664,52776,1073741824,1073741824,1073741824,1073741824,1073741824,1073741824,1073741824,1073741824,1073741824,1073741824,1073741824,1073741824,1073741824,1073741824,1073741824,1073741824,1073741824,1073741824,1073741824,1073741824,
1073741824,1073741824,1073741824,1073741824,1073741824,1073741824,1073741824,1073741824,1073741824,1073741824,1073741824,1073741824,1073741824,1073741824,1073741824,1073741824,1073741824,1073741824,1073741824,1073741824,1073741824,1073741824,1073741824,1073741824,1073741824,1073741824,1073741824,1073741824,1073741824,1073741824,1073741824,1073741824,
55728,55840,56752,56864,57780,57892,58804,58916,59832,59944,60856,60968,1073741824,1073741824,1073741824,1073741824,1073741824,1073741824,1073741824,1073741824,1073741824,1073741824,1073741824,1073741824,1073741824,1073741824,1073741824,1073741824,1073741824,1073741824,1073741824,1073741824,
1073741824,1073741824,1073741824,1073741824,1073741824,1073741824,1073741824,1073741824,1073741824,1073741824,1073741824,1073741824,1073741824,1073741824,1073741824,1073741824,1073741824,1073741824,1073741824,1073741824,1073741824,1073741824,1073741824,1073741824,1073741824,1073741824,1073741824,1073741824,1073741824,1073741824,1073741824,1073741824,
63920,64032,64944,65056,1073741824,1073741824,1073741824,1073741824,1073741824,1073741824,1073741824,1073741824,1073741824,1073741824,1073741824,1073741824,1073741824,1073741824,1073741824,1073741824,1073741824,1073741824,1073741824,1073741824,1073741824,1073741824,1073741824,1073741824,1073741824,1073741824,1073741824,1073741824,
1073741824,1073741824,1073741824,1073741824,1073741824,1073741824,1073741824,1073741824,1073741824,1073741824,1073741824,1073741824,1073741824,1073741824,1073741824,1073741824,1073741824,1073741824,1073741824,1073741824,1073741824,1073741824,1073741824,1073741824,1073741824,1073741824,1073741824,1073741824,1073741824,1073741824,1073741824,1073741824
};

__global__ void __launch_bounds__(256, 1)
fused_block(const char* x, const char* wqkv, char* ws, const char* wo, const char* w1, const char* w2, const char* b1, const char* b2, char* out) {
  asm volatile(
    ".Lfb_beg:\n"
    "v_lshrrev_b32 v244, 6, v0\n"
    "s_load_dwordx2 s[20:21], s[0:1], 0\n"
    "s_load_dwordx2 s[28:29], s[0:1], 8\n"
    "s_load_dwordx2 s[24:25], s[0:1], 16\n"
    "v_readfirstlane_b32 s16, v244\n"
    "s_lshl_b32 s3, s2, 9\n"
    "s_add_u32 s4, s4, s3\n"
    "s_addc_u32 s5, s5, 0\n"
    "s_mul_i32 s3, s16, 0x60\n"
    "s_load_dwordx16 s[64:79], s[4:5], 0\n"
    "s_load_dwordx16 s[80:95], s[4:5], 64\n"
    "s_add_u32 s4, s4, s3\n"
    "s_addc_u32 s5, s5, 0\n"
    "s_load_dwordx16 s[48:63], s[4:5], 128\n"
    "s_load_dwordx8 s[8:15], s[4:5], 192\n"
    "s_load_dwordx4 s[32:35], s[0:1], 24\n"
    "s_load_dwordx2 s[6:7], s[0:1], 40\n"
    "s_load_dwordx2 s[36:37], s[0:1], 48\n"
    "s_load_dwordx2 s[40:41], s[0:1], 56\n"
    "s_load_dwordx2 s[44:45], s[0:1], 64\n"
    "s_mov_b32 s17, s2\n"
    "v_mov_b32 v234, v0\n"
    "v_and_b32 v235, 63, v234\n"
    "v_and_b32 v236, 15, v234\n"
    "v_lshrrev_b32 v237, 4, v235\n"
    "v_and_b32 v242, 7, v234\n"
    "v_lshrrev_b32 v243, 3, v235\n"
    "v_xor_b32 v245, v242, v237\n"
    "v_lshlrev_b32 v245, 4, v245\n"
    "v_mul_u32_u24 v246, 0x500, v243\n"
    "v_add_u32 v32, v245, v246\n"
    "v_lshlrev_b32 v245, 1, v237\n"
    "v_xor_b32 v245, v245, v236\n"
    "v_lshlrev_b32 v245, 4, v245\n"
    "v_mul_u32_u24 v246, 0xf00, v237\n"
    "v_add_u32 v36, v245, v246\n"
    "v_and_b32 v238, 31, v234\n"
    "v_lshrrev_b32 v239, 5, v235\n"
    "v_bfe_u32 v240, v234, 2, 2\n"
    "v_and_b32 v241, 3, v234\n"
    "s_lshl_b32 s18, s16, 12\n"
    "s_mul_i32 s19, s16, 0x1100\n"
    "s_add_u32 s19, s19, 0x10000\n"
    "s_mov_b32 s22, 0x280000\n"
    "s_mov_b32 s23, 0x20000\n"
    "s_mov_b32 s26, 0x1750000\n"
    "s_mov_b32 s27, 0x20000\n"
    "s_mov_b32 s38, 0xc00\n"
    "s_mov_b32 s39, 0x20000\n"
    "s_mov_b32 s42, 0x500\n"
    "s_mov_b32 s43, 0x20000\n"
    "s_mov_b32 s46, 0x280000\n"
    "s_mov_b32 s47, 0x20000\n"
    "v_add_u32 v37, 0x37c0, v36\n"
    "v_add_u32 v38, 0x6f80, v36\n"
    "v_add_u32 v39, 0xa740, v36\n"
    "v_xor_b32 v245, 64, v32\n"
    "v_add_u32 v33, 0x2400, v245\n"
    "v_add_u32 v34, 0x4800, v32\n"
    "v_add_u32 v35, 0x6c00, v245\n"
    "s_mov_b32 s31, 0x20000\n"
    "s_waitcnt lgkmcnt(0)\n"
    "s_mov_b32 s30, s65\n"
    "v_add_u32 v36, s49, v36\n"
    "v_add_u32 v37, s49, v37\n"
    "v_add_u32 v38, s49, v38\n"
    "v_add_u32 v39, s49, v39\n"
    "v_add_u32 v32, s48, v32\n"
    "v_add_u32 v33, s48, v33\n"
    "v_add_u32 v34, s48, v34\n"
    "v_add_u32 v35, s48, v35\n"
    "s_mul_i32 s1, s64, 0x780\n"
    "s_add_u32 m0, s19, 0\n"
    "s_nop 0\n"
    "buffer_load_dwordx4 v36, s[28:31], s1 offen lds\n"
    "buffer_load_dwordx4 v37, s[28:31], s1 offen offset:1088 lds\n"
    "buffer_load_dwordx4 v38, s[28:31], s1 offen offset:2176 lds\n"
    "buffer_load_dwordx4 v39, s[28:31], s1 offen offset:3264 lds\n"
    "s_add_u32 m0, s18, 0\n"
    "s_nop 0\n"
    "buffer_load_dwordx4 v32, s[20:23], s64 offen lds\n"
    "buffer_load_dwordx4 v33, s[20:23], s64 offen offset:1024 lds\n"
    "buffer_load_dwordx4 v34, s[20:23], s64 offen offset:2048 lds\n"
    "buffer_load_dwordx4 v35, s[20:23], s64 offen offset:3072 lds\n"
    "s_add_u32 s64, s64, 0x80\n"
    "s_cmp_eq_u32 s64, 0x500\n"
    "s_cselect_b32 s64, 0, s64\n"
    "v_mov_b32 v254, 0\n"
    "buffer_load_dword v254, v254, s[24:27], 0 offen sc1\n"
    "s_mul_i32 s0, s16, 0x60\n"
    "s_sub_u32 s0, s76, s0\n"
    "v_lshl_add_u32 v252, v238, 2, s0\n"
    "global_load_dword v252, v252, s[4:5]\n"
    "s_cmp_eq_u32 s66, 1\n"
    "s_cselect_b64 s[32:33], s[34:35], s[32:33]\n"
    "s_cmp_eq_u32 s66, 2\n"
    "s_cselect_b64 s[32:33], s[6:7], s[32:33]\n"
    "s_mov_b32 s34, s67\n"
    "s_mov_b32 s35, 0x20000\n"
    "v_mov_b32 v233, s50\n"
    "buffer_load_dword v233, v233, s[32:35], 0 offen\n"
    "v_lshlrev_b32 v232, 7, v238\n"
    "buffer_load_dword v231, v232, s[36:39], 0 offen\n"
    "buffer_load_dword v232, v232, s[40:43], 0 offen\n"
    "s_getpc_b64 s[6:7]\n"
    ".Lfb_pc:\n"
    "s_sub_u32 s6, s6, .Lfb_pc-.Lfb_beg\n"
    "s_subb_u32 s7, s7, 0\n"
    "s_lshr_b32 s0, s17, 3\n"
    "s_lshl_b32 s0, s0, 11\n"
    "v_lshl_add_u32 v253, v236, 7, s0\n"
    "v_min_u32 v253, .Lfb_end-.Lfb_beg-4, v253\n"
    "global_load_dword v253, v253, s[6:7]\n"
    "v_lshl_add_u32 v55, v242, 4, s50\n"
    "v_mad_u32_u24 v55, v239, s51, v55\n"
    "v_and_b32 v245, 3, v243\n"
    "v_mad_u32_u24 v55, v245, s68, v55\n"
    "v_lshlrev_b32 v0, 4, v235\n"
    "v_lshlrev_b32 v25, 2, v235\n"
    "v_lshlrev_b32 v248, 4, v236\n"
    "v_and_b32 v249, 1, v237\n"
    "v_lshrrev_b32 v245, 1, v236\n"
    "v_xor_b32 v245, v245, v237\n"
    "s_mul_i32 s1, s64, 0x780\n"
    "s_add_u32 m0, s19, 0x4400\n"
    "s_nop 0\n"
    "buffer_load_dwordx4 v36, s[28:31], s1 offen lds\n"
    "v_lshlrev_b32 v245, 4, v245\n"
    "v_lshl_add_u32 v245, v236, 7, v245\n"
    "v_add_u32 v48, s55, v245\n"
    "v_xor_b32 v49, 64, v48\n"
    "v_mul_u32_u24 v245, 0x880, v237\n"
    "v_lshl_add_u32 v245, v240, 8, v245\n"
    "v_lshl_add_u32 v245, v241, 3, v245\n"
    "v_and_b32 v246, 1, v244\n"
    "v_lshl_add_u32 v245, v246, 7, v245\n"
    "v_add_u32 v245, 0x10000, v245\n"
    "buffer_load_dwordx4 v37, s[28:31], s1 offen offset:1088 lds\n"
    "v_and_b32 v247, 2, v244\n"
    "v_xor_b32 v247, v247, v240\n"
    "v_xor_b32 v50, 0, v247\n"
    "v_lshl_add_u32 v50, v50, 5, v245\n"
    "v_xor_b32 v51, 1, v247\n"
    "v_lshl_add_u32 v51, v51, 5, v245\n"
    "v_xor_b32 v52, 2, v247\n"
    "v_lshl_add_u32 v52, v52, 5, v245\n"
    "v_xor_b32 v53, 3, v247\n"
    "v_lshl_add_u32 v53, v53, 5, v245\n"
    "buffer_load_dwordx4 v38, s[28:31], s1 offen offset:2176 lds\n"
    "buffer_load_dwordx4 v39, s[28:31], s1 offen offset:3264 lds\n"
    "s_add_u32 m0, s18, 0x4000\n"
    "s_nop 0\n"
    "buffer_load_dwordx4 v32, s[20:23], s64 offen lds\n"
    "buffer_load_dwordx4 v33, s[20:23], s64 offen offset:1024 lds\n"
    "buffer_load_dwordx4 v34, s[20:23], s64 offen offset:2048 lds\n"
    "buffer_load_dwordx4 v35, s[20:23], s64 offen offset:3072 lds\n"
    "s_add_u32 s64, s64, 0x80\n"
    "s_cmp_eq_u32 s64, 0x500\n"
    "s_cselect_b32 s64, 0, s64\n"
    "s_mul_i32 s1, s64, 0x780\n"
    "s_add_u32 m0, s19, 0x8800\n"
    "s_nop 0\n"
    "buffer_load_dwordx4 v36, s[28:31], s1 offen lds\n"
    "buffer_load_dwordx4 v37, s[28:31], s1 offen offset:1088 lds\n"
    "buffer_load_dwordx4 v38, s[28:31], s1 offen offset:2176 lds\n"
    "buffer_load_dwordx4 v39, s[28:31], s1 offen offset:3264 lds\n"
    "s_add_u32 m0, s18, 0x8000\n"
    "s_nop 0\n"
    "buffer_load_dwordx4 v32, s[20:23], s64 offen lds\n"
    "buffer_load_dwordx4 v33, s[20:23], s64 offen offset:1024 lds\n"
    "buffer_load_dwordx4 v34, s[20:23], s64 offen offset:2048 lds\n"
    "buffer_load_dwordx4 v35, s[20:23], s64 offen offset:3072 lds\n"
    "s_add_u32 s64, s64, 0x80\n"
    "s_cmp_eq_u32 s64, 0x500\n"
    "s_cselect_b32 s64, 0, s64\n"
    "v_mov_b32 v144, 0x3f803f80\n"
    "v_mov_b32 v145, 0x3f803f80\n"
    "v_mov_b32 v146, 0x3f803f80\n"
    "v_mov_b32 v147, 0x3f803f80\n"
    "s_cmp_eq_u32 s74, 1\n"
    "s_cbranch_scc1 .Lfb_p1v_1\n"
    "s_cmp_eq_u32 s74, 2\n"
    "s_cbranch_scc1 .Lfb_p1s_2\n"
    "; ---- P1 QK (t~868)\n"
    "s_waitcnt vmcnt(22)\n"
    "s_barrier\n"
    "ds_read_b64_tr_b16 a[32:33], v50\n"
    "ds_read_b64_tr_b16 a[34:35], v50 offset:1088\n"
    "ds_read_b128 a[0:3], v48\n"
    "ds_read_b64_tr_b16 a[36:37], v51\n"
    "ds_read_b64_tr_b16 a[38:39], v51 offset:1088\n"
    "ds_read_b128 a[4:7], v48 offset:2048\n"
    "ds_read_b64_tr_b16 a[40:41], v52\n"
    "ds_read_b64_tr_b16 a[42:43], v52 offset:1088\n"
    "ds_read_b128 a[8:11], v48 offset:4096\n"
    "ds_read_b64_tr_b16 a[44:45], v53\n"
    "ds_read_b64_tr_b16 a[46:47], v53 offset:1088\n"
    "ds_read_b128 a[12:15], v48 offset:6144\n"
    "s_waitcnt lgkmcnt(9)\n"
    "v_mfma_f32_16x16x32_bf16 v[72:75], a[32:35], a[0:3], 0\n"
    "s_mul_i32 s1, s64, 0x780\n"
    "s_add_u32 m0, s19, 0xcc00\n"
    "s_nop 0\n"
    "buffer_load_dwordx4 v36, s[28:31], s1 offen lds\n"
    "ds_read_b64_tr_b16 a[48:49], v50 offset:8704\n"
    "s_lshl_b32 s4, s16, 10\n"
    "s_waitcnt lgkmcnt(8)\n"
    "v_mfma_f32_16x16x32_bf16 v[76:79], a[36:39], a[0:3], 0\n"
    "ds_read_b64_tr_b16 a[50:51], v50 offset:9792\n"
    "s_add_u32 s4, s4, 0x21000\n"
    "s_waitcnt lgkmcnt(8)\n"
    "v_mfma_f32_16x16x32_bf16 v[88:91], a[32:35], a[4:7], 0\n"
    "ds_read_b128 a[16:19], v49\n"
    "s_and_b32 s6, s16, 2\n"
    "v_mfma_f32_16x16x32_bf16 v[92:95], a[36:39], a[4:7], 0\n"
    "ds_read_b64_tr_b16 a[52:53], v51 offset:8704\n"
    "buffer_load_dwordx4 v37, s[28:31], s1 offen offset:1088 lds\n"
    "v_add_u32 v62, s52, v0\n"
    "v_mfma_f32_16x16x32_bf16 v[136:139], a[32:35], v[144:147], 0\n"
    "ds_read_b64_tr_b16 a[54:55], v51 offset:9792\n"
    "v_add_u32 v63, s56, v0\n"
    "v_mfma_f32_16x16x32_bf16 v[140:143], a[36:39], v[144:147], 0\n"
    "ds_read_b128 a[20:23], v49 offset:2048\n"
    "v_lshlrev_b32 v250, 10, v237\n"
    "s_waitcnt lgkmcnt(10)\n"
    "v_mfma_f32_16x16x32_bf16 v[80:83], a[40:43], a[0:3], 0\n"
    "ds_read_b64_tr_b16 a[56:57], v52 offset:8704\n"
    "v_lshl_add_u32 v250, v240, 7, v250\n"
    "buffer_load_dwordx4 v38, s[28:31], s1 offen offset:2176 lds\n"
    "s_waitcnt lgkmcnt(10)\n"
    "v_mfma_f32_16x16x32_bf16 v[104:107], a[32:35], a[8:11], 0\n"
    "ds_read_b64_tr_b16 a[58:59], v52 offset:9792\n"
    "v_add_u32 v250, 0x21000, v250\n"
    "v_mfma_f32_16x16x32_bf16 v[96:99], a[40:43], a[4:7], 0\n"
    "ds_read_b128 a[24:27], v49 offset:4096\n"
    "v_lshlrev_b32 v245, 4, v241\n"
    "v_mfma_f32_16x16x32_bf16 v[108:111], a[36:39], a[8:11], 0\n"
    "ds_read_b64_tr_b16 a[60:61], v53 offset:8704\n"
    "v_add_u32 v245, s54, v245\n"
    "v_mfma_f32_16x16x32_bf16 v[112:115], a[40:43], a[8:11], 0\n"
    "buffer_load_dwordx4 v39, s[28:31], s1 offen offset:3264 lds\n"
    "ds_read_b64_tr_b16 a[62:63], v53 offset:9792\n"
    "s_waitcnt lgkmcnt(12)\n"
    "v_mfma_f32_16x16x32_bf16 v[84:87], a[44:47], a[0:3], 0\n"
    "v_lshrrev_b32 v246, 7, v245\n"
    "ds_read_b128 a[28:31], v49 offset:6144\n"
    "s_waitcnt lgkmcnt(12)\n"
    "v_mfma_f32_16x16x32_bf16 v[120:123], a[32:35], a[12:15], 0\n"
    "v_and_b32 v245, 0x7f, v245\n"
    "v_mfma_f32_16x16x32_bf16 v[100:103], a[44:47], a[4:7], 0\n"
    "v_mfma_f32_16x16x32_bf16 v[124:127], a[36:39], a[12:15], 0\n"
    "s_add_u32 m0, s18, 0xc000\n"
    "s_nop 0\n"
    "buffer_load_dwordx4 v32, s[20:23], s64 offen lds\n"
    "v_mfma_f32_16x16x32_bf16 v[116:119], a[44:47], a[8:11], 0\n"
    "v_mfma_f32_16x16x32_bf16 v[128:131], a[40:43], a[12:15], 0\n"
    "v_mfma_f32_16x16x32_bf16 v[132:135], a[44:47], a[12:15], 0\n"
    "s_waitcnt vmcnt(13)\n"
    "s_waitcnt lgkmcnt(0)\n"
    "s_barrier\n"
    "v_readfirstlane_b32 s96, v254\n"
    "buffer_load_dwordx4 v33, s[20:23], s64 offen offset:1024 lds\n"
    "s_add_u32 s96, s96, 1\n"
    "v_mfma_f32_16x16x32_bf16 v[72:75], a[48:51], a[16:19], v[72:75]\n"
    "ds_read_b64_tr_b16 a[32:33], v50 offset:17408\n"
    "v_lshl_add_u32 v246, v246, 12, v250\n"
    "v_mfma_f32_16x16x32_bf16 v[76:79], a[52:55], a[16:19], v[76:79]\n"
    "ds_read_b64_tr_b16 a[34:35], v50 offset:18496\n"
    "v_add_u32 v56, v245, v246\n"
    "v_mfma_f32_16x16x32_bf16 v[88:91], a[48:51], a[20:23], v[88:91]\n"
    "ds_read_b128 a[0:3], v48 offset:16384\n"
    "v_lshlrev_b32 v245, 4, v241\n"
    "buffer_load_dwordx4 v34, s[20:23], s64 offen offset:2048 lds\n"
    "v_mfma_f32_16x16x32_bf16 v[92:95], a[52:55], a[20:23], v[92:95]\n"
    "ds_read_b64_tr_b16 a[36:37], v51 offset:17408\n"
    "v_add_u32 v245, s54, v245\n"
    "v_mfma_f32_16x16x32_bf16 v[136:139], a[48:51], v[144:147], v[136:139]\n"
    "ds_read_b64_tr_b16 a[38:39], v51 offset:18496\n"
    "v_add_u32 v245, 8, v245\n"
    "v_mfma_f32_16x16x32_bf16 v[140:143], a[52:55], v[144:147], v[140:143]\n"
    "ds_read_b128 a[4:7], v48 offset:18432\n"
    "v_lshrrev_b32 v246, 7, v245\n"
    "v_mfma_f32_16x16x32_bf16 v[80:83], a[56:59], a[16:19], v[80:83]\n"
    "buffer_load_dwordx4 v35, s[20:23], s64 offen offset:3072 lds\n"
    "s_add_u32 s64, s64, 0x80\n"
    "s_cmp_eq_u32 s64, 0x500\n"
    "s_cselect_b32 s64, 0, s64\n"
    "ds_read_b64_tr_b16 a[40:41], v52 offset:17408\n"
    "v_and_b32 v245, 0x7f, v245\n"
    "v_mfma_f32_16x16x32_bf16 v[104:107], a[48:51], a[24:27], v[104:107]\n"
    "ds_read_b64_tr_b16 a[42:43], v52 offset:18496\n"
    "v_lshl_add_u32 v246, v246, 12, v250\n"
    "v_mfma_f32_16x16x32_bf16 v[96:99], a[56:59], a[20:23], v[96:99]\n"
    "ds_read_b128 a[8:11], v48 offset:20480\n"
    "v_add_u32 v57, v245, v246\n"
    "v_mfma_f32_16x16x32_bf16 v[108:111], a[52:55], a[24:27], v[108:111]\n"
    "s_mul_i32 s1, s64, 0x780\n"
    "s_add_u32 m0, s19, 0\n"
    "s_nop 0\n"
    "buffer_load_dwordx4 v36, s[28:31], s1 offen lds\n"
    "ds_read_b64_tr_b16 a[44:45], v53 offset:17408\n"
    "v_lshlrev_b32 v245, s69, v241\n"
    "v_mfma_f32_16x16x32_bf16 v[112:115], a[56:59], a[24:27], v[112:115]\n"
    "ds_read_b64_tr_b16 a[46:47], v53 offset:18496\n"
    "v_mfma_f32_16x16x32_bf16 v[84:87], a[60:63], a[16:19], v[84:87]\n"
    "v_add_u32 v245, s54, v245\n"
    "ds_read_b128 a[12:15], v48 offset:22528\n"
    "v_mfma_f32_16x16x32_bf16 v[120:123], a[48:51], a[28:31], v[120:123]\n"
    "v_add_u32 v245, 64, v245\n"
    "buffer_load_dwordx4 v37, s[28:31], s1 offen offset:1088 lds\n"
    "v_mfma_f32_16x16x32_bf16 v[100:103], a[60:63], a[20:23], v[100:103]\n"
    "v_mfma_f32_16x16x32_bf16 v[124:127], a[52:55], a[28:31], v[124:127]\n"
    "v_mfma_f32_16x16x32_bf16 v[116:119], a[60:63], a[24:27], v[116:119]\n"
    "v_mfma_f32_16x16x32_bf16 v[128:131], a[56:59], a[28:31], v[128:131]\n"
    "buffer_load_dwordx4 v38, s[28:31], s1 offen offset:2176 lds\n"
    "v_mfma_f32_16x16x32_bf16 v[132:135], a[60:63], a[28:31], v[132:135]\n"
    "s_waitcnt lgkmcnt(9)\n"
    "v_mfma_f32_16x16x32_bf16 v[72:75], a[32:35], a[0:3], v[72:75]\n"
    "ds_read_b64_tr_b16 a[48:49], v50 offset:26112\n"
    "v_lshrrev_b32 v246, 7, v245\n"
    "s_waitcnt lgkmcnt(8)\n"
    "v_mfma_f32_16x16x32_bf16 v[76:79], a[36:39], a[0:3], v[76:79]\n"
    "ds_read_b64_tr_b16 a[50:51], v50 offset:27200\n"
    "v_and_b32 v245, 0x7f, v245\n"
    "s_waitcnt lgkmcnt(8)\n"
    "v_mfma_f32_16x16x32_bf16 v[88:91], a[32:35], a[4:7], v[88:91]\n"
    "buffer_load_dwordx4 v39, s[28:31], s1 offen offset:3264 lds\n"
    "ds_read_b128 a[16:19], v49 offset:16384\n"
    "v_lshl_add_u32 v246, v246, 12, v250\n"
    "v_mfma_f32_16x16x32_bf16 v[92:95], a[36:39], a[4:7], v[92:95]\n"
    "ds_read_b64_tr_b16 a[52:53], v51 offset:26112\n"
    "v_add_u32 v58, v245, v246\n"
    "v_mfma_f32_16x16x32_bf16 v[136:139], a[32:35], v[144:147], v[136:139]\n"
    "ds_read_b64_tr_b16 a[54:55], v51 offset:27200\n"
    "v_lshlrev_b32 v245, 4, v241\n"
    "v_mfma_f32_16x16x32_bf16 v[140:143], a[36:39], v[144:147], v[140:143]\n"
    "ds_read_b128 a[20:23], v49 offset:18432\n"
    "s_add_u32 m0, s18, 0\n"
    "s_nop 0\n"
    "buffer_load_dwordx4 v32, s[20:23], s64 offen lds\n"
    "v_add_u32 v245, s54, v245\n"
    "s_waitcnt lgkmcnt(10)\n"
    "v_mfma_f32_16x16x32_bf16 v[80:83], a[40:43], a[0:3], v[80:83]\n"
    "ds_read_b64_tr_b16 a[56:57], v52 offset:26112\n"
    "v_add_u32 v245, s92, v245\n"
    "s_waitcnt lgkmcnt(10)\n"
    "v_mfma_f32_16x16x32_bf16 v[104:107], a[32:35], a[8:11], v[104:107]\n"
    "ds_read_b64_tr_b16 a[58:59], v52 offset:27200\n"
    "v_lshrrev_b32 v246, 7, v245\n"
    "v_mfma_f32_16x16x32_bf16 v[96:99], a[40:43], a[4:7], v[96:99]\n"
    "ds_read_b128 a[24:27], v49 offset:20480\n"
    "v_and_b32 v245, 0x7f, v245\n"
    "buffer_load_dwordx4 v33, s[20:23], s64 offen offset:1024 lds\n"
    "v_mfma_f32_16x16x32_bf16 v[108:111], a[36:39], a[8:11], v[108:111]\n"
    "ds_read_b64_tr_b16 a[60:61], v53 offset:26112\n"
    "v_lshl_add_u32 v246, v246, 12, v250\n"
    "v_mfma_f32_16x16x32_bf16 v[112:115], a[40:43], a[8:11], v[112:115]\n"
    "ds_read_b64_tr_b16 a[62:63], v53 offset:27200\n"
    "s_waitcnt lgkmcnt(12)\n"
    "v_mfma_f32_16x16x32_bf16 v[84:87], a[44:47], a[0:3], v[84:87]\n"
    "v_add_u32 v59, v245, v246\n"
    "ds_read_b128 a[28:31], v49 offset:22528\n"
    "s_waitcnt lgkmcnt(12)\n"
    "v_mfma_f32_16x16x32_bf16 v[120:123], a[32:35], a[12:15], v[120:123]\n"
    "buffer_load_dwordx4 v34, s[20:23], s64 offen offset:2048 lds\n"
    "v_lshlrev_b32 v245, s70, v241\n"
    "v_mfma_f32_16x16x32_bf16 v[100:103], a[44:47], a[4:7], v[100:103]\n"
    "v_mfma_f32_16x16x32_bf16 v[124:127], a[36:39], a[12:15], v[124:127]\n"
    "v_mfma_f32_16x16x32_bf16 v[116:119], a[44:47], a[8:11], v[116:119]\n"
    "v_mfma_f32_16x16x32_bf16 v[128:131], a[40:43], a[12:15], v[128:131]\n"
    "buffer_load_dwordx4 v35, s[20:23], s64 offen offset:3072 lds\n"
    "s_add_u32 s64, s64, 0x80\n"
    "s_cmp_eq_u32 s64, 0x500\n"
    "s_cselect_b32 s64, 0, s64\n"
    "v_mfma_f32_16x16x32_bf16 v[132:135], a[44:47], a[12:15], v[132:135]\n"
    "s_waitcnt vmcnt(16)\n"
    "s_waitcnt lgkmcnt(0)\n"
    "s_barrier\n"
    "v_mfma_f32_16x16x32_bf16 v[72:75], a[48:51], a[16:19], v[72:75]\n"
    "ds_read_b64_tr_b16 a[32:33], v50 offset:34816\n"
    "v_add_u32 v245, s54, v245\n"
    "v_mfma_f32_16x16x32_bf16 v[76:79], a[52:55], a[16:19], v[76:79]\n"
    "ds_read_b64_tr_b16 a[34:35], v50 offset:35904\n"
    "s_mul_i32 s1, s64, 0x780\n"
    "s_add_u32 m0, s19, 0x4400\n"
    "s_nop 0\n"
    "buffer_load_dwordx4 v36, s[28:31], s1 offen lds\n"
    "v_add_u32 v245, s93, v245\n"
    "v_mfma_f32_16x16x32_bf16 v[88:91], a[48:51], a[20:23], v[88:91]\n"
    "ds_read_b128 a[0:3], v48 offset:32768\n"
    "v_lshrrev_b32 v246, 7, v245\n"
    "v_mfma_f32_16x16x32_bf16 v[92:95], a[52:55], a[20:23], v[92:95]\n"
    "ds_read_b64_tr_b16 a[36:37], v51 offset:34816\n"
    "v_and_b32 v245, 0x7f, v245\n"
    "v_mfma_f32_16x16x32_bf16 v[136:139], a[48:51], v[144:147], v[136:139]\n"
    "ds_read_b64_tr_b16 a[38:39], v51 offset:35904\n"
    "v_lshl_add_u32 v246, v246, 12, v250\n"
    "buffer_load_dwordx4 v37, s[28:31], s1 offen offset:1088 lds\n"
    "v_mfma_f32_16x16x32_bf16 v[140:143], a[52:55], v[144:147], v[140:143]\n"
    "ds_read_b128 a[4:7], v48 offset:34816\n"
    "v_add_u32 v60, v245, v246\n"
    "v_mfma_f32_16x16x32_bf16 v[80:83], a[56:59], a[16:19], v[80:83]\n"
    "ds_read_b64_tr_b16 a[40:41], v52 offset:34816\n"
    "v_lshlrev_b32 v245, 3, v241\n"
    "v_mfma_f32_16x16x32_bf16 v[104:107], a[48:51], a[24:27], v[104:107]\n"
    "ds_read_b64_tr_b16 a[42:43], v52 offset:35904\n"
    "v_add_u32 v245, s54, v245\n"
    "v_mfma_f32_16x16x32_bf16 v[96:99], a[56:59], a[20:23], v[96:99]\n"
    "buffer_load_dwordx4 v38, s[28:31], s1 offen offset:2176 lds\n"
    "ds_read_b128 a[8:11], v48 offset:36864\n"
    "v_add_u32 v245, s94, v245\n"
    "v_mfma_f32_16x16x32_bf16 v[108:111], a[52:55], a[24:27], v[108:111]\n"
    "ds_read_b64_tr_b16 a[44:45], v53 offset:34816\n"
    "v_lshrrev_b32 v246, 7, v245\n"
    "v_mfma_f32_16x16x32_bf16 v[112:115], a[56:59], a[24:27], v[112:115]\n"
    "ds_read_b64_tr_b16 a[46:47], v53 offset:35904\n"
    "v_mfma_f32_16x16x32_bf16 v[84:87], a[60:63], a[16:19], v[84:87]\n"
    "v_and_b32 v245, 0x7f, v245\n"
    "buffer_load_dwordx4 v39, s[28:31], s1 offen offset:3264 lds\n"
    "ds_read_b128 a[12:15], v48 offset:38912\n"
    "v_mfma_f32_16x16x32_bf16 v[120:123], a[48:51], a[28:31], v[120:123]\n"
    "v_lshl_add_u32 v246, v246, 12, v250\n"
    "v_mfma_f32_16x16x32_bf16 v[100:103], a[60:63], a[20:23], v[100:103]\n"
    "v_mfma_f32_16x16x32_bf16 v[124:127], a[52:55], a[28:31], v[124:127]\n"
    "v_mfma_f32_16x16x32_bf16 v[116:119], a[60:63], a[24:27], v[116:119]\n"
    "s_add_u32 m0, s18, 0x4000\n"
    "s_nop 0\n"
    "buffer_load_dwordx4 v32, s[20:23], s64 offen lds\n"
    "v_mfma_f32_16x16x32_bf16 v[128:131], a[56:59], a[28:31], v[128:131]\n"
    "v_mfma_f32_16x16x32_bf16 v[132:135], a[60:63], a[28:31], v[132:135]\n"
    "s_waitcnt lgkmcnt(9)\n"
    "v_mfma_f32_16x16x32_bf16 v[72:75], a[32:35], a[0:3], v[72:75]\n"
    "ds_read_b64_tr_b16 a[48:49], v50 offset:43520\n"
    "v_add_u32 v61, v245, v246\n"
    "s_waitcnt lgkmcnt(8)\n"
    "v_mfma_f32_16x16x32_bf16 v[76:79], a[36:39], a[0:3], v[76:79]\n"
    "buffer_load_dwordx4 v33, s[20:23], s64 offen offset:1024 lds\n"
    "ds_read_b64_tr_b16 a[50:51], v50 offset:44608\n"
    "v_and_b32 v246, 1, v244\n"
    "s_waitcnt lgkmcnt(8)\n"
    "v_mfma_f32_16x16x32_bf16 v[88:91], a[32:35], a[4:7], v[88:91]\n"
    "ds_read_b128 a[16:19], v49 offset:32768\n"
    "v_and_b32 v247, 2, v244\n"
    "v_mfma_f32_16x16x32_bf16 v[92:95], a[36:39], a[4:7], v[92:95]\n"
    "ds_read_b64_tr_b16 a[52:53], v51 offset:43520\n"
    "v_lshlrev_b32 v245, 8, v246\n"
    "v_mfma_f32_16x16x32_bf16 v[136:139], a[32:35], v[144:147], v[136:139]\n"
    "ds_read_b64_tr_b16 a[54:55], v51 offset:44608\n"
    "buffer_load_dwordx4 v34, s[20:23], s64 offen offset:2048 lds\n"
    "v_lshl_add_u32 v245, v247, 6, v245\n"
    "v_mfma_f32_16x16x32_bf16 v[140:143], a[36:39], v[144:147], v[140:143]\n"
    "ds_read_b128 a[20:23], v49 offset:34816\n"
    "v_add_u32 v245, 0x27000, v245\n"
    "s_waitcnt lgkmcnt(10)\n"
    "v_mfma_f32_16x16x32_bf16 v[80:83], a[40:43], a[0:3], v[80:83]\n"
    "ds_read_b64_tr_b16 a[56:57], v52 offset:43520\n"
    "v_lshl_add_u32 v66, v237, 4, v245\n"
    "s_waitcnt lgkmcnt(10)\n"
    "v_mfma_f32_16x16x32_bf16 v[104:107], a[32:35], a[8:11], v[104:107]\n"
    "ds_read_b64_tr_b16 a[58:59], v52 offset:44608\n"
    "v_xor_b32 v67, 0x80, v66\n"
    "buffer_load_dwordx4 v35, s[20:23], s64 offen offset:3072 lds\n"
    "s_add_u32 s64, s64, 0x80\n"
    "s_cmp_eq_u32 s64, 0x500\n"
    "s_cselect_b32 s64, 0, s64\n"
    "v_mfma_f32_16x16x32_bf16 v[96:99], a[40:43], a[4:7], v[96:99]\n"
    "ds_read_b128 a[24:27], v49 offset:36864\n"
    "v_lshlrev_b32 v245, 7, v247\n"
    "v_mfma_f32_16x16x32_bf16 v[108:111], a[36:39], a[8:11], v[108:111]\n"
    "ds_read_b64_tr_b16 a[60:61], v53 offset:43520\n"
    "v_add_u32 v245, 0x27200, v245\n"
    "v_mfma_f32_16x16x32_bf16 v[112:115], a[40:43], a[8:11], v[112:115]\n"
    "ds_read_b64_tr_b16 a[62:63], v53 offset:44608\n"
    "s_waitcnt lgkmcnt(12)\n"
    "v_mfma_f32_16x16x32_bf16 v[84:87], a[44:47], a[0:3], v[84:87]\n"
    "v_add_u32 v68, v245, v25\n"
    "ds_read_b128 a[28:31], v49 offset:38912\n"
    "s_waitcnt lgkmcnt(12)\n"
    "v_mfma_f32_16x16x32_bf16 v[120:123], a[32:35], a[12:15], v[120:123]\n"
    "v_lshl_add_u32 v69, v236, 2, v245\n"
    "v_mfma_f32_16x16x32_bf16 v[100:103], a[44:47], a[4:7], v[100:103]\n"
    "v_mfma_f32_16x16x32_bf16 v[124:127], a[36:39], a[12:15], v[124:127]\n"
    "v_mfma_f32_16x16x32_bf16 v[116:119], a[44:47], a[8:11], v[116:119]\n"
    "v_mfma_f32_16x16x32_bf16 v[128:131], a[40:43], a[12:15], v[128:131]\n"
    "v_mfma_f32_16x16x32_bf16 v[132:135], a[44:47], a[12:15], v[132:135]\n"
    "s_waitcnt vmcnt(16)\n"
    "s_waitcnt lgkmcnt(0)\n"
    "s_barrier\n"
    "v_mfma_f32_16x16x32_bf16 v[72:75], a[48:51], a[16:19], v[72:75]\n"
    "s_mul_i32 s1, s64, 0x780\n"
    "s_add_u32 m0, s19, 0x8800\n"
    "s_nop 0\n"
    "buffer_load_dwordx4 v36, s[28:31], s1 offen lds\n"
    "ds_read_b64_tr_b16 a[32:33], v50 offset:52224\n"
    "v_mfma_f32_16x16x32_bf16 v[76:79], a[52:55], a[16:19], v[76:79]\n"
    "v_and_b32 v245, 39, v235\n"
    "ds_read_b64_tr_b16 a[34:35], v50 offset:53312\n"
    "v_mfma_f32_16x16x32_bf16 v[88:91], a[48:51], a[20:23], v[88:91]\n"
    "v_lshl_add_u32 v245, v249, 3, v245\n"
    "v_mfma_f32_16x16x32_bf16 v[92:95], a[52:55], a[20:23], v[92:95]\n"
    "ds_read_b128 a[0:3], v48 offset:49152\n"
    "v_and_b32 v246, 8, v235\n"
    "buffer_load_dwordx4 v37, s[28:31], s1 offen offset:1088 lds\n"
    "v_mfma_f32_16x16x32_bf16 v[136:139], a[48:51], v[144:147], v[136:139]\n"
    "ds_read_b64_tr_b16 a[36:37], v51 offset:52224\n"
    "v_mfma_f32_16x16x32_bf16 v[140:143], a[52:55], v[144:147], v[140:143]\n"
    "v_lshl_add_u32 v245, v246, 1, v245\n"
    "ds_read_b64_tr_b16 a[38:39], v51 offset:53312\n"
    "v_mfma_f32_16x16x32_bf16 v[80:83], a[56:59], a[16:19], v[80:83]\n"
    "v_lshlrev_b32 v245, 4, v245\n"
    "v_mfma_f32_16x16x32_bf16 v[104:107], a[48:51], a[24:27], v[104:107]\n"
    "buffer_load_dwordx4 v38, s[28:31], s1 offen offset:2176 lds\n"
    "ds_read_b128 a[4:7], v48 offset:51200\n"
    "v_mfma_f32_16x16x32_bf16 v[96:99], a[56:59], a[20:23], v[96:99]\n"
    "s_cmp_eq_u32 s74, 3\n"
    "s_cselect_b64 vcc, -1, 0\n"
    "v_cndmask_b32 v54, v0, v245, vcc\n"
    "v_mfma_f32_16x16x32_bf16 v[108:111], a[52:55], a[24:27], v[108:111]\n"
    "ds_read_b64_tr_b16 a[40:41], v52 offset:52224\n"
    "ds_read_b64_tr_b16 a[42:43], v52 offset:53312\n"
    "v_mfma_f32_16x16x32_bf16 v[112:115], a[56:59], a[24:27], v[112:115]\n"
    "ds_read_b128 a[8:11], v48 offset:53248\n"
    "v_mfma_f32_16x16x32_bf16 v[84:87], a[60:63], a[16:19], v[84:87]\n"
    "buffer_load_dwordx4 v39, s[28:31], s1 offen offset:3264 lds\n"
    "ds_read_b64_tr_b16 a[44:45], v53 offset:52224\n"
    "ds_read_b64_tr_b16 a[46:47], v53 offset:53312\n"
    "v_mfma_f32_16x16x32_bf16 v[120:123], a[48:51], a[28:31], v[120:123]\n"
    "ds_read_b128 a[12:15], v48 offset:55296\n"
    "v_mfma_f32_16x16x32_bf16 v[100:103], a[60:63], a[20:23], v[100:103]\n"
    "v_mfma_f32_16x16x32_bf16 v[124:127], a[52:55], a[28:31], v[124:127]\n"
    "v_mfma_f32_16x16x32_bf16 v[116:119], a[60:63], a[24:27], v[116:119]\n"
    "s_add_u32 m0, s18, 0x8000\n"
    "s_nop 0\n"
    "buffer_load_dwordx4 v32, s[20:23], s64 offen lds\n"
    "v_mfma_f32_16x16x32_bf16 v[128:131], a[56:59], a[28:31], v[128:131]\n"
    "v_mfma_f32_16x16x32_bf16 v[132:135], a[60:63], a[28:31], v[132:135]\n"
    "s_waitcnt lgkmcnt(9)\n"
    "v_mfma_f32_16x16x32_bf16 v[72:75], a[32:35], a[0:3], v[72:75]\n"
    "ds_read_b64_tr_b16 a[48:49], v50 offset:60928\n"
    "s_waitcnt lgkmcnt(8)\n"
    "v_mfma_f32_16x16x32_bf16 v[76:79], a[36:39], a[0:3], v[76:79]\n"
    "buffer_load_dwordx4 v33, s[20:23], s64 offen offset:1024 lds\n"
    "ds_read_b64_tr_b16 a[50:51], v50 offset:62016\n"
    "s_waitcnt lgkmcnt(8)\n"
    "v_mfma_f32_16x16x32_bf16 v[88:91], a[32:35], a[4:7], v[88:91]\n"
    "ds_read_b128 a[16:19], v49 offset:49152\n"
    "v_mfma_f32_16x16x32_bf16 v[92:95], a[36:39], a[4:7], v[92:95]\n"
    "ds_read_b64_tr_b16 a[52:53], v51 offset:60928\n"
    "v_mfma_f32_16x16x32_bf16 v[136:139], a[32:35], v[144:147], v[136:139]\n"
    "ds_read_b64_tr_b16 a[54:55], v51 offset:62016\n"
    "v_mfma_f32_16x16x32_bf16 v[140:143], a[36:39], v[144:147], v[140:143]\n"
    "buffer_load_dwordx4 v34, s[20:23], s64 offen offset:2048 lds\n"
    "ds_read_b128 a[20:23], v49 offset:51200\n"
    "s_waitcnt lgkmcnt(10)\n"
    "v_mfma_f32_16x16x32_bf16 v[80:83], a[40:43], a[0:3], v[80:83]\n"
    "ds_read_b64_tr_b16 a[56:57], v52 offset:60928\n"
    "s_waitcnt lgkmcnt(10)\n"
    "v_mfma_f32_16x16x32_bf16 v[104:107], a[32:35], a[8:11], v[104:107]\n"
    "ds_read_b64_tr_b16 a[58:59], v52 offset:62016\n"
    "v_mfma_f32_16x16x32_bf16 v[96:99], a[40:43], a[4:7], v[96:99]\n"
    "ds_read_b128 a[24:27], v49 offset:53248\n"
    "v_mfma_f32_16x16x32_bf16 v[108:111], a[36:39], a[8:11], v[108:111]\n"
    "buffer_load_dwordx4 v35, s[20:23], s64 offen offset:3072 lds\n"
    "s_add_u32 s64, s64, 0x80\n"
    "s_cmp_eq_u32 s64, 0x500\n"
    "s_cselect_b32 s64, 0, s64\n"
    "ds_read_b64_tr_b16 a[60:61], v53 offset:60928\n"
    "v_mfma_f32_16x16x32_bf16 v[112:115], a[40:43], a[8:11], v[112:115]\n"
    "ds_read_b64_tr_b16 a[62:63], v53 offset:62016\n"
    "s_waitcnt lgkmcnt(12)\n"
    "v_mfma_f32_16x16x32_bf16 v[84:87], a[44:47], a[0:3], v[84:87]\n"
    "ds_read_b128 a[28:31], v49 offset:55296\n"
    "s_waitcnt lgkmcnt(12)\n"
    "v_mfma_f32_16x16x32_bf16 v[120:123], a[32:35], a[12:15], v[120:123]\n"
    "s_add_u32 m0, s4, 0\n"
    "s_nop 0\n"
    "buffer_load_dwordx4 v55, s[32:35], 0 offen lds\n"
    "v_mfma_f32_16x16x32_bf16 v[100:103], a[44:47], a[4:7], v[100:103]\n"
    "v_mfma_f32_16x16x32_bf16 v[124:127], a[36:39], a[12:15], v[124:127]\n"
    "v_mfma_f32_16x16x32_bf16 v[116:119], a[44:47], a[8:11], v[116:119]\n"
    "v_mfma_f32_16x16x32_bf16 v[128:131], a[40:43], a[12:15], v[128:131]\n"
    "s_add_u32 m0, s4, 0xf80\n"
    "s_nop 0\n"
    "buffer_load_dwordx4 v55, s[32:35], 0 offen offset:128 lds\n"
    "v_mfma_f32_16x16x32_bf16 v[132:135], a[44:47], a[12:15], v[132:135]\n"
    "s_add_u32 m0, s4, 0x1f00\n"
    "s_nop 0\n"
    "buffer_load_dwordx4 v55, s[32:35], 0 offen offset:256 lds\n"
    "s_add_u32 m0, s4, 0x2e80\n"
    "s_nop 0\n"
    "buffer_load_dwordx4 v55, s[32:35], 0 offen offset:384 lds\n"
    "s_add_u32 m0, s4, 0x3e00\n"
    "s_nop 0\n"
    "buffer_load_dwordx4 v55, s[32:35], 0 offen offset:512 lds\n"
    "s_add_u32 m0, s4, 0x4d80\n"
    "s_nop 0\n"
    "buffer_load_dwordx4 v55, s[32:35], 0 offen offset:640 lds\n"
    "s_waitcnt vmcnt(22)\n"
    "s_waitcnt lgkmcnt(0)\n"
    "s_barrier\n"
    "v_mfma_f32_16x16x32_bf16 v[72:75], a[48:51], a[16:19], v[72:75]\n"
    "ds_read_b64_tr_b16 a[32:33], v50\n"
    "v_mfma_f32_16x16x32_bf16 v[76:79], a[52:55], a[16:19], v[76:79]\n"
    "ds_read_b64_tr_b16 a[34:35], v50 offset:1088\n"
    "v_mfma_f32_16x16x32_bf16 v[88:91], a[48:51], a[20:23], v[88:91]\n"
    "ds_read_b128 a[0:3], v48\n"
    "s_mul_i32 s1, s64, 0x780\n"
    "s_add_u32 m0, s19, 0xcc00\n"
    "s_nop 0\n"
    "buffer_load_dwordx4 v36, s[28:31], s1 offen lds\n"
    "v_mfma_f32_16x16x32_bf16 v[92:95], a[52:55], a[20:23], v[92:95]\n"
    "ds_read_b64_tr_b16 a[36:37], v51\n"
    "v_mfma_f32_16x16x32_bf16 v[136:139], a[48:51], v[144:147], v[136:139]\n"
    "ds_read_b64_tr_b16 a[38:39], v51 offset:1088\n"
    "v_mfma_f32_16x16x32_bf16 v[140:143], a[52:55], v[144:147], v[140:143]\n"
    "ds_read_b128 a[4:7], v48 offset:2048\n"
    "v_mfma_f32_16x16x32_bf16 v[80:83], a[56:59], a[16:19], v[80:83]\n"
    "buffer_load_dwordx4 v37, s[28:31], s1 offen offset:1088 lds\n"
    "ds_read_b64_tr_b16 a[40:41], v52\n"
    "v_mfma_f32_16x16x32_bf16 v[104:107], a[48:51], a[24:27], v[104:107]\n"
    "ds_read_b64_tr_b16 a[42:43], v52 offset:1088\n"
    "v_mfma_f32_16x16x32_bf16 v[96:99], a[56:59], a[20:23], v[96:99]\n"
    "ds_read_b128 a[8:11], v48 offset:4096\n"
    "v_mfma_f32_16x16x32_bf16 v[108:111], a[52:55], a[24:27], v[108:111]\n"
    "ds_read_b64_tr_b16 a[44:45], v53\n"
    "v_mfma_f32_16x16x32_bf16 v[112:115], a[56:59], a[24:27], v[112:115]\n"
    "buffer_load_dwordx4 v38, s[28:31], s1 offen offset:2176 lds\n"
    "ds_read_b64_tr_b16 a[46:47], v53 offset:1088\n"
    "v_mfma_f32_16x16x32_bf16 v[84:87], a[60:63], a[16:19], v[84:87]\n"
    "ds_read_b128 a[12:15], v48 offset:6144\n"
    "v_mfma_f32_16x16x32_bf16 v[120:123], a[48:51], a[28:31], v[120:123]\n"
    "v_mfma_f32_16x16x32_bf16 v[100:103], a[60:63], a[20:23], v[100:103]\n"
    "v_mfma_f32_16x16x32_bf16 v[124:127], a[52:55], a[28:31], v[124:127]\n"
    "buffer_load_dwordx4 v39, s[28:31], s1 offen offset:3264 lds\n"
    "v_mfma_f32_16x16x32_bf16 v[116:119], a[60:63], a[24:27], v[116:119]\n"
    "v_mfma_f32_16x16x32_bf16 v[128:131], a[56:59], a[28:31], v[128:131]\n"
    "v_mfma_f32_16x16x32_bf16 v[132:135], a[60:63], a[28:31], v[132:135]\n"
    "s_waitcnt lgkmcnt(9)\n"
    "v_mfma_f32_16x16x32_bf16 v[72:75], a[32:35], a[0:3], v[72:75]\n"
    "s_add_u32 m0, s18, 0xc000\n"
    "s_nop 0\n"
    "buffer_load_dwordx4 v32, s[20:23], s64 offen lds\n"
    "ds_read_b64_tr_b16 a[48:49], v50 offset:8704\n"
    "s_waitcnt lgkmcnt(8)\n"
    "v_mfma_f32_16x16x32_bf16 v[76:79], a[36:39], a[0:3], v[76:79]\n"
    "ds_read_b64_tr_b16 a[50:51], v50 offset:9792\n"
    "s_waitcnt lgkmcnt(8)\n"
    "v_mfma_f32_16x16x32_bf16 v[88:91], a[32:35], a[4:7], v[88:91]\n"
    "ds_read_b128 a[16:19], v49\n"
    "v_mfma_f32_16x16x32_bf16 v[92:95], a[36:39], a[4:7], v[92:95]\n"
    "ds_read_b64_tr_b16 a[52:53], v51 offset:8704\n"
    "v_mfma_f32_16x16x32_bf16 v[136:139], a[32:35], v[144:147], v[136:139]\n"
    "buffer_load_dwordx4 v33, s[20:23], s64 offen offset:1024 lds\n"
    "ds_read_b64_tr_b16 a[54:55], v51 offset:9792\n"
    "v_mfma_f32_16x16x32_bf16 v[140:143], a[36:39], v[144:147], v[140:143]\n"
    "ds_read_b128 a[20:23], v49 offset:2048\n"
    "s_waitcnt lgkmcnt(10)\n"
    "v_mfma_f32_16x16x32_bf16 v[80:83], a[40:43], a[0:3], v[80:83]\n"
    "ds_read_b64_tr_b16 a[56:57], v52 offset:8704\n"
    "s_waitcnt lgkmcnt(10)\n"
    "v_mfma_f32_16x16x32_bf16 v[104:107], a[32:35], a[8:11], v[104:107]\n"
    "ds_read_b64_tr_b16 a[58:59], v52 offset:9792\n"
    "v_mfma_f32_16x16x32_bf16 v[96:99], a[40:43], a[4:7], v[96:99]\n"
    "buffer_load_dwordx4 v34, s[20:23], s64 offen offset:2048 lds\n"
    "ds_read_b128 a[24:27], v49 offset:4096\n"
    "v_mfma_f32_16x16x32_bf16 v[108:111], a[36:39], a[8:11], v[108:111]\n"
    "ds_read_b64_tr_b16 a[60:61], v53 offset:8704\n"
    "v_mfma_f32_16x16x32_bf16 v[112:115], a[40:43], a[8:11], v[112:115]\n"
    "ds_read_b64_tr_b16 a[62:63], v53 offset:9792\n"
    "s_waitcnt lgkmcnt(12)\n"
    "v_mfma_f32_16x16x32_bf16 v[84:87], a[44:47], a[0:3], v[84:87]\n"
    "ds_read_b128 a[28:31], v49 offset:6144\n"
    "s_waitcnt lgkmcnt(12)\n"
    "v_mfma_f32_16x16x32_bf16 v[120:123], a[32:35], a[12:15], v[120:123]\n"
    "buffer_load_dwordx4 v35, s[20:23], s64 offen offset:3072 lds\n"
    "s_add_u32 s64, s64, 0x80\n"
    "s_cmp_eq_u32 s64, 0x500\n"
    "s_cselect_b32 s64, 0, s64\n"
    "v_mfma_f32_16x16x32_bf16 v[100:103], a[44:47], a[4:7], v[100:103]\n"
    "v_mfma_f32_16x16x32_bf16 v[124:127], a[36:39], a[12:15], v[124:127]\n"
    "v_mfma_f32_16x16x32_bf16 v[116:119], a[44:47], a[8:11], v[116:119]\n"
    "v_mfma_f32_16x16x32_bf16 v[128:131], a[40:43], a[12:15], v[128:131]\n"
    "v_mfma_f32_16x16x32_bf16 v[132:135], a[44:47], a[12:15], v[132:135]\n"
    "s_waitcnt vmcnt(22)\n"
    "s_waitcnt lgkmcnt(0)\n"
    "s_barrier\n"
    "v_mfma_f32_16x16x32_bf16 v[72:75], a[48:51], a[16:19], v[72:75]\n"
    "s_mul_i32 s1, s64, 0x780\n"
    "s_add_u32 m0, s19, 0\n"
    "s_nop 0\n"
    "buffer_load_dwordx4 v36, s[28:31], s1 offen lds\n"
    "ds_read_b64_tr_b16 a[32:33], v50 offset:17408\n"
    "v_mfma_f32_16x16x32_bf16 v[76:79], a[52:55], a[16:19], v[76:79]\n"
    "ds_read_b64_tr_b16 a[34:35], v50 offset:18496\n"
    "v_mfma_f32_16x16x32_bf16 v[88:91], a[48:51], a[20:23], v[88:91]\n"
    "ds_read_b128 a[0:3], v48 offset:16384\n"
    "v_mfma_f32_16x16x32_bf16 v[92:95], a[52:55], a[20:23], v[92:95]\n"
    "ds_read_b64_tr_b16 a[36:37], v51 offset:17408\n"
    "v_mfma_f32_16x16x32_bf16 v[136:139], a[48:51], v[144:147], v[136:139]\n"
    "buffer_load_dwordx4 v37, s[28:31], s1 offen offset:1088 lds\n"
    "ds_read_b64_tr_b16 a[38:39], v51 offset:18496\n"
    "v_mfma_f32_16x16x32_bf16 v[140:143], a[52:55], v[144:147], v[140:143]\n"
    "ds_read_b128 a[4:7], v48 offset:18432\n"
    "v_mfma_f32_16x16x32_bf16 v[80:83], a[56:59], a[16:19], v[80:83]\n"
    "ds_read_b64_tr_b16 a[40:41], v52 offset:17408\n"
    "v_mfma_f32_16x16x32_bf16 v[104:107], a[48:51], a[24:27], v[104:107]\n"
    "ds_read_b64_tr_b16 a[42:43], v52 offset:18496\n"
    "v_mfma_f32_16x16x32_bf16 v[96:99], a[56:59], a[20:23], v[96:99]\n"
    "buffer_load_dwordx4 v38, s[28:31], s1 offen offset:2176 lds\n"
    "ds_read_b128 a[8:11], v48 offset:20480\n"
    "v_mfma_f32_16x16x32_bf16 v[108:111], a[52:55], a[24:27], v[108:111]\n"
    "ds_read_b64_tr_b16 a[44:45], v53 offset:17408\n"
    "v_mfma_f32_16x16x32_bf16 v[112:115], a[56:59], a[24:27], v[112:115]\n"
    "ds_read_b64_tr_b16 a[46:47], v53 offset:18496\n"
    "v_mfma_f32_16x16x32_bf16 v[84:87], a[60:63], a[16:19], v[84:87]\n"
    "ds_read_b128 a[12:15], v48 offset:22528\n"
    "v_mfma_f32_16x16x32_bf16 v[120:123], a[48:51], a[28:31], v[120:123]\n"
    "buffer_load_dwordx4 v39, s[28:31], s1 offen offset:3264 lds\n"
    "v_mfma_f32_16x16x32_bf16 v[100:103], a[60:63], a[20:23], v[100:103]\n"
    "v_mfma_f32_16x16x32_bf16 v[124:127], a[52:55], a[28:31], v[124:127]\n"
    "v_mfma_f32_16x16x32_bf16 v[116:119], a[60:63], a[24:27], v[116:119]\n"
    "v_mfma_f32_16x16x32_bf16 v[128:131], a[56:59], a[28:31], v[128:131]\n"
    "s_add_u32 m0, s18, 0\n"
    "s_nop 0\n"
    "buffer_load_dwordx4 v32, s[20:23], s64 offen lds\n"
    "v_mfma_f32_16x16x32_bf16 v[132:135], a[60:63], a[28:31], v[132:135]\n"
    "s_waitcnt lgkmcnt(9)\n"
    "v_mfma_f32_16x16x32_bf16 v[72:75], a[32:35], a[0:3], v[72:75]\n"
    "ds_read_b64_tr_b16 a[48:49], v50 offset:26112\n"
    "s_waitcnt lgkmcnt(8)\n"
    "v_mfma_f32_16x16x32_bf16 v[76:79], a[36:39], a[0:3], v[76:79]\n"
    "ds_read_b64_tr_b16 a[50:51], v50 offset:27200\n"
    "s_waitcnt lgkmcnt(8)\n"
    "v_mfma_f32_16x16x32_bf16 v[88:91], a[32:35], a[4:7], v[88:91]\n"
    "buffer_load_dwordx4 v33, s[20:23], s64 offen offset:1024 lds\n"
    "ds_read_b128 a[16:19], v49 offset:16384\n"
    "v_mfma_f32_16x16x32_bf16 v[92:95], a[36:39], a[4:7], v[92:95]\n"
    "ds_read_b64_tr_b16 a[52:53], v51 offset:26112\n"
    "v_mfma_f32_16x16x32_bf16 v[136:139], a[32:35], v[144:147], v[136:139]\n"
    "ds_read_b64_tr_b16 a[54:55], v51 offset:27200\n"
    "v_mfma_f32_16x16x32_bf16 v[140:143], a[36:39], v[144:147], v[140:143]\n"
    "ds_read_b128 a[20:23], v49 offset:18432\n"
    "s_waitcnt lgkmcnt(10)\n"
    "v_mfma_f32_16x16x32_bf16 v[80:83], a[40:43], a[0:3], v[80:83]\n"
    "buffer_load_dwordx4 v34, s[20:23], s64 offen offset:2048 lds\n"
    "ds_read_b64_tr_b16 a[56:57], v52 offset:26112\n"
    "s_waitcnt lgkmcnt(10)\n"
    "v_mfma_f32_16x16x32_bf16 v[104:107], a[32:35], a[8:11], v[104:107]\n"
    "ds_read_b64_tr_b16 a[58:59], v52 offset:27200\n"
    "v_mfma_f32_16x16x32_bf16 v[96:99], a[40:43], a[4:7], v[96:99]\n"
    "ds_read_b128 a[24:27], v49 offset:20480\n"
    "v_mfma_f32_16x16x32_bf16 v[108:111], a[36:39], a[8:11], v[108:111]\n"
    "ds_read_b64_tr_b16 a[60:61], v53 offset:26112\n"
    "v_mfma_f32_16x16x32_bf16 v[112:115], a[40:43], a[8:11], v[112:115]\n"
    "buffer_load_dwordx4 v35, s[20:23], s64 offen offset:3072 lds\n"
    "s_add_u32 s64, s64, 0x80\n"
    "s_cmp_eq_u32 s64, 0x500\n"
    "s_cselect_b32 s64, 0, s64\n"
    "ds_read_b64_tr_b16 a[62:63], v53 offset:27200\n"
    "s_waitcnt lgkmcnt(12)\n"
    "v_mfma_f32_16x16x32_bf16 v[84:87], a[44:47], a[0:3], v[84:87]\n"
    "ds_read_b128 a[28:31], v49 offset:22528\n"
    "s_waitcnt lgkmcnt(12)\n"
    "v_mfma_f32_16x16x32_bf16 v[120:123], a[32:35], a[12:15], v[120:123]\n"
    "v_mfma_f32_16x16x32_bf16 v[100:103], a[44:47], a[4:7], v[100:103]\n"
    "v_mfma_f32_16x16x32_bf16 v[124:127], a[36:39], a[12:15], v[124:127]\n"
    "v_mfma_f32_16x16x32_bf16 v[116:119], a[44:47], a[8:11], v[116:119]\n"
    "v_mfma_f32_16x16x32_bf16 v[128:131], a[40:43], a[12:15], v[128:131]\n"
    "v_mfma_f32_16x16x32_bf16 v[132:135], a[44:47], a[12:15], v[132:135]\n"
    "s_waitcnt vmcnt(22)\n"
    "s_waitcnt lgkmcnt(0)\n"
    "s_barrier\n"
    "buffer_load_dwordx4 v[152:155], v63, s[24:27], 0 offen sc1\n"
    "v_mfma_f32_16x16x32_bf16 v[72:75], a[48:51], a[16:19], v[72:75]\n"
    "ds_read_b64_tr_b16 a[32:33], v50 offset:34816\n"
    "v_mfma_f32_16x16x32_bf16 v[76:79], a[52:55], a[16:19], v[76:79]\n"
    "ds_read_b64_tr_b16 a[34:35], v50 offset:35904\n"
    "v_mfma_f32_16x16x32_bf16 v[88:91], a[48:51], a[20:23], v[88:91]\n"
    "ds_read_b128 a[0:3], v48 offset:32768\n"
    "v_mfma_f32_16x16x32_bf16 v[92:95], a[52:55], a[20:23], v[92:95]\n"
    "s_mul_i32 s1, s64, 0x780\n"
    "s_add_u32 m0, s19, 0x4400\n"
    "s_nop 0\n"
    "buffer_load_dwordx4 v36, s[28:31], s1 offen lds\n"
    "ds_read_b64_tr_b16 a[36:37], v51 offset:34816\n"
    "v_mfma_f32_16x16x32_bf16 v[136:139], a[48:51], v[144:147], v[136:139]\n"
    "ds_read_b64_tr_b16 a[38:39], v51 offset:35904\n"
    "v_mfma_f32_16x16x32_bf16 v[140:143], a[52:55], v[144:147], v[140:143]\n"
    "ds_read_b128 a[4:7], v48 offset:34816\n"
    "v_mfma_f32_16x16x32_bf16 v[80:83], a[56:59], a[16:19], v[80:83]\n"
    "ds_read_b64_tr_b16 a[40:41], v52 offset:34816\n"
    "v_mfma_f32_16x16x32_bf16 v[104:107], a[48:51], a[24:27], v[104:107]\n"
    "buffer_load_dwordx4 v37, s[28:31], s1 offen offset:1088 lds\n"
    "ds_read_b64_tr_b16 a[42:43], v52 offset:35904\n"
    "v_mfma_f32_16x16x32_bf16 v[96:99], a[56:59], a[20:23], v[96:99]\n"
    "ds_read_b128 a[8:11], v48 offset:36864\n"
    "v_mfma_f32_16x16x32_bf16 v[108:111], a[52:55], a[24:27], v[108:111]\n"
    "ds_read_b64_tr_b16 a[44:45], v53 offset:34816\n"
    "v_mfma_f32_16x16x32_bf16 v[112:115], a[56:59], a[24:27], v[112:115]\n"
    "ds_read_b64_tr_b16 a[46:47], v53 offset:35904\n"
    "v_mfma_f32_16x16x32_bf16 v[84:87], a[60:63], a[16:19], v[84:87]\n"
    "buffer_load_dwordx4 v38, s[28:31], s1 offen offset:2176 lds\n"
    "ds_read_b128 a[12:15], v48 offset:38912\n"
    "v_mfma_f32_16x16x32_bf16 v[120:123], a[48:51], a[28:31], v[120:123]\n"
    "v_mfma_f32_16x16x32_bf16 v[100:103], a[60:63], a[20:23], v[100:103]\n"
    "v_mfma_f32_16x16x32_bf16 v[124:127], a[52:55], a[28:31], v[124:127]\n"
    "v_mfma_f32_16x16x32_bf16 v[116:119], a[60:63], a[24:27], v[116:119]\n"
    "buffer_load_dwordx4 v39, s[28:31], s1 offen offset:3264 lds\n"
    "v_mfma_f32_16x16x32_bf16 v[128:131], a[56:59], a[28:31], v[128:131]\n"
    "v_mfma_f32_16x16x32_bf16 v[132:135], a[60:63], a[28:31], v[132:135]\n"
    "s_waitcnt lgkmcnt(9)\n"
    "v_mfma_f32_16x16x32_bf16 v[72:75], a[32:35], a[0:3], v[72:75]\n"
    "ds_read_b64_tr_b16 a[48:49], v50 offset:43520\n"
    "s_waitcnt lgkmcnt(8)\n"
    "v_mfma_f32_16x16x32_bf16 v[76:79], a[36:39], a[0:3], v[76:79]\n"
    "s_add_u32 m0, s18, 0x4000\n"
    "s_nop 0\n"
    "buffer_load_dwordx4 v32, s[20:23], s64 offen lds\n"
    "ds_read_b64_tr_b16 a[50:51], v50 offset:44608\n"
    "s_waitcnt lgkmcnt(8)\n"
    "v_mfma_f32_16x16x32_bf16 v[88:91], a[32:35], a[4:7], v[88:91]\n"
    "ds_read_b128 a[16:19], v49 offset:32768\n"
    "v_mfma_f32_16x16x32_bf16 v[92:95], a[36:39], a[4:7], v[92:95]\n"
    "ds_read_b64_tr_b16 a[52:53], v51 offset:43520\n"
    "v_mfma_f32_16x16x32_bf16 v[136:139], a[32:35], v[144:147], v[136:139]\n"
    "ds_read_b64_tr_b16 a[54:55], v51 offset:44608\n"
    "v_mfma_f32_16x16x32_bf16 v[140:143], a[36:39], v[144:147], v[140:143]\n"
    "buffer_load_dwordx4 v33, s[20:23], s64 offen offset:1024 lds\n"
    "ds_read_b128 a[20:23], v49 offset:34816\n"
    "s_waitcnt lgkmcnt(10)\n"
    "v_mfma_f32_16x16x32_bf16 v[80:83], a[40:43], a[0:3], v[80:83]\n"
    "ds_read_b64_tr_b16 a[56:57], v52 offset:43520\n"
    "s_waitcnt lgkmcnt(10)\n"
    "v_mfma_f32_16x16x32_bf16 v[104:107], a[32:35], a[8:11], v[104:107]\n"
    "ds_read_b64_tr_b16 a[58:59], v52 offset:44608\n"
    "v_mfma_f32_16x16x32_bf16 v[96:99], a[40:43], a[4:7], v[96:99]\n"
    "ds_read_b128 a[24:27], v49 offset:36864\n"
    "v_mfma_f32_16x16x32_bf16 v[108:111], a[36:39], a[8:11], v[108:111]\n"
    "buffer_load_dwordx4 v34, s[20:23], s64 offen offset:2048 lds\n"
    "ds_read_b64_tr_b16 a[60:61], v53 offset:43520\n"
    "v_mfma_f32_16x16x32_bf16 v[112:115], a[40:43], a[8:11], v[112:115]\n"
    "ds_read_b64_tr_b16 a[62:63], v53 offset:44608\n"
    "s_waitcnt lgkmcnt(12)\n"
    "v_mfma_f32_16x16x32_bf16 v[84:87], a[44:47], a[0:3], v[84:87]\n"
    "ds_read_b128 a[28:31], v49 offset:38912\n"
    "s_waitcnt lgkmcnt(12)\n"
    "v_mfma_f32_16x16x32_bf16 v[120:123], a[32:35], a[12:15], v[120:123]\n"
    "v_mfma_f32_16x16x32_bf16 v[100:103], a[44:47], a[4:7], v[100:103]\n"
    "buffer_load_dwordx4 v35, s[20:23], s64 offen offset:3072 lds\n"
    "s_add_u32 s64, s64, 0x80\n"
    "s_cmp_eq_u32 s64, 0x500\n"
    "s_cselect_b32 s64, 0, s64\n"
    "v_mfma_f32_16x16x32_bf16 v[124:127], a[36:39], a[12:15], v[124:127]\n"
    "v_mfma_f32_16x16x32_bf16 v[116:119], a[44:47], a[8:11], v[116:119]\n"
    "v_mfma_f32_16x16x32_bf16 v[128:131], a[40:43], a[12:15], v[128:131]\n"
    "v_mfma_f32_16x16x32_bf16 v[132:135], a[44:47], a[12:15], v[132:135]\n"
    "s_waitcnt vmcnt(17)\n"
    "s_waitcnt lgkmcnt(0)\n"
    "s_barrier\n"
    "v_mfma_f32_16x16x32_bf16 v[72:75], a[48:51], a[16:19], v[72:75]\n"
    "ds_read_b64_tr_b16 a[32:33], v50 offset:52224\n"
    "v_mfma_f32_16x16x32_bf16 v[76:79], a[52:55], a[16:19], v[76:79]\n"
    "ds_read_b64_tr_b16 a[34:35], v50 offset:53312\n"
    "v_mfma_f32_16x16x32_bf16 v[88:91], a[48:51], a[20:23], v[88:91]\n"
    "ds_read_b128 a[0:3], v48 offset:49152\n"
    "v_mfma_f32_16x16x32_bf16 v[92:95], a[52:55], a[20:23], v[92:95]\n"
    "ds_read_b64_tr_b16 a[36:37], v51 offset:52224\n"
    "v_mfma_f32_16x16x32_bf16 v[136:139], a[48:51], v[144:147], v[136:139]\n"
    "ds_read_b64_tr_b16 a[38:39], v51 offset:53312\n"
    "v_mfma_f32_16x16x32_bf16 v[140:143], a[52:55], v[144:147], v[140:143]\n"
    "ds_read_b128 a[4:7], v48 offset:51200\n"
    "v_mfma_f32_16x16x32_bf16 v[80:83], a[56:59], a[16:19], v[80:83]\n"
    "ds_read_b64_tr_b16 a[40:41], v52 offset:52224\n"
    "v_mfma_f32_16x16x32_bf16 v[104:107], a[48:51], a[24:27], v[104:107]\n"
    "ds_read_b64_tr_b16 a[42:43], v52 offset:53312\n"
    "v_mfma_f32_16x16x32_bf16 v[96:99], a[56:59], a[20:23], v[96:99]\n"
    "ds_read_b128 a[8:11], v48 offset:53248\n"
    "v_mfma_f32_16x16x32_bf16 v[108:111], a[52:55], a[24:27], v[108:111]\n"
    "ds_read_b64_tr_b16 a[44:45], v53 offset:52224\n"
    "v_mfma_f32_16x16x32_bf16 v[112:115], a[56:59], a[24:27], v[112:115]\n"
    "ds_read_b64_tr_b16 a[46:47], v53 offset:53312\n"
    "v_mfma_f32_16x16x32_bf16 v[84:87], a[60:63], a[16:19], v[84:87]\n"
    "ds_read_b128 a[12:15], v48 offset:55296\n"
    "v_mfma_f32_16x16x32_bf16 v[120:123], a[48:51], a[28:31], v[120:123]\n"
    "v_mfma_f32_16x16x32_bf16 v[100:103], a[60:63], a[20:23], v[100:103]\n"
    "v_mfma_f32_16x16x32_bf16 v[124:127], a[52:55], a[28:31], v[124:127]\n"
    "v_mfma_f32_16x16x32_bf16 v[116:119], a[60:63], a[24:27], v[116:119]\n"
    "v_mfma_f32_16x16x32_bf16 v[128:131], a[56:59], a[28:31], v[128:131]\n"
    "v_mfma_f32_16x16x32_bf16 v[132:135], a[60:63], a[28:31], v[132:135]\n"
    "s_waitcnt lgkmcnt(9)\n"
    "v_mfma_f32_16x16x32_bf16 v[72:75], a[32:35], a[0:3], v[72:75]\n"
    "ds_read_b64_tr_b16 a[48:49], v50 offset:60928\n"
    "s_waitcnt lgkmcnt(8)\n"
    "v_mfma_f32_16x16x32_bf16 v[76:79], a[36:39], a[0:3], v[76:79]\n"
    "ds_read_b64_tr_b16 a[50:51], v50 offset:62016\n"
    "s_waitcnt lgkmcnt(8)\n"
    "v_mfma_f32_16x16x32_bf16 v[88:91], a[32:35], a[4:7], v[88:91]\n"
    "ds_read_b128 a[16:19], v49 offset:49152\n"
    "v_mfma_f32_16x16x32_bf16 v[92:95], a[36:39], a[4:7], v[92:95]\n"
    "ds_read_b64_tr_b16 a[52:53], v51 offset:60928\n"
    "v_mfma_f32_16x16x32_bf16 v[136:139], a[32:35], v[144:147], v[136:139]\n"
    "ds_read_b64_tr_b16 a[54:55], v51 offset:62016\n"
    "v_mfma_f32_16x16x32_bf16 v[140:143], a[36:39], v[144:147], v[140:143]\n"
    "ds_read_b128 a[20:23], v49 offset:51200\n"
    "s_waitcnt lgkmcnt(10)\n"
    "v_mfma_f32_16x16x32_bf16 v[80:83], a[40:43], a[0:3], v[80:83]\n"
    "ds_read_b64_tr_b16 a[56:57], v52 offset:60928\n"
    "s_waitcnt lgkmcnt(10)\n"
    "v_mfma_f32_16x16x32_bf16 v[104:107], a[32:35], a[8:11], v[104:107]\n"
    "ds_read_b64_tr_b16 a[58:59], v52 offset:62016\n"
    "v_mfma_f32_16x16x32_bf16 v[96:99], a[40:43], a[4:7], v[96:99]\n"
    "ds_read_b128 a[24:27], v49 offset:53248\n"
    "v_mfma_f32_16x16x32_bf16 v[108:111], a[36:39], a[8:11], v[108:111]\n"
    "ds_read_b64_tr_b16 a[60:61], v53 offset:60928\n"
    "v_mfma_f32_16x16x32_bf16 v[112:115], a[40:43], a[8:11], v[112:115]\n"
    "ds_read_b64_tr_b16 a[62:63], v53 offset:62016\n"
    "s_waitcnt lgkmcnt(12)\n"
    "v_mfma_f32_16x16x32_bf16 v[84:87], a[44:47], a[0:3], v[84:87]\n"
    "ds_read_b128 a[28:31], v49 offset:55296\n"
    "s_waitcnt lgkmcnt(12)\n"
    "v_mfma_f32_16x16x32_bf16 v[120:123], a[32:35], a[12:15], v[120:123]\n"
    "v_mfma_f32_16x16x32_bf16 v[100:103], a[44:47], a[4:7], v[100:103]\n"
    "v_mfma_f32_16x16x32_bf16 v[124:127], a[36:39], a[12:15], v[124:127]\n"
    "v_mfma_f32_16x16x32_bf16 v[116:119], a[44:47], a[8:11], v[116:119]\n"
    "v_mfma_f32_16x16x32_bf16 v[128:131], a[40:43], a[12:15], v[128:131]\n"
    "v_mfma_f32_16x16x32_bf16 v[132:135], a[44:47], a[12:15], v[132:135]\n"
    "s_waitcnt vmcnt(9)\n"
    "s_waitcnt lgkmcnt(0)\n"
    "s_barrier\n"
    "v_mfma_f32_16x16x32_bf16 v[72:75], a[48:51], a[16:19], v[72:75]\n"
    "ds_read_b64_tr_b16 a[32:33], v50\n"
    "ds_read_b64_tr_b16 a[34:35], v50 offset:1088\n"
    "v_mfma_f32_16x16x32_bf16 v[76:79], a[52:55], a[16:19], v[76:79]\n"
    "ds_read_b128 a[0:3], v48\n"
    "v_mfma_f32_16x16x32_bf16 v[88:91], a[48:51], a[20:23], v[88:91]\n"
    "ds_read_b64_tr_b16 a[36:37], v51\n"
    "ds_read_b64_tr_b16 a[38:39], v51 offset:1088\n"
    "v_mfma_f32_16x16x32_bf16 v[92:95], a[52:55], a[20:23], v[92:95]\n"
    "ds_read_b128 a[4:7], v48 offset:2048\n"
    "v_mfma_f32_16x16x32_bf16 v[136:139], a[48:51], v[144:147], v[136:139]\n"
    "ds_read_b64_tr_b16 a[40:41], v52\n"
    "ds_read_b64_tr_b16 a[42:43], v52 offset:1088\n"
    "v_mfma_f32_16x16x32_bf16 v[140:143], a[52:55], v[144:147], v[140:143]\n"
    "ds_read_b128 a[8:11], v48 offset:4096\n"
    "v_mfma_f32_16x16x32_bf16 v[80:83], a[56:59], a[16:19], v[80:83]\n"
    "ds_read_b64_tr_b16 a[44:45], v53\n"
    "ds_read_b64_tr_b16 a[46:47], v53 offset:1088\n"
    "v_mfma_f32_16x16x32_bf16 v[104:107], a[48:51], a[24:27], v[104:107]\n"
    "ds_read_b128 a[12:15], v48 offset:6144\n"
    "v_mfma_f32_16x16x32_bf16 v[96:99], a[56:59], a[20:23], v[96:99]\n"
    "ds_read_b64_tr_b16 a[64:65], v56\n"
    "ds_read_b64_tr_b16 a[66:67], v56 offset:512\n"
    "v_mfma_f32_16x16x32_bf16 v[108:111], a[52:55], a[24:27], v[108:111]\n"
    "ds_read_b64_tr_b16 a[68:69], v57\n"
    "v_mfma_f32_16x16x32_bf16 v[112:115], a[56:59], a[24:27], v[112:115]\n"
    "ds_read_b64_tr_b16 a[70:71], v57 offset:512\n"
    "ds_read_b64_tr_b16 a[72:73], v58\n"
    "v_mfma_f32_16x16x32_bf16 v[84:87], a[60:63], a[16:19], v[84:87]\n"
    "ds_read_b64_tr_b16 a[74:75], v58 offset:512\n"
    "v_mfma_f32_16x16x32_bf16 v[120:123], a[48:51], a[28:31], v[120:123]\n"
    "ds_read_b64_tr_b16 a[76:77], v59\n"
    "ds_read_b64_tr_b16 a[78:79], v59 offset:512\n"
    "v_mfma_f32_16x16x32_bf16 v[100:103], a[60:63], a[20:23], v[100:103]\n"
    "ds_read_b64_tr_b16 a[80:81], v60\n"
    "v_mfma_f32_16x16x32_bf16 v[124:127], a[52:55], a[28:31], v[124:127]\n"
    "ds_read_b64_tr_b16 a[82:83], v60 offset:512\n"
    "ds_read_b64_tr_b16 a[84:85], v61\n"
    "v_mfma_f32_16x16x32_bf16 v[116:119], a[60:63], a[24:27], v[116:119]\n"
    "ds_read_b64_tr_b16 a[86:87], v61 offset:512\n"
    "v_mfma_f32_16x16x32_bf16 v[128:131], a[56:59], a[28:31], v[128:131]\n"
    "v_mfma_f32_16x16x32_bf16 v[132:135], a[60:63], a[28:31], v[132:135]\n"
    "s_waitcnt lgkmcnt(15)\n"
    "v_mfma_f32_16x16x32_bf16 v[72:75], a[32:35], a[0:3], v[72:75]\n"
    "s_waitcnt lgkmcnt(10)\n"
    "buffer_store_dwordx4 a[64:67], v62, s[24:27], 0 offen sc1\n"
    "ds_read_b64_tr_b16 a[48:49], v50 offset:8704\n"
    "v_mfma_f32_16x16x32_bf16 v[76:79], a[36:39], a[0:3], v[76:79]\n"
    "ds_read_b64_tr_b16 a[50:51], v50 offset:9792\n"
    "v_mfma_f32_16x16x32_bf16 v[88:91], a[32:35], a[4:7], v[88:91]\n"
    "ds_read_b128 a[16:19], v49\n"
    "v_mfma_f32_16x16x32_bf16 v[92:95], a[36:39], a[4:7], v[92:95]\n"
    "ds_read_b64_tr_b16 a[52:53], v51 offset:8704\n"
    "v_mfma_f32_16x16x32_bf16 v[136:139], a[32:35], v[144:147], v[136:139]\n"
    "ds_read_b64_tr_b16 a[54:55], v51 offset:9792\n"
    "v_mfma_f32_16x16x32_bf16 v[140:143], a[36:39], v[144:147], v[140:143]\n"
    "ds_read_b128 a[20:23], v49 offset:2048\n"
    "v_mfma_f32_16x16x32_bf16 v[80:83], a[40:43], a[0:3], v[80:83]\n"
    "s_waitcnt lgkmcnt(14)\n"
    "buffer_store_dwordx4 a[68:71], v62, s[24:27], 0 offen offset:1024 sc1\n"
    "ds_read_b64_tr_b16 a[56:57], v52 offset:8704\n"
    "v_mfma_f32_16x16x32_bf16 v[104:107], a[32:35], a[8:11], v[104:107]\n"
    "ds_read_b64_tr_b16 a[58:59], v52 offset:9792\n"
    "v_mfma_f32_16x16x32_bf16 v[96:99], a[40:43], a[4:7], v[96:99]\n"
    "ds_read_b128 a[24:27], v49 offset:4096\n"
    "v_mfma_f32_16x16x32_bf16 v[108:111], a[36:39], a[8:11], v[108:111]\n"
    "ds_read_b64_tr_b16 a[60:61], v53 offset:8704\n"
    "v_mfma_f32_16x16x32_bf16 v[112:115], a[40:43], a[8:11], v[112:115]\n"
    "ds_read_b64_tr_b16 a[62:63], v53 offset:9792\n"
    "v_mfma_f32_16x16x32_bf16 v[84:87], a[44:47], a[0:3], v[84:87]\n"
    "ds_read_b128 a[28:31], v49 offset:6144\n"
    "v_mfma_f32_16x16x32_bf16 v[120:123], a[32:35], a[12:15], v[120:123]\n"
    "s_waitcnt lgkmcnt(15)\n"
    "buffer_store_dwordx4 a[72:75], v62, s[24:27], 0 offen offset:2048 sc1\n"
    "v_mfma_f32_16x16x32_bf16 v[100:103], a[44:47], a[4:7], v[100:103]\n"
    "v_mfma_f32_16x16x32_bf16 v[124:127], a[36:39], a[12:15], v[124:127]\n"
    "v_mfma_f32_16x16x32_bf16 v[116:119], a[44:47], a[8:11], v[116:119]\n"
    "v_mfma_f32_16x16x32_bf16 v[128:131], a[40:43], a[12:15], v[128:131]\n"
    "v_mfma_f32_16x16x32_bf16 v[132:135], a[44:47], a[12:15], v[132:135]\n"
    "s_waitcnt vmcnt(3)\n"
    "s_waitcnt lgkmcnt(0)\n"
    "s_barrier\n"
    "v_mfma_f32_16x16x32_bf16 v[72:75], a[48:51], a[16:19], v[72:75]\n"
    "ds_read_b64_tr_b16 a[32:33], v50 offset:17408\n"
    "buffer_store_dwordx4 a[76:79], v62, s[24:27], s71 offen sc1\n"
    "v_mfma_f32_16x16x32_bf16 v[76:79], a[52:55], a[16:19], v[76:79]\n"
    "ds_read_b64_tr_b16 a[34:35], v50 offset:18496\n"
    "v_mfma_f32_16x16x32_bf16 v[88:91], a[48:51], a[20:23], v[88:91]\n"
    "ds_read_b128 a[0:3], v48 offset:16384\n"
    "v_mfma_f32_16x16x32_bf16 v[92:95], a[52:55], a[20:23], v[92:95]\n"
    "ds_read_b64_tr_b16 a[36:37], v51 offset:17408\n"
    "v_mfma_f32_16x16x32_bf16 v[136:139], a[48:51], v[144:147], v[136:139]\n"
    "ds_read_b64_tr_b16 a[38:39], v51 offset:18496\n"
    "v_mfma_f32_16x16x32_bf16 v[140:143], a[52:55], v[144:147], v[140:143]\n"
    "ds_read_b128 a[4:7], v48 offset:18432\n"
    "v_mfma_f32_16x16x32_bf16 v[80:83], a[56:59], a[16:19], v[80:83]\n"
    "ds_read_b64_tr_b16 a[40:41], v52 offset:17408\n"
    "buffer_store_dwordx4 a[80:83], v62, s[24:27], s72 offen sc1\n"
    "v_mfma_f32_16x16x32_bf16 v[104:107], a[48:51], a[24:27], v[104:107]\n"
    "ds_read_b64_tr_b16 a[42:43], v52 offset:18496\n"
    "v_mfma_f32_16x16x32_bf16 v[96:99], a[56:59], a[20:23], v[96:99]\n"
    "ds_read_b128 a[8:11], v48 offset:20480\n"
    "v_mfma_f32_16x16x32_bf16 v[108:111], a[52:55], a[24:27], v[108:111]\n"
    "ds_read_b64_tr_b16 a[44:45], v53 offset:17408\n"
    "v_mfma_f32_16x16x32_bf16 v[112:115], a[56:59], a[24:27], v[112:115]\n"
    "ds_read_b64_tr_b16 a[46:47], v53 offset:18496\n"
    "v_mfma_f32_16x16x32_bf16 v[84:87], a[60:63], a[16:19], v[84:87]\n"
    "ds_read_b128 a[12:15], v48 offset:22528\n"
    "v_mfma_f32_16x16x32_bf16 v[120:123], a[48:51], a[28:31], v[120:123]\n"
    "v_mfma_f32_16x16x32_bf16 v[100:103], a[60:63], a[20:23], v[100:103]\n"
    "buffer_store_dwordx4 a[84:87], v62, s[24:27], s73 offen sc1\n"
    "v_mfma_f32_16x16x32_bf16 v[124:127], a[52:55], a[28:31], v[124:127]\n"
    "v_mfma_f32_16x16x32_bf16 v[116:119], a[60:63], a[24:27], v[116:119]\n"
    "v_mfma_f32_16x16x32_bf16 v[128:131], a[56:59], a[28:31], v[128:131]\n"
    "v_mfma_f32_16x16x32_bf16 v[132:135], a[60:63], a[28:31], v[132:135]\n"
    "s_waitcnt lgkmcnt(9)\n"
    "v_mfma_f32_16x16x32_bf16 v[72:75], a[32:35], a[0:3], v[72:75]\n"
    "ds_read_b64_tr_b16 a[48:49], v50 offset:26112\n"
    "s_waitcnt lgkmcnt(8)\n"
    "v_mfma_f32_16x16x32_bf16 v[76:79], a[36:39], a[0:3], v[76:79]\n"
    "ds_read_b64_tr_b16 a[50:51], v50 offset:27200\n"
    "s_waitcnt lgkmcnt(8)\n"
    "v_mfma_f32_16x16x32_bf16 v[88:91], a[32:35], a[4:7], v[88:91]\n"
    "ds_read_b128 a[16:19], v49 offset:16384\n"
    "v_mfma_f32_16x16x32_bf16 v[92:95], a[36:39], a[4:7], v[92:95]\n"
    "ds_read_b64_tr_b16 a[52:53], v51 offset:26112\n"
    "v_mfma_f32_16x16x32_bf16 v[136:139], a[32:35], v[144:147], v[136:139]\n"
    "ds_read_b64_tr_b16 a[54:55], v51 offset:27200\n"
    "v_mfma_f32_16x16x32_bf16 v[140:143], a[36:39], v[144:147], v[140:143]\n"
    "ds_read_b128 a[20:23], v49 offset:18432\n"
    "s_waitcnt lgkmcnt(10)\n"
    "v_mfma_f32_16x16x32_bf16 v[80:83], a[40:43], a[0:3], v[80:83]\n"
    "ds_read_b64_tr_b16 a[56:57], v52 offset:26112\n"
    "s_waitcnt lgkmcnt(10)\n"
    "v_mfma_f32_16x16x32_bf16 v[104:107], a[32:35], a[8:11], v[104:107]\n"
    "ds_read_b64_tr_b16 a[58:59], v52 offset:27200\n"
    "v_mfma_f32_16x16x32_bf16 v[96:99], a[40:43], a[4:7], v[96:99]\n"
    "ds_read_b128 a[24:27], v49 offset:20480\n"
    "v_mfma_f32_16x16x32_bf16 v[108:111], a[36:39], a[8:11], v[108:111]\n"
    "ds_read_b64_tr_b16 a[60:61], v53 offset:26112\n"
    "v_mfma_f32_16x16x32_bf16 v[112:115], a[40:43], a[8:11], v[112:115]\n"
    "ds_read_b64_tr_b16 a[62:63], v53 offset:27200\n"
    "s_waitcnt lgkmcnt(12)\n"
    "v_mfma_f32_16x16x32_bf16 v[84:87], a[44:47], a[0:3], v[84:87]\n"
    "ds_read_b128 a[28:31], v49 offset:22528\n"
    "s_waitcnt lgkmcnt(12)\n"
    "v_mfma_f32_16x16x32_bf16 v[120:123], a[32:35], a[12:15], v[120:123]\n"
    "v_mfma_f32_16x16x32_bf16 v[100:103], a[44:47], a[4:7], v[100:103]\n"
    "v_mfma_f32_16x16x32_bf16 v[124:127], a[36:39], a[12:15], v[124:127]\n"
    "v_mfma_f32_16x16x32_bf16 v[116:119], a[44:47], a[8:11], v[116:119]\n"
    "v_mfma_f32_16x16x32_bf16 v[128:131], a[40:43], a[12:15], v[128:131]\n"
    "v_mfma_f32_16x16x32_bf16 v[132:135], a[44:47], a[12:15], v[132:135]\n"
    "s_waitcnt lgkmcnt(9)\n"
    "v_mfma_f32_16x16x32_bf16 v[72:75], a[48:51], a[16:19], v[72:75]\n"
    "s_waitcnt lgkmcnt(7)\n"
    "v_mfma_f32_16x16x32_bf16 v[76:79], a[52:55], a[16:19], v[76:79]\n"
    "v_xor_b32 v148, s96, v153\n"
    "v_xor_b32 v149, s96, v155\n"
    "v_or_b32 v148, v148, v149\n"
    "v_cmp_ne_u32 vcc, 0, v148\n"
    "s_cmp_eq_u64 vcc, 0\n"
    "s_cbranch_scc1 .Lfb_xd_5\n"
    "s_mov_b32 s97, 0\n"
    ".Lfb_xs_4:\n"
    "s_sleep 4\n"
    "buffer_load_dwordx4 v[152:155], v63, s[24:27], 0 offen sc1\n"
    "s_waitcnt vmcnt(0)\n"
    "v_xor_b32 v148, s96, v153\n"
    "v_xor_b32 v149, s96, v155\n"
    "v_or_b32 v148, v148, v149\n"
    "v_cmp_ne_u32 vcc, 0, v148\n"
    "s_cmp_eq_u64 vcc, 0\n"
    "s_cbranch_scc1 .Lfb_xd_5\n"
    "s_add_u32 s97, s97, 1\n"
    "s_cmp_lt_u32 s97, 0x10000\n"
    "s_cbranch_scc1 .Lfb_xs_4\n"
    ".Lfb_xd_5:\n"
    ".Lfb_xe_6:\n"
    "s_waitcnt lgkmcnt(6)\n"
    "v_mfma_f32_16x16x32_bf16 v[88:91], a[48:51], a[20:23], v[88:91]\n"
    "v_mul_f32 v152, s75, v152\n"
    "v_mfma_f32_16x16x32_bf16 v[92:95], a[52:55], a[20:23], v[92:95]\n"
    "v_mul_f32 v154, s75, v154\n"
    "v_mfma_f32_16x16x32_bf16 v[136:139], a[48:51], v[144:147], v[136:139]\n"
    "ds_write_b32 v68, v152\n"
    "v_mfma_f32_16x16x32_bf16 v[140:143], a[52:55], v[144:147], v[140:143]\n"
    "ds_write_b32 v68, v154 offset:512\n"
    "s_waitcnt lgkmcnt(6)\n"
    "v_mfma_f32_16x16x32_bf16 v[80:83], a[56:59], a[16:19], v[80:83]\n"
    "s_waitcnt lgkmcnt(5)\n"
    "v_mfma_f32_16x16x32_bf16 v[104:107], a[48:51], a[24:27], v[104:107]\n"
    "v_mfma_f32_16x16x32_bf16 v[96:99], a[56:59], a[20:23], v[96:99]\n"
    "v_mfma_f32_16x16x32_bf16 v[108:111], a[52:55], a[24:27], v[108:111]\n"
    "ds_write_b128 v66, v[136:139]\n"
    "v_mfma_f32_16x16x32_bf16 v[112:115], a[56:59], a[24:27], v[112:115]\n"
    "ds_write_b128 v66, v[140:143] offset:64\n"
    "s_waitcnt lgkmcnt(5)\n"
    "v_mfma_f32_16x16x32_bf16 v[84:87], a[60:63], a[16:19], v[84:87]\n"
    "s_waitcnt lgkmcnt(4)\n"
    "v_mfma_f32_16x16x32_bf16 v[120:123], a[48:51], a[28:31], v[120:123]\n"
    "v_mfma_f32_16x16x32_bf16 v[100:103], a[60:63], a[20:23], v[100:103]\n"
    "v_mfma_f32_16x16x32_bf16 v[124:127], a[52:55], a[28:31], v[124:127]\n"
    "s_waitcnt lgkmcnt(0)\n"
    "s_barrier\n"
    "v_mfma_f32_16x16x32_bf16 v[116:119], a[60:63], a[24:27], v[116:119]\n"
    "ds_read2_b32 v[164:165], v69 offset0:0 offset1:16\n"
    "ds_read2_b32 v[166:167], v69 offset0:128 offset1:144\n"
    "v_mfma_f32_16x16x32_bf16 v[128:131], a[56:59], a[28:31], v[128:131]\n"
    "ds_read2_b32 v[168:169], v69 offset0:32 offset1:48\n"
    "ds_read2_b32 v[170:171], v69 offset0:160 offset1:176\n"
    "v_mfma_f32_16x16x32_bf16 v[132:135], a[60:63], a[28:31], v[132:135]\n"
    "ds_read_b128 v[156:159], v67\n"
    "ds_read_b128 v[160:163], v67 offset:64\n"
    "s_waitcnt lgkmcnt(4)\n"
    "v_pk_mul_f32 v[196:197], v[136:137], v[166:167] op_sel_hi:[1,0]\n"
    "v_pk_fma_f32 v[196:197], v[72:73], v[164:165], v[196:197] op_sel_hi:[1,0,1]\n"
    "v_pk_mul_f32 v[198:199], v[138:139], v[166:167] op_sel_hi:[1,0]\n"
    "v_pk_fma_f32 v[198:199], v[74:75], v[164:165], v[198:199] op_sel_hi:[1,0,1]\n"
    "v_pk_mul_f32 v[200:201], v[136:137], v[166:167] op_sel:[0,1] op_sel_hi:[1,1]\n"
    "v_pk_fma_f32 v[200:201], v[88:89], v[164:165], v[200:201] op_sel:[0,1,0] op_sel_hi:[1,1,1]\n"
    "v_pk_mul_f32 v[202:203], v[138:139], v[166:167] op_sel:[0,1] op_sel_hi:[1,1]\n"
    "v_pk_fma_f32 v[202:203], v[90:91], v[164:165], v[202:203] op_sel:[0,1,0] op_sel_hi:[1,1,1]\n"
    "v_cvt_pk_bf16_f32 v212, v196, v197\n"
    "v_cvt_pk_bf16_f32 v213, v198, v199\n"
    "v_cvt_pk_bf16_f32 v214, v200, v201\n"
    "v_cvt_pk_bf16_f32 v215, v202, v203\n"
    "s_xor_b32 s99, s6, 0\n"
    "s_mul_i32 s99, s99, s79\n"
    "s_add_u32 s99, s99, s53\n"
    "v_permlane16_swap_b32 v212, v214\n"
    "v_permlane16_swap_b32 v213, v215\n"
    "buffer_store_dwordx4 v[212:215], v54, s[24:27], s99 offen sc1\n"
    "s_waitcnt lgkmcnt(2)\n"
    "v_pk_mul_f32 v[204:205], v[136:137], v[170:171] op_sel_hi:[1,0]\n"
    "v_pk_fma_f32 v[204:205], v[104:105], v[168:169], v[204:205] op_sel_hi:[1,0,1]\n"
    "v_pk_mul_f32 v[206:207], v[138:139], v[170:171] op_sel_hi:[1,0]\n"
    "v_pk_fma_f32 v[206:207], v[106:107], v[168:169], v[206:207] op_sel_hi:[1,0,1]\n"
    "v_pk_mul_f32 v[208:209], v[136:137], v[170:171] op_sel:[0,1] op_sel_hi:[1,1]\n"
    "v_pk_fma_f32 v[208:209], v[120:121], v[168:169], v[208:209] op_sel:[0,1,0] op_sel_hi:[1,1,1]\n"
    "v_pk_mul_f32 v[210:211], v[138:139], v[170:171] op_sel:[0,1] op_sel_hi:[1,1]\n"
    "v_pk_fma_f32 v[210:211], v[122:123], v[168:169], v[210:211] op_sel:[0,1,0] op_sel_hi:[1,1,1]\n"
    "v_cvt_pk_bf16_f32 v216, v204, v205\n"
    "v_cvt_pk_bf16_f32 v217, v206, v207\n"
    "v_cvt_pk_bf16_f32 v218, v208, v209\n"
    "v_cvt_pk_bf16_f32 v219, v210, v211\n"
    "s_xor_b32 s98, s6, 0\n"
    "s_mul_i32 s98, s98, s79\n"
    "s_add_u32 s98, s98, s53\n"
    "s_add_u32 s98, s98, s78\n"
    "v_permlane16_swap_b32 v216, v218\n"
    "v_permlane16_swap_b32 v217, v219\n"
    "buffer_store_dwordx4 v[216:219], v54, s[24:27], s98 offen sc1\n"
    "v_pk_mul_f32 v[196:197], v[140:141], v[166:167] op_sel_hi:[1,0]\n"
    "v_pk_fma_f32 v[196:197], v[76:77], v[164:165], v[196:197] op_sel_hi:[1,0,1]\n"
    "v_pk_mul_f32 v[198:199], v[142:143], v[166:167] op_sel_hi:[1,0]\n"
    "v_pk_fma_f32 v[198:199], v[78:79], v[164:165], v[198:199] op_sel_hi:[1,0,1]\n"
    "v_pk_mul_f32 v[200:201], v[140:141], v[166:167] op_sel:[0,1] op_sel_hi:[1,1]\n"
    "v_pk_fma_f32 v[200:201], v[92:93], v[164:165], v[200:201] op_sel:[0,1,0] op_sel_hi:[1,1,1]\n"
    "v_pk_mul_f32 v[202:203], v[142:143], v[166:167] op_sel:[0,1] op_sel_hi:[1,1]\n"
    "v_pk_fma_f32 v[202:203], v[94:95], v[164:165], v[202:203] op_sel:[0,1,0] op_sel_hi:[1,1,1]\n"
    "v_cvt_pk_bf16_f32 v220, v196, v197\n"
    "v_cvt_pk_bf16_f32 v221, v198, v199\n"
    "v_cvt_pk_bf16_f32 v222, v200, v201\n"
    "v_cvt_pk_bf16_f32 v223, v202, v203\n"
    "s_xor_b32 s99, s6, 1\n"
    "s_mul_i32 s99, s99, s79\n"
    "s_add_u32 s99, s99, s53\n"
    "v_permlane16_swap_b32 v220, v222\n"
    "v_permlane16_swap_b32 v221, v223\n"
    "buffer_store_dwordx4 v[220:223], v54, s[24:27], s99 offen sc1\n"
    "v_pk_mul_f32 v[204:205], v[140:141], v[170:171] op_sel_hi:[1,0]\n"
    "v_pk_fma_f32 v[204:205], v[108:109], v[168:169], v[204:205] op_sel_hi:[1,0,1]\n"
    "v_pk_mul_f32 v[206:207], v[142:143], v[170:171] op_sel_hi:[1,0]\n"
    "v_pk_fma_f32 v[206:207], v[110:111], v[168:169], v[206:207] op_sel_hi:[1,0,1]\n"
    "v_pk_mul_f32 v[208:209], v[140:141], v[170:171] op_sel:[0,1] op_sel_hi:[1,1]\n"
    "v_pk_fma_f32 v[208:209], v[124:125], v[168:169], v[208:209] op_sel:[0,1,0] op_sel_hi:[1,1,1]\n"
    "v_pk_mul_f32 v[210:211], v[142:143], v[170:171] op_sel:[0,1] op_sel_hi:[1,1]\n"
    "v_pk_fma_f32 v[210:211], v[126:127], v[168:169], v[210:211] op_sel:[0,1,0] op_sel_hi:[1,1,1]\n"
    "v_cvt_pk_bf16_f32 v224, v204, v205\n"
    "v_cvt_pk_bf16_f32 v225, v206, v207\n"
    "v_cvt_pk_bf16_f32 v226, v208, v209\n"
    "v_cvt_pk_bf16_f32 v227, v210, v211\n"
    "s_xor_b32 s98, s6, 1\n"
    "s_mul_i32 s98, s98, s79\n"
    "s_add_u32 s98, s98, s53\n"
    "s_add_u32 s98, s98, s78\n"
    "v_permlane16_swap_b32 v224, v226\n"
    "v_permlane16_swap_b32 v225, v227\n"
    "buffer_store_dwordx4 v[224:227], v54, s[24:27], s98 offen sc1\n"
    "s_waitcnt lgkmcnt(1)\n"
    "v_pk_mul_f32 v[196:197], v[156:157], v[166:167] op_sel_hi:[1,0]\n"
    "v_pk_fma_f32 v[196:197], v[80:81], v[164:165], v[196:197] op_sel_hi:[1,0,1]\n"
    "v_pk_mul_f32 v[198:199], v[158:159], v[166:167] op_sel_hi:[1,0]\n"
    "v_pk_fma_f32 v[198:199], v[82:83], v[164:165], v[198:199] op_sel_hi:[1,0,1]\n"
    "v_pk_mul_f32 v[200:201], v[156:157], v[166:167] op_sel:[0,1] op_sel_hi:[1,1]\n"
    "v_pk_fma_f32 v[200:201], v[96:97], v[164:165], v[200:201] op_sel:[0,1,0] op_sel_hi:[1,1,1]\n"
    "v_pk_mul_f32 v[202:203], v[158:159], v[166:167] op_sel:[0,1] op_sel_hi:[1,1]\n"
    "v_pk_fma_f32 v[202:203], v[98:99], v[164:165], v[202:203] op_sel:[0,1,0] op_sel_hi:[1,1,1]\n"
    "v_cvt_pk_bf16_f32 v212, v196, v197\n"
    "v_cvt_pk_bf16_f32 v213, v198, v199\n"
    "v_cvt_pk_bf16_f32 v214, v200, v201\n"
    "v_cvt_pk_bf16_f32 v215, v202, v203\n"
    "s_xor_b32 s99, s6, 2\n"
    "s_mul_i32 s99, s99, s79\n"
    "s_add_u32 s99, s99, s53\n"
    "v_permlane16_swap_b32 v212, v214\n"
    "v_permlane16_swap_b32 v213, v215\n"
    "buffer_store_dwordx4 v[212:215], v54, s[24:27], s99 offen sc1\n"
    "v_pk_mul_f32 v[204:205], v[156:157], v[170:171] op_sel_hi:[1,0]\n"
    "v_pk_fma_f32 v[204:205], v[112:113], v[168:169], v[204:205] op_sel_hi:[1,0,1]\n"
    "v_pk_mul_f32 v[206:207], v[158:159], v[170:171] op_sel_hi:[1,0]\n"
    "v_pk_fma_f32 v[206:207], v[114:115], v[168:169], v[206:207] op_sel_hi:[1,0,1]\n"
    "v_pk_mul_f32 v[208:209], v[156:157], v[170:171] op_sel:[0,1] op_sel_hi:[1,1]\n"
    "v_pk_fma_f32 v[208:209], v[128:129], v[168:169], v[208:209] op_sel:[0,1,0] op_sel_hi:[1,1,1]\n"
    "v_pk_mul_f32 v[210:211], v[158:159], v[170:171] op_sel:[0,1] op_sel_hi:[1,1]\n"
    "v_pk_fma_f32 v[210:211], v[130:131], v[168:169], v[210:211] op_sel:[0,1,0] op_sel_hi:[1,1,1]\n"
    "v_cvt_pk_bf16_f32 v216, v204, v205\n"
    "v_cvt_pk_bf16_f32 v217, v206, v207\n"
    "v_cvt_pk_bf16_f32 v218, v208, v209\n"
    "v_cvt_pk_bf16_f32 v219, v210, v211\n"
    "s_xor_b32 s98, s6, 2\n"
    "s_mul_i32 s98, s98, s79\n"
    "s_add_u32 s98, s98, s53\n"
    "s_add_u32 s98, s98, s78\n"
    "v_permlane16_swap_b32 v216, v218\n"
    "v_permlane16_swap_b32 v217, v219\n"
    "buffer_store_dwordx4 v[216:219], v54, s[24:27], s98 offen sc1\n"
    "s_waitcnt lgkmcnt(0)\n"
    "v_pk_mul_f32 v[196:197], v[160:161], v[166:167] op_sel_hi:[1,0]\n"
    "v_pk_fma_f32 v[196:197], v[84:85], v[164:165], v[196:197] op_sel_hi:[1,0,1]\n"
    "v_pk_mul_f32 v[198:199], v[162:163], v[166:167] op_sel_hi:[1,0]\n"
    "v_pk_fma_f32 v[198:199], v[86:87], v[164:165], v[198:199] op_sel_hi:[1,0,1]\n"
    "v_pk_mul_f32 v[200:201], v[160:161], v[166:167] op_sel:[0,1] op_sel_hi:[1,1]\n"
    "v_pk_fma_f32 v[200:201], v[100:101], v[164:165], v[200:201] op_sel:[0,1,0] op_sel_hi:[1,1,1]\n"
    "v_pk_mul_f32 v[202:203], v[162:163], v[166:167] op_sel:[0,1] op_sel_hi:[1,1]\n"
    "v_pk_fma_f32 v[202:203], v[102:103], v[164:165], v[202:203] op_sel:[0,1,0] op_sel_hi:[1,1,1]\n"
    "v_cvt_pk_bf16_f32 v220, v196, v197\n"
    "v_cvt_pk_bf16_f32 v221, v198, v199\n"
    "v_cvt_pk_bf16_f32 v222, v200, v201\n"
    "v_cvt_pk_bf16_f32 v223, v202, v203\n"
    "s_xor_b32 s99, s6, 3\n"
    "s_mul_i32 s99, s99, s79\n"
    "s_add_u32 s99, s99, s53\n"
    "v_permlane16_swap_b32 v220, v222\n"
    "v_permlane16_swap_b32 v221, v223\n"
    "buffer_store_dwordx4 v[220:223], v54, s[24:27], s99 offen sc1\n"
    "v_pk_mul_f32 v[204:205], v[160:161], v[170:171] op_sel_hi:[1,0]\n"
    "v_pk_fma_f32 v[204:205], v[116:117], v[168:169], v[204:205] op_sel_hi:[1,0,1]\n"
    "v_pk_mul_f32 v[206:207], v[162:163], v[170:171] op_sel_hi:[1,0]\n"
    "v_pk_fma_f32 v[206:207], v[118:119], v[168:169], v[206:207] op_sel_hi:[1,0,1]\n"
    "v_pk_mul_f32 v[208:209], v[160:161], v[170:171] op_sel:[0,1] op_sel_hi:[1,1]\n"
    "v_pk_fma_f32 v[208:209], v[132:133], v[168:169], v[208:209] op_sel:[0,1,0] op_sel_hi:[1,1,1]\n"
    "v_pk_mul_f32 v[210:211], v[162:163], v[170:171] op_sel:[0,1] op_sel_hi:[1,1]\n"
    "v_pk_fma_f32 v[210:211], v[134:135], v[168:169], v[210:211] op_sel:[0,1,0] op_sel_hi:[1,1,1]\n"
    "v_cvt_pk_bf16_f32 v224, v204, v205\n"
    "v_cvt_pk_bf16_f32 v225, v206, v207\n"
    "v_cvt_pk_bf16_f32 v226, v208, v209\n"
    "v_cvt_pk_bf16_f32 v227, v210, v211\n"
    "s_xor_b32 s98, s6, 3\n"
    "s_mul_i32 s98, s98, s79\n"
    "s_add_u32 s98, s98, s53\n"
    "s_add_u32 s98, s98, s78\n"
    "v_permlane16_swap_b32 v224, v226\n"
    "v_permlane16_swap_b32 v225, v227\n"
    "buffer_store_dwordx4 v[224:227], v54, s[24:27], s98 offen sc1\n"
    "s_branch .Lfb_p1j_3\n"
    ".Lfb_p1v_1:\n"
    "; ---- P1 V (t~8198)\n"
    "s_waitcnt vmcnt(22)\n"
    "s_barrier\n"
    "ds_read_b64_tr_b16 a[32:33], v50\n"
    "ds_read_b64_tr_b16 a[34:35], v50 offset:1088\n"
    "ds_read_b128 a[0:3], v48\n"
    "ds_read_b64_tr_b16 a[36:37], v51\n"
    "ds_read_b64_tr_b16 a[38:39], v51 offset:1088\n"
    "ds_read_b128 a[4:7], v48 offset:2048\n"
    "ds_read_b64_tr_b16 a[40:41], v52\n"
    "ds_read_b64_tr_b16 a[42:43], v52 offset:1088\n"
    "ds_read_b128 a[8:11], v48 offset:4096\n"
    "ds_read_b64_tr_b16 a[44:45], v53\n"
    "ds_read_b64_tr_b16 a[46:47], v53 offset:1088\n"
    "ds_read_b128 a[12:15], v48 offset:6144\n"
    "s_waitcnt lgkmcnt(9)\n"
    "v_mfma_f32_16x16x32_bf16 v[72:75], a[0:3], a[32:35], 0\n"
    "s_mul_i32 s1, s64, 0x780\n"
    "s_add_u32 m0, s19, 0xcc00\n"
    "s_nop 0\n"
    "buffer_load_dwordx4 v36, s[28:31], s1 offen lds\n"
    "ds_read_b64_tr_b16 a[48:49], v50 offset:8704\n"
    "s_lshl_b32 s4, s16, 10\n"
    "s_waitcnt lgkmcnt(8)\n"
    "v_mfma_f32_16x16x32_bf16 v[76:79], a[0:3], a[36:39], 0\n"
    "ds_read_b64_tr_b16 a[50:51], v50 offset:9792\n"
    "s_add_u32 s4, s4, 0x21000\n"
    "s_waitcnt lgkmcnt(8)\n"
    "v_mfma_f32_16x16x32_bf16 v[88:91], a[4:7], a[32:35], 0\n"
    "ds_read_b128 a[16:19], v49\n"
    "s_and_b32 s6, s16, 2\n"
    "v_mfma_f32_16x16x32_bf16 v[92:95], a[4:7], a[36:39], 0\n"
    "ds_read_b64_tr_b16 a[52:53], v51 offset:8704\n"
    "buffer_load_dwordx4 v37, s[28:31], s1 offen offset:1088 lds\n"
    "v_mov_b32 v247, 0x40000000\n"
    "v_mfma_f32_16x16x32_bf16 v[136:139], v[144:147], a[32:35], 0\n"
    "ds_read_b64_tr_b16 a[54:55], v51 offset:9792\n"
    "v_cmp_gt_u32 vcc, 8, v235\n"
    "v_mfma_f32_16x16x32_bf16 v[140:143], v[144:147], a[36:39], 0\n"
    "ds_read_b128 a[20:23], v49 offset:2048\n"
    "v_add_u32 v245, s58, v0\n"
    "s_waitcnt lgkmcnt(10)\n"
    "v_mfma_f32_16x16x32_bf16 v[80:83], a[0:3], a[40:43], 0\n"
    "ds_read_b64_tr_b16 a[56:57], v52 offset:8704\n"
    "v_mov_b32 v246, 0\n"
    "buffer_load_dwordx4 v38, s[28:31], s1 offen offset:2176 lds\n"
    "s_waitcnt lgkmcnt(10)\n"
    "v_mfma_f32_16x16x32_bf16 v[104:107], a[8:11], a[32:35], 0\n"
    "ds_read_b64_tr_b16 a[58:59], v52 offset:9792\n"
    "v_cndmask_b32 v65, v247, v245, vcc\n"
    "v_mfma_f32_16x16x32_bf16 v[96:99], a[4:7], a[40:43], 0\n"
    "ds_read_b128 a[24:27], v49 offset:4096\n"
    "v_add_u32 v62, s52, v0\n"
    "v_mfma_f32_16x16x32_bf16 v[108:111], a[8:11], a[36:39], 0\n"
    "ds_read_b64_tr_b16 a[60:61], v53 offset:8704\n"
    "v_add_u32 v63, s56, v0\n"
    "v_mfma_f32_16x16x32_bf16 v[112:115], a[8:11], a[40:43], 0\n"
    "buffer_load_dwordx4 v39, s[28:31], s1 offen offset:3264 lds\n"
    "ds_read_b64_tr_b16 a[62:63], v53 offset:9792\n"
    "s_waitcnt lgkmcnt(12)\n"
    "v_mfma_f32_16x16x32_bf16 v[84:87], a[0:3], a[44:47], 0\n"
    "v_lshlrev_b32 v250, 10, v237\n"
    "ds_read_b128 a[28:31], v49 offset:6144\n"
    "s_waitcnt lgkmcnt(12)\n"
    "v_mfma_f32_16x16x32_bf16 v[120:123], a[12:15], a[32:35], 0\n"
    "v_lshl_add_u32 v250, v240, 7, v250\n"
    "v_mfma_f32_16x16x32_bf16 v[100:103], a[4:7], a[44:47], 0\n"
    "v_mfma_f32_16x16x32_bf16 v[124:127], a[12:15], a[36:39], 0\n"
    "s_add_u32 m0, s18, 0xc000\n"
    "s_nop 0\n"
    "buffer_load_dwordx4 v32, s[20:23], s64 offen lds\n"
    "v_mfma_f32_16x16x32_bf16 v[116:119], a[8:11], a[44:47], 0\n"
    "v_mfma_f32_16x16x32_bf16 v[128:131], a[12:15], a[40:43], 0\n"
    "v_mfma_f32_16x16x32_bf16 v[132:135], a[12:15], a[44:47], 0\n"
    "s_waitcnt vmcnt(13)\n"
    "s_waitcnt lgkmcnt(0)\n"
    "s_barrier\n"
    "v_readfirstlane_b32 s96, v254\n"
    "buffer_load_dwordx4 v33, s[20:23], s64 offen offset:1024 lds\n"
    "s_add_u32 s96, s96, 1\n"
    "v_mfma_f32_16x16x32_bf16 v[72:75], a[16:19], a[48:51], v[72:75]\n"
    "ds_read_b64_tr_b16 a[32:33], v50 offset:17408\n"
    "v_add_u32 v250, 0x21000, v250\n"
    "v_mfma_f32_16x16x32_bf16 v[76:79], a[16:19], a[52:55], v[76:79]\n"
    "ds_read_b64_tr_b16 a[34:35], v50 offset:18496\n"
    "v_lshlrev_b32 v245, 4, v241\n"
    "v_mfma_f32_16x16x32_bf16 v[88:91], a[20:23], a[48:51], v[88:91]\n"
    "ds_read_b128 a[0:3], v48 offset:16384\n"
    "v_add_u32 v245, s54, v245\n"
    "buffer_load_dwordx4 v34, s[20:23], s64 offen offset:2048 lds\n"
    "v_mfma_f32_16x16x32_bf16 v[92:95], a[20:23], a[52:55], v[92:95]\n"
    "ds_read_b64_tr_b16 a[36:37], v51 offset:17408\n"
    "v_lshrrev_b32 v246, 7, v245\n"
    "v_mfma_f32_16x16x32_bf16 v[136:139], v[144:147], a[48:51], v[136:139]\n"
    "ds_read_b64_tr_b16 a[38:39], v51 offset:18496\n"
    "v_and_b32 v245, 0x7f, v245\n"
    "v_mfma_f32_16x16x32_bf16 v[140:143], v[144:147], a[52:55], v[140:143]\n"
    "ds_read_b128 a[4:7], v48 offset:18432\n"
    "v_lshl_add_u32 v246, v246, 12, v250\n"
    "v_mfma_f32_16x16x32_bf16 v[80:83], a[16:19], a[56:59], v[80:83]\n"
    "buffer_load_dwordx4 v35, s[20:23], s64 offen offset:3072 lds\n"
    "s_add_u32 s64, s64, 0x80\n"
    "s_cmp_eq_u32 s64, 0x500\n"
    "s_cselect_b32 s64, 0, s64\n"
    "ds_read_b64_tr_b16 a[40:41], v52 offset:17408\n"
    "v_add_u32 v56, v245, v246\n"
    "v_mfma_f32_16x16x32_bf16 v[104:107], a[24:27], a[48:51], v[104:107]\n"
    "ds_read_b64_tr_b16 a[42:43], v52 offset:18496\n"
    "v_lshlrev_b32 v245, 4, v241\n"
    "v_mfma_f32_16x16x32_bf16 v[96:99], a[20:23], a[56:59], v[96:99]\n"
    "ds_read_b128 a[8:11], v48 offset:20480\n"
    "v_add_u32 v245, s54, v245\n"
    "v_mfma_f32_16x16x32_bf16 v[108:111], a[24:27], a[52:55], v[108:111]\n"
    "s_mul_i32 s1, s64, 0x780\n"
    "s_add_u32 m0, s19, 0\n"
    "s_nop 0\n"
    "buffer_load_dwordx4 v36, s[28:31], s1 offen lds\n"
    "ds_read_b64_tr_b16 a[44:45], v53 offset:17408\n"
    "v_add_u32 v245, 8, v245\n"
    "v_mfma_f32_16x16x32_bf16 v[112:115], a[24:27], a[56:59], v[112:115]\n"
    "ds_read_b64_tr_b16 a[46:47], v53 offset:18496\n"
    "v_mfma_f32_16x16x32_bf16 v[84:87], a[16:19], a[60:63], v[84:87]\n"
    "v_lshrrev_b32 v246, 7, v245\n"
    "ds_read_b128 a[12:15], v48 offset:22528\n"
    "v_mfma_f32_16x16x32_bf16 v[120:123], a[28:31], a[48:51], v[120:123]\n"
    "v_and_b32 v245, 0x7f, v245\n"
    "buffer_load_dwordx4 v37, s[28:31], s1 offen offset:1088 lds\n"
    "v_mfma_f32_16x16x32_bf16 v[100:103], a[20:23], a[60:63], v[100:103]\n"
    "v_mfma_f32_16x16x32_bf16 v[124:127], a[28:31], a[52:55], v[124:127]\n"
    "v_mfma_f32_16x16x32_bf16 v[116:119], a[24:27], a[60:63], v[116:119]\n"
    "v_mfma_f32_16x16x32_bf16 v[128:131], a[28:31], a[56:59], v[128:131]\n"
    "buffer_load_dwordx4 v38, s[28:31], s1 offen offset:2176 lds\n"
    "v_mfma_f32_16x16x32_bf16 v[132:135], a[28:31], a[60:63], v[132:135]\n"
    "s_waitcnt lgkmcnt(9)\n"
    "v_mfma_f32_16x16x32_bf16 v[72:75], a[0:3], a[32:35], v[72:75]\n"
    "ds_read_b64_tr_b16 a[48:49], v50 offset:26112\n"
    "v_lshl_add_u32 v246, v246, 12, v250\n"
    "s_waitcnt lgkmcnt(8)\n"
    "v_mfma_f32_16x16x32_bf16 v[76:79], a[0:3], a[36:39], v[76:79]\n"
    "ds_read_b64_tr_b16 a[50:51], v50 offset:27200\n"
    "v_add_u32 v57, v245, v246\n"
    "s_waitcnt lgkmcnt(8)\n"
    "v_mfma_f32_16x16x32_bf16 v[88:91], a[4:7], a[32:35], v[88:91]\n"
    "buffer_load_dwordx4 v39, s[28:31], s1 offen offset:3264 lds\n"
    "ds_read_b128 a[16:19], v49 offset:16384\n"
    "v_lshlrev_b32 v245, s69, v241\n"
    "v_mfma_f32_16x16x32_bf16 v[92:95], a[4:7], a[36:39], v[92:95]\n"
    "ds_read_b64_tr_b16 a[52:53], v51 offset:26112\n"
    "v_add_u32 v245, s54, v245\n"
    "v_mfma_f32_16x16x32_bf16 v[136:139], v[144:147], a[32:35], v[136:139]\n"
    "ds_read_b64_tr_b16 a[54:55], v51 offset:27200\n"
    "v_add_u32 v245, 64, v245\n"
    "v_mfma_f32_16x16x32_bf16 v[140:143], v[144:147], a[36:39], v[140:143]\n"
    "ds_read_b128 a[20:23], v49 offset:18432\n"
    "s_add_u32 m0, s18, 0\n"
    "s_nop 0\n"
    "buffer_load_dwordx4 v32, s[20:23], s64 offen lds\n"
    "v_lshrrev_b32 v246, 7, v245\n"
    "s_waitcnt lgkmcnt(10)\n"
    "v_mfma_f32_16x16x32_bf16 v[80:83], a[0:3], a[40:43], v[80:83]\n"
    "ds_read_b64_tr_b16 a[56:57], v52 offset:26112\n"
    "v_and_b32 v245, 0x7f, v245\n"
    "s_waitcnt lgkmcnt(10)\n"
    "v_mfma_f32_16x16x32_bf16 v[104:107], a[8:11], a[32:35], v[104:107]\n"
    "ds_read_b64_tr_b16 a[58:59], v52 offset:27200\n"
    "v_lshl_add_u32 v246, v246, 12, v250\n"
    "v_mfma_f32_16x16x32_bf16 v[96:99], a[4:7], a[40:43], v[96:99]\n"
    "ds_read_b128 a[24:27], v49 offset:20480\n"
    "v_add_u32 v58, v245, v246\n"
    "buffer_load_dwordx4 v33, s[20:23], s64 offen offset:1024 lds\n"
    "v_mfma_f32_16x16x32_bf16 v[108:111], a[8:11], a[36:39], v[108:111]\n"
    "ds_read_b64_tr_b16 a[60:61], v53 offset:26112\n"
    "v_lshlrev_b32 v245, 4, v241\n"
    "v_mfma_f32_16x16x32_bf16 v[112:115], a[8:11], a[40:43], v[112:115]\n"
    "ds_read_b64_tr_b16 a[62:63], v53 offset:27200\n"
    "s_waitcnt lgkmcnt(12)\n"
    "v_mfma_f32_16x16x32_bf16 v[84:87], a[0:3], a[44:47], v[84:87]\n"
    "v_add_u32 v245, s54, v245\n"
    "ds_read_b128 a[28:31], v49 offset:22528\n"
    "s_waitcnt lgkmcnt(12)\n"
    "v_mfma_f32_16x16x32_bf16 v[120:123], a[12:15], a[32:35], v[120:123]\n"
    "buffer_load_dwordx4 v34, s[20:23], s64 offen offset:2048 lds\n"
    "v_add_u32 v245, s92, v245\n"
    "v_mfma_f32_16x16x32_bf16 v[100:103], a[4:7], a[44:47], v[100:103]\n"
    "v_mfma_f32_16x16x32_bf16 v[124:127], a[12:15], a[36:39], v[124:127]\n"
    "v_mfma_f32_16x16x32_bf16 v[116:119], a[8:11], a[44:47], v[116:119]\n"
    "v_mfma_f32_16x16x32_bf16 v[128:131], a[12:15], a[40:43], v[128:131]\n"
    "buffer_load_dwordx4 v35, s[20:23], s64 offen offset:3072 lds\n"
    "s_add_u32 s64, s64, 0x80\n"
    "s_cmp_eq_u32 s64, 0x500\n"
    "s_cselect_b32 s64, 0, s64\n"
    "v_mfma_f32_16x16x32_bf16 v[132:135], a[12:15], a[44:47], v[132:135]\n"
    "s_waitcnt vmcnt(16)\n"
    "s_waitcnt lgkmcnt(0)\n"
    "s_barrier\n"
    "v_mfma_f32_16x16x32_bf16 v[72:75], a[16:19], a[48:51], v[72:75]\n"
    "ds_read_b64_tr_b16 a[32:33], v50 offset:34816\n"
    "v_lshrrev_b32 v246, 7, v245\n"
    "v_mfma_f32_16x16x32_bf16 v[76:79], a[16:19], a[52:55], v[76:79]\n"
    "ds_read_b64_tr_b16 a[34:35], v50 offset:35904\n"
    "s_mul_i32 s1, s64, 0x780\n"
    "s_add_u32 m0, s19, 0x4400\n"
    "s_nop 0\n"
    "buffer_load_dwordx4 v36, s[28:31], s1 offen lds\n"
    "v_and_b32 v245, 0x7f, v245\n"
    "v_mfma_f32_16x16x32_bf16 v[88:91], a[20:23], a[48:51], v[88:91]\n"
    "ds_read_b128 a[0:3], v48 offset:32768\n"
    "v_lshl_add_u32 v246, v246, 12, v250\n"
    "v_mfma_f32_16x16x32_bf16 v[92:95], a[20:23], a[52:55], v[92:95]\n"
    "ds_read_b64_tr_b16 a[36:37], v51 offset:34816\n"
    "v_add_u32 v59, v245, v246\n"
    "v_mfma_f32_16x16x32_bf16 v[136:139], v[144:147], a[48:51], v[136:139]\n"
    "ds_read_b64_tr_b16 a[38:39], v51 offset:35904\n"
    "v_lshlrev_b32 v245, s70, v241\n"
    "buffer_load_dwordx4 v37, s[28:31], s1 offen offset:1088 lds\n"
    "v_mfma_f32_16x16x32_bf16 v[140:143], v[144:147], a[52:55], v[140:143]\n"
    "ds_read_b128 a[4:7], v48 offset:34816\n"
    "v_add_u32 v245, s54, v245\n"
    "v_mfma_f32_16x16x32_bf16 v[80:83], a[16:19], a[56:59], v[80:83]\n"
    "ds_read_b64_tr_b16 a[40:41], v52 offset:34816\n"
    "v_add_u32 v245, s93, v245\n"
    "v_mfma_f32_16x16x32_bf16 v[104:107], a[24:27], a[48:51], v[104:107]\n"
    "ds_read_b64_tr_b16 a[42:43], v52 offset:35904\n"
    "v_lshrrev_b32 v246, 7, v245\n"
    "v_mfma_f32_16x16x32_bf16 v[96:99], a[20:23], a[56:59], v[96:99]\n"
    "buffer_load_dwordx4 v38, s[28:31], s1 offen offset:2176 lds\n"
    "ds_read_b128 a[8:11], v48 offset:36864\n"
    "v_and_b32 v245, 0x7f, v245\n"
    "v_mfma_f32_16x16x32_bf16 v[108:111], a[24:27], a[52:55], v[108:111]\n"
    "ds_read_b64_tr_b16 a[44:45], v53 offset:34816\n"
    "v_lshl_add_u32 v246, v246, 12, v250\n"
    "v_mfma_f32_16x16x32_bf16 v[112:115], a[24:27], a[56:59], v[112:115]\n"
    "ds_read_b64_tr_b16 a[46:47], v53 offset:35904\n"
    "v_mfma_f32_16x16x32_bf16 v[84:87], a[16:19], a[60:63], v[84:87]\n"
    "v_add_u32 v60, v245, v246\n"
    "buffer_load_dwordx4 v39, s[28:31], s1 offen offset:3264 lds\n"
    "ds_read_b128 a[12:15], v48 offset:38912\n"
    "v_mfma_f32_16x16x32_bf16 v[120:123], a[28:31], a[48:51], v[120:123]\n"
    "v_lshlrev_b32 v245, 3, v241\n"
    "v_mfma_f32_16x16x32_bf16 v[100:103], a[20:23], a[60:63], v[100:103]\n"
    "v_mfma_f32_16x16x32_bf16 v[124:127], a[28:31], a[52:55], v[124:127]\n"
    "v_mfma_f32_16x16x32_bf16 v[116:119], a[24:27], a[60:63], v[116:119]\n"
    "s_add_u32 m0, s18, 0x4000\n"
    "s_nop 0\n"
    "buffer_load_dwordx4 v32, s[20:23], s64 offen lds\n"
    "v_mfma_f32_16x16x32_bf16 v[128:131], a[28:31], a[56:59], v[128:131]\n"
    "v_mfma_f32_16x16x32_bf16 v[132:135], a[28:31], a[60:63], v[132:135]\n"
    "s_waitcnt lgkmcnt(9)\n"
    "v_mfma_f32_16x16x32_bf16 v[72:75], a[0:3], a[32:35], v[72:75]\n"
    "ds_read_b64_tr_b16 a[48:49], v50 offset:43520\n"
    "v_add_u32 v245, s54, v245\n"
    "s_waitcnt lgkmcnt(8)\n"
    "v_mfma_f32_16x16x32_bf16 v[76:79], a[0:3], a[36:39], v[76:79]\n"
    "buffer_load_dwordx4 v33, s[20:23], s64 offen offset:1024 lds\n"
    "ds_read_b64_tr_b16 a[50:51], v50 offset:44608\n"
    "v_add_u32 v245, s94, v245\n"
    "s_waitcnt lgkmcnt(8)\n"
    "v_mfma_f32_16x16x32_bf16 v[88:91], a[4:7], a[32:35], v[88:91]\n"
    "ds_read_b128 a[16:19], v49 offset:32768\n"
    "v_lshrrev_b32 v246, 7, v245\n"
    "v_mfma_f32_16x16x32_bf16 v[92:95], a[4:7], a[36:39], v[92:95]\n"
    "ds_read_b64_tr_b16 a[52:53], v51 offset:43520\n"
    "v_and_b32 v245, 0x7f, v245\n"
    "v_mfma_f32_16x16x32_bf16 v[136:139], v[144:147], a[32:35], v[136:139]\n"
    "ds_read_b64_tr_b16 a[54:55], v51 offset:44608\n"
    "buffer_load_dwordx4 v34, s[20:23], s64 offen offset:2048 lds\n"
    "v_lshl_add_u32 v246, v246, 12, v250\n"
    "v_mfma_f32_16x16x32_bf16 v[140:143], v[144:147], a[36:39], v[140:143]\n"
    "ds_read_b128 a[20:23], v49 offset:34816\n"
    "v_add_u32 v61, v245, v246\n"
    "s_waitcnt lgkmcnt(10)\n"
    "v_mfma_f32_16x16x32_bf16 v[80:83], a[0:3], a[40:43], v[80:83]\n"
    "ds_read_b64_tr_b16 a[56:57], v52 offset:43520\n"
    "v_and_b32 v246, 1, v244\n"
    "s_waitcnt lgkmcnt(10)\n"
    "v_mfma_f32_16x16x32_bf16 v[104:107], a[8:11], a[32:35], v[104:107]\n"
    "ds_read_b64_tr_b16 a[58:59], v52 offset:44608\n"
    "v_and_b32 v247, 2, v244\n"
    "buffer_load_dwordx4 v35, s[20:23], s64 offen offset:3072 lds\n"
    "s_add_u32 s64, s64, 0x80\n"
    "s_cmp_eq_u32 s64, 0x500\n"
    "s_cselect_b32 s64, 0, s64\n"
    "v_mfma_f32_16x16x32_bf16 v[96:99], a[4:7], a[40:43], v[96:99]\n"
    "ds_read_b128 a[24:27], v49 offset:36864\n"
    "v_lshlrev_b32 v245, 8, v246\n"
    "v_mfma_f32_16x16x32_bf16 v[108:111], a[8:11], a[36:39], v[108:111]\n"
    "ds_read_b64_tr_b16 a[60:61], v53 offset:43520\n"
    "v_lshl_add_u32 v245, v247, 6, v245\n"
    "v_mfma_f32_16x16x32_bf16 v[112:115], a[8:11], a[40:43], v[112:115]\n"
    "ds_read_b64_tr_b16 a[62:63], v53 offset:44608\n"
    "s_waitcnt lgkmcnt(12)\n"
    "v_mfma_f32_16x16x32_bf16 v[84:87], a[0:3], a[44:47], v[84:87]\n"
    "v_add_u32 v245, 0x27000, v245\n"
    "ds_read_b128 a[28:31], v49 offset:38912\n"
    "s_waitcnt lgkmcnt(12)\n"
    "v_mfma_f32_16x16x32_bf16 v[120:123], a[12:15], a[32:35], v[120:123]\n"
    "v_lshl_add_u32 v66, v236, 2, v245\n"
    "v_mfma_f32_16x16x32_bf16 v[100:103], a[4:7], a[44:47], v[100:103]\n"
    "v_mfma_f32_16x16x32_bf16 v[124:127], a[12:15], a[36:39], v[124:127]\n"
    "v_mfma_f32_16x16x32_bf16 v[116:119], a[8:11], a[44:47], v[116:119]\n"
    "v_mfma_f32_16x16x32_bf16 v[128:131], a[12:15], a[40:43], v[128:131]\n"
    "v_mfma_f32_16x16x32_bf16 v[132:135], a[12:15], a[44:47], v[132:135]\n"
    "s_waitcnt vmcnt(16)\n"
    "s_waitcnt lgkmcnt(0)\n"
    "s_barrier\n"
    "v_mfma_f32_16x16x32_bf16 v[72:75], a[16:19], a[48:51], v[72:75]\n"
    "s_mul_i32 s1, s64, 0x780\n"
    "s_add_u32 m0, s19, 0x8800\n"
    "s_nop 0\n"
    "buffer_load_dwordx4 v36, s[28:31], s1 offen lds\n"
    "ds_read_b64_tr_b16 a[32:33], v50 offset:52224\n"
    "v_mfma_f32_16x16x32_bf16 v[76:79], a[16:19], a[52:55], v[76:79]\n"
    "v_xor_b32 v67, 0x80, v66\n"
    "ds_read_b64_tr_b16 a[34:35], v50 offset:53312\n"
    "v_mfma_f32_16x16x32_bf16 v[88:91], a[20:23], a[48:51], v[88:91]\n"
    "v_lshlrev_b32 v245, 7, v247\n"
    "v_mfma_f32_16x16x32_bf16 v[92:95], a[20:23], a[52:55], v[92:95]\n"
    "ds_read_b128 a[0:3], v48 offset:49152\n"
    "v_add_u32 v245, 0x27200, v245\n"
    "buffer_load_dwordx4 v37, s[28:31], s1 offen offset:1088 lds\n"
    "v_mfma_f32_16x16x32_bf16 v[136:139], v[144:147], a[48:51], v[136:139]\n"
    "ds_read_b64_tr_b16 a[36:37], v51 offset:52224\n"
    "v_mfma_f32_16x16x32_bf16 v[140:143], v[144:147], a[52:55], v[140:143]\n"
    "v_add_u32 v68, v245, v25\n"
    "ds_read_b64_tr_b16 a[38:39], v51 offset:53312\n"
    "v_mfma_f32_16x16x32_bf16 v[80:83], a[16:19], a[56:59], v[80:83]\n"
    "v_lshl_add_u32 v69, v237, 4, v245\n"
    "v_mfma_f32_16x16x32_bf16 v[104:107], a[24:27], a[48:51], v[104:107]\n"
    "buffer_load_dwordx4 v38, s[28:31], s1 offen offset:2176 lds\n"
    "ds_read_b128 a[4:7], v48 offset:51200\n"
    "v_mul_u32_u24 v245, 0x2800, v237\n"
    "v_mfma_f32_16x16x32_bf16 v[96:99], a[20:23], a[56:59], v[96:99]\n"
    "ds_read_b64_tr_b16 a[40:41], v52 offset:52224\n"
    "v_mfma_f32_16x16x32_bf16 v[108:111], a[24:27], a[52:55], v[108:111]\n"
    "v_add_u32 v54, v245, v248\n"
    "ds_read_b64_tr_b16 a[42:43], v52 offset:53312\n"
    "v_mfma_f32_16x16x32_bf16 v[112:115], a[24:27], a[56:59], v[112:115]\n"
    "ds_read_b128 a[8:11], v48 offset:53248\n"
    "buffer_load_dwordx4 v39, s[28:31], s1 offen offset:3264 lds\n"
    "v_mfma_f32_16x16x32_bf16 v[84:87], a[16:19], a[60:63], v[84:87]\n"
    "ds_read_b64_tr_b16 a[44:45], v53 offset:52224\n"
    "ds_read_b64_tr_b16 a[46:47], v53 offset:53312\n"
    "v_mfma_f32_16x16x32_bf16 v[120:123], a[28:31], a[48:51], v[120:123]\n"
    "ds_read_b128 a[12:15], v48 offset:55296\n"
    "v_mfma_f32_16x16x32_bf16 v[100:103], a[20:23], a[60:63], v[100:103]\n"
    "v_mfma_f32_16x16x32_bf16 v[124:127], a[28:31], a[52:55], v[124:127]\n"
    "s_add_u32 m0, s18, 0x8000\n"
    "s_nop 0\n"
    "buffer_load_dwordx4 v32, s[20:23], s64 offen lds\n"
    "v_mfma_f32_16x16x32_bf16 v[116:119], a[24:27], a[60:63], v[116:119]\n"
    "v_mfma_f32_16x16x32_bf16 v[128:131], a[28:31], a[56:59], v[128:131]\n"
    "v_mfma_f32_16x16x32_bf16 v[132:135], a[28:31], a[60:63], v[132:135]\n"
    "s_waitcnt lgkmcnt(9)\n"
    "v_mfma_f32_16x16x32_bf16 v[72:75], a[0:3], a[32:35], v[72:75]\n"
    "buffer_load_dwordx4 v33, s[20:23], s64 offen offset:1024 lds\n"
    "ds_read_b64_tr_b16 a[48:49], v50 offset:60928\n"
    "s_waitcnt lgkmcnt(8)\n"
    "v_mfma_f32_16x16x32_bf16 v[76:79], a[0:3], a[36:39], v[76:79]\n"
    "ds_read_b64_tr_b16 a[50:51], v50 offset:62016\n"
    "s_waitcnt lgkmcnt(8)\n"
    "v_mfma_f32_16x16x32_bf16 v[88:91], a[4:7], a[32:35], v[88:91]\n"
    "ds_read_b128 a[16:19], v49 offset:49152\n"
    "v_mfma_f32_16x16x32_bf16 v[92:95], a[4:7], a[36:39], v[92:95]\n"
    "ds_read_b64_tr_b16 a[52:53], v51 offset:60928\n"
    "v_mfma_f32_16x16x32_bf16 v[136:139], v[144:147], a[32:35], v[136:139]\n"
    "buffer_load_dwordx4 v34, s[20:23], s64 offen offset:2048 lds\n"
    "ds_read_b64_tr_b16 a[54:55], v51 offset:62016\n"
    "v_mfma_f32_16x16x32_bf16 v[140:143], v[144:147], a[36:39], v[140:143]\n"
    "ds_read_b128 a[20:23], v49 offset:51200\n"
    "s_waitcnt lgkmcnt(10)\n"
    "v_mfma_f32_16x16x32_bf16 v[80:83], a[0:3], a[40:43], v[80:83]\n"
    "ds_read_b64_tr_b16 a[56:57], v52 offset:60928\n"
    "s_waitcnt lgkmcnt(10)\n"
    "v_mfma_f32_16x16x32_bf16 v[104:107], a[8:11], a[32:35], v[104:107]\n"
    "ds_read_b64_tr_b16 a[58:59], v52 offset:62016\n"
    "v_mfma_f32_16x16x32_bf16 v[96:99], a[4:7], a[40:43], v[96:99]\n"
    "buffer_load_dwordx4 v35, s[20:23], s64 offen offset:3072 lds\n"
    "s_add_u32 s64, s64, 0x80\n"
    "s_cmp_eq_u32 s64, 0x500\n"
    "s_cselect_b32 s64, 0, s64\n"
    "ds_read_b128 a[24:27], v49 offset:53248\n"
    "v_mfma_f32_16x16x32_bf16 v[108:111], a[8:11], a[36:39], v[108:111]\n"
    "ds_read_b64_tr_b16 a[60:61], v53 offset:60928\n"
    "v_mfma_f32_16x16x32_bf16 v[112:115], a[8:11], a[40:43], v[112:115]\n"
    "ds_read_b64_tr_b16 a[62:63], v53 offset:62016\n"
    "s_waitcnt lgkmcnt(12)\n"
    "v_mfma_f32_16x16x32_bf16 v[84:87], a[0:3], a[44:47], v[84:87]\n"
    "s_add_u32 m0, s4, 0\n"
    "s_nop 0\n"
    "buffer_load_dwordx4 v55, s[32:35], 0 offen lds\n"
    "ds_read_b128 a[28:31], v49 offset:55296\n"
    "s_waitcnt lgkmcnt(12)\n"
    "v_mfma_f32_16x16x32_bf16 v[120:123], a[12:15], a[32:35], v[120:123]\n"
    "v_mfma_f32_16x16x32_bf16 v[100:103], a[4:7], a[44:47], v[100:103]\n"
    "v_mfma_f32_16x16x32_bf16 v[124:127], a[12:15], a[36:39], v[124:127]\n"
    "v_mfma_f32_16x16x32_bf16 v[116:119], a[8:11], a[44:47], v[116:119]\n"
    "s_add_u32 m0, s4, 0xf80\n"
    "s_nop 0\n"
    "buffer_load_dwordx4 v55, s[32:35], 0 offen offset:128 lds\n"
    "v_mfma_f32_16x16x32_bf16 v[128:131], a[12:15], a[40:43], v[128:131]\n"
    "v_mfma_f32_16x16x32_bf16 v[132:135], a[12:15], a[44:47], v[132:135]\n"
    "s_add_u32 m0, s4, 0x1f00\n"
    "s_nop 0\n"
    "buffer_load_dwordx4 v55, s[32:35], 0 offen offset:256 lds\n"
    "s_add_u32 m0, s4, 0x2e80\n"
    "s_nop 0\n"
    "buffer_load_dwordx4 v55, s[32:35], 0 offen offset:384 lds\n"
    "s_add_u32 m0, s4, 0x3e00\n"
    "s_nop 0\n"
    "buffer_load_dwordx4 v55, s[32:35], 0 offen offset:512 lds\n"
    "s_add_u32 m0, s4, 0x4d80\n"
    "s_nop 0\n"
    "buffer_load_dwordx4 v55, s[32:35], 0 offen offset:640 lds\n"
    "s_waitcnt vmcnt(22)\n"
    "s_waitcnt lgkmcnt(0)\n"
    "s_barrier\n"
    "v_mfma_f32_16x16x32_bf16 v[72:75], a[16:19], a[48:51], v[72:75]\n"
    "ds_read_b64_tr_b16 a[32:33], v50\n"
    "v_mfma_f32_16x16x32_bf16 v[76:79], a[16:19], a[52:55], v[76:79]\n"
    "ds_read_b64_tr_b16 a[34:35], v50 offset:1088\n"
    "v_mfma_f32_16x16x32_bf16 v[88:91], a[20:23], a[48:51], v[88:91]\n"
    "ds_read_b128 a[0:3], v48\n"
    "s_mul_i32 s1, s64, 0x780\n"
    "s_add_u32 m0, s19, 0xcc00\n"
    "s_nop 0\n"
    "buffer_load_dwordx4 v36, s[28:31], s1 offen lds\n"
    "v_mfma_f32_16x16x32_bf16 v[92:95], a[20:23], a[52:55], v[92:95]\n"
    "ds_read_b64_tr_b16 a[36:37], v51\n"
    "v_mfma_f32_16x16x32_bf16 v[136:139], v[144:147], a[48:51], v[136:139]\n"
    "ds_read_b64_tr_b16 a[38:39], v51 offset:1088\n"
    "v_mfma_f32_16x16x32_bf16 v[140:143], v[144:147], a[52:55], v[140:143]\n"
    "ds_read_b128 a[4:7], v48 offset:2048\n"
    "v_mfma_f32_16x16x32_bf16 v[80:83], a[16:19], a[56:59], v[80:83]\n"
    "buffer_load_dwordx4 v37, s[28:31], s1 offen offset:1088 lds\n"
    "ds_read_b64_tr_b16 a[40:41], v52\n"
    "v_mfma_f32_16x16x32_bf16 v[104:107], a[24:27], a[48:51], v[104:107]\n"
    "ds_read_b64_tr_b16 a[42:43], v52 offset:1088\n"
    "v_mfma_f32_16x16x32_bf16 v[96:99], a[20:23], a[56:59], v[96:99]\n"
    "ds_read_b128 a[8:11], v48 offset:4096\n"
    "v_mfma_f32_16x16x32_bf16 v[108:111], a[24:27], a[52:55], v[108:111]\n"
    "ds_read_b64_tr_b16 a[44:45], v53\n"
    "v_mfma_f32_16x16x32_bf16 v[112:115], a[24:27], a[56:59], v[112:115]\n"
    "buffer_load_dwordx4 v38, s[28:31], s1 offen offset:2176 lds\n"
    "ds_read_b64_tr_b16 a[46:47], v53 offset:1088\n"
    "v_mfma_f32_16x16x32_bf16 v[84:87], a[16:19], a[60:63], v[84:87]\n"
    "ds_read_b128 a[12:15], v48 offset:6144\n"
    "v_mfma_f32_16x16x32_bf16 v[120:123], a[28:31], a[48:51], v[120:123]\n"
    "v_mfma_f32_16x16x32_bf16 v[100:103], a[20:23], a[60:63], v[100:103]\n"
    "v_mfma_f32_16x16x32_bf16 v[124:127], a[28:31], a[52:55], v[124:127]\n"
    "buffer_load_dwordx4 v39, s[28:31], s1 offen offset:3264 lds\n"
    "v_mfma_f32_16x16x32_bf16 v[116:119], a[24:27], a[60:63], v[116:119]\n"
    "v_mfma_f32_16x16x32_bf16 v[128:131], a[28:31], a[56:59], v[128:131]\n"
    "v_mfma_f32_16x16x32_bf16 v[132:135], a[28:31], a[60:63], v[132:135]\n"
    "s_waitcnt lgkmcnt(9)\n"
    "v_mfma_f32_16x16x32_bf16 v[72:75], a[0:3], a[32:35], v[72:75]\n"
    "s_add_u32 m0, s18, 0xc000\n"
    "s_nop 0\n"
    "buffer_load_dwordx4 v32, s[20:23], s64 offen lds\n"
    "ds_read_b64_tr_b16 a[48:49], v50 offset:8704\n"
    "s_waitcnt lgkmcnt(8)\n"
    "v_mfma_f32_16x16x32_bf16 v[76:79], a[0:3], a[36:39], v[76:79]\n"
    "ds_read_b64_tr_b16 a[50:51], v50 offset:9792\n"
    "s_waitcnt lgkmcnt(8)\n"
    "v_mfma_f32_16x16x32_bf16 v[88:91], a[4:7], a[32:35], v[88:91]\n"
    "ds_read_b128 a[16:19], v49\n"
    "v_mfma_f32_16x16x32_bf16 v[92:95], a[4:7], a[36:39], v[92:95]\n"
    "ds_read_b64_tr_b16 a[52:53], v51 offset:8704\n"
    "v_mfma_f32_16x16x32_bf16 v[136:139], v[144:147], a[32:35], v[136:139]\n"
    "buffer_load_dwordx4 v33, s[20:23], s64 offen offset:1024 lds\n"
    "ds_read_b64_tr_b16 a[54:55], v51 offset:9792\n"
    "v_mfma_f32_16x16x32_bf16 v[140:143], v[144:147], a[36:39], v[140:143]\n"
    "ds_read_b128 a[20:23], v49 offset:2048\n"
    "s_waitcnt lgkmcnt(10)\n"
    "v_mfma_f32_16x16x32_bf16 v[80:83], a[0:3], a[40:43], v[80:83]\n"
    "ds_read_b64_tr_b16 a[56:57], v52 offset:8704\n"
    "s_waitcnt lgkmcnt(10)\n"
    "v_mfma_f32_16x16x32_bf16 v[104:107], a[8:11], a[32:35], v[104:107]\n"
    "ds_read_b64_tr_b16 a[58:59], v52 offset:9792\n"
    "v_mfma_f32_16x16x32_bf16 v[96:99], a[4:7], a[40:43], v[96:99]\n"
    "buffer_load_dwordx4 v34, s[20:23], s64 offen offset:2048 lds\n"
    "ds_read_b128 a[24:27], v49 offset:4096\n"
    "v_mfma_f32_16x16x32_bf16 v[108:111], a[8:11], a[36:39], v[108:111]\n"
    "ds_read_b64_tr_b16 a[60:61], v53 offset:8704\n"
    "v_mfma_f32_16x16x32_bf16 v[112:115], a[8:11], a[40:43], v[112:115]\n"
    "ds_read_b64_tr_b16 a[62:63], v53 offset:9792\n"
    "s_waitcnt lgkmcnt(12)\n"
    "v_mfma_f32_16x16x32_bf16 v[84:87], a[0:3], a[44:47], v[84:87]\n"
    "ds_read_b128 a[28:31], v49 offset:6144\n"
    "s_waitcnt lgkmcnt(12)\n"
    "v_mfma_f32_16x16x32_bf16 v[120:123], a[12:15], a[32:35], v[120:123]\n"
    "buffer_load_dwordx4 v35, s[20:23], s64 offen offset:3072 lds\n"
    "s_add_u32 s64, s64, 0x80\n"
    "s_cmp_eq_u32 s64, 0x500\n"
    "s_cselect_b32 s64, 0, s64\n"
    "v_mfma_f32_16x16x32_bf16 v[100:103], a[4:7], a[44:47], v[100:103]\n"
    "v_mfma_f32_16x16x32_bf16 v[124:127], a[12:15], a[36:39], v[124:127]\n"
    "v_mfma_f32_16x16x32_bf16 v[116:119], a[8:11], a[44:47], v[116:119]\n"
    "v_mfma_f32_16x16x32_bf16 v[128:131], a[12:15], a[40:43], v[128:131]\n"
    "v_mfma_f32_16x16x32_bf16 v[132:135], a[12:15], a[44:47], v[132:135]\n"
    "s_waitcnt vmcnt(22)\n"
    "s_waitcnt lgkmcnt(0)\n"
    "s_barrier\n"
    "v_mfma_f32_16x16x32_bf16 v[72:75], a[16:19], a[48:51], v[72:75]\n"
    "s_mul_i32 s1, s64, 0x780\n"
    "s_add_u32 m0, s19, 0\n"
    "s_nop 0\n"
    "buffer_load_dwordx4 v36, s[28:31], s1 offen lds\n"
    "ds_read_b64_tr_b16 a[32:33], v50 offset:17408\n"
    "v_mfma_f32_16x16x32_bf16 v[76:79], a[16:19], a[52:55], v[76:79]\n"
    "ds_read_b64_tr_b16 a[34:35], v50 offset:18496\n"
    "v_mfma_f32_16x16x32_bf16 v[88:91], a[20:23], a[48:51], v[88:91]\n"
    "ds_read_b128 a[0:3], v48 offset:16384\n"
    "v_mfma_f32_16x16x32_bf16 v[92:95], a[20:23], a[52:55], v[92:95]\n"
    "ds_read_b64_tr_b16 a[36:37], v51 offset:17408\n"
    "v_mfma_f32_16x16x32_bf16 v[136:139], v[144:147], a[48:51], v[136:139]\n"
    "buffer_load_dwordx4 v37, s[28:31], s1 offen offset:1088 lds\n"
    "ds_read_b64_tr_b16 a[38:39], v51 offset:18496\n"
    "v_mfma_f32_16x16x32_bf16 v[140:143], v[144:147], a[52:55], v[140:143]\n"
    "ds_read_b128 a[4:7], v48 offset:18432\n"
    "v_mfma_f32_16x16x32_bf16 v[80:83], a[16:19], a[56:59], v[80:83]\n"
    "ds_read_b64_tr_b16 a[40:41], v52 offset:17408\n"
    "v_mfma_f32_16x16x32_bf16 v[104:107], a[24:27], a[48:51], v[104:107]\n"
    "ds_read_b64_tr_b16 a[42:43], v52 offset:18496\n"
    "v_mfma_f32_16x16x32_bf16 v[96:99], a[20:23], a[56:59], v[96:99]\n"
    "buffer_load_dwordx4 v38, s[28:31], s1 offen offset:2176 lds\n"
    "ds_read_b128 a[8:11], v48 offset:20480\n"
    "v_mfma_f32_16x16x32_bf16 v[108:111], a[24:27], a[52:55], v[108:111]\n"
    "ds_read_b64_tr_b16 a[44:45], v53 offset:17408\n"
    "v_mfma_f32_16x16x32_bf16 v[112:115], a[24:27], a[56:59], v[112:115]\n"
    "ds_read_b64_tr_b16 a[46:47], v53 offset:18496\n"
    "v_mfma_f32_16x16x32_bf16 v[84:87], a[16:19], a[60:63], v[84:87]\n"
    "ds_read_b128 a[12:15], v48 offset:22528\n"
    "v_mfma_f32_16x16x32_bf16 v[120:123], a[28:31], a[48:51], v[120:123]\n"
    "buffer_load_dwordx4 v39, s[28:31], s1 offen offset:3264 lds\n"
    "v_mfma_f32_16x16x32_bf16 v[100:103], a[20:23], a[60:63], v[100:103]\n"
    "v_mfma_f32_16x16x32_bf16 v[124:127], a[28:31], a[52:55], v[124:127]\n"
    "v_mfma_f32_16x16x32_bf16 v[116:119], a[24:27], a[60:63], v[116:119]\n"
    "v_mfma_f32_16x16x32_bf16 v[128:131], a[28:31], a[56:59], v[128:131]\n"
    "s_add_u32 m0, s18, 0\n"
    "s_nop 0\n"
    "buffer_load_dwordx4 v32, s[20:23], s64 offen lds\n"
    "v_mfma_f32_16x16x32_bf16 v[132:135], a[28:31], a[60:63], v[132:135]\n"
    "s_waitcnt lgkmcnt(9)\n"
    "v_mfma_f32_16x16x32_bf16 v[72:75], a[0:3], a[32:35], v[72:75]\n"
    "ds_read_b64_tr_b16 a[48:49], v50 offset:26112\n"
    "s_waitcnt lgkmcnt(8)\n"
    "v_mfma_f32_16x16x32_bf16 v[76:79], a[0:3], a[36:39], v[76:79]\n"
    "ds_read_b64_tr_b16 a[50:51], v50 offset:27200\n"
    "s_waitcnt lgkmcnt(8)\n"
    "v_mfma_f32_16x16x32_bf16 v[88:91], a[4:7], a[32:35], v[88:91]\n"
    "buffer_load_dwordx4 v33, s[20:23], s64 offen offset:1024 lds\n"
    "ds_read_b128 a[16:19], v49 offset:16384\n"
    "v_mfma_f32_16x16x32_bf16 v[92:95], a[4:7], a[36:39], v[92:95]\n"
    "ds_read_b64_tr_b16 a[52:53], v51 offset:26112\n"
    "v_mfma_f32_16x16x32_bf16 v[136:139], v[144:147], a[32:35], v[136:139]\n"
    "ds_read_b64_tr_b16 a[54:55], v51 offset:27200\n"
    "v_mfma_f32_16x16x32_bf16 v[140:143], v[144:147], a[36:39], v[140:143]\n"
    "ds_read_b128 a[20:23], v49 offset:18432\n"
    "s_waitcnt lgkmcnt(10)\n"
    "v_mfma_f32_16x16x32_bf16 v[80:83], a[0:3], a[40:43], v[80:83]\n"
    "buffer_load_dwordx4 v34, s[20:23], s64 offen offset:2048 lds\n"
    "ds_read_b64_tr_b16 a[56:57], v52 offset:26112\n"
    "s_waitcnt lgkmcnt(10)\n"
    "v_mfma_f32_16x16x32_bf16 v[104:107], a[8:11], a[32:35], v[104:107]\n"
    "ds_read_b64_tr_b16 a[58:59], v52 offset:27200\n"
    "v_mfma_f32_16x16x32_bf16 v[96:99], a[4:7], a[40:43], v[96:99]\n"
    "ds_read_b128 a[24:27], v49 offset:20480\n"
    "v_mfma_f32_16x16x32_bf16 v[108:111], a[8:11], a[36:39], v[108:111]\n"
    "ds_read_b64_tr_b16 a[60:61], v53 offset:26112\n"
    "v_mfma_f32_16x16x32_bf16 v[112:115], a[8:11], a[40:43], v[112:115]\n"
    "buffer_load_dwordx4 v35, s[20:23], s64 offen offset:3072 lds\n"
    "s_add_u32 s64, s64, 0x80\n"
    "s_cmp_eq_u32 s64, 0x500\n"
    "s_cselect_b32 s64, 0, s64\n"
    "ds_read_b64_tr_b16 a[62:63], v53 offset:27200\n"
    "s_waitcnt lgkmcnt(12)\n"
    "v_mfma_f32_16x16x32_bf16 v[84:87], a[0:3], a[44:47], v[84:87]\n"
    "ds_read_b128 a[28:31], v49 offset:22528\n"
    "s_waitcnt lgkmcnt(12)\n"
    "v_mfma_f32_16x16x32_bf16 v[120:123], a[12:15], a[32:35], v[120:123]\n"
    "v_mfma_f32_16x16x32_bf16 v[100:103], a[4:7], a[44:47], v[100:103]\n"
    "buffer_store_dwordx4 v[144:147], v65, s[24:27], 0 offen sc1\n"
    "v_mfma_f32_16x16x32_bf16 v[124:127], a[12:15], a[36:39], v[124:127]\n"
    "v_mfma_f32_16x16x32_bf16 v[116:119], a[8:11], a[44:47], v[116:119]\n"
    "v_mfma_f32_16x16x32_bf16 v[128:131], a[12:15], a[40:43], v[128:131]\n"
    "v_mfma_f32_16x16x32_bf16 v[132:135], a[12:15], a[44:47], v[132:135]\n"
    "s_waitcnt vmcnt(23)\n"
    "s_waitcnt lgkmcnt(0)\n"
    "s_barrier\n"
    "buffer_load_dwordx4 v[152:155], v63, s[24:27], 0 offen sc1\n"
    "v_mfma_f32_16x16x32_bf16 v[72:75], a[16:19], a[48:51], v[72:75]\n"
    "ds_read_b64_tr_b16 a[32:33], v50 offset:34816\n"
    "v_mfma_f32_16x16x32_bf16 v[76:79], a[16:19], a[52:55], v[76:79]\n"
    "ds_read_b64_tr_b16 a[34:35], v50 offset:35904\n"
    "v_mfma_f32_16x16x32_bf16 v[88:91], a[20:23], a[48:51], v[88:91]\n"
    "ds_read_b128 a[0:3], v48 offset:32768\n"
    "v_mfma_f32_16x16x32_bf16 v[92:95], a[20:23], a[52:55], v[92:95]\n"
    "s_mul_i32 s1, s64, 0x780\n"
    "s_add_u32 m0, s19, 0x4400\n"
    "s_nop 0\n"
    "buffer_load_dwordx4 v36, s[28:31], s1 offen lds\n"
    "ds_read_b64_tr_b16 a[36:37], v51 offset:34816\n"
    "v_mfma_f32_16x16x32_bf16 v[136:139], v[144:147], a[48:51], v[136:139]\n"
    "ds_read_b64_tr_b16 a[38:39], v51 offset:35904\n"
    "v_mfma_f32_16x16x32_bf16 v[140:143], v[144:147], a[52:55], v[140:143]\n"
    "ds_read_b128 a[4:7], v48 offset:34816\n"
    "v_mfma_f32_16x16x32_bf16 v[80:83], a[16:19], a[56:59], v[80:83]\n"
    "ds_read_b64_tr_b16 a[40:41], v52 offset:34816\n"
    "v_mfma_f32_16x16x32_bf16 v[104:107], a[24:27], a[48:51], v[104:107]\n"
    "buffer_load_dwordx4 v37, s[28:31], s1 offen offset:1088 lds\n"
    "ds_read_b64_tr_b16 a[42:43], v52 offset:35904\n"
    "v_mfma_f32_16x16x32_bf16 v[96:99], a[20:23], a[56:59], v[96:99]\n"
    "ds_read_b128 a[8:11], v48 offset:36864\n"
    "v_mfma_f32_16x16x32_bf16 v[108:111], a[24:27], a[52:55], v[108:111]\n"
    "ds_read_b64_tr_b16 a[44:45], v53 offset:34816\n"
    "v_mfma_f32_16x16x32_bf16 v[112:115], a[24:27], a[56:59], v[112:115]\n"
    "ds_read_b64_tr_b16 a[46:47], v53 offset:35904\n"
    "v_mfma_f32_16x16x32_bf16 v[84:87], a[16:19], a[60:63], v[84:87]\n"
    "buffer_load_dwordx4 v38, s[28:31], s1 offen offset:2176 lds\n"
    "ds_read_b128 a[12:15], v48 offset:38912\n"
    "v_mfma_f32_16x16x32_bf16 v[120:123], a[28:31], a[48:51], v[120:123]\n"
    "v_mfma_f32_16x16x32_bf16 v[100:103], a[20:23], a[60:63], v[100:103]\n"
    "v_mfma_f32_16x16x32_bf16 v[124:127], a[28:31], a[52:55], v[124:127]\n"
    "v_mfma_f32_16x16x32_bf16 v[116:119], a[24:27], a[60:63], v[116:119]\n"
    "buffer_load_dwordx4 v39, s[28:31], s1 offen offset:3264 lds\n"
    "v_mfma_f32_16x16x32_bf16 v[128:131], a[28:31], a[56:59], v[128:131]\n"
    "v_mfma_f32_16x16x32_bf16 v[132:135], a[28:31], a[60:63], v[132:135]\n"
    "s_waitcnt lgkmcnt(9)\n"
    "v_mfma_f32_16x16x32_bf16 v[72:75], a[0:3], a[32:35], v[72:75]\n"
    "ds_read_b64_tr_b16 a[48:49], v50 offset:43520\n"
    "s_waitcnt lgkmcnt(8)\n"
    "v_mfma_f32_16x16x32_bf16 v[76:79], a[0:3], a[36:39], v[76:79]\n"
    "s_add_u32 m0, s18, 0x4000\n"
    "s_nop 0\n"
    "buffer_load_dwordx4 v32, s[20:23], s64 offen lds\n"
    "ds_read_b64_tr_b16 a[50:51], v50 offset:44608\n"
    "s_waitcnt lgkmcnt(8)\n"
    "v_mfma_f32_16x16x32_bf16 v[88:91], a[4:7], a[32:35], v[88:91]\n"
    "ds_read_b128 a[16:19], v49 offset:32768\n"
    "v_mfma_f32_16x16x32_bf16 v[92:95], a[4:7], a[36:39], v[92:95]\n"
    "ds_read_b64_tr_b16 a[52:53], v51 offset:43520\n"
    "v_mfma_f32_16x16x32_bf16 v[136:139], v[144:147], a[32:35], v[136:139]\n"
    "ds_read_b64_tr_b16 a[54:55], v51 offset:44608\n"
    "v_mfma_f32_16x16x32_bf16 v[140:143], v[144:147], a[36:39], v[140:143]\n"
    "buffer_load_dwordx4 v33, s[20:23], s64 offen offset:1024 lds\n"
    "ds_read_b128 a[20:23], v49 offset:34816\n"
    "s_waitcnt lgkmcnt(10)\n"
    "v_mfma_f32_16x16x32_bf16 v[80:83], a[0:3], a[40:43], v[80:83]\n"
    "ds_read_b64_tr_b16 a[56:57], v52 offset:43520\n"
    "s_waitcnt lgkmcnt(10)\n"
    "v_mfma_f32_16x16x32_bf16 v[104:107], a[8:11], a[32:35], v[104:107]\n"
    "ds_read_b64_tr_b16 a[58:59], v52 offset:44608\n"
    "v_mfma_f32_16x16x32_bf16 v[96:99], a[4:7], a[40:43], v[96:99]\n"
    "ds_read_b128 a[24:27], v49 offset:36864\n"
    "v_mfma_f32_16x16x32_bf16 v[108:111], a[8:11], a[36:39], v[108:111]\n"
    "buffer_load_dwordx4 v34, s[20:23], s64 offen offset:2048 lds\n"
    "ds_read_b64_tr_b16 a[60:61], v53 offset:43520\n"
    "v_mfma_f32_16x16x32_bf16 v[112:115], a[8:11], a[40:43], v[112:115]\n"
    "ds_read_b64_tr_b16 a[62:63], v53 offset:44608\n"
    "s_waitcnt lgkmcnt(12)\n"
    "v_mfma_f32_16x16x32_bf16 v[84:87], a[0:3], a[44:47], v[84:87]\n"
    "ds_read_b128 a[28:31], v49 offset:38912\n"
    "s_waitcnt lgkmcnt(12)\n"
    "v_mfma_f32_16x16x32_bf16 v[120:123], a[12:15], a[32:35], v[120:123]\n"
    "v_mfma_f32_16x16x32_bf16 v[100:103], a[4:7], a[44:47], v[100:103]\n"
    "buffer_load_dwordx4 v35, s[20:23], s64 offen offset:3072 lds\n"
    "s_add_u32 s64, s64, 0x80\n"
    "s_cmp_eq_u32 s64, 0x500\n"
    "s_cselect_b32 s64, 0, s64\n"
    "v_mfma_f32_16x16x32_bf16 v[124:127], a[12:15], a[36:39], v[124:127]\n"
    "v_mfma_f32_16x16x32_bf16 v[116:119], a[8:11], a[44:47], v[116:119]\n"
    "v_mfma_f32_16x16x32_bf16 v[128:131], a[12:15], a[40:43], v[128:131]\n"
    "v_mfma_f32_16x16x32_bf16 v[132:135], a[12:15], a[44:47], v[132:135]\n"
    "s_waitcnt vmcnt(18)\n"
    "s_waitcnt lgkmcnt(0)\n"
    "s_barrier\n"
    "v_mfma_f32_16x16x32_bf16 v[72:75], a[16:19], a[48:51], v[72:75]\n"
    "ds_read_b64_tr_b16 a[32:33], v50 offset:52224\n"
    "v_mfma_f32_16x16x32_bf16 v[76:79], a[16:19], a[52:55], v[76:79]\n"
    "ds_read_b64_tr_b16 a[34:35], v50 offset:53312\n"
    "v_mfma_f32_16x16x32_bf16 v[88:91], a[20:23], a[48:51], v[88:91]\n"
    "ds_read_b128 a[0:3], v48 offset:49152\n"
    "v_mfma_f32_16x16x32_bf16 v[92:95], a[20:23], a[52:55], v[92:95]\n"
    "ds_read_b64_tr_b16 a[36:37], v51 offset:52224\n"
    "v_mfma_f32_16x16x32_bf16 v[136:139], v[144:147], a[48:51], v[136:139]\n"
    "ds_read_b64_tr_b16 a[38:39], v51 offset:53312\n"
    "v_mfma_f32_16x16x32_bf16 v[140:143], v[144:147], a[52:55], v[140:143]\n"
    "ds_read_b128 a[4:7], v48 offset:51200\n"
    "v_mfma_f32_16x16x32_bf16 v[80:83], a[16:19], a[56:59], v[80:83]\n"
    "ds_read_b64_tr_b16 a[40:41], v52 offset:52224\n"
    "v_mfma_f32_16x16x32_bf16 v[104:107], a[24:27], a[48:51], v[104:107]\n"
    "ds_read_b64_tr_b16 a[42:43], v52 offset:53312\n"
    "v_mfma_f32_16x16x32_bf16 v[96:99], a[20:23], a[56:59], v[96:99]\n"
    "ds_read_b128 a[8:11], v48 offset:53248\n"
    "v_mfma_f32_16x16x32_bf16 v[108:111], a[24:27], a[52:55], v[108:111]\n"
    "ds_read_b64_tr_b16 a[44:45], v53 offset:52224\n"
    "v_mfma_f32_16x16x32_bf16 v[112:115], a[24:27], a[56:59], v[112:115]\n"
    "ds_read_b64_tr_b16 a[46:47], v53 offset:53312\n"
    "v_mfma_f32_16x16x32_bf16 v[84:87], a[16:19], a[60:63], v[84:87]\n"
    "ds_read_b128 a[12:15], v48 offset:55296\n"
    "v_mfma_f32_16x16x32_bf16 v[120:123], a[28:31], a[48:51], v[120:123]\n"
    "v_mfma_f32_16x16x32_bf16 v[100:103], a[20:23], a[60:63], v[100:103]\n"
    "v_mfma_f32_16x16x32_bf16 v[124:127], a[28:31], a[52:55], v[124:127]\n"
    "v_mfma_f32_16x16x32_bf16 v[116:119], a[24:27], a[60:63], v[116:119]\n"
    "v_mfma_f32_16x16x32_bf16 v[128:131], a[28:31], a[56:59], v[128:131]\n"
    "v_mfma_f32_16x16x32_bf16 v[132:135], a[28:31], a[60:63], v[132:135]\n"
    "s_waitcnt lgkmcnt(9)\n"
    "v_mfma_f32_16x16x32_bf16 v[72:75], a[0:3], a[32:35], v[72:75]\n"
    "ds_read_b64_tr_b16 a[48:49], v50 offset:60928\n"
    "s_waitcnt lgkmcnt(8)\n"
    "v_mfma_f32_16x16x32_bf16 v[76:79], a[0:3], a[36:39], v[76:79]\n"
    "ds_read_b64_tr_b16 a[50:51], v50 offset:62016\n"
    "s_waitcnt lgkmcnt(8)\n"
    "v_mfma_f32_16x16x32_bf16 v[88:91], a[4:7], a[32:35], v[88:91]\n"
    "ds_read_b128 a[16:19], v49 offset:49152\n"
    "v_mfma_f32_16x16x32_bf16 v[92:95], a[4:7], a[36:39], v[92:95]\n"
    "ds_read_b64_tr_b16 a[52:53], v51 offset:60928\n"
    "v_mfma_f32_16x16x32_bf16 v[136:139], v[144:147], a[32:35], v[136:139]\n"
    "ds_read_b64_tr_b16 a[54:55], v51 offset:62016\n"
    "v_mfma_f32_16x16x32_bf16 v[140:143], v[144:147], a[36:39], v[140:143]\n"
    "ds_read_b128 a[20:23], v49 offset:51200\n"
    "s_waitcnt lgkmcnt(10)\n"
    "v_mfma_f32_16x16x32_bf16 v[80:83], a[0:3], a[40:43], v[80:83]\n"
    "ds_read_b64_tr_b16 a[56:57], v52 offset:60928\n"
    "s_waitcnt lgkmcnt(10)\n"
    "v_mfma_f32_16x16x32_bf16 v[104:107], a[8:11], a[32:35], v[104:107]\n"
    "ds_read_b64_tr_b16 a[58:59], v52 offset:62016\n"
    "v_mfma_f32_16x16x32_bf16 v[96:99], a[4:7], a[40:43], v[96:99]\n"
    "ds_read_b128 a[24:27], v49 offset:53248\n"
    "v_mfma_f32_16x16x32_bf16 v[108:111], a[8:11], a[36:39], v[108:111]\n"
    "ds_read_b64_tr_b16 a[60:61], v53 offset:60928\n"
    "v_mfma_f32_16x16x32_bf16 v[112:115], a[8:11], a[40:43], v[112:115]\n"
    "ds_read_b64_tr_b16 a[62:63], v53 offset:62016\n"
    "s_waitcnt lgkmcnt(12)\n"
    "v_mfma_f32_16x16x32_bf16 v[84:87], a[0:3], a[44:47], v[84:87]\n"
    "ds_read_b128 a[28:31], v49 offset:55296\n"
    "s_waitcnt lgkmcnt(12)\n"
    "v_mfma_f32_16x16x32_bf16 v[120:123], a[12:15], a[32:35], v[120:123]\n"
    "v_mfma_f32_16x16x32_bf16 v[100:103], a[4:7], a[44:47], v[100:103]\n"
    "v_mfma_f32_16x16x32_bf16 v[124:127], a[12:15], a[36:39], v[124:127]\n"
    "v_mfma_f32_16x16x32_bf16 v[116:119], a[8:11], a[44:47], v[116:119]\n"
    "v_mfma_f32_16x16x32_bf16 v[128:131], a[12:15], a[40:43], v[128:131]\n"
    "v_mfma_f32_16x16x32_bf16 v[132:135], a[12:15], a[44:47], v[132:135]\n"
    "s_waitcnt vmcnt(10)\n"
    "s_waitcnt lgkmcnt(0)\n"
    "s_barrier\n"
    "v_mfma_f32_16x16x32_bf16 v[72:75], a[16:19], a[48:51], v[72:75]\n"
    "ds_read_b64_tr_b16 a[32:33], v50\n"
    "ds_read_b64_tr_b16 a[34:35], v50 offset:1088\n"
    "v_mfma_f32_16x16x32_bf16 v[76:79], a[16:19], a[52:55], v[76:79]\n"
    "ds_read_b128 a[0:3], v48\n"
    "v_mfma_f32_16x16x32_bf16 v[88:91], a[20:23], a[48:51], v[88:91]\n"
    "ds_read_b64_tr_b16 a[36:37], v51\n"
    "ds_read_b64_tr_b16 a[38:39], v51 offset:1088\n"
    "v_mfma_f32_16x16x32_bf16 v[92:95], a[20:23], a[52:55], v[92:95]\n"
    "ds_read_b128 a[4:7], v48 offset:2048\n"
    "v_mfma_f32_16x16x32_bf16 v[136:139], v[144:147], a[48:51], v[136:139]\n"
    "ds_read_b64_tr_b16 a[40:41], v52\n"
    "ds_read_b64_tr_b16 a[42:43], v52 offset:1088\n"
    "v_mfma_f32_16x16x32_bf16 v[140:143], v[144:147], a[52:55], v[140:143]\n"
    "ds_read_b128 a[8:11], v48 offset:4096\n"
    "v_mfma_f32_16x16x32_bf16 v[80:83], a[16:19], a[56:59], v[80:83]\n"
    "ds_read_b64_tr_b16 a[44:45], v53\n"
    "ds_read_b64_tr_b16 a[46:47], v53 offset:1088\n"
    "v_mfma_f32_16x16x32_bf16 v[104:107], a[24:27], a[48:51], v[104:107]\n"
    "ds_read_b128 a[12:15], v48 offset:6144\n"
    "v_mfma_f32_16x16x32_bf16 v[96:99], a[20:23], a[56:59], v[96:99]\n"
    "ds_read_b64_tr_b16 a[64:65], v56\n"
    "ds_read_b64_tr_b16 a[66:67], v56 offset:512\n"
    "v_mfma_f32_16x16x32_bf16 v[108:111], a[24:27], a[52:55], v[108:111]\n"
    "ds_read_b64_tr_b16 a[68:69], v57\n"
    "v_mfma_f32_16x16x32_bf16 v[112:115], a[24:27], a[56:59], v[112:115]\n"
    "ds_read_b64_tr_b16 a[70:71], v57 offset:512\n"
    "ds_read_b64_tr_b16 a[72:73], v58\n"
    "v_mfma_f32_16x16x32_bf16 v[84:87], a[16:19], a[60:63], v[84:87]\n"
    "ds_read_b64_tr_b16 a[74:75], v58 offset:512\n"
    "v_mfma_f32_16x16x32_bf16 v[120:123], a[28:31], a[48:51], v[120:123]\n"
    "ds_read_b64_tr_b16 a[76:77], v59\n"
    "ds_read_b64_tr_b16 a[78:79], v59 offset:512\n"
    "v_mfma_f32_16x16x32_bf16 v[100:103], a[20:23], a[60:63], v[100:103]\n"
    "ds_read_b64_tr_b16 a[80:81], v60\n"
    "v_mfma_f32_16x16x32_bf16 v[124:127], a[28:31], a[52:55], v[124:127]\n"
    "ds_read_b64_tr_b16 a[82:83], v60 offset:512\n"
    "ds_read_b64_tr_b16 a[84:85], v61\n"
    "v_mfma_f32_16x16x32_bf16 v[116:119], a[24:27], a[60:63], v[116:119]\n"
    "ds_read_b64_tr_b16 a[86:87], v61 offset:512\n"
    "v_mfma_f32_16x16x32_bf16 v[128:131], a[28:31], a[56:59], v[128:131]\n"
    "v_mfma_f32_16x16x32_bf16 v[132:135], a[28:31], a[60:63], v[132:135]\n"
    "s_waitcnt lgkmcnt(15)\n"
    "v_mfma_f32_16x16x32_bf16 v[72:75], a[0:3], a[32:35], v[72:75]\n"
    "s_waitcnt lgkmcnt(10)\n"
    "buffer_store_dwordx4 a[64:67], v62, s[24:27], 0 offen sc1\n"
    "ds_read_b64_tr_b16 a[48:49], v50 offset:8704\n"
    "v_mfma_f32_16x16x32_bf16 v[76:79], a[0:3], a[36:39], v[76:79]\n"
    "ds_read_b64_tr_b16 a[50:51], v50 offset:9792\n"
    "v_mfma_f32_16x16x32_bf16 v[88:91], a[4:7], a[32:35], v[88:91]\n"
    "ds_read_b128 a[16:19], v49\n"
    "v_mfma_f32_16x16x32_bf16 v[92:95], a[4:7], a[36:39], v[92:95]\n"
    "ds_read_b64_tr_b16 a[52:53], v51 offset:8704\n"
    "v_mfma_f32_16x16x32_bf16 v[136:139], v[144:147], a[32:35], v[136:139]\n"
    "ds_read_b64_tr_b16 a[54:55], v51 offset:9792\n"
    "v_mfma_f32_16x16x32_bf16 v[140:143], v[144:147], a[36:39], v[140:143]\n"
    "ds_read_b128 a[20:23], v49 offset:2048\n"
    "v_mfma_f32_16x16x32_bf16 v[80:83], a[0:3], a[40:43], v[80:83]\n"
    "s_waitcnt lgkmcnt(14)\n"
    "buffer_store_dwordx4 a[68:71], v62, s[24:27], 0 offen offset:1024 sc1\n"
    "ds_read_b64_tr_b16 a[56:57], v52 offset:8704\n"
    "v_mfma_f32_16x16x32_bf16 v[104:107], a[8:11], a[32:35], v[104:107]\n"
    "ds_read_b64_tr_b16 a[58:59], v52 offset:9792\n"
    "v_mfma_f32_16x16x32_bf16 v[96:99], a[4:7], a[40:43], v[96:99]\n"
    "ds_read_b128 a[24:27], v49 offset:4096\n"
    "v_mfma_f32_16x16x32_bf16 v[108:111], a[8:11], a[36:39], v[108:111]\n"
    "ds_read_b64_tr_b16 a[60:61], v53 offset:8704\n"
    "v_mfma_f32_16x16x32_bf16 v[112:115], a[8:11], a[40:43], v[112:115]\n"
    "ds_read_b64_tr_b16 a[62:63], v53 offset:9792\n"
    "v_mfma_f32_16x16x32_bf16 v[84:87], a[0:3], a[44:47], v[84:87]\n"
    "ds_read_b128 a[28:31], v49 offset:6144\n"
    "v_mfma_f32_16x16x32_bf16 v[120:123], a[12:15], a[32:35], v[120:123]\n"
    "s_waitcnt lgkmcnt(15)\n"
    "buffer_store_dwordx4 a[72:75], v62, s[24:27], 0 offen offset:2048 sc1\n"
    "v_mfma_f32_16x16x32_bf16 v[100:103], a[4:7], a[44:47], v[100:103]\n"
    "v_mfma_f32_16x16x32_bf16 v[124:127], a[12:15], a[36:39], v[124:127]\n"
    "v_mfma_f32_16x16x32_bf16 v[116:119], a[8:11], a[44:47], v[116:119]\n"
    "v_mfma_f32_16x16x32_bf16 v[128:131], a[12:15], a[40:43], v[128:131]\n"
    "v_mfma_f32_16x16x32_bf16 v[132:135], a[12:15], a[44:47], v[132:135]\n"
    "s_waitcnt vmcnt(3)\n"
    "s_waitcnt lgkmcnt(0)\n"
    "s_barrier\n"
    "v_mfma_f32_16x16x32_bf16 v[72:75], a[16:19], a[48:51], v[72:75]\n"
    "ds_read_b64_tr_b16 a[32:33], v50 offset:17408\n"
    "buffer_store_dwordx4 a[76:79], v62, s[24:27], s71 offen sc1\n"
    "v_mfma_f32_16x16x32_bf16 v[76:79], a[16:19], a[52:55], v[76:79]\n"
    "ds_read_b64_tr_b16 a[34:35], v50 offset:18496\n"
    "v_mfma_f32_16x16x32_bf16 v[88:91], a[20:23], a[48:51], v[88:91]\n"
    "ds_read_b128 a[0:3], v48 offset:16384\n"
    "v_mfma_f32_16x16x32_bf16 v[92:95], a[20:23], a[52:55], v[92:95]\n"
    "ds_read_b64_tr_b16 a[36:37], v51 offset:17408\n"
    "v_mfma_f32_16x16x32_bf16 v[136:139], v[144:147], a[48:51], v[136:139]\n"
    "ds_read_b64_tr_b16 a[38:39], v51 offset:18496\n"
    "v_mfma_f32_16x16x32_bf16 v[140:143], v[144:147], a[52:55], v[140:143]\n"
    "ds_read_b128 a[4:7], v48 offset:18432\n"
    "v_mfma_f32_16x16x32_bf16 v[80:83], a[16:19], a[56:59], v[80:83]\n"
    "ds_read_b64_tr_b16 a[40:41], v52 offset:17408\n"
    "buffer_store_dwordx4 a[80:83], v62, s[24:27], s72 offen sc1\n"
    "v_mfma_f32_16x16x32_bf16 v[104:107], a[24:27], a[48:51], v[104:107]\n"
    "ds_read_b64_tr_b16 a[42:43], v52 offset:18496\n"
    "v_mfma_f32_16x16x32_bf16 v[96:99], a[20:23], a[56:59], v[96:99]\n"
    "ds_read_b128 a[8:11], v48 offset:20480\n"
    "v_mfma_f32_16x16x32_bf16 v[108:111], a[24:27], a[52:55], v[108:111]\n"
    "ds_read_b64_tr_b16 a[44:45], v53 offset:17408\n"
    "v_mfma_f32_16x16x32_bf16 v[112:115], a[24:27], a[56:59], v[112:115]\n"
    "ds_read_b64_tr_b16 a[46:47], v53 offset:18496\n"
    "v_mfma_f32_16x16x32_bf16 v[84:87], a[16:19], a[60:63], v[84:87]\n"
    "ds_read_b128 a[12:15], v48 offset:22528\n"
    "v_mfma_f32_16x16x32_bf16 v[120:123], a[28:31], a[48:51], v[120:123]\n"
    "v_mfma_f32_16x16x32_bf16 v[100:103], a[20:23], a[60:63], v[100:103]\n"
    "buffer_store_dwordx4 a[84:87], v62, s[24:27], s73 offen sc1\n"
    "v_mfma_f32_16x16x32_bf16 v[124:127], a[28:31], a[52:55], v[124:127]\n"
    "v_mfma_f32_16x16x32_bf16 v[116:119], a[24:27], a[60:63], v[116:119]\n"
    "v_mfma_f32_16x16x32_bf16 v[128:131], a[28:31], a[56:59], v[128:131]\n"
    "v_mfma_f32_16x16x32_bf16 v[132:135], a[28:31], a[60:63], v[132:135]\n"
    "s_waitcnt lgkmcnt(9)\n"
    "v_mfma_f32_16x16x32_bf16 v[72:75], a[0:3], a[32:35], v[72:75]\n"
    "ds_read_b64_tr_b16 a[48:49], v50 offset:26112\n"
    "s_waitcnt lgkmcnt(8)\n"
    "v_mfma_f32_16x16x32_bf16 v[76:79], a[0:3], a[36:39], v[76:79]\n"
    "ds_read_b64_tr_b16 a[50:51], v50 offset:27200\n"
    "s_waitcnt lgkmcnt(8)\n"
    "v_mfma_f32_16x16x32_bf16 v[88:91], a[4:7], a[32:35], v[88:91]\n"
    "ds_read_b128 a[16:19], v49 offset:16384\n"
    "v_mfma_f32_16x16x32_bf16 v[92:95], a[4:7], a[36:39], v[92:95]\n"
    "ds_read_b64_tr_b16 a[52:53], v51 offset:26112\n"
    "v_mfma_f32_16x16x32_bf16 v[136:139], v[144:147], a[32:35], v[136:139]\n"
    "ds_read_b64_tr_b16 a[54:55], v51 offset:27200\n"
    "v_mfma_f32_16x16x32_bf16 v[140:143], v[144:147], a[36:39], v[140:143]\n"
    "ds_read_b128 a[20:23], v49 offset:18432\n"
    "s_waitcnt lgkmcnt(10)\n"
    "v_mfma_f32_16x16x32_bf16 v[80:83], a[0:3], a[40:43], v[80:83]\n"
    "ds_read_b64_tr_b16 a[56:57], v52 offset:26112\n"
    "s_waitcnt lgkmcnt(10)\n"
    "v_mfma_f32_16x16x32_bf16 v[104:107], a[8:11], a[32:35], v[104:107]\n"
    "ds_read_b64_tr_b16 a[58:59], v52 offset:27200\n"
    "v_mfma_f32_16x16x32_bf16 v[96:99], a[4:7], a[40:43], v[96:99]\n"
    "ds_read_b128 a[24:27], v49 offset:20480\n"
    "v_mfma_f32_16x16x32_bf16 v[108:111], a[8:11], a[36:39], v[108:111]\n"
    "ds_read_b64_tr_b16 a[60:61], v53 offset:26112\n"
    "v_mfma_f32_16x16x32_bf16 v[112:115], a[8:11], a[40:43], v[112:115]\n"
    "ds_read_b64_tr_b16 a[62:63], v53 offset:27200\n"
    "s_waitcnt lgkmcnt(12)\n"
    "v_mfma_f32_16x16x32_bf16 v[84:87], a[0:3], a[44:47], v[84:87]\n"
    "ds_read_b128 a[28:31], v49 offset:22528\n"
    "s_waitcnt lgkmcnt(12)\n"
    "v_mfma_f32_16x16x32_bf16 v[120:123], a[12:15], a[32:35], v[120:123]\n"
    "v_mfma_f32_16x16x32_bf16 v[100:103], a[4:7], a[44:47], v[100:103]\n"
    "v_mfma_f32_16x16x32_bf16 v[124:127], a[12:15], a[36:39], v[124:127]\n"
    "v_mfma_f32_16x16x32_bf16 v[116:119], a[8:11], a[44:47], v[116:119]\n"
    "v_mfma_f32_16x16x32_bf16 v[128:131], a[12:15], a[40:43], v[128:131]\n"
    "v_mfma_f32_16x16x32_bf16 v[132:135], a[12:15], a[44:47], v[132:135]\n"
    "s_waitcnt lgkmcnt(9)\n"
    "v_mfma_f32_16x16x32_bf16 v[72:75], a[16:19], a[48:51], v[72:75]\n"
    "s_waitcnt lgkmcnt(7)\n"
    "v_mfma_f32_16x16x32_bf16 v[76:79], a[16:19], a[52:55], v[76:79]\n"
    "v_xor_b32 v148, s96, v153\n"
    "v_xor_b32 v149, s96, v155\n"
    "v_or_b32 v148, v148, v149\n"
    "v_cmp_ne_u32 vcc, 0, v148\n"
    "s_cmp_eq_u64 vcc, 0\n"
    "s_cbranch_scc1 .Lfb_xd_8\n"
    "s_mov_b32 s97, 0\n"
    ".Lfb_xs_7:\n"
    "s_sleep 4\n"
    "buffer_load_dwordx4 v[152:155], v63, s[24:27], 0 offen sc1\n"
    "s_waitcnt vmcnt(0)\n"
    "v_xor_b32 v148, s96, v153\n"
    "v_xor_b32 v149, s96, v155\n"
    "v_or_b32 v148, v148, v149\n"
    "v_cmp_ne_u32 vcc, 0, v148\n"
    "s_cmp_eq_u64 vcc, 0\n"
    "s_cbranch_scc1 .Lfb_xd_8\n"
    "s_add_u32 s97, s97, 1\n"
    "s_cmp_lt_u32 s97, 0x10000\n"
    "s_cbranch_scc1 .Lfb_xs_7\n"
    ".Lfb_xd_8:\n"
    ".Lfb_xe_9:\n"
    "s_waitcnt lgkmcnt(6)\n"
    "v_mfma_f32_16x16x32_bf16 v[88:91], a[20:23], a[48:51], v[88:91]\n"
    "v_mul_f32 v152, s75, v152\n"
    "v_mfma_f32_16x16x32_bf16 v[92:95], a[20:23], a[52:55], v[92:95]\n"
    "v_mul_f32 v154, s75, v154\n"
    "v_mfma_f32_16x16x32_bf16 v[136:139], v[144:147], a[48:51], v[136:139]\n"
    "ds_write_b32 v68, v152\n"
    "v_mfma_f32_16x16x32_bf16 v[140:143], v[144:147], a[52:55], v[140:143]\n"
    "ds_write_b32 v68, v154 offset:512\n"
    "s_waitcnt lgkmcnt(6)\n"
    "v_mfma_f32_16x16x32_bf16 v[80:83], a[16:19], a[56:59], v[80:83]\n"
    "s_waitcnt lgkmcnt(5)\n"
    "v_mfma_f32_16x16x32_bf16 v[104:107], a[24:27], a[48:51], v[104:107]\n"
    "v_mfma_f32_16x16x32_bf16 v[96:99], a[20:23], a[56:59], v[96:99]\n"
    "v_mfma_f32_16x16x32_bf16 v[108:111], a[24:27], a[52:55], v[108:111]\n"
    "ds_write_b32 v66, v136\n"
    "v_mfma_f32_16x16x32_bf16 v[112:115], a[24:27], a[56:59], v[112:115]\n"
    "ds_write_b32 v66, v140 offset:64\n"
    "s_waitcnt lgkmcnt(5)\n"
    "v_mfma_f32_16x16x32_bf16 v[84:87], a[16:19], a[60:63], v[84:87]\n"
    "s_waitcnt lgkmcnt(4)\n"
    "v_mfma_f32_16x16x32_bf16 v[120:123], a[28:31], a[48:51], v[120:123]\n"
    "v_mfma_f32_16x16x32_bf16 v[100:103], a[20:23], a[60:63], v[100:103]\n"
    "v_mfma_f32_16x16x32_bf16 v[124:127], a[28:31], a[52:55], v[124:127]\n"
    "s_waitcnt lgkmcnt(0)\n"
    "s_barrier\n"
    "v_mfma_f32_16x16x32_bf16 v[116:119], a[24:27], a[60:63], v[116:119]\n"
    "ds_read2_b32 v[156:157], v67 offset0:0 offset1:16\n"
    "ds_read_b128 v[164:167], v69\n"
    "ds_read_b128 v[168:171], v69 offset:512\n"
    "v_mfma_f32_16x16x32_bf16 v[128:131], a[28:31], a[56:59], v[128:131]\n"
    "ds_read_b128 v[172:175], v69 offset:64\n"
    "ds_read_b128 v[176:179], v69 offset:576\n"
    "ds_read_b128 v[180:183], v69 offset:128\n"
    "v_mfma_f32_16x16x32_bf16 v[132:135], a[28:31], a[60:63], v[132:135]\n"
    "ds_read_b128 v[184:187], v69 offset:640\n"
    "ds_read_b128 v[188:191], v69 offset:192\n"
    "ds_read_b128 v[192:195], v69 offset:704\n"
    "s_waitcnt lgkmcnt(6)\n"
    "v_pk_mul_f32 v[196:197], v[168:169], v[136:137] op_sel_hi:[1,0]\n"
    "v_pk_fma_f32 v[196:197], v[72:73], v[164:165], v[196:197]\n"
    "v_pk_mul_f32 v[198:199], v[170:171], v[136:137] op_sel_hi:[1,0]\n"
    "v_pk_fma_f32 v[198:199], v[74:75], v[166:167], v[198:199]\n"
    "s_waitcnt lgkmcnt(4)\n"
    "v_pk_mul_f32 v[200:201], v[176:177], v[136:137] op_sel_hi:[1,0]\n"
    "v_pk_fma_f32 v[200:201], v[88:89], v[172:173], v[200:201]\n"
    "v_pk_mul_f32 v[202:203], v[178:179], v[136:137] op_sel_hi:[1,0]\n"
    "v_pk_fma_f32 v[202:203], v[90:91], v[174:175], v[202:203]\n"
    "v_cvt_pk_bf16_f32 v212, v196, v197\n"
    "v_cvt_pk_bf16_f32 v213, v198, v199\n"
    "v_cvt_pk_bf16_f32 v214, v200, v201\n"
    "v_cvt_pk_bf16_f32 v215, v202, v203\n"
    "s_xor_b32 s99, s6, 0\n"
    "s_mul_i32 s99, s99, s79\n"
    "s_add_u32 s99, s99, s53\n"
    "buffer_store_dwordx4 v[212:215], v54, s[24:27], s99 offen sc1\n"
    "s_waitcnt lgkmcnt(2)\n"
    "v_pk_mul_f32 v[204:205], v[184:185], v[136:137] op_sel_hi:[1,0]\n"
    "v_pk_fma_f32 v[204:205], v[104:105], v[180:181], v[204:205]\n"
    "v_pk_mul_f32 v[206:207], v[186:187], v[136:137] op_sel_hi:[1,0]\n"
    "v_pk_fma_f32 v[206:207], v[106:107], v[182:183], v[206:207]\n"
    "s_waitcnt lgkmcnt(0)\n"
    "v_pk_mul_f32 v[208:209], v[192:193], v[136:137] op_sel_hi:[1,0]\n"
    "v_pk_fma_f32 v[208:209], v[120:121], v[188:189], v[208:209]\n"
    "v_pk_mul_f32 v[210:211], v[194:195], v[136:137] op_sel_hi:[1,0]\n"
    "v_pk_fma_f32 v[210:211], v[122:123], v[190:191], v[210:211]\n"
    "v_cvt_pk_bf16_f32 v216, v204, v205\n"
    "v_cvt_pk_bf16_f32 v217, v206, v207\n"
    "v_cvt_pk_bf16_f32 v218, v208, v209\n"
    "v_cvt_pk_bf16_f32 v219, v210, v211\n"
    "s_xor_b32 s98, s6, 0\n"
    "s_mul_i32 s98, s98, s79\n"
    "s_add_u32 s98, s98, s53\n"
    "s_add_u32 s98, s98, s78\n"
    "buffer_store_dwordx4 v[216:219], v54, s[24:27], s98 offen sc1\n"
    "v_pk_mul_f32 v[196:197], v[168:169], v[140:141] op_sel_hi:[1,0]\n"
    "v_pk_fma_f32 v[196:197], v[76:77], v[164:165], v[196:197]\n"
    "v_pk_mul_f32 v[198:199], v[170:171], v[140:141] op_sel_hi:[1,0]\n"
    "v_pk_fma_f32 v[198:199], v[78:79], v[166:167], v[198:199]\n"
    "v_pk_mul_f32 v[200:201], v[176:177], v[140:141] op_sel_hi:[1,0]\n"
    "v_pk_fma_f32 v[200:201], v[92:93], v[172:173], v[200:201]\n"
    "v_pk_mul_f32 v[202:203], v[178:179], v[140:141] op_sel_hi:[1,0]\n"
    "v_pk_fma_f32 v[202:203], v[94:95], v[174:175], v[202:203]\n"
    "v_cvt_pk_bf16_f32 v220, v196, v197\n"
    "v_cvt_pk_bf16_f32 v221, v198, v199\n"
    "v_cvt_pk_bf16_f32 v222, v200, v201\n"
    "v_cvt_pk_bf16_f32 v223, v202, v203\n"
    "s_xor_b32 s99, s6, 1\n"
    "s_mul_i32 s99, s99, s79\n"
    "s_add_u32 s99, s99, s53\n"
    "buffer_store_dwordx4 v[220:223], v54, s[24:27], s99 offen sc1\n"
    "v_pk_mul_f32 v[204:205], v[184:185], v[140:141] op_sel_hi:[1,0]\n"
    "v_pk_fma_f32 v[204:205], v[108:109], v[180:181], v[204:205]\n"
    "v_pk_mul_f32 v[206:207], v[186:187], v[140:141] op_sel_hi:[1,0]\n"
    "v_pk_fma_f32 v[206:207], v[110:111], v[182:183], v[206:207]\n"
    "v_pk_mul_f32 v[208:209], v[192:193], v[140:141] op_sel_hi:[1,0]\n"
    "v_pk_fma_f32 v[208:209], v[124:125], v[188:189], v[208:209]\n"
    "v_pk_mul_f32 v[210:211], v[194:195], v[140:141] op_sel_hi:[1,0]\n"
    "v_pk_fma_f32 v[210:211], v[126:127], v[190:191], v[210:211]\n"
    "v_cvt_pk_bf16_f32 v224, v204, v205\n"
    "v_cvt_pk_bf16_f32 v225, v206, v207\n"
    "v_cvt_pk_bf16_f32 v226, v208, v209\n"
    "v_cvt_pk_bf16_f32 v227, v210, v211\n"
    "s_xor_b32 s98, s6, 1\n"
    "s_mul_i32 s98, s98, s79\n"
    "s_add_u32 s98, s98, s53\n"
    "s_add_u32 s98, s98, s78\n"
    "buffer_store_dwordx4 v[224:227], v54, s[24:27], s98 offen sc1\n"
    "v_pk_mul_f32 v[196:197], v[168:169], v[156:157] op_sel_hi:[1,0]\n"
    "v_pk_fma_f32 v[196:197], v[80:81], v[164:165], v[196:197]\n"
    "v_pk_mul_f32 v[198:199], v[170:171], v[156:157] op_sel_hi:[1,0]\n"
    "v_pk_fma_f32 v[198:199], v[82:83], v[166:167], v[198:199]\n"
    "v_pk_mul_f32 v[200:201], v[176:177], v[156:157] op_sel_hi:[1,0]\n"
    "v_pk_fma_f32 v[200:201], v[96:97], v[172:173], v[200:201]\n"
    "v_pk_mul_f32 v[202:203], v[178:179], v[156:157] op_sel_hi:[1,0]\n"
    "v_pk_fma_f32 v[202:203], v[98:99], v[174:175], v[202:203]\n"
    "v_cvt_pk_bf16_f32 v212, v196, v197\n"
    "v_cvt_pk_bf16_f32 v213, v198, v199\n"
    "v_cvt_pk_bf16_f32 v214, v200, v201\n"
    "v_cvt_pk_bf16_f32 v215, v202, v203\n"
    "s_xor_b32 s99, s6, 2\n"
    "s_mul_i32 s99, s99, s79\n"
    "s_add_u32 s99, s99, s53\n"
    "buffer_store_dwordx4 v[212:215], v54, s[24:27], s99 offen sc1\n"
    "v_pk_mul_f32 v[204:205], v[184:185], v[156:157] op_sel_hi:[1,0]\n"
    "v_pk_fma_f32 v[204:205], v[112:113], v[180:181], v[204:205]\n"
    "v_pk_mul_f32 v[206:207], v[186:187], v[156:157] op_sel_hi:[1,0]\n"
    "v_pk_fma_f32 v[206:207], v[114:115], v[182:183], v[206:207]\n"
    "v_pk_mul_f32 v[208:209], v[192:193], v[156:157] op_sel_hi:[1,0]\n"
    "v_pk_fma_f32 v[208:209], v[128:129], v[188:189], v[208:209]\n"
    "v_pk_mul_f32 v[210:211], v[194:195], v[156:157] op_sel_hi:[1,0]\n"
    "v_pk_fma_f32 v[210:211], v[130:131], v[190:191], v[210:211]\n"
    "v_cvt_pk_bf16_f32 v216, v204, v205\n"
    "v_cvt_pk_bf16_f32 v217, v206, v207\n"
    "v_cvt_pk_bf16_f32 v218, v208, v209\n"
    "v_cvt_pk_bf16_f32 v219, v210, v211\n"
    "s_xor_b32 s98, s6, 2\n"
    "s_mul_i32 s98, s98, s79\n"
    "s_add_u32 s98, s98, s53\n"
    "s_add_u32 s98, s98, s78\n"
    "buffer_store_dwordx4 v[216:219], v54, s[24:27], s98 offen sc1\n"
    "v_pk_mul_f32 v[196:197], v[168:169], v[156:157] op_sel:[0,1] op_sel_hi:[1,1]\n"
    "v_pk_fma_f32 v[196:197], v[84:85], v[164:165], v[196:197]\n"
    "v_pk_mul_f32 v[198:199], v[170:171], v[156:157] op_sel:[0,1] op_sel_hi:[1,1]\n"
    "v_pk_fma_f32 v[198:199], v[86:87], v[166:167], v[198:199]\n"
    "v_pk_mul_f32 v[200:201], v[176:177], v[156:157] op_sel:[0,1] op_sel_hi:[1,1]\n"
    "v_pk_fma_f32 v[200:201], v[100:101], v[172:173], v[200:201]\n"
    "v_pk_mul_f32 v[202:203], v[178:179], v[156:157] op_sel:[0,1] op_sel_hi:[1,1]\n"
    "v_pk_fma_f32 v[202:203], v[102:103], v[174:175], v[202:203]\n"
    "v_cvt_pk_bf16_f32 v220, v196, v197\n"
    "v_cvt_pk_bf16_f32 v221, v198, v199\n"
    "v_cvt_pk_bf16_f32 v222, v200, v201\n"
    "v_cvt_pk_bf16_f32 v223, v202, v203\n"
    "s_xor_b32 s99, s6, 3\n"
    "s_mul_i32 s99, s99, s79\n"
    "s_add_u32 s99, s99, s53\n"
    "buffer_store_dwordx4 v[220:223], v54, s[24:27], s99 offen sc1\n"
    "v_pk_mul_f32 v[204:205], v[184:185], v[156:157] op_sel:[0,1] op_sel_hi:[1,1]\n"
    "v_pk_fma_f32 v[204:205], v[116:117], v[180:181], v[204:205]\n"
    "v_pk_mul_f32 v[206:207], v[186:187], v[156:157] op_sel:[0,1] op_sel_hi:[1,1]\n"
    "v_pk_fma_f32 v[206:207], v[118:119], v[182:183], v[206:207]\n"
    "v_pk_mul_f32 v[208:209], v[192:193], v[156:157] op_sel:[0,1] op_sel_hi:[1,1]\n"
    "v_pk_fma_f32 v[208:209], v[132:133], v[188:189], v[208:209]\n"
    "v_pk_mul_f32 v[210:211], v[194:195], v[156:157] op_sel:[0,1] op_sel_hi:[1,1]\n"
    "v_pk_fma_f32 v[210:211], v[134:135], v[190:191], v[210:211]\n"
    "v_cvt_pk_bf16_f32 v224, v204, v205\n"
    "v_cvt_pk_bf16_f32 v225, v206, v207\n"
    "v_cvt_pk_bf16_f32 v226, v208, v209\n"
    "v_cvt_pk_bf16_f32 v227, v210, v211\n"
    "s_xor_b32 s98, s6, 3\n"
    "s_mul_i32 s98, s98, s79\n"
    "s_add_u32 s98, s98, s53\n"
    "s_add_u32 s98, s98, s78\n"
    "buffer_store_dwordx4 v[224:227], v54, s[24:27], s98 offen sc1\n"
    "s_branch .Lfb_p1j_3\n"
    ".Lfb_p1s_2:\n"
    "; ---- P1 stats (t~15482)\n"
    "v_mov_b32 v72, 0\n"
    "v_mov_b32 v73, 0\n"
    "v_mov_b32 v74, 0\n"
    "v_mov_b32 v75, 0\n"
    "v_mov_b32 v247, 0x40000000\n"
    "v_cmp_eq_u32 vcc, 0, v249\n"
    "v_lshl_add_u32 v245, v237, 7, v248\n"
    "v_add_u32 v245, s57, v245\n"
    "v_cndmask_b32 v64, v247, v245, vcc\n"
    "s_add_u32 m0, s18, 0xc000\n"
    "s_nop 0\n"
    "buffer_load_dwordx4 v32, s[20:23], s64 offen lds\n"
    "buffer_load_dwordx4 v33, s[20:23], s64 offen offset:1024 lds\n"
    "buffer_load_dwordx4 v34, s[20:23], s64 offen offset:2048 lds\n"
    "buffer_load_dwordx4 v35, s[20:23], s64 offen offset:3072 lds\n"
    "s_add_u32 s64, s64, 0x80\n"
    "s_cmp_eq_u32 s64, 0x500\n"
    "s_cselect_b32 s64, 0, s64\n"
    "s_waitcnt vmcnt(26)\n"
    "ds_read_b128 v[80:83], v48\n"
    "ds_read_b128 v[84:87], v49\n"
    "ds_read_b128 v[88:91], v48 offset:2048\n"
    "ds_read_b128 v[92:95], v49 offset:2048\n"
    "s_waitcnt lgkmcnt(0)\n"
    "s_add_u32 m0, s18, 0\n"
    "s_nop 0\n"
    "buffer_load_dwordx4 v32, s[20:23], s64 offen lds\n"
    "buffer_load_dwordx4 v33, s[20:23], s64 offen offset:1024 lds\n"
    "buffer_load_dwordx4 v34, s[20:23], s64 offen offset:2048 lds\n"
    "buffer_load_dwordx4 v35, s[20:23], s64 offen offset:3072 lds\n"
    "s_add_u32 s64, s64, 0x80\n"
    "s_cmp_eq_u32 s64, 0x500\n"
    "s_cselect_b32 s64, 0, s64\n"
    "v_dot2c_f32_bf16 v72, v144, v80\n"
    "v_dot2c_f32_bf16 v73, v80, v80\n"
    "v_dot2c_f32_bf16 v72, v144, v81\n"
    "v_dot2c_f32_bf16 v73, v81, v81\n"
    "v_dot2c_f32_bf16 v72, v144, v82\n"
    "v_dot2c_f32_bf16 v73, v82, v82\n"
    "v_dot2c_f32_bf16 v72, v144, v83\n"
    "v_dot2c_f32_bf16 v73, v83, v83\n"
    "v_dot2c_f32_bf16 v72, v144, v84\n"
    "v_dot2c_f32_bf16 v73, v84, v84\n"
    "v_dot2c_f32_bf16 v72, v144, v85\n"
    "v_dot2c_f32_bf16 v73, v85, v85\n"
    "v_dot2c_f32_bf16 v72, v144, v86\n"
    "v_dot2c_f32_bf16 v73, v86, v86\n"
    "v_dot2c_f32_bf16 v72, v144, v87\n"
    "v_dot2c_f32_bf16 v73, v87, v87\n"
    "v_dot2c_f32_bf16 v74, v144, v88\n"
    "v_dot2c_f32_bf16 v75, v88, v88\n"
    "v_dot2c_f32_bf16 v74, v144, v89\n"
    "v_dot2c_f32_bf16 v75, v89, v89\n"
    "v_dot2c_f32_bf16 v74, v144, v90\n"
    "v_dot2c_f32_bf16 v75, v90, v90\n"
    "v_dot2c_f32_bf16 v74, v144, v91\n"
    "v_dot2c_f32_bf16 v75, v91, v91\n"
    "v_dot2c_f32_bf16 v74, v144, v92\n"
    "v_dot2c_f32_bf16 v75, v92, v92\n"
    "v_dot2c_f32_bf16 v74, v144, v93\n"
    "v_dot2c_f32_bf16 v75, v93, v93\n"
    "v_dot2c_f32_bf16 v74, v144, v94\n"
    "v_dot2c_f32_bf16 v75, v94, v94\n"
    "v_dot2c_f32_bf16 v74, v144, v95\n"
    "v_dot2c_f32_bf16 v75, v95, v95\n"
    "s_waitcnt vmcnt(16)\n"
    "ds_read_b128 v[96:99], v48 offset:16384\n"
    "ds_read_b128 v[100:103], v49 offset:16384\n"
    "ds_read_b128 v[104:107], v48 offset:18432\n"
    "ds_read_b128 v[108:111], v49 offset:18432\n"
    "s_waitcnt lgkmcnt(0)\n"
    "s_add_u32 m0, s18, 0x4000\n"
    "s_nop 0\n"
    "buffer_load_dwordx4 v32, s[20:23], s64 offen lds\n"
    "buffer_load_dwordx4 v33, s[20:23], s64 offen offset:1024 lds\n"
    "buffer_load_dwordx4 v34, s[20:23], s64 offen offset:2048 lds\n"
    "buffer_load_dwordx4 v35, s[20:23], s64 offen offset:3072 lds\n"
    "s_add_u32 s64, s64, 0x80\n"
    "s_cmp_eq_u32 s64, 0x500\n"
    "s_cselect_b32 s64, 0, s64\n"
    "v_dot2c_f32_bf16 v72, v144, v96\n"
    "v_dot2c_f32_bf16 v73, v96, v96\n"
    "v_dot2c_f32_bf16 v72, v144, v97\n"
    "v_dot2c_f32_bf16 v73, v97, v97\n"
    "v_dot2c_f32_bf16 v72, v144, v98\n"
    "v_dot2c_f32_bf16 v73, v98, v98\n"
    "v_dot2c_f32_bf16 v72, v144, v99\n"
    "v_dot2c_f32_bf16 v73, v99, v99\n"
    "v_dot2c_f32_bf16 v72, v144, v100\n"
    "v_dot2c_f32_bf16 v73, v100, v100\n"
    "v_dot2c_f32_bf16 v72, v144, v101\n"
    "v_dot2c_f32_bf16 v73, v101, v101\n"
    "v_dot2c_f32_bf16 v72, v144, v102\n"
    "v_dot2c_f32_bf16 v73, v102, v102\n"
    "v_dot2c_f32_bf16 v72, v144, v103\n"
    "v_dot2c_f32_bf16 v73, v103, v103\n"
    "v_dot2c_f32_bf16 v74, v144, v104\n"
    "v_dot2c_f32_bf16 v75, v104, v104\n"
    "v_dot2c_f32_bf16 v74, v144, v105\n"
    "v_dot2c_f32_bf16 v75, v105, v105\n"
    "v_dot2c_f32_bf16 v74, v144, v106\n"
    "v_dot2c_f32_bf16 v75, v106, v106\n"
    "v_dot2c_f32_bf16 v74, v144, v107\n"
    "v_dot2c_f32_bf16 v75, v107, v107\n"
    "v_dot2c_f32_bf16 v74, v144, v108\n"
    "v_dot2c_f32_bf16 v75, v108, v108\n"
    "v_dot2c_f32_bf16 v74, v144, v109\n"
    "v_dot2c_f32_bf16 v75, v109, v109\n"
    "v_dot2c_f32_bf16 v74, v144, v110\n"
    "v_dot2c_f32_bf16 v75, v110, v110\n"
    "v_dot2c_f32_bf16 v74, v144, v111\n"
    "v_dot2c_f32_bf16 v75, v111, v111\n"
    "s_waitcnt vmcnt(12)\n"
    "ds_read_b128 v[80:83], v48 offset:32768\n"
    "ds_read_b128 v[84:87], v49 offset:32768\n"
    "ds_read_b128 v[88:91], v48 offset:34816\n"
    "ds_read_b128 v[92:95], v49 offset:34816\n"
    "s_waitcnt lgkmcnt(0)\n"
    "s_add_u32 m0, s18, 0x8000\n"
    "s_nop 0\n"
    "buffer_load_dwordx4 v32, s[20:23], s64 offen lds\n"
    "buffer_load_dwordx4 v33, s[20:23], s64 offen offset:1024 lds\n"
    "buffer_load_dwordx4 v34, s[20:23], s64 offen offset:2048 lds\n"
    "buffer_load_dwordx4 v35, s[20:23], s64 offen offset:3072 lds\n"
    "s_add_u32 s64, s64, 0x80\n"
    "s_cmp_eq_u32 s64, 0x500\n"
    "s_cselect_b32 s64, 0, s64\n"
    "v_dot2c_f32_bf16 v72, v144, v80\n"
    "v_dot2c_f32_bf16 v73, v80, v80\n"
    "v_dot2c_f32_bf16 v72, v144, v81\n"
    "v_dot2c_f32_bf16 v73, v81, v81\n"
    "v_dot2c_f32_bf16 v72, v144, v82\n"
    "v_dot2c_f32_bf16 v73, v82, v82\n"
    "v_dot2c_f32_bf16 v72, v144, v83\n"
    "v_dot2c_f32_bf16 v73, v83, v83\n"
    "v_dot2c_f32_bf16 v72, v144, v84\n"
    "v_dot2c_f32_bf16 v73, v84, v84\n"
    "v_dot2c_f32_bf16 v72, v144, v85\n"
    "v_dot2c_f32_bf16 v73, v85, v85\n"
    "v_dot2c_f32_bf16 v72, v144, v86\n"
    "v_dot2c_f32_bf16 v73, v86, v86\n"
    "v_dot2c_f32_bf16 v72, v144, v87\n"
    "v_dot2c_f32_bf16 v73, v87, v87\n"
    "v_dot2c_f32_bf16 v74, v144, v88\n"
    "v_dot2c_f32_bf16 v75, v88, v88\n"
    "v_dot2c_f32_bf16 v74, v144, v89\n"
    "v_dot2c_f32_bf16 v75, v89, v89\n"
    "v_dot2c_f32_bf16 v74, v144, v90\n"
    "v_dot2c_f32_bf16 v75, v90, v90\n"
    "v_dot2c_f32_bf16 v74, v144, v91\n"
    "v_dot2c_f32_bf16 v75, v91, v91\n"
    "v_dot2c_f32_bf16 v74, v144, v92\n"
    "v_dot2c_f32_bf16 v75, v92, v92\n"
    "v_dot2c_f32_bf16 v74, v144, v93\n"
    "v_dot2c_f32_bf16 v75, v93, v93\n"
    "v_dot2c_f32_bf16 v74, v144, v94\n"
    "v_dot2c_f32_bf16 v75, v94, v94\n"
    "v_dot2c_f32_bf16 v74, v144, v95\n"
    "v_dot2c_f32_bf16 v75, v95, v95\n"
    "s_waitcnt vmcnt(12)\n"
    "ds_read_b128 v[96:99], v48 offset:49152\n"
    "ds_read_b128 v[100:103], v49 offset:49152\n"
    "ds_read_b128 v[104:107], v48 offset:51200\n"
    "ds_read_b128 v[108:111], v49 offset:51200\n"
    "s_waitcnt lgkmcnt(0)\n"
    "s_add_u32 m0, s18, 0xc000\n"
    "s_nop 0\n"
    "buffer_load_dwordx4 v32, s[20:23], s64 offen lds\n"
    "buffer_load_dwordx4 v33, s[20:23], s64 offen offset:1024 lds\n"
    "buffer_load_dwordx4 v34, s[20:23], s64 offen offset:2048 lds\n"
    "buffer_load_dwordx4 v35, s[20:23], s64 offen offset:3072 lds\n"
    "s_add_u32 s64, s64, 0x80\n"
    "s_cmp_eq_u32 s64, 0x500\n"
    "s_cselect_b32 s64, 0, s64\n"
    "v_dot2c_f32_bf16 v72, v144, v96\n"
    "v_dot2c_f32_bf16 v73, v96, v96\n"
    "v_dot2c_f32_bf16 v72, v144, v97\n"
    "v_dot2c_f32_bf16 v73, v97, v97\n"
    "v_dot2c_f32_bf16 v72, v144, v98\n"
    "v_dot2c_f32_bf16 v73, v98, v98\n"
    "v_dot2c_f32_bf16 v72, v144, v99\n"
    "v_dot2c_f32_bf16 v73, v99, v99\n"
    "v_dot2c_f32_bf16 v72, v144, v100\n"
    "v_dot2c_f32_bf16 v73, v100, v100\n"
    "v_dot2c_f32_bf16 v72, v144, v101\n"
    "v_dot2c_f32_bf16 v73, v101, v101\n"
    "v_dot2c_f32_bf16 v72, v144, v102\n"
    "v_dot2c_f32_bf16 v73, v102, v102\n"
    "v_dot2c_f32_bf16 v72, v144, v103\n"
    "v_dot2c_f32_bf16 v73, v103, v103\n"
    "v_dot2c_f32_bf16 v74, v144, v104\n"
    "v_dot2c_f32_bf16 v75, v104, v104\n"
    "v_dot2c_f32_bf16 v74, v144, v105\n"
    "v_dot2c_f32_bf16 v75, v105, v105\n"
    "v_dot2c_f32_bf16 v74, v144, v106\n"
    "v_dot2c_f32_bf16 v75, v106, v106\n"
    "v_dot2c_f32_bf16 v74, v144, v107\n"
    "v_dot2c_f32_bf16 v75, v107, v107\n"
    "v_dot2c_f32_bf16 v74, v144, v108\n"
    "v_dot2c_f32_bf16 v75, v108, v108\n"
    "v_dot2c_f32_bf16 v74, v144, v109\n"
    "v_dot2c_f32_bf16 v75, v109, v109\n"
    "v_dot2c_f32_bf16 v74, v144, v110\n"
    "v_dot2c_f32_bf16 v75, v110, v110\n"
    "v_dot2c_f32_bf16 v74, v144, v111\n"
    "v_dot2c_f32_bf16 v75, v111, v111\n"
    "s_waitcnt vmcnt(12)\n"
    "ds_read_b128 v[80:83], v48\n"
    "ds_read_b128 v[84:87], v49\n"
    "ds_read_b128 v[88:91], v48 offset:2048\n"
    "ds_read_b128 v[92:95], v49 offset:2048\n"
    "s_waitcnt lgkmcnt(0)\n"
    "s_add_u32 m0, s18, 0\n"
    "s_nop 0\n"
    "buffer_load_dwordx4 v32, s[20:23], s64 offen lds\n"
    "buffer_load_dwordx4 v33, s[20:23], s64 offen offset:1024 lds\n"
    "buffer_load_dwordx4 v34, s[20:23], s64 offen offset:2048 lds\n"
    "buffer_load_dwordx4 v35, s[20:23], s64 offen offset:3072 lds\n"
    "s_add_u32 s64, s64, 0x80\n"
    "s_cmp_eq_u32 s64, 0x500\n"
    "s_cselect_b32 s64, 0, s64\n"
    "v_dot2c_f32_bf16 v72, v144, v80\n"
    "v_dot2c_f32_bf16 v73, v80, v80\n"
    "v_dot2c_f32_bf16 v72, v144, v81\n"
    "v_dot2c_f32_bf16 v73, v81, v81\n"
    "v_dot2c_f32_bf16 v72, v144, v82\n"
    "v_dot2c_f32_bf16 v73, v82, v82\n"
    "v_dot2c_f32_bf16 v72, v144, v83\n"
    "v_dot2c_f32_bf16 v73, v83, v83\n"
    "v_dot2c_f32_bf16 v72, v144, v84\n"
    "v_dot2c_f32_bf16 v73, v84, v84\n"
    "v_dot2c_f32_bf16 v72, v144, v85\n"
    "v_dot2c_f32_bf16 v73, v85, v85\n"
    "v_dot2c_f32_bf16 v72, v144, v86\n"
    "v_dot2c_f32_bf16 v73, v86, v86\n"
    "v_dot2c_f32_bf16 v72, v144, v87\n"
    "v_dot2c_f32_bf16 v73, v87, v87\n"
    "v_dot2c_f32_bf16 v74, v144, v88\n"
    "v_dot2c_f32_bf16 v75, v88, v88\n"
    "v_dot2c_f32_bf16 v74, v144, v89\n"
    "v_dot2c_f32_bf16 v75, v89, v89\n"
    "v_dot2c_f32_bf16 v74, v144, v90\n"
    "v_dot2c_f32_bf16 v75, v90, v90\n"
    "v_dot2c_f32_bf16 v74, v144, v91\n"
    "v_dot2c_f32_bf16 v75, v91, v91\n"
    "v_dot2c_f32_bf16 v74, v144, v92\n"
    "v_dot2c_f32_bf16 v75, v92, v92\n"
    "v_dot2c_f32_bf16 v74, v144, v93\n"
    "v_dot2c_f32_bf16 v75, v93, v93\n"
    "v_dot2c_f32_bf16 v74, v144, v94\n"
    "v_dot2c_f32_bf16 v75, v94, v94\n"
    "v_dot2c_f32_bf16 v74, v144, v95\n"
    "v_dot2c_f32_bf16 v75, v95, v95\n"
    "s_waitcnt vmcnt(12)\n"
    "ds_read_b128 v[96:99], v48 offset:16384\n"
    "ds_read_b128 v[100:103], v49 offset:16384\n"
    "ds_read_b128 v[104:107], v48 offset:18432\n"
    "ds_read_b128 v[108:111], v49 offset:18432\n"
    "s_waitcnt lgkmcnt(0)\n"
    "s_add_u32 m0, s18, 0x4000\n"
    "s_nop 0\n"
    "buffer_load_dwordx4 v32, s[20:23], s64 offen lds\n"
    "buffer_load_dwordx4 v33, s[20:23], s64 offen offset:1024 lds\n"
    "buffer_load_dwordx4 v34, s[20:23], s64 offen offset:2048 lds\n"
    "buffer_load_dwordx4 v35, s[20:23], s64 offen offset:3072 lds\n"
    "s_add_u32 s64, s64, 0x80\n"
    "s_cmp_eq_u32 s64, 0x500\n"
    "s_cselect_b32 s64, 0, s64\n"
    "v_dot2c_f32_bf16 v72, v144, v96\n"
    "v_dot2c_f32_bf16 v73, v96, v96\n"
    "v_dot2c_f32_bf16 v72, v144, v97\n"
    "v_dot2c_f32_bf16 v73, v97, v97\n"
    "v_dot2c_f32_bf16 v72, v144, v98\n"
    "v_dot2c_f32_bf16 v73, v98, v98\n"
    "v_dot2c_f32_bf16 v72, v144, v99\n"
    "v_dot2c_f32_bf16 v73, v99, v99\n"
    "v_dot2c_f32_bf16 v72, v144, v100\n"
    "v_dot2c_f32_bf16 v73, v100, v100\n"
    "v_dot2c_f32_bf16 v72, v144, v101\n"
    "v_dot2c_f32_bf16 v73, v101, v101\n"
    "v_dot2c_f32_bf16 v72, v144, v102\n"
    "v_dot2c_f32_bf16 v73, v102, v102\n"
    "v_dot2c_f32_bf16 v72, v144, v103\n"
    "v_dot2c_f32_bf16 v73, v103, v103\n"
    "v_dot2c_f32_bf16 v74, v144, v104\n"
    "v_dot2c_f32_bf16 v75, v104, v104\n"
    "v_dot2c_f32_bf16 v74, v144, v105\n"
    "v_dot2c_f32_bf16 v75, v105, v105\n"
    "v_dot2c_f32_bf16 v74, v144, v106\n"
    "v_dot2c_f32_bf16 v75, v106, v106\n"
    "v_dot2c_f32_bf16 v74, v144, v107\n"
    "v_dot2c_f32_bf16 v75, v107, v107\n"
    "v_dot2c_f32_bf16 v74, v144, v108\n"
    "v_dot2c_f32_bf16 v75, v108, v108\n"
    "v_dot2c_f32_bf16 v74, v144, v109\n"
    "v_dot2c_f32_bf16 v75, v109, v109\n"
    "v_dot2c_f32_bf16 v74, v144, v110\n"
    "v_dot2c_f32_bf16 v75, v110, v110\n"
    "v_dot2c_f32_bf16 v74, v144, v111\n"
    "v_dot2c_f32_bf16 v75, v111, v111\n"
    "s_waitcnt vmcnt(12)\n"
    "ds_read_b128 v[80:83], v48 offset:32768\n"
    "ds_read_b128 v[84:87], v49 offset:32768\n"
    "ds_read_b128 v[88:91], v48 offset:34816\n"
    "ds_read_b128 v[92:95], v49 offset:34816\n"
    "s_waitcnt lgkmcnt(0)\n"
    "v_dot2c_f32_bf16 v72, v144, v80\n"
    "v_dot2c_f32_bf16 v73, v80, v80\n"
    "v_dot2c_f32_bf16 v72, v144, v81\n"
    "v_dot2c_f32_bf16 v73, v81, v81\n"
    "v_dot2c_f32_bf16 v72, v144, v82\n"
    "v_dot2c_f32_bf16 v73, v82, v82\n"
    "v_dot2c_f32_bf16 v72, v144, v83\n"
    "v_dot2c_f32_bf16 v73, v83, v83\n"
    "v_dot2c_f32_bf16 v72, v144, v84\n"
    "v_dot2c_f32_bf16 v73, v84, v84\n"
    "v_dot2c_f32_bf16 v72, v144, v85\n"
    "v_dot2c_f32_bf16 v73, v85, v85\n"
    "v_dot2c_f32_bf16 v72, v144, v86\n"
    "v_dot2c_f32_bf16 v73, v86, v86\n"
    "v_dot2c_f32_bf16 v72, v144, v87\n"
    "v_dot2c_f32_bf16 v73, v87, v87\n"
    "v_dot2c_f32_bf16 v74, v144, v88\n"
    "v_dot2c_f32_bf16 v75, v88, v88\n"
    "v_dot2c_f32_bf16 v74, v144, v89\n"
    "v_dot2c_f32_bf16 v75, v89, v89\n"
    "v_dot2c_f32_bf16 v74, v144, v90\n"
    "v_dot2c_f32_bf16 v75, v90, v90\n"
    "v_dot2c_f32_bf16 v74, v144, v91\n"
    "v_dot2c_f32_bf16 v75, v91, v91\n"
    "v_dot2c_f32_bf16 v74, v144, v92\n"
    "v_dot2c_f32_bf16 v75, v92, v92\n"
    "v_dot2c_f32_bf16 v74, v144, v93\n"
    "v_dot2c_f32_bf16 v75, v93, v93\n"
    "v_dot2c_f32_bf16 v74, v144, v94\n"
    "v_dot2c_f32_bf16 v75, v94, v94\n"
    "v_dot2c_f32_bf16 v74, v144, v95\n"
    "v_dot2c_f32_bf16 v75, v95, v95\n"
    "s_waitcnt vmcnt(8)\n"
    "ds_read_b128 v[96:99], v48 offset:49152\n"
    "ds_read_b128 v[100:103], v49 offset:49152\n"
    "ds_read_b128 v[104:107], v48 offset:51200\n"
    "ds_read_b128 v[108:111], v49 offset:51200\n"
    "s_waitcnt lgkmcnt(0)\n"
    "v_dot2c_f32_bf16 v72, v144, v96\n"
    "v_dot2c_f32_bf16 v73, v96, v96\n"
    "v_dot2c_f32_bf16 v72, v144, v97\n"
    "v_dot2c_f32_bf16 v73, v97, v97\n"
    "v_dot2c_f32_bf16 v72, v144, v98\n"
    "v_dot2c_f32_bf16 v73, v98, v98\n"
    "v_dot2c_f32_bf16 v72, v144, v99\n"
    "v_dot2c_f32_bf16 v73, v99, v99\n"
    "v_dot2c_f32_bf16 v72, v144, v100\n"
    "v_dot2c_f32_bf16 v73, v100, v100\n"
    "v_dot2c_f32_bf16 v72, v144, v101\n"
    "v_dot2c_f32_bf16 v73, v101, v101\n"
    "v_dot2c_f32_bf16 v72, v144, v102\n"
    "v_dot2c_f32_bf16 v73, v102, v102\n"
    "v_dot2c_f32_bf16 v72, v144, v103\n"
    "v_dot2c_f32_bf16 v73, v103, v103\n"
    "v_dot2c_f32_bf16 v74, v144, v104\n"
    "v_dot2c_f32_bf16 v75, v104, v104\n"
    "v_dot2c_f32_bf16 v74, v144, v105\n"
    "v_dot2c_f32_bf16 v75, v105, v105\n"
    "v_dot2c_f32_bf16 v74, v144, v106\n"
    "v_dot2c_f32_bf16 v75, v106, v106\n"
    "v_dot2c_f32_bf16 v74, v144, v107\n"
    "v_dot2c_f32_bf16 v75, v107, v107\n"
    "v_dot2c_f32_bf16 v74, v144, v108\n"
    "v_dot2c_f32_bf16 v75, v108, v108\n"
    "v_dot2c_f32_bf16 v74, v144, v109\n"
    "v_dot2c_f32_bf16 v75, v109, v109\n"
    "v_dot2c_f32_bf16 v74, v144, v110\n"
    "v_dot2c_f32_bf16 v75, v110, v110\n"
    "v_dot2c_f32_bf16 v74, v144, v111\n"
    "v_dot2c_f32_bf16 v75, v111, v111\n"
    "s_waitcnt vmcnt(4)\n"
    "ds_read_b128 v[80:83], v48\n"
    "ds_read_b128 v[84:87], v49\n"
    "ds_read_b128 v[88:91], v48 offset:2048\n"
    "ds_read_b128 v[92:95], v49 offset:2048\n"
    "s_waitcnt lgkmcnt(0)\n"
    "v_dot2c_f32_bf16 v72, v144, v80\n"
    "v_dot2c_f32_bf16 v73, v80, v80\n"
    "v_dot2c_f32_bf16 v72, v144, v81\n"
    "v_dot2c_f32_bf16 v73, v81, v81\n"
    "v_dot2c_f32_bf16 v72, v144, v82\n"
    "v_dot2c_f32_bf16 v73, v82, v82\n"
    "v_dot2c_f32_bf16 v72, v144, v83\n"
    "v_dot2c_f32_bf16 v73, v83, v83\n"
    "v_dot2c_f32_bf16 v72, v144, v84\n"
    "v_dot2c_f32_bf16 v73, v84, v84\n"
    "v_dot2c_f32_bf16 v72, v144, v85\n"
    "v_dot2c_f32_bf16 v73, v85, v85\n"
    "v_dot2c_f32_bf16 v72, v144, v86\n"
    "v_dot2c_f32_bf16 v73, v86, v86\n"
    "v_dot2c_f32_bf16 v72, v144, v87\n"
    "v_dot2c_f32_bf16 v73, v87, v87\n"
    "v_dot2c_f32_bf16 v74, v144, v88\n"
    "v_dot2c_f32_bf16 v75, v88, v88\n"
    "v_dot2c_f32_bf16 v74, v144, v89\n"
    "v_dot2c_f32_bf16 v75, v89, v89\n"
    "v_dot2c_f32_bf16 v74, v144, v90\n"
    "v_dot2c_f32_bf16 v75, v90, v90\n"
    "v_dot2c_f32_bf16 v74, v144, v91\n"
    "v_dot2c_f32_bf16 v75, v91, v91\n"
    "v_dot2c_f32_bf16 v74, v144, v92\n"
    "v_dot2c_f32_bf16 v75, v92, v92\n"
    "v_dot2c_f32_bf16 v74, v144, v93\n"
    "v_dot2c_f32_bf16 v75, v93, v93\n"
    "v_dot2c_f32_bf16 v74, v144, v94\n"
    "v_dot2c_f32_bf16 v75, v94, v94\n"
    "v_dot2c_f32_bf16 v74, v144, v95\n"
    "v_dot2c_f32_bf16 v75, v95, v95\n"
    "s_waitcnt vmcnt(0)\n"
    "ds_read_b128 v[96:99], v48 offset:16384\n"
    "ds_read_b128 v[100:103], v49 offset:16384\n"
    "ds_read_b128 v[104:107], v48 offset:18432\n"
    "ds_read_b128 v[108:111], v49 offset:18432\n"
    "s_waitcnt lgkmcnt(0)\n"
    "v_dot2c_f32_bf16 v72, v144, v96\n"
    "v_dot2c_f32_bf16 v73, v96, v96\n"
    "v_dot2c_f32_bf16 v72, v144, v97\n"
    "v_dot2c_f32_bf16 v73, v97, v97\n"
    "v_dot2c_f32_bf16 v72, v144, v98\n"
    "v_dot2c_f32_bf16 v73, v98, v98\n"
    "v_dot2c_f32_bf16 v72, v144, v99\n"
    "v_dot2c_f32_bf16 v73, v99, v99\n"
    "v_dot2c_f32_bf16 v72, v144, v100\n"
    "v_dot2c_f32_bf16 v73, v100, v100\n"
    "v_dot2c_f32_bf16 v72, v144, v101\n"
    "v_dot2c_f32_bf16 v73, v101, v101\n"
    "v_dot2c_f32_bf16 v72, v144, v102\n"
    "v_dot2c_f32_bf16 v73, v102, v102\n"
    "v_dot2c_f32_bf16 v72, v144, v103\n"
    "v_dot2c_f32_bf16 v73, v103, v103\n"
    "v_dot2c_f32_bf16 v74, v144, v104\n"
    "v_dot2c_f32_bf16 v75, v104, v104\n"
    "v_dot2c_f32_bf16 v74, v144, v105\n"
    "v_dot2c_f32_bf16 v75, v105, v105\n"
    "v_dot2c_f32_bf16 v74, v144, v106\n"
    "v_dot2c_f32_bf16 v75, v106, v106\n"
    "v_dot2c_f32_bf16 v74, v144, v107\n"
    "v_dot2c_f32_bf16 v75, v107, v107\n"
    "v_dot2c_f32_bf16 v74, v144, v108\n"
    "v_dot2c_f32_bf16 v75, v108, v108\n"
    "v_dot2c_f32_bf16 v74, v144, v109\n"
    "v_dot2c_f32_bf16 v75, v109, v109\n"
    "v_dot2c_f32_bf16 v74, v144, v110\n"
    "v_dot2c_f32_bf16 v75, v110, v110\n"
    "v_dot2c_f32_bf16 v74, v144, v111\n"
    "v_dot2c_f32_bf16 v75, v111, v111\n"
    "v_readfirstlane_b32 s96, v254\n"
    "s_add_u32 s96, s96, 1\n"
    "v_permlane16_swap_b32 v72, v73\n"
    "v_permlane16_swap_b32 v74, v75\n"
    "v_add_f32 v72, v72, v73\n"
    "v_add_f32 v74, v74, v75\n"
    "s_nop 1\n"
    "v_permlane32_swap_b32 v72, v74\n"
    "v_add_f32 v72, v72, v74\n"
    "v_mov_b32 v73, v72\n"
    "s_nop 1\n"
    "v_permlane16_swap_b32 v72, v73\n"
    "v_mul_f32 v72, 0x3acccccd, v72\n"
    "v_mul_f32 v73, 0x3acccccd, v73\n"
    "v_fma_f32 v73, -v72, v72, v73\n"
    "v_max_f32 v73, 0, v73\n"
    "v_add_f32 v73, 0x3727c5ac, v73\n"
    "v_rsq_f32 v152, v73\n"
    "v_mov_b32 v153, s96\n"
    "v_mov_b32 v155, s96\n"
    "v_mul_f32_e64 v154, -v72, v152\n"
    "buffer_store_dwordx4 v[152:155], v64, s[24:27], 0 offen sc1\n"
    "v_mov_b32 v62, 0x40000000\n"
    "v_mov_b32 v56, 0\n"
    "v_mov_b32 v57, 0\n"
    "v_mov_b32 v58, 0\n"
    "v_mov_b32 v59, 0\n"
    "v_mov_b32 v60, 0\n"
    "v_mov_b32 v61, 0\n"
    ".Lfb_p1j_3:\n"
    "; ---- H1 (t~17546)\n"
    "s_waitcnt vmcnt(0)\n"
    "s_barrier\n"
    "s_cmp_lg_u32 s16, 0\n"
    "s_cbranch_scc1 .Lfb_fs_10\n"
    "s_mov_b32 exec_hi, 0\n"
    "v_mov_b32 v148, s96\n"
    "buffer_store_dword v148, v252, s[24:27], 0 offen sc1\n"
    "s_mov_b64 exec, -1\n"
    ".Lfb_fs_10:\n"
    "v_add_u32 v24, s86, v25\n"
    "v_add_u32 v1, 0x1000, v0\n"
    "v_lshlrev_b32 v2, 4, v234\n"
    "v_cmp_gt_u32 vcc, 32, v235\n"
    "v_lshlrev_b32 v245, 8, v239\n"
    "v_add_u32 v245, v245, v248\n"
    "v_lshl_add_u32 v11, v249, 3, v245\n"
    "v_add_u32 v11, 0x800, v11\n"
    "v_mul_u32_u24 v246, 0xc000, v249\n"
    "v_add_u32 v13, v245, v246\n"
    "v_add_u32 v13, 0x400, v13\n"
    "v_mul_u32_u24 v246, 0x5000, v249\n"
    "v_add3_u32 v7, v245, v246, s12\n"
    "v_mul_u32_u24 v246, 0x5000, v237\n"
    "v_lshl_add_u32 v246, v236, 3, v246\n"
    "v_add_u32 v8, s13, v246\n"
    "v_lshlrev_b32 v246, 8, v237\n"
    "v_lshl_add_u32 v246, v236, 3, v246\n"
    "v_add_u32 v231, 0x1000, v246\n"
    "s_add_u32 s0, s16, 1\n"
    "s_and_b32 s0, s0, 3\n"
    "s_mul_i32 s0, s0, 0x3c00\n"
    "s_add_u32 s0, s0, 0\n"
    "v_add_u32 v14, s0, v0\n"
    "s_add_u32 s0, s16, 2\n"
    "s_and_b32 s0, s0, 3\n"
    "s_mul_i32 s0, s0, 0x3c00\n"
    "s_add_u32 s0, s0, 0x1400\n"
    "v_add_u32 v15, s0, v0\n"
    "s_add_u32 s0, s16, 3\n"
    "s_and_b32 s0, s0, 3\n"
    "s_mul_i32 s0, s0, 0x3c00\n"
    "s_add_u32 s0, s0, 0x2800\n"
    "v_add_u32 v16, s0, v0\n"
    "s_mul_i32 s0, s16, 0x3c00\n"
    "v_add_u32 v17, s0, v0\n"
    "v_lshlrev_b32 v245, 3, v236\n"
    "v_add_u32 v19, 0x14000, v245\n"
    "v_lshl_add_u32 v18, v244, 7, v19\n"
    "v_mov_b32 v247, 0x40000000\n"
    "s_and_b32 s0, s82, 31\n"
    "s_lshl_b32 s0, s0, 5\n"
    "s_add_u32 s0, s0, s8\n"
    "v_add_u32 v245, s0, v25\n"
    "v_cndmask_b32 v12, v247, v245, vcc\n"
    "v_add_u32 v245, s89, v25\n"
    "v_add_u32 v246, s90, v25\n"
    "v_cndmask_b32 v31, v246, v245, vcc\n"
    "v_add_u32 v3, s9, v0\n"
    "v_add_u32 v26, 0xa000, v3\n"
    "v_lshl_add_u32 v4, v238, 4, s10\n"
    "v_add_u32 v27, 0xa000, v4\n"
    "v_cndmask_b32 v5, v27, v4, vcc\n"
    "v_cmp_gt_u32 vcc, 20, v238\n"
    "v_mul_u32_u24 v245, 0x2800, v239\n"
    "v_lshl_add_u32 v245, v238, 4, v245\n"
    "v_add_u32 v245, s11, v245\n"
    "v_add_u32 v246, 0x5000, v245\n"
    "v_mov_b32 v247, s87\n"
    "v_cndmask_b32 v6, v247, v245, vcc\n"
    "v_cndmask_b32 v28, v247, v246, vcc\n"
    "v_lshl_add_u32 v22, v241, 4, s88\n"
    "v_lshl_add_u32 v23, v241, 3, s88\n"
    "v_add_u32 v23, 0x80, v23\n"
    "v_lshrrev_b32 v246, 2, v235\n"
    "v_mul_u32_u24 v245, 0x500, v246\n"
    "v_add3_u32 v9, v245, v22, s14\n"
    "v_add3_u32 v10, v245, v23, s14\n"
    "v_lshl_add_u32 v245, v241, 2, v246\n"
    "v_and_b32 v245, 15, v245\n"
    "v_lshl_add_u32 v245, v241, 4, v245\n"
    "v_lshlrev_b32 v251, 4, v245\n"
    "v_lshl_add_u32 v245, v237, 2, v236\n"
    "v_and_b32 v245, 15, v245\n"
    "v_lshl_add_u32 v245, v237, 4, v245\n"
    "v_lshlrev_b32 v230, 4, v245\n"
    "v_lshl_add_u32 v20, v237, 4, s15\n"
    "v_lshl_add_u32 v21, v237, 3, s15\n"
    "v_add_u32 v21, 64, v21\n"
    "s_mov_b64 s[10:11], s[80:81]\n"
    "s_mov_b64 s[12:13], s[82:83]\n"
    "s_mov_b64 s[14:15], s[84:85]\n"
    "s_mov_b32 s9, s91\n"
    "s_mov_b32 s5, s95\n"
    "s_mov_b32 s7, s77\n"
    "s_cmp_lg_u32 s16, 0\n"
    "s_cbranch_scc1 .Lfb_fs_11\n"
    "s_mov_b64 exec, 1\n"
    "v_mov_b32 v148, s96\n"
    "buffer_store_dword v148, v25, s[24:27], s7 offen sc1\n"
    "s_mov_b64 exec, -1\n"
    ".Lfb_fs_11:\n"
    "s_mul_i32 s18, s16, 0x5000\n"
    "s_mul_i32 s19, s16, 0x4c00\n"
    "s_add_u32 s19, s19, 0x14400\n"
    "s_and_b32 s6, s16, 1\n"
    "v_add_u32 v29, s18, v0\n"
    "v_add_u32 v30, s19, v0\n"
    "s_add_u32 s48, s16, 1\n"
    "s_and_b32 s48, s48, 3\n"
    "s_add_u32 s48, s48, s5\n"
    "s_add_u32 s49, s16, 2\n"
    "s_and_b32 s49, s49, 3\n"
    "s_add_u32 s49, s49, s5\n"
    "s_add_u32 s50, s16, 3\n"
    "s_and_b32 s50, s50, 3\n"
    "s_add_u32 s50, s50, s5\n"
    "s_add_u32 s51, s16, 4\n"
    "s_and_b32 s51, s51, 3\n"
    "s_add_u32 s51, s51, s5\n"
    "s_mov_b32 s64, 0\n"
    "s_mov_b32 s65, 15\n"
    "s_mov_b32 s66, 1\n"
    "s_mov_b32 s67, 31\n"
    "s_mov_b32 s68, 3\n"
    "s_mov_b32 s69, 63\n"
    "s_mov_b32 s70, 7\n"
    "s_mov_b32 s71, 0x7f\n"
    "s_mov_b32 s72, 0xffff\n"
    "s_mov_b32 s73, 0xfffff\n"
    "s_mov_b32 s74, 0x1ffff\n"
    "s_mov_b32 s75, 0x1fffff\n"
    "s_mov_b32 s76, 0x3ffff\n"
    "s_mov_b32 s77, 0x3fffff\n"
    "s_mov_b32 s78, 0x7ffff\n"
    "s_mov_b32 s79, 0x7fffff\n"
    "s_mov_b32 s80, 0xff\n"
    "s_mov_b32 s81, 0xfff\n"
    "s_mov_b32 s82, 0x1ff\n"
    "s_mov_b32 s83, 0x1fff\n"
    "s_mov_b32 s84, 0x3ff\n"
    "s_mov_b32 s85, 0x3fff\n"
    "s_mov_b32 s86, 0x7ff\n"
    "s_mov_b32 s87, 0x7fff\n"
    "s_mov_b32 s88, 0xffffff\n"
    "s_mov_b32 s89, 0xfffffff\n"
    "s_mov_b32 s90, 0x1ffffff\n"
    "s_mov_b32 s91, 0x1fffffff\n"
    "s_mov_b32 s92, 0x3ffffff\n"
    "s_mov_b32 s93, 0x3fffffff\n"
    "s_mov_b32 s94, 0x7ffffff\n"
    "s_mov_b32 s95, 0x7fffffff\n"
    "v_mov_b32 v229, 0xffff0000\n"
    "v_cndmask_b32_e64 v40, 0, v229, s[66:67]\n"
    "v_cndmask_b32_e64 v40, v40, -1, s[64:65]\n"
    "v_cndmask_b32_e64 v41, 0, v229, s[70:71]\n"
    "v_cndmask_b32_e64 v41, v41, -1, s[68:69]\n"
    "v_cndmask_b32_e64 v42, 0, v229, s[74:75]\n"
    "v_cndmask_b32_e64 v42, v42, -1, s[72:73]\n"
    "v_cndmask_b32_e64 v43, 0, v229, s[78:79]\n"
    "v_cndmask_b32_e64 v43, v43, -1, s[76:77]\n"
    "v_cndmask_b32_e64 v44, 0, v229, s[82:83]\n"
    "v_cndmask_b32_e64 v44, v44, -1, s[80:81]\n"
    "v_cndmask_b32_e64 v45, 0, v229, s[86:87]\n"
    "v_cndmask_b32_e64 v45, v45, -1, s[84:85]\n"
    "v_cndmask_b32_e64 v46, 0, v229, s[90:91]\n"
    "v_cndmask_b32_e64 v46, v46, -1, s[88:89]\n"
    "v_cndmask_b32_e64 v47, 0, v229, s[94:95]\n"
    "v_cndmask_b32_e64 v47, v47, -1, s[92:93]\n"
    "s_mul_i32 s0, s14, 0xa000\n"
    "s_add_u32 s2, s0, 0x18000\n"
    "s_mul_i32 s1, s14, 0xa400\n"
    "s_add_u32 s28, s0, 0x248000\n"
    "s_add_u32 s33, s1, 0x4c6000\n"
    "s_cmp_ge_i32 s14, 8\n"
    "s_cselect_b32 s28, s28, 0x40000000\n"
    "s_cselect_b32 s33, s33, 0x40000000\n"
    "s_add_u32 s34, s33, 0xa400\n"
    "s_add_u32 s29, s0, 0x25c000\n"
    "s_add_u32 s35, s1, 0x4da800\n"
    "s_cmp_ge_i32 s14, 6\n"
    "s_cselect_b32 s29, s29, 0x40000000\n"
    "s_cselect_b32 s35, s35, 0x40000000\n"
    "s_add_u32 s52, s35, 0xa400\n"
    "s_add_u32 s30, s0, 0x270000\n"
    "s_add_u32 s53, s1, 0x4ef000\n"
    "s_cmp_ge_i32 s14, 4\n"
    "s_cselect_b32 s30, s30, 0x40000000\n"
    "s_cselect_b32 s53, s53, 0x40000000\n"
    "s_add_u32 s54, s53, 0xa400\n"
    "s_add_u32 s31, s0, 0x284000\n"
    "s_add_u32 s55, s1, 0x503800\n"
    "s_cmp_ge_i32 s14, 2\n"
    "s_cselect_b32 s31, s31, 0x40000000\n"
    "s_cselect_b32 s55, s55, 0x40000000\n"
    "s_add_u32 s56, s55, 0xa400\n"
    "s_add_u32 s32, s0, 0x298000\n"
    "s_add_u32 s57, s1, 0x518000\n"
    "s_cmp_ge_i32 s14, 0\n"
    "s_cselect_b32 s32, s32, 0x40000000\n"
    "s_cselect_b32 s57, s57, 0x40000000\n"
    "s_add_u32 s58, s57, 0xa400\n"
    "s_cmp_lg_u32 s16, 0\n"
    "s_cbranch_scc1 .Lfb_ps_14\n"
    "s_mov_b64 exec, s[10:11]\n"
    "s_mov_b32 s97, 0\n"
    ".Lfb_pl_12:\n"
    "buffer_load_dword v229, v24, s[24:27], 0 offen sc1\n"
    "s_waitcnt vmcnt(0)\n"
    "v_cmp_ne_u32 vcc, s96, v229\n"
    "s_cmp_eq_u64 vcc, 0\n"
    "s_cbranch_scc1 .Lfb_pd_13\n"
    "s_sleep 1\n"
    "s_add_u32 s97, s97, 1\n"
    "s_cmp_lt_u32 s97, 0x10000\n"
    "s_cbranch_scc1 .Lfb_pl_12\n"
    ".Lfb_pd_13:\n"
    "s_mov_b64 exec, -1\n"
    ".Lfb_ps_14:\n"
    "s_barrier\n"
    ".Lfb_c2:\n"
    "; ---- P2 loads (t~18198)\n"
    "buffer_load_dwordx4 v[48:51], v4, s[24:27], s2 offen sc1\n"
    "buffer_load_dwordx4 v[32:35], v3, s[24:27], s2 offen sc1\n"
    "buffer_load_dwordx4 a[32:35], v3, s[24:27], s32 offen sc1\n"
    "buffer_load_dwordx4 a[56:59], v5, s[24:27], s32 offen sc1\n"
    "buffer_load_dwordx4 a[0:3], v3, s[24:27], s28 offen sc1\n"
    "buffer_load_dwordx4 a[40:43], v5, s[24:27], s28 offen sc1\n"
    "buffer_load_dwordx4 a[4:7], v26, s[24:27], s28 offen sc1\n"
    "buffer_load_dwordx4 a[60:63], v6, s[24:27], s33 offen sc1\n"
    "buffer_load_dwordx4 a[124:127], v6, s[24:27], s57 offen sc1\n"
    "s_mov_b32 s4, 0x5d800000\n"
    "s_cmp_eq_u32 s6, 0\n"
    "s_cselect_b32 vcc_lo, -1, 0\n"
    "s_mov_b32 vcc_hi, 0\n"
    "s_waitcnt vmcnt(8)\n"
    "v_cndmask_b32 v56, 0, v48, vcc\n"
    "v_cndmask_b32 v57, 0, v49, vcc\n"
    "s_cmp_eq_u32 s6, 1\n"
    "s_cselect_b32 vcc_lo, -1, 0\n"
    "s_mov_b32 vcc_hi, 0\n"
    "v_cndmask_b32 v58, 0, v50, vcc\n"
    "v_cndmask_b32 v59, 0, v51, vcc\n"
    "s_cmp_eq_u32 s6, 0\n"
    "s_cselect_b32 vcc_hi, -1, 0\n"
    "s_mov_b32 vcc_lo, 0\n"
    "v_cndmask_b32 v60, 0, v48, vcc\n"
    "v_cndmask_b32 v61, 0, v49, vcc\n"
    "s_cmp_eq_u32 s6, 1\n"
    "s_cselect_b32 vcc_hi, -1, 0\n"
    "s_mov_b32 vcc_lo, 0\n"
    "v_cndmask_b32 v62, 0, v50, vcc\n"
    "buffer_load_dwordx4 a[8:11], v3, s[24:27], s29 offen sc1\n"
    "v_cndmask_b32 v63, 0, v51, vcc\n"
    "s_waitcnt vmcnt(7)\n"
    "v_mfma_f32_32x32x16_bf16 v[120:135], a[32:35], v[32:35], 0\n"
    "s_waitcnt vmcnt(5)\n"
    "v_mfma_f32_32x32x16_bf16 v[104:119], a[0:3], v[32:35], 0\n"
    "v_mfma_f32_32x32x16_bf16 v[120:135], a[56:59], v[56:59], v[120:135]\n"
    "buffer_load_dwordx4 a[44:47], v5, s[24:27], s29 offen sc1\n"
    "s_waitcnt vmcnt(5)\n"
    "v_mfma_f32_32x32x16_bf16 v[104:119], a[40:43], v[56:59], v[104:119]\n"
    "s_cmp_ge_i32 s14, 8\n"
    "s_cbranch_scc1 .Lfb_fv_15\n"
    "s_nop 9\n"
    "v_mov_b32 v104, 0xff61b1e6\n"
    "v_mov_b32 v105, 0xff61b1e6\n"
    "v_mov_b32 v106, 0xff61b1e6\n"
    "v_mov_b32 v107, 0xff61b1e6\n"
    "v_mov_b32 v108, 0xff61b1e6\n"
    "v_mov_b32 v109, 0xff61b1e6\n"
    "v_mov_b32 v110, 0xff61b1e6\n"
    "v_mov_b32 v111, 0xff61b1e6\n"
    "v_mov_b32 v112, 0xff61b1e6\n"
    "v_mov_b32 v113, 0xff61b1e6\n"
    "v_mov_b32 v114, 0xff61b1e6\n"
    "v_mov_b32 v115, 0xff61b1e6\n"
    "v_mov_b32 v116, 0xff61b1e6\n"
    "v_mov_b32 v117, 0xff61b1e6\n"
    "v_mov_b32 v118, 0xff61b1e6\n"
    "v_mov_b32 v119, 0xff61b1e6\n"
    ".Lfb_fv_15:\n"
    "s_nop 9\n"
    "v_cndmask_b32_e64 v120, v120, v104, s[64:65]\n"
    "buffer_load_dwordx4 a[64:67], v28, s[24:27], s33 offen sc1\n"
    "v_cndmask_b32_e64 v121, v121, v105, s[66:67]\n"
    "v_cndmask_b32_e64 v122, v122, v106, s[68:69]\n"
    "v_cndmask_b32_e64 v123, v123, v107, s[70:71]\n"
    "v_cndmask_b32_e64 v124, v124, v108, s[72:73]\n"
    "v_cndmask_b32_e64 v125, v125, v109, s[74:75]\n"
    "v_cndmask_b32_e64 v126, v126, v110, s[76:77]\n"
    "v_cndmask_b32_e64 v127, v127, v111, s[78:79]\n"
    "v_cndmask_b32_e64 v128, v128, v112, s[80:81]\n"
    "v_cndmask_b32_e64 v129, v129, v113, s[82:83]\n"
    "v_cndmask_b32_e64 v130, v130, v114, s[84:85]\n"
    "v_cndmask_b32_e64 v131, v131, v115, s[86:87]\n"
    "v_cndmask_b32_e64 v132, v132, v116, s[88:89]\n"
    "v_cndmask_b32_e64 v133, v133, v117, s[90:91]\n"
    "buffer_load_dwordx4 a[128:131], v28, s[24:27], s57 offen sc1\n"
    "v_cndmask_b32_e64 v134, v134, v118, s[92:93]\n"
    "v_cndmask_b32_e64 v135, v135, v119, s[94:95]\n"
    "v_max3_f32 v192, v120, v121, v122\n"
    "v_max3_f32 v192, v192, v122, v123\n"
    "v_max3_f32 v192, v192, v124, v125\n"
    "v_max3_f32 v192, v192, v126, v127\n"
    "v_max3_f32 v192, v192, v128, v129\n"
    "v_max3_f32 v192, v192, v130, v131\n"
    "v_max3_f32 v192, v192, v132, v133\n"
    "v_max3_f32 v192, v192, v134, v135\n"
    "v_mov_b32 v193, v192\n"
    "s_nop 1\n"
    "v_permlane32_swap_b32 v192, v193\n"
    "v_max_f32 v192, v192, v193\n"
    "buffer_load_dwordx4 a[68:71], v6, s[24:27], s34 offen sc1\n"
    "v_sub_f32 v136, 0, v192\n"
    "v_sub_f32 v137, 0, v192\n"
    "v_sub_f32 v138, 0, v192\n"
    "v_sub_f32 v139, 0, v192\n"
    "v_sub_f32 v140, 0, v192\n"
    "v_sub_f32 v141, 0, v192\n"
    "v_sub_f32 v142, 0, v192\n"
    "v_sub_f32 v143, 0, v192\n"
    "v_sub_f32 v144, 0, v192\n"
    "v_sub_f32 v145, 0, v192\n"
    "v_sub_f32 v146, 0, v192\n"
    "v_sub_f32 v147, 0, v192\n"
    "v_sub_f32 v148, 0, v192\n"
    "buffer_load_dwordx4 a[12:15], v26, s[24:27], s29 offen sc1\n"
    "v_sub_f32 v149, 0, v192\n"
    "v_sub_f32 v150, 0, v192\n"
    "v_sub_f32 v151, 0, v192\n"
    "s_waitcnt vmcnt(8)\n"
    "s_nop 0\n"
    "v_mfma_f32_32x32x16_bf16 v[88:103], a[4:7], v[32:35], v[136:151]\n"
    "; ---- P2 q0 (t~18806)\n"
    "v_add_f32 v120, v120, v136\n"
    "v_add_f32 v121, v121, v136\n"
    "v_add_f32 v122, v122, v136\n"
    "v_add_f32 v123, v123, v136\n"
    "v_exp_f32 v196, v120\n"
    "v_exp_f32 v197, v121\n"
    "buffer_load_dwordx4 a[72:75], v28, s[24:27], s34 offen sc1\n"
    "v_exp_f32 v198, v122\n"
    "v_exp_f32 v199, v123\n"
    "v_cvt_pk_bf16_f32 v196, v196, v197\n"
    "v_cvt_pk_bf16_f32 v198, v198, v199\n"
    "v_and_b32 v168, v40, v196\n"
    "v_xor_b32 v184, v168, v196\n"
    "v_and_b32 v169, v41, v198\n"
    "v_xor_b32 v185, v169, v198\n"
    "v_mfma_f32_32x32x16_bf16 v[88:103], a[40:43], v[60:63], v[88:103]\n"
    "v_add_f32 v124, v124, v136\n"
    "buffer_load_dwordx4 a[76:79], v6, s[24:27], s35 offen sc1\n"
    "v_add_f32 v125, v125, v136\n"
    "v_add_f32 v126, v126, v136\n"
    "v_add_f32 v127, v127, v136\n"
    "v_exp_f32 v196, v124\n"
    "v_exp_f32 v197, v125\n"
    "v_exp_f32 v198, v126\n"
    "v_exp_f32 v199, v127\n"
    "v_cvt_pk_bf16_f32 v196, v196, v197\n"
    "v_cvt_pk_bf16_f32 v198, v198, v199\n"
    "buffer_load_dwordx4 a[16:19], v3, s[24:27], s30 offen sc1\n"
    "v_and_b32 v170, v42, v196\n"
    "v_xor_b32 v186, v170, v196\n"
    "v_and_b32 v171, v43, v198\n"
    "v_xor_b32 v187, v171, v198\n"
    "v_add_f32 v128, v128, v136\n"
    "v_add_f32 v129, v129, v136\n"
    "v_add_f32 v130, v130, v136\n"
    "v_add_f32 v131, v131, v136\n"
    "v_exp_f32 v196, v128\n"
    "v_exp_f32 v197, v129\n"
    "v_exp_f32 v198, v130\n"
    "buffer_load_dwordx4 a[48:51], v5, s[24:27], s30 offen sc1\n"
    "v_exp_f32 v199, v131\n"
    "v_cvt_pk_bf16_f32 v196, v196, v197\n"
    "v_cvt_pk_bf16_f32 v198, v198, v199\n"
    "v_and_b32 v172, v44, v196\n"
    "v_xor_b32 v188, v172, v196\n"
    "v_and_b32 v173, v45, v198\n"
    "v_xor_b32 v189, v173, v198\n"
    "s_waitcnt vmcnt(11)\n"
    "v_mfma_f32_32x32x16_bf16 v[152:167], a[60:63], v[168:171], 0\n"
    "v_add_f32 v132, v132, v136\n"
    "v_add_f32 v133, v133, v136\n"
    "v_add_f32 v134, v134, v136\n"
    "buffer_load_dwordx4 a[80:83], v28, s[24:27], s35 offen sc1\n"
    "v_add_f32 v135, v135, v136\n"
    "v_exp_f32 v196, v132\n"
    "v_exp_f32 v197, v133\n"
    "v_exp_f32 v198, v134\n"
    "v_exp_f32 v199, v135\n"
    "v_cvt_pk_bf16_f32 v196, v196, v197\n"
    "v_cvt_pk_bf16_f32 v198, v198, v199\n"
    "v_and_b32 v174, v46, v196\n"
    "v_xor_b32 v190, v174, v196\n"
    "buffer_load_dwordx4 a[84:87], v6, s[24:27], s52 offen sc1\n"
    "v_and_b32 v175, v47, v198\n"
    "v_xor_b32 v191, v175, v198\n"
    "s_waitcnt vmcnt(12)\n"
    "v_mfma_f32_32x32x16_bf16 v[152:167], a[124:127], v[184:187], v[152:167]\n"
    "s_waitcnt vmcnt(11)\n"
    "v_mfma_f32_32x32x16_bf16 v[72:87], a[8:11], v[32:35], v[136:151]\n"
    "v_exp_f32 v196, v88\n"
    "buffer_load_dword v228, v31, s[24:27], 0 offen sc1\n"
    "v_exp_f32 v197, v89\n"
    "v_exp_f32 v198, v90\n"
    "v_exp_f32 v199, v91\n"
    "v_cvt_pk_bf16_f32 v176, v196, v197\n"
    "v_cvt_pk_bf16_f32 v177, v198, v199\n"
    "s_waitcnt vmcnt(11)\n"
    "v_mfma_f32_32x32x16_bf16 v[72:87], a[44:47], v[56:59], v[72:87]\n"
    "s_waitcnt vmcnt(10)\n"
    "v_mfma_f32_32x32x16_bf16 v[152:167], a[64:67], v[172:175], v[152:167]\n"
    "buffer_load_dwordx4 a[20:23], v26, s[24:27], s30 offen sc1\n"
    "v_exp_f32 v196, v92\n"
    "v_exp_f32 v197, v93\n"
    "v_exp_f32 v198, v94\n"
    "v_exp_f32 v199, v95\n"
    "v_cvt_pk_bf16_f32 v178, v196, v197\n"
    "v_cvt_pk_bf16_f32 v179, v198, v199\n"
    "s_waitcnt vmcnt(10)\n"
    "v_mfma_f32_32x32x16_bf16 v[152:167], a[128:131], v[188:191], v[152:167]\n"
    "v_exp_f32 v196, v96\n"
    "buffer_load_dwordx4 a[88:91], v28, s[24:27], s52 offen sc1\n"
    "v_exp_f32 v197, v97\n"
    "v_exp_f32 v198, v98\n"
    "v_exp_f32 v199, v99\n"
    "v_cvt_pk_bf16_f32 v180, v196, v197\n"
    "v_cvt_pk_bf16_f32 v181, v198, v199\n"
    "s_waitcnt vmcnt(10)\n"
    "v_mfma_f32_32x32x16_bf16 v[152:167], a[68:71], v[176:179], v[152:167]\n"
    "v_exp_f32 v196, v100\n"
    "v_exp_f32 v197, v101\n"
    "buffer_load_dwordx4 a[92:95], v6, s[24:27], s53 offen sc1\n"
    "v_exp_f32 v198, v102\n"
    "v_exp_f32 v199, v103\n"
    "v_cvt_pk_bf16_f32 v182, v196, v197\n"
    "v_cvt_pk_bf16_f32 v183, v198, v199\n"
    "s_waitcnt vmcnt(10)\n"
    "v_mfma_f32_32x32x16_bf16 v[88:103], a[12:15], v[32:35], v[136:151]\n"
    "v_exp_f32 v196, v72\n"
    "v_exp_f32 v197, v73\n"
    "v_exp_f32 v198, v74\n"
    "buffer_load_dwordx4 a[24:27], v3, s[24:27], s31 offen sc1\n"
    "v_exp_f32 v199, v75\n"
    "v_cvt_pk_bf16_f32 v168, v196, v197\n"
    "v_cvt_pk_bf16_f32 v169, v198, v199\n"
    "v_mfma_f32_32x32x16_bf16 v[88:103], a[44:47], v[60:63], v[88:103]\n"
    "v_exp_f32 v196, v76\n"
    "v_exp_f32 v197, v77\n"
    "v_exp_f32 v198, v78\n"
    "v_exp_f32 v199, v79\n"
    "buffer_load_dwordx4 a[52:55], v5, s[24:27], s31 offen sc1\n"
    "v_cvt_pk_bf16_f32 v170, v196, v197\n"
    "v_cvt_pk_bf16_f32 v171, v198, v199\n"
    "s_waitcnt vmcnt(11)\n"
    "v_mfma_f32_32x32x16_bf16 v[152:167], a[72:75], v[180:183], v[152:167]\n"
    "v_exp_f32 v196, v80\n"
    "v_exp_f32 v197, v81\n"
    "v_exp_f32 v198, v82\n"
    "v_exp_f32 v199, v83\n"
    "v_cvt_pk_bf16_f32 v172, v196, v197\n"
    "buffer_load_dwordx4 a[96:99], v28, s[24:27], s53 offen sc1\n"
    "v_cvt_pk_bf16_f32 v173, v198, v199\n"
    "s_waitcnt vmcnt(11)\n"
    "v_mfma_f32_32x32x16_bf16 v[152:167], a[76:79], v[168:171], v[152:167]\n"
    "v_exp_f32 v196, v84\n"
    "v_exp_f32 v197, v85\n"
    "v_exp_f32 v198, v86\n"
    "v_exp_f32 v199, v87\n"
    "v_cvt_pk_bf16_f32 v174, v196, v197\n"
    "v_cvt_pk_bf16_f32 v175, v198, v199\n"
    "buffer_load_dwordx4 a[100:103], v6, s[24:27], s54 offen sc1\n"
    "s_waitcnt vmcnt(7)\n"
    "s_mov_b32 exec_lo, 0xfffff\n"
    "s_mov_b32 exec_hi, 0xfffff\n"
    "v_cmp_ne_u32 vcc, s96, v228\n"
    "s_cmp_eq_u64 vcc, 0\n"
    "s_cbranch_scc1 .Lfb_xd_17\n"
    "s_mov_b32 s97, 0\n"
    ".Lfb_xs_16:\n"
    "s_sleep 4\n"
    "buffer_load_dword v228, v31, s[24:27], 0 offen sc1\n"
    "s_waitcnt vmcnt(0)\n"
    "v_cmp_ne_u32 vcc, s96, v228\n"
    "s_cmp_eq_u64 vcc, 0\n"
    "s_cbranch_scc1 .Lfb_xd_17\n"
    "s_add_u32 s97, s97, 1\n"
    "s_cmp_lt_u32 s97, 0x10000\n"
    "s_cbranch_scc1 .Lfb_xs_16\n"
    ".Lfb_xd_17:\n"
    "s_mov_b64 exec, -1\n"
    ".Lfb_xe_18:\n"
    "v_mfma_f32_32x32x16_bf16 v[72:87], a[16:19], v[32:35], v[136:151]\n"
    "v_exp_f32 v196, v88\n"
    "v_exp_f32 v197, v89\n"
    "v_exp_f32 v198, v90\n"
    "buffer_load_dwordx4 a[28:31], v26, s[24:27], s31 offen sc1\n"
    "v_exp_f32 v199, v91\n"
    "v_cvt_pk_bf16_f32 v176, v196, v197\n"
    "v_cvt_pk_bf16_f32 v177, v198, v199\n"
    "v_mfma_f32_32x32x16_bf16 v[72:87], a[48:51], v[56:59], v[72:87]\n"
    "v_exp_f32 v196, v92\n"
    "v_exp_f32 v197, v93\n"
    "v_exp_f32 v198, v94\n"
    "v_exp_f32 v199, v95\n"
    "buffer_load_dwordx4 a[104:107], v28, s[24:27], s54 offen sc1\n"
    "v_cvt_pk_bf16_f32 v178, v196, v197\n"
    "v_cvt_pk_bf16_f32 v179, v198, v199\n"
    "v_mfma_f32_32x32x16_bf16 v[152:167], a[80:83], v[172:175], v[152:167]\n"
    "v_exp_f32 v196, v96\n"
    "v_exp_f32 v197, v97\n"
    "v_exp_f32 v198, v98\n"
    "v_exp_f32 v199, v99\n"
    "v_cvt_pk_bf16_f32 v180, v196, v197\n"
    "buffer_load_dwordx4 a[108:111], v6, s[24:27], s55 offen sc1\n"
    "v_cvt_pk_bf16_f32 v181, v198, v199\n"
    "v_mfma_f32_32x32x16_bf16 v[152:167], a[84:87], v[176:179], v[152:167]\n"
    "v_exp_f32 v196, v100\n"
    "v_exp_f32 v197, v101\n"
    "v_exp_f32 v198, v102\n"
    "v_exp_f32 v199, v103\n"
    "v_cvt_pk_bf16_f32 v182, v196, v197\n"
    "v_cvt_pk_bf16_f32 v183, v198, v199\n"
    "buffer_load_dwordx4 a[112:115], v28, s[24:27], s55 offen sc1\n"
    "s_waitcnt vmcnt(10)\n"
    "v_mfma_f32_32x32x16_bf16 v[88:103], a[20:23], v[32:35], v[136:151]\n"
    "v_exp_f32 v196, v72\n"
    "v_exp_f32 v197, v73\n"
    "v_exp_f32 v198, v74\n"
    "v_exp_f32 v199, v75\n"
    "v_cvt_pk_bf16_f32 v168, v196, v197\n"
    "v_cvt_pk_bf16_f32 v169, v198, v199\n"
    "v_mfma_f32_32x32x16_bf16 v[88:103], a[48:51], v[60:63], v[88:103]\n"
    "buffer_load_dwordx4 a[116:119], v6, s[24:27], s56 offen sc1\n"
    "v_exp_f32 v196, v76\n"
    "v_exp_f32 v197, v77\n"
    "v_exp_f32 v198, v78\n"
    "v_exp_f32 v199, v79\n"
    "v_cvt_pk_bf16_f32 v170, v196, v197\n"
    "v_cvt_pk_bf16_f32 v171, v198, v199\n"
    "s_waitcnt vmcnt(10)\n"
    "v_mfma_f32_32x32x16_bf16 v[152:167], a[88:91], v[180:183], v[152:167]\n"
    "v_exp_f32 v196, v80\n"
    "buffer_load_dwordx4 a[120:123], v28, s[24:27], s56 offen sc1\n"
    "v_exp_f32 v197, v81\n"
    "v_exp_f32 v198, v82\n"
    "v_exp_f32 v199, v83\n"
    "v_cvt_pk_bf16_f32 v172, v196, v197\n"
    "v_cvt_pk_bf16_f32 v173, v198, v199\n"
    "s_waitcnt vmcnt(10)\n"
    "v_mfma_f32_32x32x16_bf16 v[152:167], a[92:95], v[168:171], v[152:167]\n"
    "v_exp_f32 v196, v84\n"
    "v_exp_f32 v197, v85\n"
    "buffer_load_dwordx4 v[52:55], v27, s[24:27], s2 offen sc1\n"
    "v_exp_f32 v198, v86\n"
    "v_exp_f32 v199, v87\n"
    "v_cvt_pk_bf16_f32 v174, v196, v197\n"
    "v_cvt_pk_bf16_f32 v175, v198, v199\n"
    "s_waitcnt vmcnt(10)\n"
    "v_mfma_f32_32x32x16_bf16 v[72:87], a[24:27], v[32:35], v[136:151]\n"
    "v_exp_f32 v196, v88\n"
    "v_exp_f32 v197, v89\n"
    "v_exp_f32 v198, v90\n"
    "buffer_load_dwordx4 v[36:39], v26, s[24:27], s2 offen sc1\n"
    "v_exp_f32 v199, v91\n"
    "v_cvt_pk_bf16_f32 v176, v196, v197\n"
    "v_cvt_pk_bf16_f32 v177, v198, v199\n"
    "s_waitcnt vmcnt(10)\n"
    "v_mfma_f32_32x32x16_bf16 v[72:87], a[52:55], v[56:59], v[72:87]\n"
    "v_exp_f32 v196, v92\n"
    "v_exp_f32 v197, v93\n"
    "v_exp_f32 v198, v94\n"
    "v_exp_f32 v199, v95\n"
    "buffer_load_dwordx4 a[36:39], v26, s[24:27], s32 offen sc1\n"
    "v_cvt_pk_bf16_f32 v178, v196, v197\n"
    "v_cvt_pk_bf16_f32 v179, v198, v199\n"
    "s_waitcnt vmcnt(10)\n"
    "v_mfma_f32_32x32x16_bf16 v[152:167], a[96:99], v[172:175], v[152:167]\n"
    "v_exp_f32 v196, v96\n"
    "v_exp_f32 v197, v97\n"
    "v_exp_f32 v198, v98\n"
    "v_exp_f32 v199, v99\n"
    "v_cvt_pk_bf16_f32 v180, v196, v197\n"
    "buffer_load_dwordx4 a[132:135], v6, s[24:27], s58 offen sc1\n"
    "v_cvt_pk_bf16_f32 v181, v198, v199\n"
    "s_waitcnt vmcnt(10)\n"
    "v_mfma_f32_32x32x16_bf16 v[152:167], a[100:103], v[176:179], v[152:167]\n"
    "v_exp_f32 v196, v100\n"
    "v_exp_f32 v197, v101\n"
    "v_exp_f32 v198, v102\n"
    "v_exp_f32 v199, v103\n"
    "v_cvt_pk_bf16_f32 v182, v196, v197\n"
    "v_cvt_pk_bf16_f32 v183, v198, v199\n"
    "buffer_load_dwordx4 a[136:139], v28, s[24:27], s58 offen sc1\n"
    "s_cmp_eq_u32 s6, 0\n"
    "s_cselect_b32 vcc_lo, -1, 0\n"
    "s_mov_b32 vcc_hi, 0\n"
    "s_waitcnt vmcnt(4)\n"
    "v_cndmask_b32 v64, 0, v52, vcc\n"
    "v_cndmask_b32 v65, 0, v53, vcc\n"
    "s_cmp_eq_u32 s6, 1\n"
    "s_cselect_b32 vcc_lo, -1, 0\n"
    "s_mov_b32 vcc_hi, 0\n"
    "v_cndmask_b32 v66, 0, v54, vcc\n"
    "v_cndmask_b32 v67, 0, v55, vcc\n"
    "s_cmp_eq_u32 s6, 0\n"
    "s_cselect_b32 vcc_hi, -1, 0\n"
    "s_mov_b32 vcc_lo, 0\n"
    "v_cndmask_b32 v68, 0, v52, vcc\n"
    "v_cndmask_b32 v69, 0, v53, vcc\n"
    "s_cmp_eq_u32 s6, 1\n"
    "s_cselect_b32 vcc_hi, -1, 0\n"
    "s_mov_b32 vcc_lo, 0\n"
    "v_cndmask_b32 v70, 0, v54, vcc\n"
    "v_cndmask_b32 v71, 0, v55, vcc\n"
    "v_mfma_f32_32x32x16_bf16 v[88:103], a[28:31], v[32:35], v[136:151]\n"
    "s_add_u32 s3, s59, 0\n"
    "buffer_load_dwordx4 a[140:143], v0, s[24:27], s3 offen sc1\n"
    "v_exp_f32 v196, v72\n"
    "v_exp_f32 v197, v73\n"
    "v_exp_f32 v198, v74\n"
    "v_exp_f32 v199, v75\n"
    "v_cvt_pk_bf16_f32 v168, v196, v197\n"
    "v_cvt_pk_bf16_f32 v169, v198, v199\n"
    "v_mfma_f32_32x32x16_bf16 v[88:103], a[52:55], v[60:63], v[88:103]\n"
    "v_exp_f32 v196, v76\n"
    "v_exp_f32 v197, v77\n"
    "buffer_load_dwordx4 a[144:147], v0, s[24:27], s3 offen offset:1024 sc1\n"
    "v_exp_f32 v198, v78\n"
    "v_exp_f32 v199, v79\n"
    "v_cvt_pk_bf16_f32 v170, v196, v197\n"
    "v_cvt_pk_bf16_f32 v171, v198, v199\n"
    "v_mfma_f32_32x32x16_bf16 v[152:167], a[104:107], v[180:183], v[152:167]\n"
    "v_exp_f32 v196, v80\n"
    "v_exp_f32 v197, v81\n"
    "v_exp_f32 v198, v82\n"
    "v_exp_f32 v199, v83\n"
    "buffer_load_dwordx4 a[148:151], v0, s[24:27], s3 offen offset:2048 sc1\n"
    "v_cvt_pk_bf16_f32 v172, v196, v197\n"
    "v_cvt_pk_bf16_f32 v173, v198, v199\n"
    "v_mfma_f32_32x32x16_bf16 v[152:167], a[108:111], v[168:171], v[152:167]\n"
    "v_exp_f32 v196, v84\n"
    "v_exp_f32 v197, v85\n"
    "v_exp_f32 v198, v86\n"
    "v_exp_f32 v199, v87\n"
    "v_cvt_pk_bf16_f32 v174, v196, v197\n"
    "v_cvt_pk_bf16_f32 v175, v198, v199\n"
    "s_waitcnt vmcnt(5)\n"
    "v_mfma_f32_32x32x16_bf16 v[120:135], a[36:39], v[36:39], 0\n"
    "buffer_load_dwordx4 a[152:155], v0, s[24:27], s3 offen offset:3072 sc1\n"
    "v_mfma_f32_32x32x16_bf16 v[104:119], a[4:7], v[36:39], 0\n"
    "v_exp_f32 v196, v88\n"
    "v_exp_f32 v197, v89\n"
    "v_exp_f32 v198, v90\n"
    "v_exp_f32 v199, v91\n"
    "v_cvt_pk_bf16_f32 v176, v196, v197\n"
    "v_cvt_pk_bf16_f32 v177, v198, v199\n"
    "v_mfma_f32_32x32x16_bf16 v[120:135], a[56:59], v[68:71], v[120:135]\n"
    "buffer_load_dwordx4 a[156:159], v1, s[24:27], s3 offen sc1\n"
    "v_mfma_f32_32x32x16_bf16 v[104:119], a[40:43], v[68:71], v[104:119]\n"
    "v_exp_f32 v196, v92\n"
    "v_exp_f32 v197, v93\n"
    "v_exp_f32 v198, v94\n"
    "v_exp_f32 v199, v95\n"
    "v_cvt_pk_bf16_f32 v178, v196, v197\n"
    "v_cvt_pk_bf16_f32 v179, v198, v199\n"
    "v_mfma_f32_32x32x16_bf16 v[152:167], a[112:115], v[172:175], v[152:167]\n"
    "buffer_load_dwordx4 a[160:163], v1, s[24:27], s3 offen offset:1024 sc1\n"
    "v_exp_f32 v196, v96\n"
    "v_exp_f32 v197, v97\n"
    "v_exp_f32 v198, v98\n"
    "v_exp_f32 v199, v99\n"
    "v_cvt_pk_bf16_f32 v180, v196, v197\n"
    "v_cvt_pk_bf16_f32 v181, v198, v199\n"
    "v_mfma_f32_32x32x16_bf16 v[152:167], a[116:119], v[176:179], v[152:167]\n"
    "v_exp_f32 v196, v100\n"
    "v_exp_f32 v197, v101\n"
    "buffer_load_dwordx4 a[164:167], v1, s[24:27], s3 offen offset:2048 sc1\n"
    "v_exp_f32 v198, v102\n"
    "v_exp_f32 v199, v103\n"
    "v_cvt_pk_bf16_f32 v182, v196, v197\n"
    "v_cvt_pk_bf16_f32 v183, v198, v199\n"
    "s_nop 1\n"
    "v_mfma_f32_32x32x16_bf16 v[152:167], a[120:123], v[180:183], v[152:167]\n"
    "s_cmp_ge_i32 s14, 8\n"
    "s_cbranch_scc1 .Lfb_fv_19\n"
    "v_mov_b32 v104, 0xff61b1e6\n"
    "v_mov_b32 v105, 0xff61b1e6\n"
    "v_mov_b32 v106, 0xff61b1e6\n"
    "v_mov_b32 v107, 0xff61b1e6\n"
    "v_mov_b32 v108, 0xff61b1e6\n"
    "v_mov_b32 v109, 0xff61b1e6\n"
    "v_mov_b32 v110, 0xff61b1e6\n"
    "v_mov_b32 v111, 0xff61b1e6\n"
    "v_mov_b32 v112, 0xff61b1e6\n"
    "v_mov_b32 v113, 0xff61b1e6\n"
    "v_mov_b32 v114, 0xff61b1e6\n"
    "v_mov_b32 v115, 0xff61b1e6\n"
    "v_mov_b32 v116, 0xff61b1e6\n"
    "v_mov_b32 v117, 0xff61b1e6\n"
    "v_mov_b32 v118, 0xff61b1e6\n"
    "v_mov_b32 v119, 0xff61b1e6\n"
    ".Lfb_fv_19:\n"
    "v_cndmask_b32_e64 v120, v120, v104, s[64:65]\n"
    "buffer_load_dwordx4 a[168:171], v1, s[24:27], s3 offen offset:3072 sc1\n"
    "v_cndmask_b32_e64 v121, v121, v105, s[66:67]\n"
    "v_cndmask_b32_e64 v122, v122, v106, s[68:69]\n"
    "v_cndmask_b32_e64 v123, v123, v107, s[70:71]\n"
    "v_cndmask_b32_e64 v124, v124, v108, s[72:73]\n"
    "v_cndmask_b32_e64 v125, v125, v109, s[74:75]\n"
    "v_cndmask_b32_e64 v126, v126, v110, s[76:77]\n"
    "v_cndmask_b32_e64 v127, v127, v111, s[78:79]\n"
    "v_cndmask_b32_e64 v128, v128, v112, s[80:81]\n"
    "v_cndmask_b32_e64 v129, v129, v113, s[82:83]\n"
    "v_cndmask_b32_e64 v130, v130, v114, s[84:85]\n"
    "v_cndmask_b32_e64 v131, v131, v115, s[86:87]\n"
    "v_cndmask_b32_e64 v132, v132, v116, s[88:89]\n"
    "v_cndmask_b32_e64 v133, v133, v117, s[90:91]\n"
    "v_cndmask_b32_e64 v134, v134, v118, s[92:93]\n"
    "v_cndmask_b32_e64 v135, v135, v119, s[94:95]\n"
    "v_max3_f32 v192, v120, v121, v122\n"
    "s_add_u32 s3, s59, 0x2000\n"
    "buffer_load_dwordx4 a[172:175], v0, s[24:27], s3 offen sc1\n"
    "v_max3_f32 v192, v192, v122, v123\n"
    "v_max3_f32 v192, v192, v124, v125\n"
    "v_max3_f32 v192, v192, v126, v127\n"
    "v_max3_f32 v192, v192, v128, v129\n"
    "v_max3_f32 v192, v192, v130, v131\n"
    "v_max3_f32 v192, v192, v132, v133\n"
    "v_max3_f32 v192, v192, v134, v135\n"
    "v_mov_b32 v193, v192\n"
    "s_nop 1\n"
    "v_permlane32_swap_b32 v192, v193\n"
    "v_max_f32 v192, v192, v193\n"
    "v_sub_f32 v136, 0, v192\n"
    "v_sub_f32 v137, 0, v192\n"
    "v_sub_f32 v138, 0, v192\n"
    "v_sub_f32 v139, 0, v192\n"
    "v_sub_f32 v140, 0, v192\n"
    "v_sub_f32 v141, 0, v192\n"
    "buffer_load_dwordx4 a[176:179], v0, s[24:27], s3 offen offset:1024 sc1\n"
    "v_sub_f32 v142, 0, v192\n"
    "v_sub_f32 v143, 0, v192\n"
    "v_sub_f32 v144, 0, v192\n"
    "v_sub_f32 v145, 0, v192\n"
    "v_sub_f32 v146, 0, v192\n"
    "v_sub_f32 v147, 0, v192\n"
    "v_sub_f32 v148, 0, v192\n"
    "v_sub_f32 v149, 0, v192\n"
    "v_sub_f32 v150, 0, v192\n"
    "v_sub_f32 v151, 0, v192\n"
    "s_nop 1\n"
    "v_mfma_f32_32x32x16_bf16 v[88:103], a[8:11], v[36:39], v[136:151]\n"
    "v_mov_b32 v196, v160\n"
    "v_mov_b32 v197, v160\n"
    "s_nop 1\n"
    "v_permlane32_swap_b32 v196, v197\n"
    "v_cmp_nlt_f32_e64 vcc, v197, s4\n"
    "s_cmp_lg_u64 vcc, 0\n"
    "s_cbranch_scc1 .Lfb_ool_in_20\n"
    ".Lfb_ool_out_21:\n"
    "v_rcp_f32 v196, v197\n"
    "buffer_load_dwordx4 a[180:183], v0, s[24:27], s3 offen offset:2048 sc1\n"
    "v_mul_f32 v152, v152, v196\n"
    "v_mul_f32 v153, v153, v196\n"
    "v_mul_f32 v154, v154, v196\n"
    "v_mul_f32 v155, v155, v196\n"
    "v_mul_f32 v156, v156, v196\n"
    "v_mul_f32 v157, v157, v196\n"
    "v_mul_f32 v158, v158, v196\n"
    "v_mul_f32 v159, v159, v196\n"
    "v_mul_f32 v160, v160, v196\n"
    "v_mul_f32 v161, v161, v196\n"
    "v_mul_f32 v162, v162, v196\n"
    "v_mul_f32 v163, v163, v196\n"
    "v_cvt_pk_bf16_f32 v200, v152, v153\n"
    "v_cvt_pk_bf16_f32 v201, v154, v155\n"
    "v_cvt_pk_bf16_f32 v202, v156, v157\n"
    "v_cvt_pk_bf16_f32 v203, v158, v159\n"
    "buffer_load_dwordx4 a[184:187], v0, s[24:27], s3 offen offset:3072 sc1\n"
    "buffer_store_dwordx4 v[200:203], v7, s[24:27], 0 offen sc1\n"
    "v_cvt_pk_bf16_f32 v204, v160, v161\n"
    "v_cvt_pk_bf16_f32 v205, v162, v163\n"
    "; ---- P2 q1 (t~21560)\n"
    "v_add_f32 v120, v120, v136\n"
    "v_add_f32 v121, v121, v136\n"
    "v_add_f32 v122, v122, v136\n"
    "v_add_f32 v123, v123, v136\n"
    "v_exp_f32 v196, v120\n"
    "v_exp_f32 v197, v121\n"
    "v_exp_f32 v198, v122\n"
    "v_exp_f32 v199, v123\n"
    "v_cvt_pk_bf16_f32 v196, v196, v197\n"
    "v_cvt_pk_bf16_f32 v198, v198, v199\n"
    "buffer_load_dwordx4 a[188:191], v1, s[24:27], s3 offen sc1\n"
    "v_and_b32 v168, v40, v196\n"
    "v_xor_b32 v184, v168, v196\n"
    "v_and_b32 v169, v41, v198\n"
    "v_xor_b32 v185, v169, v198\n"
    "v_mfma_f32_32x32x16_bf16 v[88:103], a[44:47], v[64:67], v[88:103]\n"
    "v_add_f32 v124, v124, v136\n"
    "v_add_f32 v125, v125, v136\n"
    "v_add_f32 v126, v126, v136\n"
    "v_add_f32 v127, v127, v136\n"
    "v_exp_f32 v196, v124\n"
    "v_exp_f32 v197, v125\n"
    "v_exp_f32 v198, v126\n"
    "buffer_load_dwordx4 a[192:195], v1, s[24:27], s3 offen offset:1024 sc1\n"
    "v_exp_f32 v199, v127\n"
    "v_cvt_pk_bf16_f32 v196, v196, v197\n"
    "v_cvt_pk_bf16_f32 v198, v198, v199\n"
    "v_and_b32 v170, v42, v196\n"
    "v_xor_b32 v186, v170, v196\n"
    "v_and_b32 v171, v43, v198\n"
    "v_xor_b32 v187, v171, v198\n"
    "v_add_f32 v128, v128, v136\n"
    "v_add_f32 v129, v129, v136\n"
    "v_add_f32 v130, v130, v136\n"
    "v_add_f32 v131, v131, v136\n"
    "v_exp_f32 v196, v128\n"
    "v_exp_f32 v197, v129\n"
    "buffer_load_dwordx4 a[196:199], v1, s[24:27], s3 offen offset:2048 sc1\n"
    "v_exp_f32 v198, v130\n"
    "v_exp_f32 v199, v131\n"
    "v_cvt_pk_bf16_f32 v196, v196, v197\n"
    "v_cvt_pk_bf16_f32 v198, v198, v199\n"
    "v_and_b32 v172, v44, v196\n"
    "v_xor_b32 v188, v172, v196\n"
    "v_and_b32 v173, v45, v198\n"
    "v_xor_b32 v189, v173, v198\n"
    "v_mfma_f32_32x32x16_bf16 v[152:167], a[68:71], v[168:171], 0\n"
    "v_add_f32 v132, v132, v136\n"
    "v_add_f32 v133, v133, v136\n"
    "v_add_f32 v134, v134, v136\n"
    "v_add_f32 v135, v135, v136\n"
    "buffer_load_dwordx4 a[200:203], v1, s[24:27], s3 offen offset:3072 sc1\n"
    "v_exp_f32 v196, v132\n"
    "v_exp_f32 v197, v133\n"
    "v_exp_f32 v198, v134\n"
    "v_exp_f32 v199, v135\n"
    "v_cvt_pk_bf16_f32 v196, v196, v197\n"
    "v_cvt_pk_bf16_f32 v198, v198, v199\n"
    "v_and_b32 v174, v46, v196\n"
    "v_xor_b32 v190, v174, v196\n"
    "v_and_b32 v175, v47, v198\n"
    "v_xor_b32 v191, v175, v198\n"
    "s_waitcnt vmcnt(18)\n"
    "v_mfma_f32_32x32x16_bf16 v[152:167], a[132:135], v[184:187], v[152:167]\n"
    "s_add_u32 s3, s59, 0x4000\n"
    "buffer_load_dwordx4 a[204:207], v0, s[24:27], s3 offen sc1\n"
    "v_mfma_f32_32x32x16_bf16 v[72:87], a[12:15], v[36:39], v[136:151]\n"
    "v_exp_f32 v196, v88\n"
    "v_exp_f32 v197, v89\n"
    "v_exp_f32 v198, v90\n"
    "v_exp_f32 v199, v91\n"
    "v_cvt_pk_bf16_f32 v176, v196, v197\n"
    "v_cvt_pk_bf16_f32 v177, v198, v199\n"
    "v_mfma_f32_32x32x16_bf16 v[72:87], a[44:47], v[68:71], v[72:87]\n"
    "buffer_load_dwordx4 a[208:211], v0, s[24:27], s3 offen offset:1024 sc1\n"
    "v_mfma_f32_32x32x16_bf16 v[152:167], a[72:75], v[172:175], v[152:167]\n"
    "v_exp_f32 v196, v92\n"
    "v_exp_f32 v197, v93\n"
    "v_exp_f32 v198, v94\n"
    "v_exp_f32 v199, v95\n"
    "v_cvt_pk_bf16_f32 v178, v196, v197\n"
    "v_cvt_pk_bf16_f32 v179, v198, v199\n"
    "s_waitcnt vmcnt(19)\n"
    "v_mfma_f32_32x32x16_bf16 v[152:167], a[136:139], v[188:191], v[152:167]\n"
    "buffer_load_dwordx4 a[212:215], v0, s[24:27], s3 offen offset:2048 sc1\n"
    "v_exp_f32 v196, v96\n"
    "v_exp_f32 v197, v97\n"
    "v_exp_f32 v198, v98\n"
    "v_exp_f32 v199, v99\n"
    "v_cvt_pk_bf16_f32 v180, v196, v197\n"
    "v_cvt_pk_bf16_f32 v181, v198, v199\n"
    "v_mfma_f32_32x32x16_bf16 v[152:167], a[76:79], v[176:179], v[152:167]\n"
    "v_exp_f32 v196, v100\n"
    "v_exp_f32 v197, v101\n"
    "buffer_load_dwordx4 a[216:219], v0, s[24:27], s3 offen offset:3072 sc1\n"
    "v_exp_f32 v198, v102\n"
    "v_exp_f32 v199, v103\n"
    "v_cvt_pk_bf16_f32 v182, v196, v197\n"
    "v_cvt_pk_bf16_f32 v183, v198, v199\n"
    "v_mfma_f32_32x32x16_bf16 v[88:103], a[16:19], v[36:39], v[136:151]\n"
    "v_exp_f32 v196, v72\n"
    "v_exp_f32 v197, v73\n"
    "v_exp_f32 v198, v74\n"
    "v_exp_f32 v199, v75\n"
    "buffer_load_dwordx4 a[220:223], v1, s[24:27], s3 offen sc1\n"
    "v_cvt_pk_bf16_f32 v168, v196, v197\n"
    "v_cvt_pk_bf16_f32 v169, v198, v199\n"
    "v_mfma_f32_32x32x16_bf16 v[88:103], a[48:51], v[64:67], v[88:103]\n"
    "v_exp_f32 v196, v76\n"
    "v_exp_f32 v197, v77\n"
    "v_exp_f32 v198, v78\n"
    "v_exp_f32 v199, v79\n"
    "v_cvt_pk_bf16_f32 v170, v196, v197\n"
    "v_cvt_pk_bf16_f32 v171, v198, v199\n"
    "v_mfma_f32_32x32x16_bf16 v[152:167], a[80:83], v[180:183], v[152:167]\n"
    "buffer_load_dwordx4 a[224:227], v1, s[24:27], s3 offen offset:1024 sc1\n"
    "v_exp_f32 v196, v80\n"
    "v_exp_f32 v197, v81\n"
    "v_exp_f32 v198, v82\n"
    "v_exp_f32 v199, v83\n"
    "v_cvt_pk_bf16_f32 v172, v196, v197\n"
    "v_cvt_pk_bf16_f32 v173, v198, v199\n"
    "v_mfma_f32_32x32x16_bf16 v[152:167], a[84:87], v[168:171], v[152:167]\n"
    "v_exp_f32 v196, v84\n"
    "v_exp_f32 v197, v85\n"
    "buffer_load_dwordx4 a[228:231], v1, s[24:27], s3 offen offset:2048 sc1\n"
    "v_exp_f32 v198, v86\n"
    "v_exp_f32 v199, v87\n"
    "v_cvt_pk_bf16_f32 v174, v196, v197\n"
    "v_cvt_pk_bf16_f32 v175, v198, v199\n"
    "v_mfma_f32_32x32x16_bf16 v[72:87], a[20:23], v[36:39], v[136:151]\n"
    "v_exp_f32 v196, v88\n"
    "v_exp_f32 v197, v89\n"
    "v_exp_f32 v198, v90\n"
    "v_exp_f32 v199, v91\n"
    "buffer_load_dwordx4 a[232:235], v1, s[24:27], s3 offen offset:3072 sc1\n"
    "v_cvt_pk_bf16_f32 v176, v196, v197\n"
    "v_cvt_pk_bf16_f32 v177, v198, v199\n"
    "v_mfma_f32_32x32x16_bf16 v[72:87], a[48:51], v[68:71], v[72:87]\n"
    "v_exp_f32 v196, v92\n"
    "v_exp_f32 v197, v93\n"
    "v_exp_f32 v198, v94\n"
    "v_exp_f32 v199, v95\n"
    "v_cvt_pk_bf16_f32 v178, v196, v197\n"
    "v_cvt_pk_bf16_f32 v179, v198, v199\n"
    "v_mfma_f32_32x32x16_bf16 v[152:167], a[88:91], v[172:175], v[152:167]\n"
    "s_add_u32 s3, s59, 0x6000\n"
    "buffer_load_dwordx4 a[236:239], v0, s[24:27], s3 offen sc1\n"
    "v_exp_f32 v196, v96\n"
    "v_exp_f32 v197, v97\n"
    "v_exp_f32 v198, v98\n"
    "v_exp_f32 v199, v99\n"
    "v_cvt_pk_bf16_f32 v180, v196, v197\n"
    "v_cvt_pk_bf16_f32 v181, v198, v199\n"
    "v_mfma_f32_32x32x16_bf16 v[152:167], a[92:95], v[176:179], v[152:167]\n"
    "v_exp_f32 v196, v100\n"
    "v_exp_f32 v197, v101\n"
    "s_add_u32 s3, s60, 0\n"
    "buffer_load_dwordx4 a[240:243], v0, s[24:27], s3 offen sc1\n"
    "v_exp_f32 v198, v102\n"
    "v_exp_f32 v199, v103\n"
    "v_cvt_pk_bf16_f32 v182, v196, v197\n"
    "v_cvt_pk_bf16_f32 v183, v198, v199\n"
    "v_mfma_f32_32x32x16_bf16 v[88:103], a[24:27], v[36:39], v[136:151]\n"
    "v_exp_f32 v196, v72\n"
    "v_exp_f32 v197, v73\n"
    "v_exp_f32 v198, v74\n"
    "v_exp_f32 v199, v75\n"
    "buffer_load_dwordx4 a[244:247], v0, s[24:27], s3 offen offset:1024 sc1\n"
    "v_cvt_pk_bf16_f32 v168, v196, v197\n"
    "v_cvt_pk_bf16_f32 v169, v198, v199\n"
    "v_mfma_f32_32x32x16_bf16 v[88:103], a[52:55], v[64:67], v[88:103]\n"
    "v_exp_f32 v196, v76\n"
    "v_exp_f32 v197, v77\n"
    "v_exp_f32 v198, v78\n"
    "v_exp_f32 v199, v79\n"
    "v_cvt_pk_bf16_f32 v170, v196, v197\n"
    "v_cvt_pk_bf16_f32 v171, v198, v199\n"
    "v_mfma_f32_32x32x16_bf16 v[152:167], a[96:99], v[180:183], v[152:167]\n"
    "buffer_load_dwordx4 a[248:251], v0, s[24:27], s3 offen offset:2048 sc1\n"
    "v_exp_f32 v196, v80\n"
    "v_exp_f32 v197, v81\n"
    "v_exp_f32 v198, v82\n"
    "v_exp_f32 v199, v83\n"
    "v_cvt_pk_bf16_f32 v172, v196, v197\n"
    "v_cvt_pk_bf16_f32 v173, v198, v199\n"
    "v_mfma_f32_32x32x16_bf16 v[152:167], a[100:103], v[168:171], v[152:167]\n"
    "v_exp_f32 v196, v84\n"
    "v_exp_f32 v197, v85\n"
    "buffer_load_dwordx4 a[252:255], v0, s[24:27], s3 offen offset:3072 sc1\n"
    "v_exp_f32 v198, v86\n"
    "v_exp_f32 v199, v87\n"
    "v_cvt_pk_bf16_f32 v174, v196, v197\n"
    "v_cvt_pk_bf16_f32 v175, v198, v199\n"
    "v_mfma_f32_32x32x16_bf16 v[72:87], a[28:31], v[36:39], v[136:151]\n"
    "v_exp_f32 v196, v88\n"
    "v_exp_f32 v197, v89\n"
    "v_exp_f32 v198, v90\n"
    "v_exp_f32 v199, v91\n"
    "s_add_u32 m0, s19, 0\n"
    "s_nop 0\n"
    "buffer_load_dwordx4 v1, s[24:27], s3 offen sc1 lds\n"
    "v_cvt_pk_bf16_f32 v176, v196, v197\n"
    "v_cvt_pk_bf16_f32 v177, v198, v199\n"
    "v_mfma_f32_32x32x16_bf16 v[72:87], a[52:55], v[68:71], v[72:87]\n"
    "v_exp_f32 v196, v92\n"
    "v_exp_f32 v197, v93\n"
    "v_exp_f32 v198, v94\n"
    "v_exp_f32 v199, v95\n"
    "v_cvt_pk_bf16_f32 v178, v196, v197\n"
    "v_cvt_pk_bf16_f32 v179, v198, v199\n"
    "v_mfma_f32_32x32x16_bf16 v[152:167], a[104:107], v[172:175], v[152:167]\n"
    "buffer_load_dwordx4 v1, s[24:27], s3 offen offset:1024 sc1 lds\n"
    "v_exp_f32 v196, v96\n"
    "v_exp_f32 v197, v97\n"
    "v_exp_f32 v198, v98\n"
    "v_exp_f32 v199, v99\n"
    "v_cvt_pk_bf16_f32 v180, v196, v197\n"
    "v_cvt_pk_bf16_f32 v181, v198, v199\n"
    "v_mfma_f32_32x32x16_bf16 v[152:167], a[108:111], v[176:179], v[152:167]\n"
    "v_exp_f32 v196, v100\n"
    "v_exp_f32 v197, v101\n"
    "buffer_load_dwordx4 v1, s[24:27], s3 offen offset:2048 sc1 lds\n"
    "v_exp_f32 v198, v102\n"
    "v_exp_f32 v199, v103\n"
    "v_cvt_pk_bf16_f32 v182, v196, v197\n"
    "v_cvt_pk_bf16_f32 v183, v198, v199\n"
    "v_mfma_f32_32x32x16_bf16 v[88:103], a[32:35], v[36:39], v[136:151]\n"
    "v_exp_f32 v196, v72\n"
    "v_exp_f32 v197, v73\n"
    "v_exp_f32 v198, v74\n"
    "v_exp_f32 v199, v75\n"
    "buffer_load_dwordx4 v1, s[24:27], s3 offen offset:3072 sc1 lds\n"
    "v_cvt_pk_bf16_f32 v168, v196, v197\n"
    "v_cvt_pk_bf16_f32 v169, v198, v199\n"
    "v_mfma_f32_32x32x16_bf16 v[88:103], a[56:59], v[64:67], v[88:103]\n"
    "v_exp_f32 v196, v76\n"
    "v_exp_f32 v197, v77\n"
    "v_exp_f32 v198, v78\n"
    "v_exp_f32 v199, v79\n"
    "v_cvt_pk_bf16_f32 v170, v196, v197\n"
    "v_cvt_pk_bf16_f32 v171, v198, v199\n"
    "v_mfma_f32_32x32x16_bf16 v[152:167], a[112:115], v[180:183], v[152:167]\n"
    "s_add_u32 s3, s60, 0x2000\n"
    "s_add_u32 m0, s19, 0x1000\n"
    "s_nop 0\n"
    "buffer_load_dwordx4 v0, s[24:27], s3 offen sc1 lds\n"
    "v_exp_f32 v196, v80\n"
    "v_exp_f32 v197, v81\n"
    "v_exp_f32 v198, v82\n"
    "v_exp_f32 v199, v83\n"
    "v_cvt_pk_bf16_f32 v172, v196, v197\n"
    "v_cvt_pk_bf16_f32 v173, v198, v199\n"
    "v_mfma_f32_32x32x16_bf16 v[152:167], a[116:119], v[168:171], v[152:167]\n"
    "v_exp_f32 v196, v84\n"
    "v_exp_f32 v197, v85\n"
    "buffer_load_dwordx4 v0, s[24:27], s3 offen offset:1024 sc1 lds\n"
    "v_exp_f32 v198, v86\n"
    "v_exp_f32 v199, v87\n"
    "v_cvt_pk_bf16_f32 v174, v196, v197\n"
    "v_cvt_pk_bf16_f32 v175, v198, v199\n"
    "v_exp_f32 v196, v88\n"
    "v_exp_f32 v197, v89\n"
    "v_exp_f32 v198, v90\n"
    "v_exp_f32 v199, v91\n"
    "v_cvt_pk_bf16_f32 v176, v196, v197\n"
    "v_cvt_pk_bf16_f32 v177, v198, v199\n"
    "buffer_load_dwordx4 v0, s[24:27], s3 offen offset:2048 sc1 lds\n"
    "v_exp_f32 v196, v92\n"
    "v_exp_f32 v197, v93\n"
    "v_exp_f32 v198, v94\n"
    "v_exp_f32 v199, v95\n"
    "v_cvt_pk_bf16_f32 v178, v196, v197\n"
    "v_cvt_pk_bf16_f32 v179, v198, v199\n"
    "v_mfma_f32_32x32x16_bf16 v[152:167], a[120:123], v[172:175], v[152:167]\n"
    "v_exp_f32 v196, v96\n"
    "v_exp_f32 v197, v97\n"
    "buffer_load_dwordx4 v0, s[24:27], s3 offen offset:3072 sc1 lds\n"
    "v_exp_f32 v198, v98\n"
    "v_exp_f32 v199, v99\n"
    "v_cvt_pk_bf16_f32 v180, v196, v197\n"
    "v_cvt_pk_bf16_f32 v181, v198, v199\n"
    "v_mfma_f32_32x32x16_bf16 v[152:167], a[124:127], v[176:179], v[152:167]\n"
    "v_exp_f32 v196, v100\n"
    "v_exp_f32 v197, v101\n"
    "v_exp_f32 v198, v102\n"
    "v_exp_f32 v199, v103\n"
    "v_cvt_pk_bf16_f32 v182, v196, v197\n"
    "v_cvt_pk_bf16_f32 v183, v198, v199\n"
    "s_nop 1\n"
    "v_mfma_f32_32x32x16_bf16 v[152:167], a[128:131], v[180:183], v[152:167]\n"
    "s_nop 11\n"
    "v_mov_b32 v196, v160\n"
    "v_mov_b32 v197, v160\n"
    "s_nop 1\n"
    "v_permlane32_swap_b32 v196, v197\n"
    "v_cmp_nlt_f32_e64 vcc, v197, s4\n"
    "s_cmp_lg_u64 vcc, 0\n"
    "s_cbranch_scc1 .Lfb_ool_in_22\n"
    ".Lfb_ool_out_23:\n"
    "v_rcp_f32 v196, v197\n"
    "s_nop 0\n"
    "v_pk_mul_f32 v[152:153], v[152:153], v[196:197] op_sel_hi:[1,0]\n"
    "v_pk_mul_f32 v[154:155], v[154:155], v[196:197] op_sel_hi:[1,0]\n"
    "v_pk_mul_f32 v[156:157], v[156:157], v[196:197] op_sel_hi:[1,0]\n"
    "v_pk_mul_f32 v[158:159], v[158:159], v[196:197] op_sel_hi:[1,0]\n"
    "v_pk_mul_f32 v[160:161], v[160:161], v[196:197] op_sel_hi:[1,0]\n"
    "v_pk_mul_f32 v[162:163], v[162:163], v[196:197] op_sel_hi:[1,0]\n"
    "v_cvt_pk_bf16_f32 v200, v152, v153\n"
    "v_cvt_pk_bf16_f32 v201, v154, v155\n"
    "v_cvt_pk_bf16_f32 v202, v156, v157\n"
    "v_cvt_pk_bf16_f32 v203, v158, v159\n"
    "s_mov_b32 s0, 0xa000\n"
    "buffer_store_dwordx4 v[200:203], v7, s[24:27], s0 offen sc1\n"
    "v_cvt_pk_bf16_f32 v206, v160, v161\n"
    "v_cvt_pk_bf16_f32 v207, v162, v163\n"
    "s_nop 0\n"
    "v_permlane32_swap_b32 v204, v206\n"
    "v_permlane32_swap_b32 v205, v207\n"
    "buffer_store_dwordx2 v[204:205], v8, s[24:27], 0 offen sc1\n"
    ".Lfb_c3:\n"
    "; ---- H2 (t~23718)\n"
    "buffer_load_dwordx4 v[232:235], v9, s[20:23], 0 offen\n"
    "buffer_load_dwordx4 v[236:239], v9, s[20:23], 0 offen offset:64\n"
    "buffer_load_dwordx2 v[240:241], v10, s[20:23], 0 offen\n"
    "s_add_u32 m0, s19, 0x2000\n"
    "s_nop 0\n"
    "buffer_load_dwordx4 v1, s[24:27], s3 offen sc1 lds\n"
    "buffer_load_dwordx4 v1, s[24:27], s3 offen offset:1024 sc1 lds\n"
    "buffer_load_dwordx4 v1, s[24:27], s3 offen offset:2048 sc1 lds\n"
    "buffer_load_dwordx4 v1, s[24:27], s3 offen offset:3072 sc1 lds\n"
    "s_mul_i32 s0, s16, 0x1400\n"
    "s_add_u32 s0, s0, 0x7a8000\n"
    "s_mul_i32 s28, s48, 0x5000\n"
    "s_add_u32 s28, s28, s0\n"
    "s_mul_i32 s29, s49, 0x5000\n"
    "s_add_u32 s29, s29, s0\n"
    "s_mul_i32 s30, s50, 0x5000\n"
    "s_add_u32 s30, s30, s0\n"
    "s_mul_i32 s31, s51, 0x5000\n"
    "s_add_u32 s31, s31, s0\n"
    "s_add_u32 s0, s13, 0x2000\n"
    "s_waitcnt vmcnt(7)\n"
    "s_barrier\n"
    "s_cmp_lg_u32 s16, 0\n"
    "s_cbranch_scc1 .Lfb_fs_24\n"
    "s_mov_b64 exec, 1\n"
    "v_mov_b32 v229, s96\n"
    "s_add_u32 s97, s12, 0x2000\n"
    "buffer_store_dword v229, v25, s[24:27], s97 offen sc1\n"
    "s_mov_b64 exec, -1\n"
    ".Lfb_fs_24:\n"
    "s_getpc_b64 vcc\n"
    ".Lfb_pc_25:\n"
    "s_bfe_u32 s97, s17, 0x20003\n"
    "s_lshl_b32 s97, s97, 2\n"
    "s_add_u32 s97, s97, s16\n"
    "s_lshl_b32 s97, s97, 10\n"
    "v_and_b32 v253, 0x70, v0\n"
    "v_lshl_add_u32 v253, v253, 3, s97\n"
    "v_min_u32 v253, .Lfb_c5-.Lfb_c4, v253\n"
    "v_add_u32 v253, .Lfb_c4-.Lfb_pc_25, v253\n"
    "global_load_dword v253, v253, vcc\n"
    "s_add_u32 s3, s60, 0x4000\n"
    "s_add_u32 m0, s19, 0x3000\n"
    "s_nop 0\n"
    "buffer_load_dwordx4 v0, s[24:27], s3 offen sc1 lds\n"
    "buffer_load_dwordx4 v0, s[24:27], s3 offen offset:1024 sc1 lds\n"
    "buffer_load_dwordx4 v0, s[24:27], s3 offen offset:2048 sc1 lds\n"
    "buffer_load_dwordx4 v0, s[24:27], s3 offen offset:3072 sc1 lds\n"
    "s_add_u32 m0, s19, 0x4000\n"
    "s_nop 0\n"
    "buffer_load_dwordx4 v1, s[24:27], s3 offen sc1 lds\n"
    "buffer_load_dwordx4 v1, s[24:27], s3 offen offset:1024 sc1 lds\n"
    "buffer_load_dwordx4 v1, s[24:27], s3 offen offset:2048 sc1 lds\n"
    "buffer_load_dwordx4 a[0:3], v1, s[24:27], s3 offen offset:3072 sc1\n"
    "s_add_u32 s3, s60, 0x6000\n"
    "buffer_load_dwordx4 a[4:7], v0, s[24:27], s3 offen sc1\n"
    "buffer_load_dwordx4 a[8:11], v0, s[24:27], s3 offen offset:1024 sc1\n"
    "buffer_load_dwordx4 a[12:15], v0, s[24:27], s3 offen offset:2048 sc1\n"
    "buffer_load_dwordx4 a[16:19], v0, s[24:27], s3 offen offset:3072 sc1\n"
    "s_cmp_lg_u32 s16, 0\n"
    "s_cbranch_scc1 .Lfb_ps_28\n"
    "s_mov_b64 exec, 0xff\n"
    "s_mov_b32 s97, 0\n"
    ".Lfb_pl_26:\n"
    "buffer_load_dword v229, v25, s[24:27], s0 offen sc1\n"
    "s_waitcnt vmcnt(0)\n"
    "v_cmp_ne_u32 vcc, s96, v229\n"
    "s_cmp_eq_u64 vcc, 0\n"
    "s_cbranch_scc1 .Lfb_pd_27\n"
    "s_sleep 1\n"
    "s_add_u32 s97, s97, 1\n"
    "s_cmp_lt_u32 s97, 0x10000\n"
    "s_cbranch_scc1 .Lfb_pl_26\n"
    ".Lfb_pd_27:\n"
    "s_mov_b64 exec, -1\n"
    ".Lfb_ps_28:\n"
    "s_cmp_lg_u32 s16, 1\n"
    "s_cbranch_scc1 .Lfb_ps_31\n"
    "s_mov_b32 exec_lo, -1\n"
    "s_mov_b32 exec_hi, 0xffff\n"
    "s_mov_b32 s97, 0\n"
    ".Lfb_pl_29:\n"
    "buffer_load_dword v229, v25, s[24:27], s9 offen sc1\n"
    "s_waitcnt vmcnt(0)\n"
    "v_cmp_ne_u32 vcc, s96, v229\n"
    "s_cmp_eq_u64 vcc, 0\n"
    "s_cbranch_scc1 .Lfb_pd_30\n"
    "s_sleep 4\n"
    "s_add_u32 s97, s97, 1\n"
    "s_cmp_lt_u32 s97, 0x10000\n"
    "s_cbranch_scc1 .Lfb_pl_29\n"
    ".Lfb_pd_30:\n"
    "s_mov_b64 exec, -1\n"
    ".Lfb_ps_31:\n"
    "s_barrier\n"
    "; ---- P3 (t~24198)\n"
    "buffer_load_dwordx4 v[104:107], v0, s[24:27], s28 offen sc1\n"
    "buffer_load_dwordx4 v[108:111], v0, s[24:27], s28 offen offset:1024 sc1\n"
    "buffer_load_dwordx4 v[112:115], v0, s[24:27], s28 offen offset:2048 sc1\n"
    "buffer_load_dwordx4 v[116:119], v0, s[24:27], s28 offen offset:3072 sc1\n"
    "buffer_load_dwordx2 v[120:121], v231, s[24:27], s28 offen sc1\n"
    "buffer_load_dwordx2 v[122:123], v231, s[24:27], s28 offen offset:128 sc1\n"
    "buffer_load_dwordx4 v[124:127], v0, s[24:27], s29 offen sc1\n"
    "buffer_load_dwordx4 v[128:131], v0, s[24:27], s29 offen offset:1024 sc1\n"
    "s_mul_i32 s1, s16, 0xc00\n"
    "s_add_u32 s1, s1, 0xf000\n"
    "v_add_u32 v242, s1, v251\n"
    "v_add_u32 v243, s1, v230\n"
    "s_waitcnt vmcnt(27)\n"
    "ds_write_b128 v242, v[232:235]\n"
    "s_waitcnt vmcnt(26)\n"
    "ds_write_b128 v242, v[236:239] offset:1024\n"
    "s_waitcnt vmcnt(25)\n"
    "ds_write_b64 v242, v[240:241] offset:2048\n"
    "ds_read_b128 v[232:235], v243\n"
    "ds_read_b128 v[236:239], v243 offset:1024\n"
    "ds_read_b64 v[240:241], v243 offset:2048\n"
    "s_waitcnt vmcnt(7)\n"
    "v_mfma_f32_16x16x32_bf16 v[32:35], a[140:143], v[104:107], 0\n"
    "buffer_load_dwordx4 v[132:135], v0, s[24:27], s29 offen offset:2048 sc1\n"
    "v_mfma_f32_16x16x32_bf16 v[36:39], a[144:147], v[104:107], 0\n"
    "v_mfma_f32_16x16x32_bf16 v[40:43], a[148:151], v[104:107], 0\n"
    "v_mfma_f32_16x16x32_bf16 v[44:47], a[152:155], v[104:107], 0\n"
    "v_mfma_f32_16x16x32_bf16 v[48:51], a[156:159], v[104:107], 0\n"
    "s_waitcnt vmcnt(7)\n"
    "v_mfma_f32_16x16x32_bf16 v[32:35], a[160:163], v[108:111], v[32:35]\n"
    "buffer_load_dwordx4 v[136:139], v0, s[24:27], s29 offen offset:3072 sc1\n"
    "v_mfma_f32_16x16x32_bf16 v[36:39], a[164:167], v[108:111], v[36:39]\n"
    "v_mfma_f32_16x16x32_bf16 v[40:43], a[168:171], v[108:111], v[40:43]\n"
    "v_mfma_f32_16x16x32_bf16 v[44:47], a[172:175], v[108:111], v[44:47]\n"
    "v_mfma_f32_16x16x32_bf16 v[48:51], a[176:179], v[108:111], v[48:51]\n"
    "s_waitcnt vmcnt(7)\n"
    "v_mfma_f32_16x16x32_bf16 v[32:35], a[180:183], v[112:115], v[32:35]\n"
    "buffer_load_dwordx2 v[104:105], v231, s[24:27], s29 offen sc1\n"
    "buffer_load_dwordx2 v[106:107], v231, s[24:27], s29 offen offset:128 sc1\n"
    "v_mfma_f32_16x16x32_bf16 v[36:39], a[184:187], v[112:115], v[36:39]\n"
    "v_mfma_f32_16x16x32_bf16 v[40:43], a[188:191], v[112:115], v[40:43]\n"
    "v_mfma_f32_16x16x32_bf16 v[44:47], a[192:195], v[112:115], v[44:47]\n"
    "v_mfma_f32_16x16x32_bf16 v[48:51], a[196:199], v[112:115], v[48:51]\n"
    "s_waitcnt vmcnt(8)\n"
    "v_mfma_f32_16x16x32_bf16 v[32:35], a[200:203], v[116:119], v[32:35]\n"
    "buffer_load_dwordx4 v[108:111], v0, s[24:27], s30 offen sc1\n"
    "v_mfma_f32_16x16x32_bf16 v[36:39], a[204:207], v[116:119], v[36:39]\n"
    "v_mfma_f32_16x16x32_bf16 v[40:43], a[208:211], v[116:119], v[40:43]\n"
    "v_mfma_f32_16x16x32_bf16 v[44:47], a[212:215], v[116:119], v[44:47]\n"
    "v_mfma_f32_16x16x32_bf16 v[48:51], a[216:219], v[116:119], v[48:51]\n"
    "s_waitcnt vmcnt(7)\n"
    "v_mfma_f32_16x16x32_bf16 v[32:35], a[220:223], v[120:123], v[32:35]\n"
    "buffer_load_dwordx4 v[112:115], v0, s[24:27], s30 offen offset:1024 sc1\n"
    "v_mfma_f32_16x16x32_bf16 v[36:39], a[224:227], v[120:123], v[36:39]\n"
    "v_mfma_f32_16x16x32_bf16 v[40:43], a[228:231], v[120:123], v[40:43]\n"
    "v_mfma_f32_16x16x32_bf16 v[44:47], a[232:235], v[120:123], v[44:47]\n"
    "v_mfma_f32_16x16x32_bf16 v[48:51], a[236:239], v[120:123], v[48:51]\n"
    "s_waitcnt vmcnt(7)\n"
    "v_mfma_f32_16x16x32_bf16 v[52:55], a[140:143], v[124:127], 0\n"
    "buffer_load_dwordx4 v[116:119], v0, s[24:27], s30 offen offset:2048 sc1\n"
    "v_mfma_f32_16x16x32_bf16 v[56:59], a[144:147], v[124:127], 0\n"
    "v_mfma_f32_16x16x32_bf16 v[60:63], a[148:151], v[124:127], 0\n"
    "v_mfma_f32_16x16x32_bf16 v[64:67], a[152:155], v[124:127], 0\n"
    "v_mfma_f32_16x16x32_bf16 v[68:71], a[156:159], v[124:127], 0\n"
    "ds_write_b128 v14, v[32:35]\n"
    "s_waitcnt vmcnt(7)\n"
    "v_mfma_f32_16x16x32_bf16 v[52:55], a[160:163], v[128:131], v[52:55]\n"
    "buffer_load_dwordx4 v[120:123], v0, s[24:27], s30 offen offset:3072 sc1\n"
    "ds_write_b128 v14, v[36:39] offset:1024\n"
    "v_mfma_f32_16x16x32_bf16 v[56:59], a[164:167], v[128:131], v[56:59]\n"
    "ds_write_b128 v14, v[40:43] offset:2048\n"
    "v_mfma_f32_16x16x32_bf16 v[60:63], a[168:171], v[128:131], v[60:63]\n"
    "ds_write_b128 v14, v[44:47] offset:3072\n"
    "v_mfma_f32_16x16x32_bf16 v[64:67], a[172:175], v[128:131], v[64:67]\n"
    "ds_write_b128 v14, v[48:51] offset:4096\n"
    "v_mfma_f32_16x16x32_bf16 v[68:71], a[176:179], v[128:131], v[68:71]\n"
    "s_waitcnt lgkmcnt(7)\n"
    "v_lshlrev_b32 v84, 16, v232\n"
    "s_waitcnt vmcnt(7)\n"
    "v_mfma_f32_16x16x32_bf16 v[52:55], a[180:183], v[132:135], v[52:55]\n"
    "buffer_load_dwordx2 v[124:125], v231, s[24:27], s30 offen sc1\n"
    "buffer_load_dwordx2 v[126:127], v231, s[24:27], s30 offen offset:128 sc1\n"
    "v_and_b32 v85, 0xffff0000, v232\n"
    "v_mfma_f32_16x16x32_bf16 v[56:59], a[184:187], v[132:135], v[56:59]\n"
    "v_lshlrev_b32 v86, 16, v233\n"
    "v_mfma_f32_16x16x32_bf16 v[60:63], a[188:191], v[132:135], v[60:63]\n"
    "v_and_b32 v87, 0xffff0000, v233\n"
    "v_mfma_f32_16x16x32_bf16 v[64:67], a[192:195], v[132:135], v[64:67]\n"
    "v_lshlrev_b32 v88, 16, v234\n"
    "v_mfma_f32_16x16x32_bf16 v[68:71], a[196:199], v[132:135], v[68:71]\n"
    "v_and_b32 v89, 0xffff0000, v234\n"
    "s_waitcnt vmcnt(8)\n"
    "v_mfma_f32_16x16x32_bf16 v[52:55], a[200:203], v[136:139], v[52:55]\n"
    "buffer_load_dwordx4 v[128:131], v0, s[24:27], s31 offen sc1\n"
    "v_lshlrev_b32 v90, 16, v235\n"
    "v_mfma_f32_16x16x32_bf16 v[56:59], a[204:207], v[136:139], v[56:59]\n"
    "v_and_b32 v91, 0xffff0000, v235\n"
    "v_mfma_f32_16x16x32_bf16 v[60:63], a[208:211], v[136:139], v[60:63]\n"
    "s_waitcnt lgkmcnt(6)\n"
    "v_lshlrev_b32 v92, 16, v236\n"
    "v_mfma_f32_16x16x32_bf16 v[64:67], a[212:215], v[136:139], v[64:67]\n"
    "v_and_b32 v93, 0xffff0000, v236\n"
    "v_mfma_f32_16x16x32_bf16 v[68:71], a[216:219], v[136:139], v[68:71]\n"
    "v_lshlrev_b32 v94, 16, v237\n"
    "s_waitcnt vmcnt(7)\n"
    "v_mfma_f32_16x16x32_bf16 v[52:55], a[220:223], v[104:107], v[52:55]\n"
    "buffer_load_dwordx4 v[132:135], v0, s[24:27], s31 offen offset:1024 sc1\n"
    "v_and_b32 v95, 0xffff0000, v237\n"
    "v_mfma_f32_16x16x32_bf16 v[56:59], a[224:227], v[104:107], v[56:59]\n"
    "v_lshlrev_b32 v96, 16, v238\n"
    "v_mfma_f32_16x16x32_bf16 v[60:63], a[228:231], v[104:107], v[60:63]\n"
    "v_and_b32 v97, 0xffff0000, v238\n"
    "v_mfma_f32_16x16x32_bf16 v[64:67], a[232:235], v[104:107], v[64:67]\n"
    "v_lshlrev_b32 v98, 16, v239\n"
    "v_mfma_f32_16x16x32_bf16 v[68:71], a[236:239], v[104:107], v[68:71]\n"
    "v_and_b32 v99, 0xffff0000, v239\n"
    "s_waitcnt vmcnt(7)\n"
    "v_mfma_f32_16x16x32_bf16 v[32:35], a[140:143], v[108:111], 0\n"
    "buffer_load_dwordx4 v[136:139], v0, s[24:27], s31 offen offset:2048 sc1\n"
    "v_mfma_f32_16x16x32_bf16 v[36:39], a[144:147], v[108:111], 0\n"
    "v_mfma_f32_16x16x32_bf16 v[40:43], a[148:151], v[108:111], 0\n"
    "v_mfma_f32_16x16x32_bf16 v[44:47], a[152:155], v[108:111], 0\n"
    "v_mfma_f32_16x16x32_bf16 v[48:51], a[156:159], v[108:111], 0\n"
    "ds_write_b128 v15, v[52:55]\n"
    "s_waitcnt vmcnt(7)\n"
    "v_mfma_f32_16x16x32_bf16 v[32:35], a[160:163], v[112:115], v[32:35]\n"
    "buffer_load_dwordx4 v[104:107], v0, s[24:27], s31 offen offset:3072 sc1\n"
    "ds_write_b128 v15, v[56:59] offset:1024\n"
    "v_mfma_f32_16x16x32_bf16 v[36:39], a[164:167], v[112:115], v[36:39]\n"
    "ds_write_b128 v15, v[60:63] offset:2048\n"
    "v_mfma_f32_16x16x32_bf16 v[40:43], a[168:171], v[112:115], v[40:43]\n"
    "ds_write_b128 v15, v[64:67] offset:3072\n"
    "v_mfma_f32_16x16x32_bf16 v[44:47], a[172:175], v[112:115], v[44:47]\n"
    "ds_write_b128 v15, v[68:71] offset:4096\n"
    "v_mfma_f32_16x16x32_bf16 v[48:51], a[176:179], v[112:115], v[48:51]\n"
    "s_waitcnt lgkmcnt(10)\n"
    "v_lshlrev_b32 v100, 16, v240\n"
    "s_waitcnt vmcnt(7)\n"
    "v_mfma_f32_16x16x32_bf16 v[32:35], a[180:183], v[116:119], v[32:35]\n"
    "buffer_load_dwordx2 v[108:109], v231, s[24:27], s31 offen sc1\n"
    "buffer_load_dwordx2 v[110:111], v231, s[24:27], s31 offen offset:128 sc1\n"
    "v_and_b32 v101, 0xffff0000, v240\n"
    "v_mfma_f32_16x16x32_bf16 v[36:39], a[184:187], v[116:119], v[36:39]\n"
    "v_lshlrev_b32 v102, 16, v241\n"
    "v_mfma_f32_16x16x32_bf16 v[40:43], a[188:191], v[116:119], v[40:43]\n"
    "v_and_b32 v103, 0xffff0000, v241\n"
    "v_mfma_f32_16x16x32_bf16 v[44:47], a[192:195], v[116:119], v[44:47]\n"
    "v_mfma_f32_16x16x32_bf16 v[48:51], a[196:199], v[116:119], v[48:51]\n"
    "s_waitcnt vmcnt(8)\n"
    "v_mfma_f32_16x16x32_bf16 v[32:35], a[200:203], v[120:123], v[32:35]\n"
    "v_mfma_f32_16x16x32_bf16 v[36:39], a[204:207], v[120:123], v[36:39]\n"
    "buffer_load_dwordx4 a[20:23], v1, s[24:27], s3 offen sc1\n"
    "v_mfma_f32_16x16x32_bf16 v[40:43], a[208:211], v[120:123], v[40:43]\n"
    "v_mfma_f32_16x16x32_bf16 v[44:47], a[212:215], v[120:123], v[44:47]\n"
    "v_mfma_f32_16x16x32_bf16 v[48:51], a[216:219], v[120:123], v[48:51]\n"
    "s_waitcnt vmcnt(7)\n"
    "v_mfma_f32_16x16x32_bf16 v[32:35], a[220:223], v[124:127], v[32:35]\n"
    "buffer_load_dwordx4 a[24:27], v1, s[24:27], s3 offen offset:1024 sc1\n"
    "v_mfma_f32_16x16x32_bf16 v[36:39], a[224:227], v[124:127], v[36:39]\n"
    "v_mfma_f32_16x16x32_bf16 v[40:43], a[228:231], v[124:127], v[40:43]\n"
    "v_mfma_f32_16x16x32_bf16 v[44:47], a[232:235], v[124:127], v[44:47]\n"
    "v_mfma_f32_16x16x32_bf16 v[48:51], a[236:239], v[124:127], v[48:51]\n"
    "buffer_load_dwordx4 a[28:31], v1, s[24:27], s3 offen offset:2048 sc1\n"
    "s_waitcnt vmcnt(8)\n"
    "v_mfma_f32_16x16x32_bf16 v[52:55], a[140:143], v[128:131], v[84:87]\n"
    "v_mfma_f32_16x16x32_bf16 v[56:59], a[144:147], v[128:131], v[88:91]\n"
    "v_mfma_f32_16x16x32_bf16 v[60:63], a[148:151], v[128:131], v[92:95]\n"
    "v_mfma_f32_16x16x32_bf16 v[64:67], a[152:155], v[128:131], v[96:99]\n"
    "buffer_load_dwordx4 a[32:35], v1, s[24:27], s3 offen offset:3072 sc1\n"
    "v_mfma_f32_16x16x32_bf16 v[68:71], a[156:159], v[128:131], v[100:103]\n"
    "ds_write_b128 v16, v[32:35]\n"
    "s_waitcnt vmcnt(8)\n"
    "v_mfma_f32_16x16x32_bf16 v[52:55], a[160:163], v[132:135], v[52:55]\n"
    "ds_write_b128 v16, v[36:39] offset:1024\n"
    "v_mfma_f32_16x16x32_bf16 v[56:59], a[164:167], v[132:135], v[56:59]\n"
    "ds_write_b128 v16, v[40:43] offset:2048\n"
    "v_mfma_f32_16x16x32_bf16 v[60:63], a[168:171], v[132:135], v[60:63]\n"
    "s_add_u32 s3, s60, 0x8000\n"
    "buffer_load_dwordx4 a[36:39], v0, s[24:27], s3 offen sc1\n"
    "ds_write_b128 v16, v[44:47] offset:3072\n"
    "v_mfma_f32_16x16x32_bf16 v[64:67], a[172:175], v[132:135], v[64:67]\n"
    "ds_write_b128 v16, v[48:51] offset:4096\n"
    "v_mfma_f32_16x16x32_bf16 v[68:71], a[176:179], v[132:135], v[68:71]\n"
    "s_waitcnt vmcnt(8)\n"
    "v_mfma_f32_16x16x32_bf16 v[52:55], a[180:183], v[136:139], v[52:55]\n"
    "v_mfma_f32_16x16x32_bf16 v[56:59], a[184:187], v[136:139], v[56:59]\n"
    "buffer_load_dwordx4 a[40:43], v0, s[24:27], s3 offen offset:1024 sc1\n"
    "v_mfma_f32_16x16x32_bf16 v[60:63], a[188:191], v[136:139], v[60:63]\n"
    "v_mfma_f32_16x16x32_bf16 v[64:67], a[192:195], v[136:139], v[64:67]\n"
    "v_mfma_f32_16x16x32_bf16 v[68:71], a[196:199], v[136:139], v[68:71]\n"
    "s_waitcnt lgkmcnt(0)\n"
    "s_barrier\n"
    "s_waitcnt vmcnt(8)\n"
    "v_mfma_f32_16x16x32_bf16 v[52:55], a[200:203], v[104:107], v[52:55]\n"
    "buffer_load_dwordx4 a[44:47], v0, s[24:27], s3 offen offset:2048 sc1\n"
    "ds_read_b128 v[32:35], v17\n"
    "ds_read_b128 v[140:143], v17 offset:5120\n"
    "v_mfma_f32_16x16x32_bf16 v[56:59], a[204:207], v[104:107], v[56:59]\n"
    "ds_read_b128 v[160:163], v17 offset:10240\n"
    "ds_read_b128 v[36:39], v17 offset:1024\n"
    "v_mfma_f32_16x16x32_bf16 v[60:63], a[208:211], v[104:107], v[60:63]\n"
    "ds_read_b128 v[144:147], v17 offset:6144\n"
    "ds_read_b128 v[164:167], v17 offset:11264\n"
    "v_mfma_f32_16x16x32_bf16 v[64:67], a[212:215], v[104:107], v[64:67]\n"
    "ds_read_b128 v[40:43], v17 offset:2048\n"
    "buffer_load_dwordx4 a[48:51], v0, s[24:27], s3 offen offset:3072 sc1\n"
    "ds_read_b128 v[148:151], v17 offset:7168\n"
    "v_mfma_f32_16x16x32_bf16 v[68:71], a[216:219], v[104:107], v[68:71]\n"
    "ds_read_b128 v[168:171], v17 offset:12288\n"
    "ds_read_b128 v[44:47], v17 offset:3072\n"
    "s_waitcnt vmcnt(8)\n"
    "v_mfma_f32_16x16x32_bf16 v[52:55], a[220:223], v[108:111], v[52:55]\n"
    "ds_read_b128 v[152:155], v17 offset:8192\n"
    "ds_read_b128 v[172:175], v17 offset:13312\n"
    "v_mfma_f32_16x16x32_bf16 v[56:59], a[224:227], v[108:111], v[56:59]\n"
    "ds_read_b128 v[48:51], v17 offset:4096\n"
    "ds_read_b128 v[156:159], v17 offset:9216\n"
    "buffer_load_dwordx4 a[52:55], v1, s[24:27], s3 offen sc1\n"
    "v_mfma_f32_16x16x32_bf16 v[60:63], a[228:231], v[108:111], v[60:63]\n"
    "ds_read_b128 v[176:179], v17 offset:14336\n"
    "v_mfma_f32_16x16x32_bf16 v[64:67], a[232:235], v[108:111], v[64:67]\n"
    "v_mfma_f32_16x16x32_bf16 v[68:71], a[236:239], v[108:111], v[68:71]\n"
    "v_mov_b32 v84, 0\n"
    "v_mov_b32 v85, 0\n"
    "v_mov_b32 v86, 0x3f803f80\n"
    "s_waitcnt lgkmcnt(12)\n"
    "v_pk_add_f32 v[140:141], v[140:141], v[160:161]\n"
    "v_pk_add_f32 v[52:53], v[52:53], v[32:33]\n"
    "v_pk_add_f32 v[142:143], v[142:143], v[162:163]\n"
    "v_pk_add_f32 v[54:55], v[54:55], v[34:35]\n"
    "v_pk_add_f32 v[52:53], v[52:53], v[140:141]\n"
    "v_pk_add_f32 v[54:55], v[54:55], v[142:143]\n"
    "v_cvt_pk_bf16_f32 v72, v52, v53\n"
    "v_cvt_pk_bf16_f32 v73, v54, v55\n"
    "s_waitcnt lgkmcnt(9)\n"
    "v_pk_add_f32 v[144:145], v[144:145], v[164:165]\n"
    "v_pk_add_f32 v[56:57], v[56:57], v[36:37]\n"
    "v_pk_add_f32 v[146:147], v[146:147], v[166:167]\n"
    "v_pk_add_f32 v[58:59], v[58:59], v[38:39]\n"
    "v_pk_add_f32 v[56:57], v[56:57], v[144:145]\n"
    "v_pk_add_f32 v[58:59], v[58:59], v[146:147]\n"
    "v_cvt_pk_bf16_f32 v74, v56, v57\n"
    "v_cvt_pk_bf16_f32 v75, v58, v59\n"
    "buffer_store_dwordx4 v[72:75], v0, s[24:27], s62 offen sc1\n"
    "s_waitcnt lgkmcnt(6)\n"
    "v_pk_add_f32 v[148:149], v[148:149], v[168:169]\n"
    "v_pk_add_f32 v[60:61], v[60:61], v[40:41]\n"
    "v_pk_add_f32 v[150:151], v[150:151], v[170:171]\n"
    "v_pk_add_f32 v[62:63], v[62:63], v[42:43]\n"
    "v_pk_add_f32 v[60:61], v[60:61], v[148:149]\n"
    "v_pk_add_f32 v[62:63], v[62:63], v[150:151]\n"
    "v_cvt_pk_bf16_f32 v76, v60, v61\n"
    "v_cvt_pk_bf16_f32 v77, v62, v63\n"
    "s_waitcnt lgkmcnt(3)\n"
    "v_pk_add_f32 v[152:153], v[152:153], v[172:173]\n"
    "v_pk_add_f32 v[64:65], v[64:65], v[44:45]\n"
    "v_pk_add_f32 v[154:155], v[154:155], v[174:175]\n"
    "v_pk_add_f32 v[66:67], v[66:67], v[46:47]\n"
    "v_pk_add_f32 v[64:65], v[64:65], v[152:153]\n"
    "v_pk_add_f32 v[66:67], v[66:67], v[154:155]\n"
    "v_cvt_pk_bf16_f32 v78, v64, v65\n"
    "v_cvt_pk_bf16_f32 v79, v66, v67\n"
    "buffer_store_dwordx4 v[76:79], v0, s[24:27], s62 offen offset:1024 sc1\n"
    "s_waitcnt lgkmcnt(0)\n"
    "v_pk_add_f32 v[156:157], v[156:157], v[176:177]\n"
    "v_pk_add_f32 v[68:69], v[68:69], v[48:49]\n"
    "v_pk_add_f32 v[158:159], v[158:159], v[178:179]\n"
    "v_pk_add_f32 v[70:71], v[70:71], v[50:51]\n"
    "v_pk_add_f32 v[68:69], v[68:69], v[156:157]\n"
    "v_pk_add_f32 v[70:71], v[70:71], v[158:159]\n"
    "v_cvt_pk_bf16_f32 v80, v68, v69\n"
    "v_cvt_pk_bf16_f32 v81, v70, v71\n"
    "buffer_store_dwordx2 v[80:81], v11, s[24:27], s62 offen sc1\n"
    "v_dot2c_f32_bf16 v84, v86, v72\n"
    "v_dot2c_f32_bf16 v85, v72, v72\n"
    "v_dot2c_f32_bf16 v84, v86, v73\n"
    "v_dot2c_f32_bf16 v85, v73, v73\n"
    "v_dot2c_f32_bf16 v84, v86, v74\n"
    "v_dot2c_f32_bf16 v85, v74, v74\n"
    "v_dot2c_f32_bf16 v84, v86, v75\n"
    "v_dot2c_f32_bf16 v85, v75, v75\n"
    "v_dot2c_f32_bf16 v84, v86, v76\n"
    "v_dot2c_f32_bf16 v85, v76, v76\n"
    "v_dot2c_f32_bf16 v84, v86, v77\n"
    "v_dot2c_f32_bf16 v85, v77, v77\n"
    "v_dot2c_f32_bf16 v84, v86, v78\n"
    "v_dot2c_f32_bf16 v85, v78, v78\n"
    "v_dot2c_f32_bf16 v84, v86, v79\n"
    "v_dot2c_f32_bf16 v85, v79, v79\n"
    "v_dot2c_f32_bf16 v84, v86, v80\n"
    "v_dot2c_f32_bf16 v85, v80, v80\n"
    "v_dot2c_f32_bf16 v84, v86, v81\n"
    "v_dot2c_f32_bf16 v85, v81, v81\n"
    "s_nop 2\n"
    "v_permlane16_swap_b32 v84, v85\n"
    "v_add_f32 v84, v84, v85\n"
    "v_mov_b32 v85, v84\n"
    "s_nop 1\n"
    "v_permlane32_swap_b32 v84, v85\n"
    "v_add_f32 v84, v84, v85\n"
    "buffer_store_dword v84, v12, s[24:27], 0 offen sc1\n"
    "; ---- H3 (t~26502)\n"
    "buffer_load_dwordx4 v[56:59], v20, s[36:39], 0 offen\n"
    "buffer_load_dwordx2 v[60:61], v21, s[36:39], 0 offen\n"
    "buffer_load_dwordx4 a[80:83], v1, s[24:27], s3 offen offset:1024 sc1\n"
    "buffer_load_dwordx4 a[84:87], v1, s[24:27], s3 offen offset:2048 sc1\n"
    "buffer_load_dwordx4 a[88:91], v1, s[24:27], s3 offen offset:3072 sc1\n"
    "s_add_u32 s3, s60, 0xa000\n"
    "buffer_load_dwordx4 a[92:95], v0, s[24:27], s3 offen sc1\n"
    "buffer_load_dwordx4 a[96:99], v0, s[24:27], s3 offen offset:1024 sc1\n"
    "buffer_load_dwordx4 a[100:103], v0, s[24:27], s3 offen offset:2048 sc1\n"
    "buffer_load_dwordx4 a[104:107], v0, s[24:27], s3 offen offset:3072 sc1\n"
    "buffer_load_dwordx4 a[108:111], v1, s[24:27], s3 offen sc1\n"
    "ds_read_b128 a[112:115], v30\n"
    "ds_read_b128 a[116:119], v30 offset:1024\n"
    "ds_read_b128 a[120:123], v30 offset:2048\n"
    "ds_read_b128 a[124:127], v30 offset:3072\n"
    "ds_read_b128 a[128:131], v30 offset:4096\n"
    "ds_read_b128 a[132:135], v30 offset:5120\n"
    "ds_read_b128 a[136:139], v30 offset:6144\n"
    "ds_read_b128 a[140:143], v30 offset:7168\n"
    "ds_read_b128 a[144:147], v30 offset:8192\n"
    "ds_read_b128 a[148:151], v30 offset:9216\n"
    "ds_read_b128 a[152:155], v30 offset:10240\n"
    "ds_read_b128 a[156:159], v30 offset:11264\n"
    "ds_read_b128 a[160:163], v30 offset:12288\n"
    "ds_read_b128 a[164:167], v30 offset:13312\n"
    "ds_read_b128 a[168:171], v30 offset:14336\n"
    "ds_read_b128 a[172:175], v30 offset:15360\n"
    "ds_read_b128 a[176:179], v30 offset:16384\n"
    "ds_read_b128 a[180:183], v30 offset:17408\n"
    "ds_read_b128 a[184:187], v30 offset:18432\n"
    "v_mov_b32 v64, 0x3f803f80\n"
    "v_mov_b32 v65, 0x3f803f80\n"
    "v_mov_b32 v66, 0x3f803f80\n"
    "v_mov_b32 v67, 0x3f803f80\n"
    "s_mov_b32 s30, 0xbdd2d3e8\n"
    "s_mov_b32 s31, 0xbdd2d3e8\n"
    "v_mov_b32 v142, 0xc0135761\n"
    "v_mov_b32 v143, 0xc0135761\n"
    "v_add_u32 v31, 0xa000, v0\n"
    "s_lshl_b32 s32, s16, 10\n"
    "s_mov_b32 s28, s15\n"
    "s_add_u32 s0, s13, 0x2020\n"
    "s_waitcnt vmcnt(10)\n"
    "s_waitcnt lgkmcnt(0)\n"
    "s_barrier\n"
    "s_cmp_lg_u32 s16, 0\n"
    "s_cbranch_scc1 .Lfb_fs_32\n"
    "s_mov_b64 exec, 1\n"
    "v_mov_b32 v229, s96\n"
    "s_add_u32 s97, s12, 0x2020\n"
    "buffer_store_dword v229, v25, s[24:27], s97 offen sc1\n"
    "s_mov_b64 exec, -1\n"
    ".Lfb_fs_32:\n"
    "s_getpc_b64 vcc\n"
    ".Lfb_pc_33:\n"
    "s_bfe_u32 s97, s17, 0x20003\n"
    "s_lshl_b32 s97, s97, 2\n"
    "s_add_u32 s97, s97, s16\n"
    "s_lshl_b32 s97, s97, 10\n"
    "v_and_b32 v253, 0x70, v0\n"
    "v_lshl_add_u32 v253, v253, 3, s97\n"
    "v_min_u32 v253, .Lfb_end-.Lfb_c5, v253\n"
    "v_add_u32 v253, .Lfb_c5-.Lfb_pc_33, v253\n"
    "global_load_dword v253, v253, vcc\n"
    "v_mfma_f32_16x16x32_bf16 v[32:35], a[240:243], v[64:67], 0\n"
    "v_mfma_f32_16x16x32_bf16 v[36:39], a[244:247], v[64:67], 0\n"
    "v_mfma_f32_16x16x32_bf16 v[40:43], a[248:251], v[64:67], 0\n"
    "buffer_load_dwordx4 a[188:191], v1, s[24:27], s3 offen offset:1024 sc1\n"
    "v_mfma_f32_16x16x32_bf16 v[32:35], a[252:255], v[64:67], v[32:35]\n"
    "v_mfma_f32_16x16x32_bf16 v[36:39], a[112:115], v[64:67], v[36:39]\n"
    "v_mfma_f32_16x16x32_bf16 v[40:43], a[116:119], v[64:67], v[40:43]\n"
    "buffer_load_dwordx4 a[192:195], v1, s[24:27], s3 offen offset:2048 sc1\n"
    "v_mfma_f32_16x16x32_bf16 v[32:35], a[120:123], v[64:67], v[32:35]\n"
    "v_mfma_f32_16x16x32_bf16 v[36:39], a[124:127], v[64:67], v[36:39]\n"
    "v_mfma_f32_16x16x32_bf16 v[40:43], a[128:131], v[64:67], v[40:43]\n"
    "buffer_load_dwordx4 a[196:199], v1, s[24:27], s3 offen offset:3072 sc1\n"
    "v_mfma_f32_16x16x32_bf16 v[32:35], a[132:135], v[64:67], v[32:35]\n"
    "v_mfma_f32_16x16x32_bf16 v[36:39], a[136:139], v[64:67], v[36:39]\n"
    "v_mfma_f32_16x16x32_bf16 v[40:43], a[140:143], v[64:67], v[40:43]\n"
    "s_add_u32 s3, s60, 0xc000\n"
    "buffer_load_dwordx4 a[200:203], v0, s[24:27], s3 offen sc1\n"
    "v_mfma_f32_16x16x32_bf16 v[32:35], a[144:147], v[64:67], v[32:35]\n"
    "v_mfma_f32_16x16x32_bf16 v[36:39], a[148:151], v[64:67], v[36:39]\n"
    "v_mfma_f32_16x16x32_bf16 v[40:43], a[152:155], v[64:67], v[40:43]\n"
    "buffer_load_dwordx4 a[204:207], v0, s[24:27], s3 offen offset:1024 sc1\n"
    "v_mfma_f32_16x16x32_bf16 v[32:35], a[156:159], v[64:67], v[32:35]\n"
    "v_mfma_f32_16x16x32_bf16 v[36:39], a[160:163], v[64:67], v[36:39]\n"
    "v_mfma_f32_16x16x32_bf16 v[40:43], a[164:167], v[64:67], v[40:43]\n"
    "buffer_load_dwordx4 a[208:211], v0, s[24:27], s3 offen offset:2048 sc1\n"
    "v_mfma_f32_16x16x32_bf16 v[32:35], a[168:171], v[64:67], v[32:35]\n"
    "v_mfma_f32_16x16x32_bf16 v[36:39], a[172:175], v[64:67], v[36:39]\n"
    "v_mfma_f32_16x16x32_bf16 v[40:43], a[176:179], v[64:67], v[40:43]\n"
    "buffer_load_dwordx4 a[212:215], v0, s[24:27], s3 offen offset:3072 sc1\n"
    "v_mfma_f32_16x16x32_bf16 v[32:35], a[180:183], v[64:67], v[32:35]\n"
    "v_mfma_f32_16x16x32_bf16 v[36:39], a[184:187], v[64:67], v[36:39]\n"
    "v_mfma_f32_16x16x32_bf16 v[40:43], a[0:3], v[64:67], v[40:43]\n"
    "buffer_load_dwordx4 a[216:219], v1, s[24:27], s3 offen sc1\n"
    "v_mfma_f32_16x16x32_bf16 v[32:35], a[4:7], v[64:67], v[32:35]\n"
    "v_mfma_f32_16x16x32_bf16 v[36:39], a[8:11], v[64:67], v[36:39]\n"
    "v_mfma_f32_16x16x32_bf16 v[40:43], a[12:15], v[64:67], v[40:43]\n"
    "buffer_load_dwordx4 a[220:223], v1, s[24:27], s3 offen offset:1024 sc1\n"
    "v_mfma_f32_16x16x32_bf16 v[32:35], a[16:19], v[64:67], v[32:35]\n"
    "v_mfma_f32_16x16x32_bf16 v[36:39], a[20:23], v[64:67], v[36:39]\n"
    "v_mfma_f32_16x16x32_bf16 v[40:43], a[24:27], v[64:67], v[40:43]\n"
    "buffer_load_dwordx4 a[224:227], v1, s[24:27], s3 offen offset:2048 sc1\n"
    "buffer_load_dwordx4 a[228:231], v1, s[24:27], s3 offen offset:3072 sc1\n"
    "s_add_u32 s3, s60, 0xe000\n"
    "buffer_load_dwordx4 a[232:235], v0, s[24:27], s3 offen sc1\n"
    "buffer_load_dwordx4 a[236:239], v0, s[24:27], s3 offen offset:1024 sc1\n"
    "buffer_load_dwordx4 v[148:151], v0, s[24:27], s3 offen offset:2048 sc1\n"
    "buffer_load_dwordx4 v[152:155], v0, s[24:27], s3 offen offset:3072 sc1\n"
    "s_cmp_lg_u32 s16, 0\n"
    "s_cbranch_scc1 .Lfb_ps_36\n"
    "s_mov_b64 exec, 0xff\n"
    "s_mov_b32 s97, 0\n"
    ".Lfb_pl_34:\n"
    "buffer_load_dword v229, v25, s[24:27], s0 offen sc1\n"
    "s_waitcnt vmcnt(0)\n"
    "v_cmp_ne_u32 vcc, s96, v229\n"
    "s_cmp_eq_u64 vcc, 0\n"
    "s_cbranch_scc1 .Lfb_pd_35\n"
    "s_sleep 1\n"
    "s_add_u32 s97, s97, 1\n"
    "s_cmp_lt_u32 s97, 0x10000\n"
    "s_cbranch_scc1 .Lfb_pl_34\n"
    ".Lfb_pd_35:\n"
    "s_mov_b64 exec, -1\n"
    ".Lfb_ps_36:\n"
    "s_barrier\n"
    ".Lfb_c4:\n"
    "; ---- P4 (t~27504)\n"
    "s_add_u32 m0, s32, 0\n"
    "s_nop 0\n"
    "buffer_load_dwordx4 v2, s[24:27], s28 offen sc1 lds\n"
    "s_add_u32 s28, s28, 0x1000\n"
    "s_add_u32 m0, s32, 0x1000\n"
    "s_nop 0\n"
    "buffer_load_dwordx4 v2, s[24:27], s28 offen sc1 lds\n"
    "s_add_u32 s28, s28, 0x1000\n"
    "s_add_u32 m0, s32, 0x2000\n"
    "s_nop 0\n"
    "buffer_load_dwordx4 v2, s[24:27], s28 offen sc1 lds\n"
    "s_add_u32 s28, s28, 0x1000\n"
    "s_add_u32 m0, s32, 0x3000\n"
    "s_nop 0\n"
    "buffer_load_dwordx4 v2, s[24:27], s28 offen sc1 lds\n"
    "s_add_u32 s28, s28, 0x1000\n"
    "s_add_u32 m0, s32, 0x4000\n"
    "s_nop 0\n"
    "buffer_load_dwordx4 v2, s[24:27], s28 offen sc1 lds\n"
    "s_add_u32 s28, s28, 0x1000\n"
    "buffer_load_dword v144, v25, s[24:27], s8 offen sc1\n"
    "buffer_load_dword v145, v25, s[24:27], s8 offen offset:256 sc1\n"
    "buffer_load_dword v146, v25, s[24:27], s8 offen offset:512 sc1\n"
    "buffer_load_dword v147, v25, s[24:27], s8 offen offset:768 sc1\n"
    "v_mfma_f32_16x16x32_bf16 v[32:35], a[28:31], v[64:67], v[32:35]\n"
    "v_mfma_f32_16x16x32_bf16 v[36:39], a[32:35], v[64:67], v[36:39]\n"
    "v_mfma_f32_16x16x32_bf16 v[40:43], a[36:39], v[64:67], v[40:43]\n"
    "v_mfma_f32_16x16x32_bf16 v[32:35], a[40:43], v[64:67], v[32:35]\n"
    "s_add_u32 m0, s32, 0x5000\n"
    "s_nop 0\n"
    "buffer_load_dwordx4 v2, s[24:27], s28 offen sc1 lds\n"
    "s_add_u32 s28, s28, 0x1000\n"
    "v_mfma_f32_16x16x32_bf16 v[36:39], a[44:47], v[64:67], v[36:39]\n"
    "v_mfma_f32_16x16x32_bf16 v[40:43], a[48:51], v[64:67], v[40:43]\n"
    "v_mfma_f32_16x16x32_bf16 v[32:35], a[52:55], v[64:67], v[32:35]\n"
    "s_waitcnt vmcnt(33)\n"
    "v_mfma_f32_16x16x32_bf16 v[36:39], a[80:83], v[64:67], v[36:39]\n"
    "s_add_u32 m0, s32, 0x6000\n"
    "s_nop 0\n"
    "buffer_load_dwordx4 v2, s[24:27], s28 offen sc1 lds\n"
    "s_add_u32 s28, s28, 0x1000\n"
    "s_waitcnt vmcnt(33)\n"
    "v_mfma_f32_16x16x32_bf16 v[40:43], a[84:87], v[64:67], v[40:43]\n"
    "s_waitcnt vmcnt(32)\n"
    "v_mfma_f32_16x16x32_bf16 v[32:35], a[88:91], v[64:67], v[32:35]\n"
    "s_waitcnt vmcnt(31)\n"
    "v_mfma_f32_16x16x32_bf16 v[36:39], a[92:95], v[64:67], v[36:39]\n"
    "s_waitcnt vmcnt(30)\n"
    "v_mfma_f32_16x16x32_bf16 v[40:43], a[96:99], v[64:67], v[40:43]\n"
    "s_add_u32 m0, s32, 0x7000\n"
    "s_nop 0\n"
    "buffer_load_dwordx4 v2, s[24:27], s28 offen sc1 lds\n"
    "s_add_u32 s28, s28, 0x1000\n"
    "s_waitcnt vmcnt(30)\n"
    "v_mfma_f32_16x16x32_bf16 v[32:35], a[100:103], v[64:67], v[32:35]\n"
    "s_waitcnt vmcnt(29)\n"
    "v_mfma_f32_16x16x32_bf16 v[36:39], a[104:107], v[64:67], v[36:39]\n"
    "s_waitcnt vmcnt(28)\n"
    "v_mfma_f32_16x16x32_bf16 v[40:43], a[108:111], v[64:67], v[40:43]\n"
    "s_waitcnt vmcnt(26)\n"
    "v_mfma_f32_16x16x32_bf16 v[32:35], a[188:191], v[64:67], v[32:35]\n"
    "s_add_u32 m0, s32, 0x8000\n"
    "s_nop 0\n"
    "buffer_load_dwordx4 v2, s[24:27], s28 offen sc1 lds\n"
    "s_add_u32 s28, s28, 0x1000\n"
    "s_waitcnt vmcnt(26)\n"
    "v_mfma_f32_16x16x32_bf16 v[36:39], a[192:195], v[64:67], v[36:39]\n"
    "s_waitcnt vmcnt(25)\n"
    "v_mfma_f32_16x16x32_bf16 v[40:43], a[196:199], v[64:67], v[40:43]\n"
    "s_waitcnt vmcnt(24)\n"
    "v_mfma_f32_16x16x32_bf16 v[32:35], a[200:203], v[64:67], v[32:35]\n"
    "s_waitcnt vmcnt(23)\n"
    "v_mfma_f32_16x16x32_bf16 v[36:39], a[204:207], v[64:67], v[36:39]\n"
    "s_add_u32 m0, s32, 0x9000\n"
    "s_nop 0\n"
    "buffer_load_dwordx4 v2, s[24:27], s28 offen sc1 lds\n"
    "s_add_u32 s28, s28, 0x1000\n"
    "s_waitcnt vmcnt(23)\n"
    "v_mfma_f32_16x16x32_bf16 v[40:43], a[208:211], v[64:67], v[40:43]\n"
    "s_waitcnt vmcnt(22)\n"
    "v_mfma_f32_16x16x32_bf16 v[32:35], a[212:215], v[64:67], v[32:35]\n"
    "s_waitcnt vmcnt(21)\n"
    "v_mfma_f32_16x16x32_bf16 v[36:39], a[216:219], v[64:67], v[36:39]\n"
    "s_waitcnt vmcnt(20)\n"
    "v_mfma_f32_16x16x32_bf16 v[40:43], a[220:223], v[64:67], v[40:43]\n"
    "s_add_u32 m0, s32, 0xa000\n"
    "s_nop 0\n"
    "buffer_load_dwordx4 v2, s[24:27], s28 offen sc1 lds\n"
    "s_add_u32 s28, s28, 0x1000\n"
    "s_waitcnt vmcnt(20)\n"
    "v_mfma_f32_16x16x32_bf16 v[32:35], a[224:227], v[64:67], v[32:35]\n"
    "s_waitcnt vmcnt(19)\n"
    "v_mfma_f32_16x16x32_bf16 v[36:39], a[228:231], v[64:67], v[36:39]\n"
    "s_waitcnt vmcnt(18)\n"
    "v_mfma_f32_16x16x32_bf16 v[40:43], a[232:235], v[64:67], v[40:43]\n"
    "s_waitcnt vmcnt(17)\n"
    "v_mfma_f32_16x16x32_bf16 v[32:35], a[236:239], v[64:67], v[32:35]\n"
    "s_add_u32 m0, s32, 0xb000\n"
    "s_nop 0\n"
    "buffer_load_dwordx4 v2, s[24:27], s28 offen sc1 lds\n"
    "s_add_u32 s28, s28, 0x1000\n"
    "s_waitcnt vmcnt(17)\n"
    "v_mfma_f32_16x16x32_bf16 v[36:39], v[148:151], v[64:67], v[36:39]\n"
    "s_waitcnt vmcnt(16)\n"
    "v_mfma_f32_16x16x32_bf16 v[40:43], v[152:155], v[64:67], v[40:43]\n"
    "v_lshlrev_b32 v44, 16, v56\n"
    "v_and_b32 v45, 0xffff0000, v56\n"
    "v_lshlrev_b32 v46, 16, v57\n"
    "v_and_b32 v47, 0xffff0000, v57\n"
    "v_lshlrev_b32 v48, 16, v58\n"
    "v_and_b32 v49, 0xffff0000, v58\n"
    "s_add_u32 m0, s32, 0xc000\n"
    "s_nop 0\n"
    "buffer_load_dwordx4 v2, s[24:27], s28 offen sc1 lds\n"
    "s_add_u32 s28, s28, 0x1000\n"
    "v_lshlrev_b32 v50, 16, v59\n"
    "v_and_b32 v51, 0xffff0000, v59\n"
    "v_lshlrev_b32 v52, 16, v60\n"
    "v_and_b32 v53, 0xffff0000, v60\n"
    "v_lshlrev_b32 v54, 16, v61\n"
    "v_and_b32 v55, 0xffff0000, v61\n"
    "v_and_b32 v128, 16, v0\n"
    "v_and_b32 v116, 0x3e0, v0\n"
    "v_lshl_add_u32 v128, v116, 2, v128\n"
    "s_mov_b32 s1, 0x2000\n"
    "s_xor_b32 s97, s16, 1\n"
    "s_or_b32 s97, s97, s17\n"
    "s_cbranch_scc1 .Lfb_ps_39\n"
    "s_mov_b64 exec, -1\n"
    "s_mov_b32 s97, 0\n"
    ".Lfb_pl_37:\n"
    "buffer_load_dwordx4 v[116:119], v128, s[24:27], s1 offen sc1\n"
    "s_waitcnt vmcnt(0)\n"
    "v_xor_b32 v116, s96, v116\n"
    "v_xor_b32 v117, s96, v117\n"
    "v_xor_b32 v118, s96, v118\n"
    "v_xor_b32 v119, s96, v119\n"
    "v_or3_b32 v116, v116, v117, v118\n"
    "v_or_b32 v116, v116, v119\n"
    "v_cmp_ne_u32 vcc, 0, v116\n"
    "s_cmp_eq_u64 vcc, 0\n"
    "s_cbranch_scc1 .Lfb_pd_38\n"
    "s_sleep 4\n"
    "s_add_u32 s97, s97, 1\n"
    "s_cmp_lt_u32 s97, 0x10000\n"
    "s_cbranch_scc1 .Lfb_pl_37\n"
    ".Lfb_pd_38:\n"
    "s_mov_b64 exec, 1\n"
    "v_mov_b32 v116, s96\n"
    "buffer_store_dword v116, v25, s[24:27], 0 offen sc1\n"
    "s_mov_b64 exec, -1\n"
    ".Lfb_ps_39:\n"
    "s_waitcnt vmcnt(12)\n"
    "s_barrier\n"
    "ds_read_b128 a[56:59], v0\n"
    "ds_read_b128 a[60:63], v0 offset:1024\n"
    "ds_read_b128 a[64:67], v0 offset:2048\n"
    "ds_read_b128 a[68:71], v0 offset:3072\n"
    "s_mul_i32 s33, s16, 0x1000\n"
    "v_add_u32 v56, s33, v0\n"
    "s_mul_i32 s0, s16, 0x3000\n"
    "s_add_u32 s0, s0, 0xcc8000\n"
    "s_xor_b32 s48, s6, 0\n"
    "s_add_u32 s52, s48, s5\n"
    "s_mul_i32 s52, s52, 0xc000\n"
    "s_add_u32 s52, s52, s0\n"
    "s_add_u32 s54, s52, 0x18000\n"
    "s_xor_b32 s49, s6, 1\n"
    "s_add_u32 s53, s49, s5\n"
    "s_mul_i32 s53, s53, 0xc000\n"
    "s_add_u32 m0, s32, 0xd000\n"
    "s_nop 0\n"
    "buffer_load_dwordx4 v2, s[24:27], s28 offen sc1 lds\n"
    "s_add_u32 s28, s28, 0x1000\n"
    "s_add_u32 s53, s53, s0\n"
    "s_add_u32 s55, s53, 0x18000\n"
    "v_add_u32 v57, 0x2000, v0\n"
    "v_and_b32 v58, 4, v25\n"
    "s_lshl_b32 s0, s16, 3\n"
    "s_add_u32 s0, s0, s13\n"
    "s_add_u32 s0, s0, 0x2040\n"
    "v_add_u32 v58, s0, v58\n"
    "s_waitcnt lgkmcnt(3)\n"
    "v_mfma_f32_16x16x32_bf16 v[68:71], a[240:243], a[56:59], 0\n"
    "ds_read_b128 a[72:75], v0 offset:4096\n"
    "v_mfma_f32_16x16x32_bf16 v[84:87], a[244:247], a[56:59], 0\n"
    "v_mfma_f32_16x16x32_bf16 v[88:91], a[248:251], a[56:59], 0\n"
    "s_add_u32 m0, s32, 0xe000\n"
    "s_nop 0\n"
    "buffer_load_dwordx4 v2, s[24:27], s28 offen sc1 lds\n"
    "s_add_u32 s28, s28, 0x1000\n"
    "s_waitcnt vmcnt(12)\n"
    "v_add_f32 v144, v144, v145\n"
    "s_waitcnt lgkmcnt(3)\n"
    "v_mfma_f32_16x16x32_bf16 v[68:71], a[252:255], a[60:63], v[68:71]\n"
    "ds_read_b128 a[76:79], v0 offset:5120\n"
    "s_waitcnt vmcnt(10)\n"
    "v_add_f32 v146, v146, v147\n"
    "v_mfma_f32_16x16x32_bf16 v[84:87], a[112:115], a[60:63], v[84:87]\n"
    "v_add_f32 v144, v144, v146\n"
    "v_mfma_f32_16x16x32_bf16 v[88:91], a[116:119], a[60:63], v[88:91]\n"
    "v_mov_b32 v145, v144\n"
    "s_add_u32 m0, s32, 0xf000\n"
    "s_nop 0\n"
    "buffer_load_dwordx4 v2, s[24:27], s28 offen sc1 lds\n"
    "s_add_u32 s28, s28, 0x1000\n"
    "s_waitcnt lgkmcnt(3)\n"
    "v_mfma_f32_16x16x32_bf16 v[68:71], a[120:123], a[64:67], v[68:71]\n"
    "ds_read_b128 a[56:59], v0 offset:6144\n"
    "v_permlane32_swap_b32 v144, v145\n"
    "v_mfma_f32_16x16x32_bf16 v[84:87], a[124:127], a[64:67], v[84:87]\n"
    "v_add_f32 v144, v144, v145\n"
    "v_mfma_f32_16x16x32_bf16 v[88:91], a[128:131], a[64:67], v[88:91]\n"
    "v_mov_b32 v145, v144\n"
    "s_waitcnt lgkmcnt(3)\n"
    "v_mfma_f32_16x16x32_bf16 v[68:71], a[132:135], a[68:71], v[68:71]\n"
    "s_add_u32 m0, s32, 0x10000\n"
    "s_nop 0\n"
    "buffer_load_dwordx4 v2, s[24:27], s28 offen sc1 lds\n"
    "s_add_u32 s28, s28, 0x1000\n"
    "ds_read_b128 a[60:63], v0 offset:7168\n"
    "v_permlane16_swap_b32 v144, v145\n"
    "v_mfma_f32_16x16x32_bf16 v[84:87], a[136:139], a[68:71], v[84:87]\n"
    "v_mul_f32 v144, 0x3acccccd, v144\n"
    "v_mfma_f32_16x16x32_bf16 v[88:91], a[140:143], a[68:71], v[88:91]\n"
    "s_waitcnt lgkmcnt(3)\n"
    "v_mfma_f32_16x16x32_bf16 v[68:71], a[144:147], a[72:75], v[68:71]\n"
    "s_add_u32 m0, s32, 0x11000\n"
    "s_nop 0\n"
    "buffer_load_dwordx4 v2, s[24:27], s28 offen sc1 lds\n"
    "s_add_u32 s28, s28, 0x1000\n"
    "ds_read_b128 a[64:67], v0 offset:8192\n"
    "v_mul_f32 v145, 0x3acccccd, v145\n"
    "v_mfma_f32_16x16x32_bf16 v[84:87], a[148:151], a[72:75], v[84:87]\n"
    "v_mfma_f32_16x16x32_bf16 v[88:91], a[152:155], a[72:75], v[88:91]\n"
    "v_fma_f32 v145, -v144, v144, v145\n"
    "s_waitcnt lgkmcnt(3)\n"
    "v_mfma_f32_16x16x32_bf16 v[68:71], a[156:159], a[76:79], v[68:71]\n"
    "s_add_u32 m0, s32, 0x12000\n"
    "s_nop 0\n"
    "buffer_load_dwordx4 v2, s[24:27], s28 offen sc1 lds\n"
    "s_add_u32 s28, s28, 0x1000\n"
    "ds_read_b128 a[68:71], v0 offset:9216\n"
    "v_mfma_f32_16x16x32_bf16 v[84:87], a[160:163], a[76:79], v[84:87]\n"
    "v_max_f32 v145, 0, v145\n"
    "v_mfma_f32_16x16x32_bf16 v[88:91], a[164:167], a[76:79], v[88:91]\n"
    "s_waitcnt lgkmcnt(3)\n"
    "v_mfma_f32_16x16x32_bf16 v[68:71], a[168:171], a[56:59], v[68:71]\n"
    "ds_read_b128 a[72:75], v0 offset:10240\n"
    "s_add_u32 m0, s32, 0x13000\n"
    "s_nop 0\n"
    "buffer_load_dwordx4 v2, s[24:27], s28 offen sc1 lds\n"
    "s_add_u32 s28, s28, 0x1000\n"
    "v_add_f32 v146, 0x3727c5ac, v145\n"
    "v_mfma_f32_16x16x32_bf16 v[84:87], a[172:175], a[56:59], v[84:87]\n"
    "v_mfma_f32_16x16x32_bf16 v[88:91], a[176:179], a[56:59], v[88:91]\n"
    "v_rsq_f32 v146, v146\n"
    "s_waitcnt lgkmcnt(3)\n"
    "v_mfma_f32_16x16x32_bf16 v[68:71], a[180:183], a[60:63], v[68:71]\n"
    "s_add_u32 s3, s61, 0\n"
    "s_add_u32 m0, s19, 0\n"
    "s_nop 0\n"
    "buffer_load_dwordx4 v0, s[24:27], s3 offen sc1 lds\n"
    "ds_read_b128 a[76:79], v0 offset:11264\n"
    "v_mfma_f32_16x16x32_bf16 v[84:87], a[184:187], a[60:63], v[84:87]\n"
    "v_mul_f32 v145, v145, v146\n"
    "v_mfma_f32_16x16x32_bf16 v[88:91], a[0:3], a[60:63], v[88:91]\n"
    "s_waitcnt lgkmcnt(3)\n"
    "v_mfma_f32_16x16x32_bf16 v[68:71], a[4:7], a[64:67], v[68:71]\n"
    "buffer_load_dwordx4 v0, s[24:27], s3 offen offset:1024 sc1 lds\n"
    "ds_read_b128 a[56:59], v0 offset:12288\n"
    "v_mul_f32 v145, v145, v146\n"
    "v_mfma_f32_16x16x32_bf16 v[84:87], a[8:11], a[64:67], v[84:87]\n"
    "v_mfma_f32_16x16x32_bf16 v[88:91], a[12:15], a[64:67], v[88:91]\n"
    "v_add_f32 v145, 0x3727c5ac, v145\n"
    "s_waitcnt lgkmcnt(3)\n"
    "v_mfma_f32_16x16x32_bf16 v[68:71], a[16:19], a[68:71], v[68:71]\n"
    "buffer_load_dwordx4 v0, s[24:27], s3 offen offset:2048 sc1 lds\n"
    "ds_read_b128 a[60:63], v0 offset:13312\n"
    "v_mfma_f32_16x16x32_bf16 v[84:87], a[20:23], a[68:71], v[84:87]\n"
    "v_rsq_f32 v145, v145\n"
    "v_mfma_f32_16x16x32_bf16 v[88:91], a[24:27], a[68:71], v[88:91]\n"
    "s_waitcnt lgkmcnt(3)\n"
    "v_mfma_f32_16x16x32_bf16 v[68:71], a[28:31], a[72:75], v[68:71]\n"
    "buffer_load_dwordx4 v0, s[24:27], s3 offen offset:3072 sc1 lds\n"
    "ds_read_b128 a[64:67], v0 offset:14336\n"
    "v_mul_f32 v146, v146, v145\n"
    "v_mfma_f32_16x16x32_bf16 v[84:87], a[32:35], a[72:75], v[84:87]\n"
    "v_mfma_f32_16x16x32_bf16 v[88:91], a[36:39], a[72:75], v[88:91]\n"
    "v_mul_f32_e64 v147, -v144, v146\n"
    "s_waitcnt lgkmcnt(3)\n"
    "v_mfma_f32_16x16x32_bf16 v[68:71], a[40:43], a[76:79], v[68:71]\n"
    "s_add_u32 m0, s19, 0x1000\n"
    "s_nop 0\n"
    "buffer_load_dwordx4 v1, s[24:27], s3 offen sc1 lds\n"
    "ds_read_b128 a[68:71], v0 offset:15360\n"
    "v_mfma_f32_16x16x32_bf16 v[84:87], a[44:47], a[76:79], v[84:87]\n"
    "ds_write_b64 v18, v[146:147]\n"
    "v_mfma_f32_16x16x32_bf16 v[88:91], a[48:51], a[76:79], v[88:91]\n"
    "s_waitcnt lgkmcnt(4)\n"
    "v_mfma_f32_16x16x32_bf16 v[68:71], a[52:55], a[56:59], v[68:71]\n"
    "buffer_load_dwordx4 v1, s[24:27], s3 offen offset:1024 sc1 lds\n"
    "ds_read_b128 a[72:75], v0 offset:16384\n"
    "v_mfma_f32_16x16x32_bf16 v[84:87], a[80:83], a[56:59], v[84:87]\n"
    "v_mfma_f32_16x16x32_bf16 v[88:91], a[84:87], a[56:59], v[88:91]\n"
    "s_waitcnt lgkmcnt(4)\n"
    "v_mfma_f32_16x16x32_bf16 v[68:71], a[88:91], a[60:63], v[68:71]\n"
    "buffer_load_dwordx4 v1, s[24:27], s3 offen offset:2048 sc1 lds\n"
    "ds_read_b128 a[76:79], v0 offset:17408\n"
    "v_mfma_f32_16x16x32_bf16 v[84:87], a[92:95], a[60:63], v[84:87]\n"
    "v_mfma_f32_16x16x32_bf16 v[88:91], a[96:99], a[60:63], v[88:91]\n"
    "s_waitcnt lgkmcnt(4)\n"
    "v_mfma_f32_16x16x32_bf16 v[68:71], a[100:103], a[64:67], v[68:71]\n"
    "buffer_load_dwordx4 v1, s[24:27], s3 offen offset:3072 sc1 lds\n"
    "ds_read_b128 a[56:59], v0 offset:18432\n"
    "v_mfma_f32_16x16x32_bf16 v[84:87], a[104:107], a[64:67], v[84:87]\n"
    "v_mfma_f32_16x16x32_bf16 v[88:91], a[108:111], a[64:67], v[88:91]\n"
    "s_waitcnt vmcnt(18)\n"
    "s_waitcnt lgkmcnt(3)\n"
    "s_barrier\n"
    "v_mfma_f32_16x16x32_bf16 v[68:71], a[188:191], a[68:71], v[68:71]\n"
    "s_add_u32 s3, s61, 0x2000\n"
    "s_add_u32 m0, s19, 0x2000\n"
    "s_nop 0\n"
    "buffer_load_dwordx4 v0, s[24:27], s3 offen sc1 lds\n"
    "ds_read_b128 a[60:63], v0 offset:19456\n"
    "v_mfma_f32_16x16x32_bf16 v[84:87], a[192:195], a[68:71], v[84:87]\n"
    "v_mfma_f32_16x16x32_bf16 v[88:91], a[196:199], a[68:71], v[88:91]\n"
    "s_waitcnt lgkmcnt(3)\n"
    "v_mfma_f32_16x16x32_bf16 v[68:71], a[200:203], a[72:75], v[68:71]\n"
    "buffer_load_dwordx4 v0, s[24:27], s3 offen offset:1024 sc1 lds\n"
    "ds_read_b128 a[64:67], v0 offset:20480\n"
    "v_mfma_f32_16x16x32_bf16 v[84:87], a[204:207], a[72:75], v[84:87]\n"
    "v_mfma_f32_16x16x32_bf16 v[88:91], a[208:211], a[72:75], v[88:91]\n"
    "s_waitcnt lgkmcnt(3)\n"
    "v_mfma_f32_16x16x32_bf16 v[68:71], a[212:215], a[76:79], v[68:71]\n"
    "buffer_load_dwordx4 v0, s[24:27], s3 offen offset:2048 sc1 lds\n"
    "ds_read_b128 a[68:71], v0 offset:21504\n"
    "v_mfma_f32_16x16x32_bf16 v[84:87], a[216:219], a[76:79], v[84:87]\n"
    "ds_read_b64 v[140:141], v19\n"
    "v_mfma_f32_16x16x32_bf16 v[88:91], a[220:223], a[76:79], v[88:91]\n"
    "s_waitcnt lgkmcnt(4)\n"
    "v_mfma_f32_16x16x32_bf16 v[68:71], a[224:227], a[56:59], v[68:71]\n"
    "buffer_load_dwordx4 v0, s[24:27], s3 offen offset:3072 sc1 lds\n"
    "ds_read_b128 a[72:75], v0 offset:22528\n"
    "v_mfma_f32_16x16x32_bf16 v[84:87], a[228:231], a[56:59], v[84:87]\n"
    "v_mfma_f32_16x16x32_bf16 v[88:91], a[232:235], a[56:59], v[88:91]\n"
    "s_waitcnt lgkmcnt(4)\n"
    "v_mfma_f32_16x16x32_bf16 v[68:71], a[236:239], a[60:63], v[68:71]\n"
    "s_add_u32 m0, s19, 0x3000\n"
    "s_nop 0\n"
    "buffer_load_dwordx4 v1, s[24:27], s3 offen sc1 lds\n"
    "ds_read_b128 a[76:79], v0 offset:23552\n"
    "v_mfma_f32_16x16x32_bf16 v[84:87], v[148:151], a[60:63], v[84:87]\n"
    "v_mfma_f32_16x16x32_bf16 v[88:91], v[152:155], a[60:63], v[88:91]\n"
    "s_waitcnt lgkmcnt(2)\n"
    "v_pk_fma_f32 v[128:129], v[32:33], v[140:141], v[44:45] op_sel:[0,1,0] op_sel_hi:[1,1,1]\n"
    "buffer_load_dwordx4 v1, s[24:27], s3 offen offset:1024 sc1 lds\n"
    "v_pk_fma_f32 v[130:131], v[34:35], v[140:141], v[46:47] op_sel:[0,1,0] op_sel_hi:[1,1,1]\n"
    "v_pk_fma_f32 v[132:133], v[36:37], v[140:141], v[48:49] op_sel:[0,1,0] op_sel_hi:[1,1,1]\n"
    "v_pk_fma_f32 v[134:135], v[38:39], v[140:141], v[50:51] op_sel:[0,1,0] op_sel_hi:[1,1,1]\n"
    "v_pk_fma_f32 v[136:137], v[40:41], v[140:141], v[52:53] op_sel:[0,1,0] op_sel_hi:[1,1,1]\n"
    "v_pk_fma_f32 v[138:139], v[42:43], v[140:141], v[54:55] op_sel:[0,1,0] op_sel_hi:[1,1,1]\n"
    "v_pk_fma_f32 v[116:117], v[68:69], v[140:141], v[128:129] op_sel_hi:[1,0,1]\n"
    "v_pk_fma_f32 v[118:119], v[70:71], v[140:141], v[130:131] op_sel_hi:[1,0,1]\n"
    "v_pk_fma_f32 v[120:121], v[84:85], v[140:141], v[132:133] op_sel_hi:[1,0,1]\n"
    "v_pk_fma_f32 v[122:123], v[86:87], v[140:141], v[134:135] op_sel_hi:[1,0,1]\n"
    "v_pk_fma_f32 v[124:125], v[88:89], v[140:141], v[136:137] op_sel_hi:[1,0,1]\n"
    "v_pk_fma_f32 v[126:127], v[90:91], v[140:141], v[138:139] op_sel_hi:[1,0,1]\n"
    "buffer_load_dwordx4 v1, s[24:27], s3 offen offset:2048 sc1 lds\n"
    "v_pk_mul_f32 v[128:129], v[116:117], v[116:117]\n"
    "v_pk_mul_f32 v[130:131], v[118:119], v[118:119]\n"
    "v_pk_mul_f32 v[132:133], v[120:121], v[120:121]\n"
    "v_pk_mul_f32 v[134:135], v[122:123], v[122:123]\n"
    "v_pk_mul_f32 v[136:137], v[124:125], v[124:125]\n"
    "v_pk_mul_f32 v[138:139], v[126:127], v[126:127]\n"
    "v_pk_fma_f32 v[128:129], v[128:129], s[30:31], v[142:143]\n"
    "v_pk_fma_f32 v[130:131], v[130:131], s[30:31], v[142:143]\n"
    "v_pk_fma_f32 v[132:133], v[132:133], s[30:31], v[142:143]\n"
    "v_pk_fma_f32 v[134:135], v[134:135], s[30:31], v[142:143]\n"
    "v_pk_fma_f32 v[136:137], v[136:137], s[30:31], v[142:143]\n"
    "buffer_load_dwordx4 v1, s[24:27], s3 offen offset:3072 sc1 lds\n"
    "v_pk_fma_f32 v[138:139], v[138:139], s[30:31], v[142:143]\n"
    "v_pk_mul_f32 v[128:129], v[128:129], v[116:117]\n"
    "v_pk_mul_f32 v[130:131], v[130:131], v[118:119]\n"
    "v_pk_mul_f32 v[132:133], v[132:133], v[120:121]\n"
    "v_pk_mul_f32 v[134:135], v[134:135], v[122:123]\n"
    "v_pk_mul_f32 v[136:137], v[136:137], v[124:125]\n"
    "v_pk_mul_f32 v[138:139], v[138:139], v[126:127]\n"
    "v_mfma_f32_16x16x32_bf16 v[92:95], a[240:243], a[64:67], 0\n"
    "ds_read_b128 a[56:59], v0 offset:24576\n"
    "v_exp_f32 v128, v128\n"
    "s_add_u32 s3, s61, 0x4000\n"
    "s_add_u32 m0, s19, 0x4000\n"
    "s_nop 0\n"
    "buffer_load_dwordx4 v0, s[24:27], s3 offen sc1 lds\n"
    "v_mfma_f32_16x16x32_bf16 v[96:99], a[244:247], a[64:67], 0\n"
    "v_exp_f32 v129, v129\n"
    "v_mfma_f32_16x16x32_bf16 v[100:103], a[248:251], a[64:67], 0\n"
    "v_exp_f32 v130, v130\n"
    "v_mfma_f32_16x16x32_bf16 v[92:95], a[252:255], a[68:71], v[92:95]\n"
    "ds_read_b128 a[60:63], v0 offset:25600\n"
    "buffer_load_dwordx4 v0, s[24:27], s3 offen offset:1024 sc1 lds\n"
    "v_exp_f32 v131, v131\n"
    "v_mfma_f32_16x16x32_bf16 v[96:99], a[112:115], a[68:71], v[96:99]\n"
    "v_add_f32 v128, 1.0, v128\n"
    "v_mfma_f32_16x16x32_bf16 v[100:103], a[116:119], a[68:71], v[100:103]\n"
    "v_add_f32 v129, 1.0, v129\n"
    "s_waitcnt lgkmcnt(3)\n"
    "v_mfma_f32_16x16x32_bf16 v[92:95], a[120:123], a[72:75], v[92:95]\n"
    "buffer_load_dwordx4 v0, s[24:27], s3 offen offset:2048 sc1 lds\n"
    "ds_read_b128 a[64:67], v0 offset:26624\n"
    "v_add_f32 v130, 1.0, v130\n"
    "v_mfma_f32_16x16x32_bf16 v[96:99], a[124:127], a[72:75], v[96:99]\n"
    "v_add_f32 v131, 1.0, v131\n"
    "v_mfma_f32_16x16x32_bf16 v[100:103], a[128:131], a[72:75], v[100:103]\n"
    "v_rcp_f32 v128, v128\n"
    "s_waitcnt lgkmcnt(3)\n"
    "v_mfma_f32_16x16x32_bf16 v[92:95], a[132:135], a[76:79], v[92:95]\n"
    "buffer_load_dwordx4 v[60:63], v0, s[24:27], s3 offen offset:3072 sc1\n"
    "ds_read_b128 a[68:71], v0 offset:27648\n"
    "v_rcp_f32 v129, v129\n"
    "v_mfma_f32_16x16x32_bf16 v[96:99], a[136:139], a[76:79], v[96:99]\n"
    "v_rcp_f32 v130, v130\n"
    "v_mfma_f32_16x16x32_bf16 v[100:103], a[140:143], a[76:79], v[100:103]\n"
    "v_rcp_f32 v131, v131\n"
    "buffer_load_dwordx4 v[64:67], v1, s[24:27], s3 offen sc1\n"
    "s_waitcnt lgkmcnt(3)\n"
    "v_mfma_f32_16x16x32_bf16 v[92:95], a[144:147], a[56:59], v[92:95]\n"
    "ds_read_b128 a[72:75], v0 offset:28672\n"
    "v_mul_f32 v116, v116, v128\n"
    "v_mfma_f32_16x16x32_bf16 v[96:99], a[148:151], a[56:59], v[96:99]\n"
    "v_mul_f32 v117, v117, v129\n"
    "v_mfma_f32_16x16x32_bf16 v[100:103], a[152:155], a[56:59], v[100:103]\n"
    "v_mul_f32 v118, v118, v130\n"
    "buffer_load_dwordx4 v[156:159], v1, s[24:27], s3 offen offset:1024 sc1\n"
    "s_waitcnt lgkmcnt(3)\n"
    "v_mfma_f32_16x16x32_bf16 v[92:95], a[156:159], a[60:63], v[92:95]\n"
    "ds_read_b128 a[76:79], v0 offset:29696\n"
    "v_mul_f32 v119, v119, v131\n"
    "v_mfma_f32_16x16x32_bf16 v[96:99], a[160:163], a[60:63], v[96:99]\n"
    "v_cvt_pk_bf16_f32 v104, v116, v117\n"
    "v_mfma_f32_16x16x32_bf16 v[100:103], a[164:167], a[60:63], v[100:103]\n"
    "v_cvt_pk_bf16_f32 v105, v118, v119\n"
    "buffer_load_dwordx4 v[160:163], v1, s[24:27], s3 offen offset:2048 sc1\n"
    "s_waitcnt lgkmcnt(3)\n"
    "v_mfma_f32_16x16x32_bf16 v[92:95], a[168:171], a[64:67], v[92:95]\n"
    "ds_read_b128 a[56:59], v0 offset:30720\n"
    "v_exp_f32 v132, v132\n"
    "v_mfma_f32_16x16x32_bf16 v[96:99], a[172:175], a[64:67], v[96:99]\n"
    "v_exp_f32 v133, v133\n"
    "v_mfma_f32_16x16x32_bf16 v[100:103], a[176:179], a[64:67], v[100:103]\n"
    "buffer_load_dwordx4 v[164:167], v1, s[24:27], s3 offen offset:3072 sc1\n"
    "v_exp_f32 v134, v134\n"
    "s_waitcnt lgkmcnt(3)\n"
    "v_mfma_f32_16x16x32_bf16 v[92:95], a[180:183], a[68:71], v[92:95]\n"
    "ds_read_b128 a[60:63], v0 offset:31744\n"
    "v_exp_f32 v135, v135\n"
    "v_mfma_f32_16x16x32_bf16 v[96:99], a[184:187], a[68:71], v[96:99]\n"
    "v_add_f32 v132, 1.0, v132\n"
    "v_mfma_f32_16x16x32_bf16 v[100:103], a[0:3], a[68:71], v[100:103]\n"
    "s_add_u32 s3, s61, 0x6000\n"
    "buffer_load_dwordx4 v[168:171], v0, s[24:27], s3 offen sc1\n"
    "v_add_f32 v133, 1.0, v133\n"
    "s_waitcnt lgkmcnt(3)\n"
    "v_mfma_f32_16x16x32_bf16 v[92:95], a[4:7], a[72:75], v[92:95]\n"
    "ds_read_b128 a[64:67], v0 offset:32768\n"
    "v_add_f32 v134, 1.0, v134\n"
    "v_mfma_f32_16x16x32_bf16 v[96:99], a[8:11], a[72:75], v[96:99]\n"
    "v_add_f32 v135, 1.0, v135\n"
    "v_mfma_f32_16x16x32_bf16 v[100:103], a[12:15], a[72:75], v[100:103]\n"
    "buffer_load_dwordx4 v[172:175], v0, s[24:27], s3 offen offset:1024 sc1\n"
    "v_rcp_f32 v132, v132\n"
    "s_waitcnt lgkmcnt(3)\n"
    "v_mfma_f32_16x16x32_bf16 v[92:95], a[16:19], a[76:79], v[92:95]\n"
    "ds_read_b128 a[68:71], v0 offset:33792\n"
    "v_rcp_f32 v133, v133\n"
    "v_mfma_f32_16x16x32_bf16 v[96:99], a[20:23], a[76:79], v[96:99]\n"
    "v_rcp_f32 v134, v134\n"
    "buffer_load_dwordx4 v[176:179], v0, s[24:27], s3 offen offset:2048 sc1\n"
    "v_mfma_f32_16x16x32_bf16 v[100:103], a[24:27], a[76:79], v[100:103]\n"
    "v_rcp_f32 v135, v135\n"
    "s_waitcnt lgkmcnt(3)\n"
    "v_mfma_f32_16x16x32_bf16 v[92:95], a[28:31], a[56:59], v[92:95]\n"
    "ds_read_b128 a[72:75], v0 offset:34816\n"
    "v_mul_f32 v120, v120, v132\n"
    "v_mfma_f32_16x16x32_bf16 v[96:99], a[32:35], a[56:59], v[96:99]\n"
    "v_mul_f32 v121, v121, v133\n"
    "buffer_load_dwordx4 v[180:183], v0, s[24:27], s3 offen offset:3072 sc1\n"
    "v_mfma_f32_16x16x32_bf16 v[100:103], a[36:39], a[56:59], v[100:103]\n"
    "v_mul_f32 v122, v122, v134\n"
    "s_waitcnt lgkmcnt(3)\n"
    "v_mfma_f32_16x16x32_bf16 v[92:95], a[40:43], a[60:63], v[92:95]\n"
    "ds_read_b128 a[76:79], v0 offset:35840\n"
    "v_mul_f32 v123, v123, v135\n"
    "v_mfma_f32_16x16x32_bf16 v[96:99], a[44:47], a[60:63], v[96:99]\n"
    "v_cvt_pk_bf16_f32 v106, v120, v121\n"
    "buffer_load_dwordx4 v[184:187], v1, s[24:27], s3 offen sc1\n"
    "v_mfma_f32_16x16x32_bf16 v[100:103], a[48:51], a[60:63], v[100:103]\n"
    "v_cvt_pk_bf16_f32 v107, v122, v123\n"
    "s_waitcnt lgkmcnt(3)\n"
    "v_mfma_f32_16x16x32_bf16 v[92:95], a[52:55], a[64:67], v[92:95]\n"
    "ds_read_b128 a[56:59], v0 offset:36864\n"
    "s_add_u32 s29, s63, 0\n"
    "v_mfma_f32_16x16x32_bf16 v[96:99], a[80:83], a[64:67], v[96:99]\n"
    "v_mfma_f32_16x16x32_bf16 v[100:103], a[84:87], a[64:67], v[100:103]\n"
    "buffer_load_dwordx4 v[188:191], v1, s[24:27], s3 offen offset:1024 sc1\n"
    "s_waitcnt lgkmcnt(3)\n"
    "v_mfma_f32_16x16x32_bf16 v[92:95], a[88:91], a[68:71], v[92:95]\n"
    "ds_read_b128 a[60:63], v0 offset:37888\n"
    "v_mfma_f32_16x16x32_bf16 v[96:99], a[92:95], a[68:71], v[96:99]\n"
    "v_mfma_f32_16x16x32_bf16 v[100:103], a[96:99], a[68:71], v[100:103]\n"
    "s_waitcnt lgkmcnt(3)\n"
    "v_mfma_f32_16x16x32_bf16 v[92:95], a[100:103], a[72:75], v[92:95]\n"
    "buffer_load_dwordx4 v[192:195], v1, s[24:27], s3 offen offset:2048 sc1\n"
    "ds_read_b128 a[64:67], v0 offset:38912\n"
    "v_mfma_f32_16x16x32_bf16 v[96:99], a[104:107], a[72:75], v[96:99]\n"
    "v_mfma_f32_16x16x32_bf16 v[100:103], a[108:111], a[72:75], v[100:103]\n"
    "s_waitcnt vmcnt(36)\n"
    "s_barrier\n"
    "buffer_store_dwordx4 v[104:107], v0, s[24:27], s29 offen sc1\n"
    "s_waitcnt lgkmcnt(3)\n"
    "v_mfma_f32_16x16x32_bf16 v[92:95], a[188:191], a[76:79], v[92:95]\n"
    "ds_read_b128 a[68:71], v0 offset:39936\n"
    "v_exp_f32 v136, v136\n"
    "v_mfma_f32_16x16x32_bf16 v[96:99], a[192:195], a[76:79], v[96:99]\n"
    "v_exp_f32 v137, v137\n"
    "v_mfma_f32_16x16x32_bf16 v[100:103], a[196:199], a[76:79], v[100:103]\n"
    "s_add_u32 s3, s61, 0xc000\n"
    "s_add_u32 m0, s33, 0\n"
    "s_nop 0\n"
    "buffer_load_dwordx4 v1, s[24:27], s3 offen sc1 lds\n"
    "v_exp_f32 v138, v138\n"
    "s_waitcnt lgkmcnt(3)\n"
    "v_mfma_f32_16x16x32_bf16 v[92:95], a[200:203], a[56:59], v[92:95]\n"
    "ds_read_b128 a[72:75], v31\n"
    "v_exp_f32 v139, v139\n"
    "v_mfma_f32_16x16x32_bf16 v[96:99], a[204:207], a[56:59], v[96:99]\n"
    "v_add_f32 v136, 1.0, v136\n"
    "v_add_f32 v137, 1.0, v137\n"
    "buffer_load_dwordx4 v1, s[24:27], s3 offen offset:1024 sc1 lds\n"
    "v_mfma_f32_16x16x32_bf16 v[100:103], a[208:211], a[56:59], v[100:103]\n"
    "v_add_f32 v138, 1.0, v138\n"
    "v_add_f32 v139, 1.0, v139\n"
    "s_waitcnt lgkmcnt(3)\n"
    "v_mfma_f32_16x16x32_bf16 v[92:95], a[212:215], a[60:63], v[92:95]\n"
    "ds_read_b128 a[76:79], v31 offset:1024\n"
    "v_rcp_f32 v136, v136\n"
    "v_mfma_f32_16x16x32_bf16 v[96:99], a[216:219], a[60:63], v[96:99]\n"
    "buffer_load_dwordx4 v1, s[24:27], s3 offen offset:2048 sc1 lds\n"
    "ds_read_b64 v[140:141], v19 offset:128\n"
    "v_rcp_f32 v137, v137\n"
    "v_mfma_f32_16x16x32_bf16 v[100:103], a[220:223], a[60:63], v[100:103]\n"
    "v_rcp_f32 v138, v138\n"
    "s_waitcnt lgkmcnt(4)\n"
    "v_mfma_f32_16x16x32_bf16 v[92:95], a[224:227], a[64:67], v[92:95]\n"
    "ds_read_b128 a[56:59], v31 offset:2048\n"
    "v_rcp_f32 v139, v139\n"
    "buffer_load_dwordx4 v1, s[24:27], s3 offen offset:3072 sc1 lds\n"
    "v_mfma_f32_16x16x32_bf16 v[96:99], a[228:231], a[64:67], v[96:99]\n"
    "v_mul_f32 v124, v124, v136\n"
    "v_mfma_f32_16x16x32_bf16 v[100:103], a[232:235], a[64:67], v[100:103]\n"
    "v_mul_f32 v125, v125, v137\n"
    "s_waitcnt lgkmcnt(4)\n"
    "v_mfma_f32_16x16x32_bf16 v[92:95], a[236:239], a[68:71], v[92:95]\n"
    "ds_read_b128 a[60:63], v31 offset:3072\n"
    "s_add_u32 s3, s61, 0x6000\n"
    "buffer_load_dwordx4 v[196:199], v1, s[24:27], s3 offen offset:3072 sc1\n"
    "v_mul_f32 v126, v126, v138\n"
    "v_mfma_f32_16x16x32_bf16 v[96:99], v[148:151], a[68:71], v[96:99]\n"
    "v_mul_f32 v127, v127, v139\n"
    "v_cvt_pk_bf16_f32 v112, v124, v125\n"
    "v_mfma_f32_16x16x32_bf16 v[100:103], v[152:155], a[68:71], v[100:103]\n"
    "v_cvt_pk_bf16_f32 v113, v126, v127\n"
    "s_waitcnt lgkmcnt(2)\n"
    "v_pk_fma_f32 v[128:129], v[32:33], v[140:141], v[44:45] op_sel:[0,1,0] op_sel_hi:[1,1,1]\n"
    "s_add_u32 s3, s61, 0x8000\n"
    "buffer_load_dwordx4 v[200:203], v0, s[24:27], s3 offen sc1\n"
    "v_pk_fma_f32 v[130:131], v[34:35], v[140:141], v[46:47] op_sel:[0,1,0] op_sel_hi:[1,1,1]\n"
    "v_pk_fma_f32 v[132:133], v[36:37], v[140:141], v[48:49] op_sel:[0,1,0] op_sel_hi:[1,1,1]\n"
    "v_pk_fma_f32 v[134:135], v[38:39], v[140:141], v[50:51] op_sel:[0,1,0] op_sel_hi:[1,1,1]\n"
    "v_pk_fma_f32 v[136:137], v[40:41], v[140:141], v[52:53] op_sel:[0,1,0] op_sel_hi:[1,1,1]\n"
    "v_pk_fma_f32 v[138:139], v[42:43], v[140:141], v[54:55] op_sel:[0,1,0] op_sel_hi:[1,1,1]\n"
    "v_pk_fma_f32 v[116:117], v[92:93], v[140:141], v[128:129] op_sel_hi:[1,0,1]\n"
    "v_pk_fma_f32 v[118:119], v[94:95], v[140:141], v[130:131] op_sel_hi:[1,0,1]\n"
    "v_pk_fma_f32 v[120:121], v[96:97], v[140:141], v[132:133] op_sel_hi:[1,0,1]\n"
    "v_pk_fma_f32 v[122:123], v[98:99], v[140:141], v[134:135] op_sel_hi:[1,0,1]\n"
    "v_pk_fma_f32 v[124:125], v[100:101], v[140:141], v[136:137] op_sel_hi:[1,0,1]\n"
    "v_pk_fma_f32 v[126:127], v[102:103], v[140:141], v[138:139] op_sel_hi:[1,0,1]\n"
    "buffer_load_dwordx4 v[204:207], v0, s[24:27], s3 offen offset:1024 sc1\n"
    "v_pk_mul_f32 v[128:129], v[116:117], v[116:117]\n"
    "v_pk_mul_f32 v[130:131], v[118:119], v[118:119]\n"
    "v_pk_mul_f32 v[132:133], v[120:121], v[120:121]\n"
    "v_pk_mul_f32 v[134:135], v[122:123], v[122:123]\n"
    "v_pk_mul_f32 v[136:137], v[124:125], v[124:125]\n"
    "v_pk_mul_f32 v[138:139], v[126:127], v[126:127]\n"
    "v_pk_fma_f32 v[128:129], v[128:129], s[30:31], v[142:143]\n"
    "v_pk_fma_f32 v[130:131], v[130:131], s[30:31], v[142:143]\n"
    "v_pk_fma_f32 v[132:133], v[132:133], s[30:31], v[142:143]\n"
    "v_pk_fma_f32 v[134:135], v[134:135], s[30:31], v[142:143]\n"
    "v_pk_fma_f32 v[136:137], v[136:137], s[30:31], v[142:143]\n"
    "buffer_load_dwordx4 v[208:211], v0, s[24:27], s3 offen offset:2048 sc1\n"
    "v_pk_fma_f32 v[138:139], v[138:139], s[30:31], v[142:143]\n"
    "v_pk_mul_f32 v[128:129], v[128:129], v[116:117]\n"
    "v_pk_mul_f32 v[130:131], v[130:131], v[118:119]\n"
    "v_pk_mul_f32 v[132:133], v[132:133], v[120:121]\n"
    "v_pk_mul_f32 v[134:135], v[134:135], v[122:123]\n"
    "v_pk_mul_f32 v[136:137], v[136:137], v[124:125]\n"
    "v_pk_mul_f32 v[138:139], v[138:139], v[126:127]\n"
    "v_mfma_f32_16x16x32_bf16 v[68:71], a[240:243], a[72:75], 0\n"
    "ds_read_b128 a[64:67], v31 offset:4096\n"
    "v_exp_f32 v128, v128\n"
    "buffer_load_dwordx4 v[212:215], v0, s[24:27], s3 offen offset:3072 sc1\n"
    "v_mfma_f32_16x16x32_bf16 v[84:87], a[244:247], a[72:75], 0\n"
    "v_exp_f32 v129, v129\n"
    "v_mfma_f32_16x16x32_bf16 v[88:91], a[248:251], a[72:75], 0\n"
    "v_exp_f32 v130, v130\n"
    "v_mfma_f32_16x16x32_bf16 v[68:71], a[252:255], a[76:79], v[68:71]\n"
    "ds_read_b128 a[68:71], v31 offset:5120\n"
    "buffer_load_dwordx4 v[216:219], v1, s[24:27], s3 offen sc1\n"
    "v_exp_f32 v131, v131\n"
    "v_mfma_f32_16x16x32_bf16 v[84:87], a[112:115], a[76:79], v[84:87]\n"
    "v_add_f32 v128, 1.0, v128\n"
    "v_add_f32 v129, 1.0, v129\n"
    "v_mfma_f32_16x16x32_bf16 v[88:91], a[116:119], a[76:79], v[88:91]\n"
    "v_add_f32 v130, 1.0, v130\n"
    "v_add_f32 v131, 1.0, v131\n"
    "s_waitcnt lgkmcnt(3)\n"
    "v_mfma_f32_16x16x32_bf16 v[68:71], a[120:123], a[56:59], v[68:71]\n"
    "buffer_load_dwordx4 v[220:223], v1, s[24:27], s3 offen offset:1024 sc1\n"
    "ds_read_b128 a[72:75], v31 offset:6144\n"
    "v_rcp_f32 v128, v128\n"
    "v_mfma_f32_16x16x32_bf16 v[84:87], a[124:127], a[56:59], v[84:87]\n"
    "v_rcp_f32 v129, v129\n"
    "v_mfma_f32_16x16x32_bf16 v[88:91], a[128:131], a[56:59], v[88:91]\n"
    "v_rcp_f32 v130, v130\n"
    "buffer_load_dwordx4 v[224:227], v1, s[24:27], s3 offen offset:2048 sc1\n"
    "s_waitcnt lgkmcnt(3)\n"
    "v_mfma_f32_16x16x32_bf16 v[68:71], a[132:135], a[60:63], v[68:71]\n"
    "ds_read_b128 a[76:79], v31 offset:7168\n"
    "v_rcp_f32 v131, v131\n"
    "v_mfma_f32_16x16x32_bf16 v[84:87], a[136:139], a[60:63], v[84:87]\n"
    "v_mul_f32 v116, v116, v128\n"
    "v_mul_f32 v117, v117, v129\n"
    "v_mfma_f32_16x16x32_bf16 v[88:91], a[140:143], a[60:63], v[88:91]\n"
    "v_mul_f32 v118, v118, v130\n"
    "v_mul_f32 v119, v119, v131\n"
    "s_waitcnt lgkmcnt(3)\n"
    "v_mfma_f32_16x16x32_bf16 v[68:71], a[144:147], a[64:67], v[68:71]\n"
    "ds_read_b128 a[56:59], v31 offset:8192\n"
    "v_cvt_pk_bf16_f32 v108, v116, v117\n"
    "v_cvt_pk_bf16_f32 v109, v118, v119\n"
    "v_mfma_f32_16x16x32_bf16 v[84:87], a[148:151], a[64:67], v[84:87]\n"
    "v_exp_f32 v132, v132\n"
    "v_mfma_f32_16x16x32_bf16 v[88:91], a[152:155], a[64:67], v[88:91]\n"
    "v_exp_f32 v133, v133\n"
    "s_waitcnt lgkmcnt(3)\n"
    "v_mfma_f32_16x16x32_bf16 v[68:71], a[156:159], a[68:71], v[68:71]\n"
    "ds_read_b128 a[60:63], v31 offset:9216\n"
    "v_exp_f32 v134, v134\n"
    "v_mfma_f32_16x16x32_bf16 v[84:87], a[160:163], a[68:71], v[84:87]\n"
    "v_exp_f32 v135, v135\n"
    "v_mfma_f32_16x16x32_bf16 v[88:91], a[164:167], a[68:71], v[88:91]\n"
    "v_add_f32 v132, 1.0, v132\n"
    "v_add_f32 v133, 1.0, v133\n"
    "s_waitcnt lgkmcnt(3)\n"
    "v_mfma_f32_16x16x32_bf16 v[68:71], a[168:171], a[72:75], v[68:71]\n"
    "ds_read_b128 a[64:67], v31 offset:10240\n"
    "v_add_f32 v134, 1.0, v134\n"
    "v_add_f32 v135, 1.0, v135\n"
    "v_mfma_f32_16x16x32_bf16 v[84:87], a[172:175], a[72:75], v[84:87]\n"
    "v_rcp_f32 v132, v132\n"
    "v_mfma_f32_16x16x32_bf16 v[88:91], a[176:179], a[72:75], v[88:91]\n"
    "v_rcp_f32 v133, v133\n"
    "s_waitcnt lgkmcnt(3)\n"
    "v_mfma_f32_16x16x32_bf16 v[68:71], a[180:183], a[76:79], v[68:71]\n"
    "ds_read_b128 a[68:71], v31 offset:11264\n"
    "v_rcp_f32 v134, v134\n"
    "v_mfma_f32_16x16x32_bf16 v[84:87], a[184:187], a[76:79], v[84:87]\n"
    "v_rcp_f32 v135, v135\n"
    "v_mfma_f32_16x16x32_bf16 v[88:91], a[0:3], a[76:79], v[88:91]\n"
    "v_mul_f32 v120, v120, v132\n"
    "v_mul_f32 v121, v121, v133\n"
    "s_waitcnt lgkmcnt(3)\n"
    "v_mfma_f32_16x16x32_bf16 v[68:71], a[4:7], a[56:59], v[68:71]\n"
    "ds_read_b128 a[72:75], v31 offset:12288\n"
    "v_mul_f32 v122, v122, v134\n"
    "v_mul_f32 v123, v123, v135\n"
    "v_cvt_pk_bf16_f32 v110, v120, v121\n"
    "v_mfma_f32_16x16x32_bf16 v[84:87], a[8:11], a[56:59], v[84:87]\n"
    "v_cvt_pk_bf16_f32 v111, v122, v123\n"
    "s_add_u32 s29, s63, 0xc000\n"
    "v_mfma_f32_16x16x32_bf16 v[88:91], a[12:15], a[56:59], v[88:91]\n"
    "buffer_store_dwordx4 v[108:111], v0, s[24:27], s29 offen sc1\n"
    "s_waitcnt lgkmcnt(3)\n"
    "v_mfma_f32_16x16x32_bf16 v[68:71], a[16:19], a[60:63], v[68:71]\n"
    "ds_read_b128 a[76:79], v31 offset:13312\n"
    "v_exp_f32 v136, v136\n"
    "v_mfma_f32_16x16x32_bf16 v[84:87], a[20:23], a[60:63], v[84:87]\n"
    "v_exp_f32 v137, v137\n"
    "v_mfma_f32_16x16x32_bf16 v[88:91], a[24:27], a[60:63], v[88:91]\n"
    "v_exp_f32 v138, v138\n"
    "s_waitcnt lgkmcnt(3)\n"
    "v_mfma_f32_16x16x32_bf16 v[68:71], a[28:31], a[64:67], v[68:71]\n"
    "ds_read_b128 a[56:59], v31 offset:14336\n"
    "v_exp_f32 v139, v139\n"
    "v_add_f32 v136, 1.0, v136\n"
    "v_mfma_f32_16x16x32_bf16 v[84:87], a[32:35], a[64:67], v[84:87]\n"
    "v_add_f32 v137, 1.0, v137\n"
    "v_add_f32 v138, 1.0, v138\n"
    "v_add_f32 v139, 1.0, v139\n"
    "v_mfma_f32_16x16x32_bf16 v[88:91], a[36:39], a[64:67], v[88:91]\n"
    "v_rcp_f32 v136, v136\n"
    "s_waitcnt lgkmcnt(3)\n"
    "v_mfma_f32_16x16x32_bf16 v[68:71], a[40:43], a[68:71], v[68:71]\n"
    "ds_read_b128 a[60:63], v31 offset:15360\n"
    "v_rcp_f32 v137, v137\n"
    "v_mfma_f32_16x16x32_bf16 v[84:87], a[44:47], a[68:71], v[84:87]\n"
    "v_rcp_f32 v138, v138\n"
    "v_mfma_f32_16x16x32_bf16 v[88:91], a[48:51], a[68:71], v[88:91]\n"
    "v_rcp_f32 v139, v139\n"
    "v_mul_f32 v124, v124, v136\n"
    "s_waitcnt lgkmcnt(3)\n"
    "v_mfma_f32_16x16x32_bf16 v[68:71], a[52:55], a[72:75], v[68:71]\n"
    "ds_read_b128 a[64:67], v31 offset:16384\n"
    "v_mul_f32 v125, v125, v137\n"
    "v_mul_f32 v126, v126, v138\n"
    "v_mul_f32 v127, v127, v139\n"
    "v_mfma_f32_16x16x32_bf16 v[84:87], a[80:83], a[72:75], v[84:87]\n"
    "v_cvt_pk_bf16_f32 v114, v124, v125\n"
    "v_cvt_pk_bf16_f32 v115, v126, v127\n"
    "s_nop 0\n"
    "v_permlane16_swap_b32 v112, v114\n"
    "v_mfma_f32_16x16x32_bf16 v[88:91], a[84:87], a[72:75], v[88:91]\n"
    "v_permlane16_swap_b32 v113, v115\n"
    "s_add_u32 s29, s63, 0\n"
    "s_waitcnt lgkmcnt(3)\n"
    "v_mfma_f32_16x16x32_bf16 v[68:71], a[88:91], a[76:79], v[68:71]\n"
    "ds_read_b128 a[68:71], v31 offset:17408\n"
    "buffer_store_dwordx4 v[112:115], v13, s[24:27], s29 offen sc1\n"
    "v_mfma_f32_16x16x32_bf16 v[84:87], a[92:95], a[76:79], v[84:87]\n"
    "v_mfma_f32_16x16x32_bf16 v[88:91], a[96:99], a[76:79], v[88:91]\n"
    "s_waitcnt lgkmcnt(3)\n"
    "v_mfma_f32_16x16x32_bf16 v[68:71], a[100:103], a[56:59], v[68:71]\n"
    "ds_read_b128 a[72:75], v31 offset:18432\n"
    "v_mfma_f32_16x16x32_bf16 v[84:87], a[104:107], a[56:59], v[84:87]\n"
    "v_mfma_f32_16x16x32_bf16 v[88:91], a[108:111], a[56:59], v[88:91]\n"
    "s_waitcnt vmcnt(46)\n"
    "s_barrier\n"
    "s_waitcnt lgkmcnt(3)\n"
    "v_mfma_f32_16x16x32_bf16 v[68:71], a[188:191], a[60:63], v[68:71]\n"
    "s_add_u32 s3, s61, 0xe000\n"
    "s_add_u32 m0, s33, 0x5000\n"
    "s_nop 0\n"
    "buffer_load_dwordx4 v0, s[24:27], s3 offen sc1 lds\n"
    "ds_read_b128 a[76:79], v31 offset:19456\n"
    "v_mfma_f32_16x16x32_bf16 v[84:87], a[192:195], a[60:63], v[84:87]\n"
    "v_mfma_f32_16x16x32_bf16 v[88:91], a[196:199], a[60:63], v[88:91]\n"
    "s_waitcnt lgkmcnt(3)\n"
    "v_mfma_f32_16x16x32_bf16 v[68:71], a[200:203], a[64:67], v[68:71]\n"
    "buffer_load_dwordx4 v0, s[24:27], s3 offen offset:1024 sc1 lds\n"
    "ds_read_b128 a[56:59], v31 offset:20480\n"
    "v_mfma_f32_16x16x32_bf16 v[84:87], a[204:207], a[64:67], v[84:87]\n"
    "v_mfma_f32_16x16x32_bf16 v[88:91], a[208:211], a[64:67], v[88:91]\n"
    "s_waitcnt lgkmcnt(3)\n"
    "v_mfma_f32_16x16x32_bf16 v[68:71], a[212:215], a[68:71], v[68:71]\n"
    "buffer_load_dwordx4 v0, s[24:27], s3 offen offset:2048 sc1 lds\n"
    "ds_read_b128 a[60:63], v31 offset:21504\n"
    "v_mfma_f32_16x16x32_bf16 v[84:87], a[216:219], a[68:71], v[84:87]\n"
    "ds_read_b64 v[140:141], v19 offset:256\n"
    "v_mfma_f32_16x16x32_bf16 v[88:91], a[220:223], a[68:71], v[88:91]\n"
    "s_waitcnt lgkmcnt(4)\n"
    "v_mfma_f32_16x16x32_bf16 v[68:71], a[224:227], a[72:75], v[68:71]\n"
    "buffer_load_dwordx4 v0, s[24:27], s3 offen offset:3072 sc1 lds\n"
    "ds_read_b128 a[64:67], v31 offset:22528\n"
    "v_mfma_f32_16x16x32_bf16 v[84:87], a[228:231], a[72:75], v[84:87]\n"
    "v_mfma_f32_16x16x32_bf16 v[88:91], a[232:235], a[72:75], v[88:91]\n"
    "s_waitcnt lgkmcnt(4)\n"
    "v_mfma_f32_16x16x32_bf16 v[68:71], a[236:239], a[76:79], v[68:71]\n"
    "ds_read_b128 a[68:71], v31 offset:23552\n"
    "v_mfma_f32_16x16x32_bf16 v[84:87], v[148:151], a[76:79], v[84:87]\n"
    "v_mfma_f32_16x16x32_bf16 v[88:91], v[152:155], a[76:79], v[88:91]\n"
    "s_waitcnt lgkmcnt(2)\n"
    "v_pk_fma_f32 v[128:129], v[32:33], v[140:141], v[44:45] op_sel:[0,1,0] op_sel_hi:[1,1,1]\n"
    "v_pk_fma_f32 v[130:131], v[34:35], v[140:141], v[46:47] op_sel:[0,1,0] op_sel_hi:[1,1,1]\n"
    "v_pk_fma_f32 v[132:133], v[36:37], v[140:141], v[48:49] op_sel:[0,1,0] op_sel_hi:[1,1,1]\n"
    "v_pk_fma_f32 v[134:135], v[38:39], v[140:141], v[50:51] op_sel:[0,1,0] op_sel_hi:[1,1,1]\n"
    "v_pk_fma_f32 v[136:137], v[40:41], v[140:141], v[52:53] op_sel:[0,1,0] op_sel_hi:[1,1,1]\n"
    "v_pk_fma_f32 v[138:139], v[42:43], v[140:141], v[54:55] op_sel:[0,1,0] op_sel_hi:[1,1,1]\n"
    "v_pk_fma_f32 v[116:117], v[68:69], v[140:141], v[128:129] op_sel_hi:[1,0,1]\n"
    "v_pk_fma_f32 v[118:119], v[70:71], v[140:141], v[130:131] op_sel_hi:[1,0,1]\n"
    "v_pk_fma_f32 v[120:121], v[84:85], v[140:141], v[132:133] op_sel_hi:[1,0,1]\n"
    "v_pk_fma_f32 v[122:123], v[86:87], v[140:141], v[134:135] op_sel_hi:[1,0,1]\n"
    "v_pk_fma_f32 v[124:125], v[88:89], v[140:141], v[136:137] op_sel_hi:[1,0,1]\n"
    "v_pk_fma_f32 v[126:127], v[90:91], v[140:141], v[138:139] op_sel_hi:[1,0,1]\n"
    "v_pk_mul_f32 v[128:129], v[116:117], v[116:117]\n"
    "v_pk_mul_f32 v[130:131], v[118:119], v[118:119]\n"
    "v_pk_mul_f32 v[132:133], v[120:121], v[120:121]\n"
    "v_pk_mul_f32 v[134:135], v[122:123], v[122:123]\n"
    "v_pk_mul_f32 v[136:137], v[124:125], v[124:125]\n"
    "v_pk_mul_f32 v[138:139], v[126:127], v[126:127]\n"
    "v_pk_fma_f32 v[128:129], v[128:129], s[30:31], v[142:143]\n"
    "v_pk_fma_f32 v[130:131], v[130:131], s[30:31], v[142:143]\n"
    "v_pk_fma_f32 v[132:133], v[132:133], s[30:31], v[142:143]\n"
    "v_pk_fma_f32 v[134:135], v[134:135], s[30:31], v[142:143]\n"
    "v_pk_fma_f32 v[136:137], v[136:137], s[30:31], v[142:143]\n"
    "v_pk_fma_f32 v[138:139], v[138:139], s[30:31], v[142:143]\n"
    "v_pk_mul_f32 v[128:129], v[128:129], v[116:117]\n"
    "v_pk_mul_f32 v[130:131], v[130:131], v[118:119]\n"
    "v_pk_mul_f32 v[132:133], v[132:133], v[120:121]\n"
    "v_pk_mul_f32 v[134:135], v[134:135], v[122:123]\n"
    "v_pk_mul_f32 v[136:137], v[136:137], v[124:125]\n"
    "v_pk_mul_f32 v[138:139], v[138:139], v[126:127]\n"
    "v_mfma_f32_16x16x32_bf16 v[92:95], a[240:243], a[56:59], 0\n"
    "ds_read_b128 a[72:75], v31 offset:24576\n"
    "s_add_u32 s3, s61, 0x8000\n"
    "buffer_load_dwordx4 a[240:243], v1, s[24:27], s3 offen offset:3072 sc1\n"
    "v_exp_f32 v128, v128\n"
    "v_mfma_f32_16x16x32_bf16 v[96:99], a[244:247], a[56:59], 0\n"
    "v_exp_f32 v129, v129\n"
    "v_mfma_f32_16x16x32_bf16 v[100:103], a[248:251], a[56:59], 0\n"
    "s_waitcnt vmcnt(5)\n"
    "s_barrier\n"
    "s_cmp_lg_u32 s16, 0\n"
    "s_cbranch_scc1 .Lfb_fs_40\n"
    "s_mov_b64 exec, 1\n"
    "v_mov_b32 v229, s96\n"
    "s_add_u32 s97, s12, 0x2040\n"
    "buffer_store_dword v229, v25, s[24:27], s97 offen sc1\n"
    "s_mov_b64 exec, -1\n"
    ".Lfb_fs_40:\n"
    "v_exp_f32 v130, v130\n"
    "v_mfma_f32_16x16x32_bf16 v[92:95], a[252:255], a[60:63], v[92:95]\n"
    "ds_read_b128 a[76:79], v31 offset:25600\n"
    "v_exp_f32 v131, v131\n"
    "v_mfma_f32_16x16x32_bf16 v[96:99], a[112:115], a[60:63], v[96:99]\n"
    "v_add_f32 v128, 1.0, v128\n"
    "v_mfma_f32_16x16x32_bf16 v[100:103], a[116:119], a[60:63], v[100:103]\n"
    "s_add_u32 s3, s61, 0xa000\n"
    "buffer_load_dwordx4 a[112:115], v0, s[24:27], s3 offen sc1\n"
    "v_add_f32 v129, 1.0, v129\n"
    "s_waitcnt lgkmcnt(3)\n"
    "v_mfma_f32_16x16x32_bf16 v[92:95], a[120:123], a[64:67], v[92:95]\n"
    "ds_read_b128 a[56:59], v31 offset:26624\n"
    "v_add_f32 v130, 1.0, v130\n"
    "v_mfma_f32_16x16x32_bf16 v[96:99], a[124:127], a[64:67], v[96:99]\n"
    "v_add_f32 v131, 1.0, v131\n"
    "v_mfma_f32_16x16x32_bf16 v[100:103], a[128:131], a[64:67], v[100:103]\n"
    "buffer_load_dwordx4 a[116:119], v0, s[24:27], s3 offen offset:1024 sc1\n"
    "v_rcp_f32 v128, v128\n"
    "s_waitcnt lgkmcnt(3)\n"
    "v_mfma_f32_16x16x32_bf16 v[92:95], a[132:135], a[68:71], v[92:95]\n"
    "ds_read_b128 a[60:63], v31 offset:27648\n"
    "v_rcp_f32 v129, v129\n"
    "v_mfma_f32_16x16x32_bf16 v[96:99], a[136:139], a[68:71], v[96:99]\n"
    "v_rcp_f32 v130, v130\n"
    "buffer_load_dwordx4 a[120:123], v0, s[24:27], s3 offen offset:2048 sc1\n"
    "v_mfma_f32_16x16x32_bf16 v[100:103], a[140:143], a[68:71], v[100:103]\n"
    "v_rcp_f32 v131, v131\n"
    "s_waitcnt lgkmcnt(3)\n"
    "v_mfma_f32_16x16x32_bf16 v[92:95], a[144:147], a[72:75], v[92:95]\n"
    "ds_read_b128 a[64:67], v31 offset:28672\n"
    "v_mul_f32 v116, v116, v128\n"
    "v_mfma_f32_16x16x32_bf16 v[96:99], a[148:151], a[72:75], v[96:99]\n"
    "v_mul_f32 v117, v117, v129\n"
    "buffer_load_dwordx4 a[124:127], v0, s[24:27], s3 offen offset:3072 sc1\n"
    "v_mfma_f32_16x16x32_bf16 v[100:103], a[152:155], a[72:75], v[100:103]\n"
    "ds_read_b128 a[128:131], v30\n"
    "v_mul_f32 v118, v118, v130\n"
    "s_waitcnt lgkmcnt(4)\n"
    "v_mfma_f32_16x16x32_bf16 v[92:95], a[156:159], a[76:79], v[92:95]\n"
    "ds_read_b128 a[68:71], v31 offset:29696\n"
    "v_mul_f32 v119, v119, v131\n"
    "v_mfma_f32_16x16x32_bf16 v[96:99], a[160:163], a[76:79], v[96:99]\n"
    "v_cvt_pk_bf16_f32 v104, v116, v117\n"
    "buffer_load_dwordx4 a[132:135], v1, s[24:27], s3 offen sc1\n"
    "v_mfma_f32_16x16x32_bf16 v[100:103], a[164:167], a[76:79], v[100:103]\n"
    "ds_read_b128 a[136:139], v30 offset:1024\n"
    "v_cvt_pk_bf16_f32 v105, v118, v119\n"
    "s_waitcnt lgkmcnt(5)\n"
    "v_mfma_f32_16x16x32_bf16 v[92:95], a[168:171], a[56:59], v[92:95]\n"
    "ds_read_b128 a[72:75], v31 offset:30720\n"
    "v_exp_f32 v132, v132\n"
    "v_mfma_f32_16x16x32_bf16 v[96:99], a[172:175], a[56:59], v[96:99]\n"
    "buffer_load_dwordx4 a[140:143], v1, s[24:27], s3 offen offset:1024 sc1\n"
    "v_exp_f32 v133, v133\n"
    "v_mfma_f32_16x16x32_bf16 v[100:103], a[176:179], a[56:59], v[100:103]\n"
    "ds_read_b128 a[144:147], v30 offset:2048\n"
    "v_exp_f32 v134, v134\n"
    "s_waitcnt lgkmcnt(6)\n"
    "v_mfma_f32_16x16x32_bf16 v[92:95], a[180:183], a[60:63], v[92:95]\n"
    "ds_read_b128 a[76:79], v31 offset:31744\n"
    "v_exp_f32 v135, v135\n"
    "buffer_load_dwordx4 a[148:151], v1, s[24:27], s3 offen offset:2048 sc1\n"
    "v_mfma_f32_16x16x32_bf16 v[96:99], a[184:187], a[60:63], v[96:99]\n"
    "v_add_f32 v132, 1.0, v132\n"
    "v_mfma_f32_16x16x32_bf16 v[100:103], a[0:3], a[60:63], v[100:103]\n"
    "ds_read_b128 a[0:3], v30 offset:3072\n"
    "v_add_f32 v133, 1.0, v133\n"
    "s_waitcnt lgkmcnt(7)\n"
    "v_mfma_f32_16x16x32_bf16 v[92:95], a[4:7], a[64:67], v[92:95]\n"
    "ds_read_b128 a[56:59], v31 offset:32768\n"
    "buffer_load_dwordx4 a[4:7], v1, s[24:27], s3 offen offset:3072 sc1\n"
    "v_add_f32 v134, 1.0, v134\n"
    "v_mfma_f32_16x16x32_bf16 v[96:99], a[8:11], a[64:67], v[96:99]\n"
    "v_add_f32 v135, 1.0, v135\n"
    "v_mfma_f32_16x16x32_bf16 v[100:103], a[12:15], a[64:67], v[100:103]\n"
    "ds_read_b128 a[8:11], v30 offset:4096\n"
    "v_rcp_f32 v132, v132\n"
    "s_waitcnt lgkmcnt(7)\n"
    "v_mfma_f32_16x16x32_bf16 v[92:95], a[16:19], a[68:71], v[92:95]\n"
    "s_add_u32 s3, s61, 0xc000\n"
    "buffer_load_dwordx4 a[12:15], v0, s[24:27], s3 offen sc1\n"
    "ds_read_b128 a[60:63], v31 offset:33792\n"
    "v_rcp_f32 v133, v133\n"
    "v_mfma_f32_16x16x32_bf16 v[96:99], a[20:23], a[68:71], v[96:99]\n"
    "v_rcp_f32 v134, v134\n"
    "v_mfma_f32_16x16x32_bf16 v[100:103], a[24:27], a[68:71], v[100:103]\n"
    "ds_read_b128 a[16:19], v30 offset:5120\n"
    "v_rcp_f32 v135, v135\n"
    "buffer_load_dwordx4 a[20:23], v0, s[24:27], s3 offen offset:1024 sc1\n"
    "s_waitcnt lgkmcnt(7)\n"
    "v_mfma_f32_16x16x32_bf16 v[92:95], a[28:31], a[72:75], v[92:95]\n"
    "ds_read_b128 a[64:67], v31 offset:34816\n"
    "v_mul_f32 v120, v120, v132\n"
    "v_mfma_f32_16x16x32_bf16 v[96:99], a[32:35], a[72:75], v[96:99]\n"
    "v_mul_f32 v121, v121, v133\n"
    "v_mfma_f32_16x16x32_bf16 v[100:103], a[36:39], a[72:75], v[100:103]\n"
    "ds_read_b128 a[24:27], v30 offset:6144\n"
    "buffer_load_dwordx4 a[28:31], v0, s[24:27], s3 offen offset:2048 sc1\n"
    "v_mul_f32 v122, v122, v134\n"
    "s_waitcnt lgkmcnt(7)\n"
    "v_mfma_f32_16x16x32_bf16 v[92:95], a[40:43], a[76:79], v[92:95]\n"
    "ds_read_b128 a[68:71], v31 offset:35840\n"
    "v_mul_f32 v123, v123, v135\n"
    "v_mfma_f32_16x16x32_bf16 v[96:99], a[44:47], a[76:79], v[96:99]\n"
    "v_cvt_pk_bf16_f32 v106, v120, v121\n"
    "v_mfma_f32_16x16x32_bf16 v[100:103], a[48:51], a[76:79], v[100:103]\n"
    "buffer_load_dwordx4 a[32:35], v0, s[24:27], s3 offen offset:3072 sc1\n"
    "ds_read_b128 a[36:39], v30 offset:7168\n"
    "v_cvt_pk_bf16_f32 v107, v122, v123\n"
    "s_waitcnt lgkmcnt(7)\n"
    "v_mfma_f32_16x16x32_bf16 v[92:95], a[52:55], a[56:59], v[92:95]\n"
    "ds_read_b128 a[72:75], v31 offset:36864\n"
    "s_add_u32 s29, s63, 0x18000\n"
    "v_mfma_f32_16x16x32_bf16 v[96:99], a[80:83], a[56:59], v[96:99]\n"
    "buffer_load_dword v59, v58, s[24:27], 0 offen sc1\n"
    "v_mfma_f32_16x16x32_bf16 v[100:103], a[84:87], a[56:59], v[100:103]\n"
    "ds_read_b128 a[40:43], v30 offset:8192\n"
    "s_waitcnt lgkmcnt(7)\n"
    "v_mfma_f32_16x16x32_bf16 v[92:95], a[88:91], a[60:63], v[92:95]\n"
    "ds_read_b128 a[76:79], v31 offset:37888\n"
    "v_mfma_f32_16x16x32_bf16 v[96:99], a[92:95], a[60:63], v[96:99]\n"
    "v_mfma_f32_16x16x32_bf16 v[100:103], a[96:99], a[60:63], v[100:103]\n"
    "ds_read_b128 a[44:47], v30 offset:9216\n"
    "s_waitcnt lgkmcnt(7)\n"
    "v_mfma_f32_16x16x32_bf16 v[92:95], a[100:103], a[64:67], v[92:95]\n"
    "ds_read_b128 a[56:59], v31 offset:38912\n"
    "v_mfma_f32_16x16x32_bf16 v[96:99], a[104:107], a[64:67], v[96:99]\n"
    "v_mfma_f32_16x16x32_bf16 v[100:103], a[108:111], a[64:67], v[100:103]\n"
    "ds_read_b128 a[48:51], v30 offset:10240\n"
    "buffer_store_dwordx4 v[104:107], v0, s[24:27], s29 offen sc1\n"
    "s_waitcnt lgkmcnt(7)\n"
    "v_mfma_f32_16x16x32_bf16 v[92:95], a[188:191], a[68:71], v[92:95]\n"
    "ds_read_b128 a[60:63], v31 offset:39936\n"
    "v_exp_f32 v136, v136\n"
    "v_mfma_f32_16x16x32_bf16 v[96:99], a[192:195], a[68:71], v[96:99]\n"
    "v_exp_f32 v137, v137\n"
    "v_mfma_f32_16x16x32_bf16 v[100:103], a[196:199], a[68:71], v[100:103]\n"
    "ds_read_b128 a[52:55], v30 offset:11264\n"
    "v_exp_f32 v138, v138\n"
    "s_waitcnt lgkmcnt(7)\n"
    "v_mfma_f32_16x16x32_bf16 v[92:95], a[200:203], a[72:75], v[92:95]\n"
    "v_exp_f32 v139, v139\n"
    "v_mfma_f32_16x16x32_bf16 v[96:99], a[204:207], a[72:75], v[96:99]\n"
    "v_add_f32 v136, 1.0, v136\n"
    "v_add_f32 v137, 1.0, v137\n"
    "v_mfma_f32_16x16x32_bf16 v[100:103], a[208:211], a[72:75], v[100:103]\n"
    "ds_read_b128 a[80:83], v30 offset:12288\n"
    "v_add_f32 v138, 1.0, v138\n"
    "v_add_f32 v139, 1.0, v139\n"
    "s_waitcnt lgkmcnt(6)\n"
    "v_mfma_f32_16x16x32_bf16 v[92:95], a[212:215], a[76:79], v[92:95]\n"
    "v_rcp_f32 v136, v136\n"
    "v_mfma_f32_16x16x32_bf16 v[96:99], a[216:219], a[76:79], v[96:99]\n"
    "ds_read_b64 v[140:141], v19 offset:384\n"
    "v_rcp_f32 v137, v137\n"
    "v_mfma_f32_16x16x32_bf16 v[100:103], a[220:223], a[76:79], v[100:103]\n"
    "ds_read_b128 a[84:87], v30 offset:13312\n"
    "v_rcp_f32 v138, v138\n"
    "s_waitcnt lgkmcnt(6)\n"
    "v_mfma_f32_16x16x32_bf16 v[92:95], a[224:227], a[56:59], v[92:95]\n"
    "v_rcp_f32 v139, v139\n"
    "v_mfma_f32_16x16x32_bf16 v[96:99], a[228:231], a[56:59], v[96:99]\n"
    "s_waitcnt vmcnt(1)\n"
    "v_cmp_ne_u32 vcc, s96, v59\n"
    "s_cmp_eq_u64 vcc, 0\n"
    "s_cbranch_scc1 .Lfb_xd_42\n"
    "s_mov_b32 s97, 0\n"
    ".Lfb_xs_41:\n"
    "s_sleep 2\n"
    "buffer_load_dword v59, v58, s[24:27], 0 offen sc1\n"
    "s_waitcnt vmcnt(0)\n"
    "v_cmp_ne_u32 vcc, s96, v59\n"
    "s_cmp_eq_u64 vcc, 0\n"
    "s_cbranch_scc1 .Lfb_xd_42\n"
    "s_add_u32 s97, s97, 1\n"
    "s_cmp_lt_u32 s97, 0x10000\n"
    "s_cbranch_scc1 .Lfb_xs_41\n"
    ".Lfb_xd_42:\n"
    ".Lfb_xe_43:\n"
    "v_mul_f32 v124, v124, v136\n"
    "v_mfma_f32_16x16x32_bf16 v[100:103], a[232:235], a[56:59], v[100:103]\n"
    "ds_read_b128 a[88:91], v30 offset:14336\n"
    "v_mul_f32 v125, v125, v137\n"
    "buffer_load_dwordx4 a[92:95], v0, s[24:27], s52 offen sc1\n"
    "s_waitcnt lgkmcnt(5)\n"
    "v_mfma_f32_16x16x32_bf16 v[92:95], a[236:239], a[60:63], v[92:95]\n"
    "v_mul_f32 v126, v126, v138\n"
    "v_mfma_f32_16x16x32_bf16 v[96:99], v[148:151], a[60:63], v[96:99]\n"
    "v_mul_f32 v127, v127, v139\n"
    "v_cvt_pk_bf16_f32 v112, v124, v125\n"
    "v_mfma_f32_16x16x32_bf16 v[100:103], v[152:155], a[60:63], v[100:103]\n"
    "buffer_load_dwordx4 a[96:99], v0, s[24:27], s52 offen offset:1024 sc1\n"
    "ds_read_b128 a[100:103], v30 offset:15360\n"
    "v_cvt_pk_bf16_f32 v113, v126, v127\n"
    "s_waitcnt lgkmcnt(3)\n"
    "v_pk_fma_f32 v[128:129], v[32:33], v[140:141], v[44:45] op_sel:[0,1,0] op_sel_hi:[1,1,1]\n"
    "v_pk_fma_f32 v[130:131], v[34:35], v[140:141], v[46:47] op_sel:[0,1,0] op_sel_hi:[1,1,1]\n"
    "v_pk_fma_f32 v[132:133], v[36:37], v[140:141], v[48:49] op_sel:[0,1,0] op_sel_hi:[1,1,1]\n"
    "v_pk_fma_f32 v[134:135], v[38:39], v[140:141], v[50:51] op_sel:[0,1,0] op_sel_hi:[1,1,1]\n"
    "v_pk_fma_f32 v[136:137], v[40:41], v[140:141], v[52:53] op_sel:[0,1,0] op_sel_hi:[1,1,1]\n"
    "v_pk_fma_f32 v[138:139], v[42:43], v[140:141], v[54:55] op_sel:[0,1,0] op_sel_hi:[1,1,1]\n"
    "v_pk_fma_f32 v[116:117], v[92:93], v[140:141], v[128:129] op_sel_hi:[1,0,1]\n"
    "v_pk_fma_f32 v[118:119], v[94:95], v[140:141], v[130:131] op_sel_hi:[1,0,1]\n"
    "buffer_load_dwordx4 a[104:107], v0, s[24:27], s52 offen offset:2048 sc1\n"
    "v_pk_fma_f32 v[120:121], v[96:97], v[140:141], v[132:133] op_sel_hi:[1,0,1]\n"
    "v_pk_fma_f32 v[122:123], v[98:99], v[140:141], v[134:135] op_sel_hi:[1,0,1]\n"
    "v_pk_fma_f32 v[124:125], v[100:101], v[140:141], v[136:137] op_sel_hi:[1,0,1]\n"
    "v_pk_fma_f32 v[126:127], v[102:103], v[140:141], v[138:139] op_sel_hi:[1,0,1]\n"
    "v_pk_mul_f32 v[128:129], v[116:117], v[116:117]\n"
    "v_pk_mul_f32 v[130:131], v[118:119], v[118:119]\n"
    "v_pk_mul_f32 v[132:133], v[120:121], v[120:121]\n"
    "v_pk_mul_f32 v[134:135], v[122:123], v[122:123]\n"
    "v_pk_mul_f32 v[136:137], v[124:125], v[124:125]\n"
    "v_pk_mul_f32 v[138:139], v[126:127], v[126:127]\n"
    "buffer_load_dwordx4 a[108:111], v0, s[24:27], s52 offen offset:3072 sc1\n"
    "v_pk_fma_f32 v[128:129], v[128:129], s[30:31], v[142:143]\n"
    "v_pk_fma_f32 v[130:131], v[130:131], s[30:31], v[142:143]\n"
    "v_pk_fma_f32 v[132:133], v[132:133], s[30:31], v[142:143]\n"
    "v_pk_fma_f32 v[134:135], v[134:135], s[30:31], v[142:143]\n"
    "v_pk_fma_f32 v[136:137], v[136:137], s[30:31], v[142:143]\n"
    "v_pk_fma_f32 v[138:139], v[138:139], s[30:31], v[142:143]\n"
    "v_pk_mul_f32 v[128:129], v[128:129], v[116:117]\n"
    "v_pk_mul_f32 v[130:131], v[130:131], v[118:119]\n"
    "v_pk_mul_f32 v[132:133], v[132:133], v[120:121]\n"
    "v_pk_mul_f32 v[134:135], v[134:135], v[122:123]\n"
    "buffer_load_dwordx4 a[152:155], v1, s[24:27], s52 offen sc1\n"
    "v_pk_mul_f32 v[136:137], v[136:137], v[124:125]\n"
    "v_pk_mul_f32 v[138:139], v[138:139], v[126:127]\n"
    ".Lfb_c5:\n"
    "; ---- P4 tail (t~34048)\n"
    "v_exp_f32 v128, v128\n"
    "v_exp_f32 v129, v129\n"
    "v_exp_f32 v130, v130\n"
    "v_exp_f32 v131, v131\n"
    "buffer_load_dwordx4 a[156:159], v1, s[24:27], s52 offen offset:1024 sc1\n"
    "v_pk_add_f32 v[128:129], v[128:129], 1.0 op_sel_hi:[1,0]\n"
    "v_pk_add_f32 v[130:131], v[130:131], 1.0 op_sel_hi:[1,0]\n"
    "v_rcp_f32 v128, v128\n"
    "v_rcp_f32 v129, v129\n"
    "v_rcp_f32 v130, v130\n"
    "v_rcp_f32 v131, v131\n"
    "buffer_load_dwordx4 a[160:163], v1, s[24:27], s52 offen offset:2048 sc1\n"
    "v_pk_mul_f32 v[116:117], v[116:117], v[128:129]\n"
    "v_pk_mul_f32 v[118:119], v[118:119], v[130:131]\n"
    "v_cvt_pk_bf16_f32 v108, v116, v117\n"
    "v_cvt_pk_bf16_f32 v109, v118, v119\n"
    "v_exp_f32 v132, v132\n"
    "v_exp_f32 v133, v133\n"
    "v_exp_f32 v134, v134\n"
    "buffer_load_dwordx4 a[164:167], v1, s[24:27], s52 offen offset:3072 sc1\n"
    "v_exp_f32 v135, v135\n"
    "v_pk_add_f32 v[132:133], v[132:133], 1.0 op_sel_hi:[1,0]\n"
    "v_pk_add_f32 v[134:135], v[134:135], 1.0 op_sel_hi:[1,0]\n"
    "v_rcp_f32 v132, v132\n"
    "v_rcp_f32 v133, v133\n"
    "v_rcp_f32 v134, v134\n"
    "buffer_load_dwordx4 a[168:171], v57, s[24:27], s52 offen sc1\n"
    "v_rcp_f32 v135, v135\n"
    "v_pk_mul_f32 v[120:121], v[120:121], v[132:133]\n"
    "v_pk_mul_f32 v[122:123], v[122:123], v[134:135]\n"
    "v_cvt_pk_bf16_f32 v110, v120, v121\n"
    "v_cvt_pk_bf16_f32 v111, v122, v123\n"
    "s_add_u32 s29, s63, 0x24000\n"
    "buffer_store_dwordx4 v[108:111], v0, s[24:27], s29 offen sc1\n"
    "v_exp_f32 v136, v136\n"
    "v_exp_f32 v137, v137\n"
    "v_exp_f32 v138, v138\n"
    "v_exp_f32 v139, v139\n"
    "v_pk_add_f32 v[136:137], v[136:137], 1.0 op_sel_hi:[1,0]\n"
    "v_pk_add_f32 v[138:139], v[138:139], 1.0 op_sel_hi:[1,0]\n"
    "buffer_load_dwordx4 a[172:175], v57, s[24:27], s52 offen offset:1024 sc1\n"
    "v_rcp_f32 v136, v136\n"
    "v_rcp_f32 v137, v137\n"
    "v_rcp_f32 v138, v138\n"
    "v_rcp_f32 v139, v139\n"
    "v_pk_mul_f32 v[124:125], v[124:125], v[136:137]\n"
    "v_pk_mul_f32 v[126:127], v[126:127], v[138:139]\n"
    "buffer_load_dwordx4 a[176:179], v57, s[24:27], s52 offen offset:2048 sc1\n"
    "v_cvt_pk_bf16_f32 v114, v124, v125\n"
    "v_cvt_pk_bf16_f32 v115, v126, v127\n"
    "s_nop 0\n"
    "v_permlane16_swap_b32 v112, v114\n"
    "v_permlane16_swap_b32 v113, v115\n"
    "s_add_u32 s29, s63, 0x18000\n"
    "buffer_store_dwordx4 v[112:115], v13, s[24:27], s29 offen sc1\n"
    "; ---- P5 (t~34436)\n"
    "buffer_load_dwordx4 a[196:199], v57, s[24:27], s52 offen offset:3072 sc1\n"
    "buffer_load_dwordx4 a[200:203], v0, s[24:27], s53 offen sc1\n"
    "buffer_load_dwordx4 a[204:207], v0, s[24:27], s53 offen offset:1024 sc1\n"
    "buffer_load_dwordx4 a[208:211], v0, s[24:27], s53 offen offset:2048 sc1\n"
    "buffer_load_dwordx4 a[212:215], v0, s[24:27], s53 offen offset:3072 sc1\n"
    "ds_read_b128 a[216:219], v30 offset:16384\n"
    "ds_read_b128 a[220:223], v30 offset:17408\n"
    "ds_read_b128 a[224:227], v30 offset:18432\n"
    "ds_read_b128 a[228:231], v56\n"
    "ds_read_b128 a[232:235], v56 offset:1024\n"
    "ds_read_b128 a[236:239], v56 offset:2048\n"
    "ds_read_b128 a[244:247], v56 offset:3072\n"
    "ds_read_b128 a[248:251], v56 offset:20480\n"
    "ds_read_b128 a[252:255], v56 offset:21504\n"
    "ds_read_b128 v[140:143], v56 offset:22528\n"
    "ds_read_b128 v[144:147], v56 offset:23552\n"
    "s_waitcnt vmcnt(17)\n"
    "v_mfma_f32_16x16x32_bf16 a[56:59], a[128:131], a[92:95], 0\n"
    "buffer_load_dwordx4 v[108:111], v1, s[24:27], s53 offen sc1\n"
    "v_mfma_f32_16x16x32_bf16 a[60:63], a[136:139], a[92:95], 0\n"
    "v_mfma_f32_16x16x32_bf16 a[64:67], a[144:147], a[92:95], 0\n"
    "v_mfma_f32_16x16x32_bf16 a[68:71], a[0:3], a[92:95], 0\n"
    "v_mfma_f32_16x16x32_bf16 a[72:75], a[8:11], a[92:95], 0\n"
    "s_waitcnt vmcnt(17)\n"
    "v_mfma_f32_16x16x32_bf16 a[56:59], a[16:19], a[96:99], a[56:59]\n"
    "buffer_load_dwordx4 v[112:115], v1, s[24:27], s53 offen offset:1024 sc1\n"
    "v_mfma_f32_16x16x32_bf16 a[60:63], a[24:27], a[96:99], a[60:63]\n"
    "v_mfma_f32_16x16x32_bf16 a[64:67], a[36:39], a[96:99], a[64:67]\n"
    "v_mfma_f32_16x16x32_bf16 a[68:71], a[40:43], a[96:99], a[68:71]\n"
    "v_mfma_f32_16x16x32_bf16 a[72:75], a[44:47], a[96:99], a[72:75]\n"
    "s_waitcnt vmcnt(17)\n"
    "v_mfma_f32_16x16x32_bf16 a[56:59], a[48:51], a[104:107], a[56:59]\n"
    "buffer_load_dwordx4 a[92:95], v1, s[24:27], s53 offen offset:2048 sc1\n"
    "v_mfma_f32_16x16x32_bf16 a[60:63], a[52:55], a[104:107], a[60:63]\n"
    "v_mfma_f32_16x16x32_bf16 a[64:67], a[80:83], a[104:107], a[64:67]\n"
    "s_waitcnt lgkmcnt(13)\n"
    "v_mfma_f32_16x16x32_bf16 a[68:71], a[84:87], a[104:107], a[68:71]\n"
    "s_waitcnt lgkmcnt(12)\n"
    "v_mfma_f32_16x16x32_bf16 a[72:75], a[88:91], a[104:107], a[72:75]\n"
    "s_waitcnt vmcnt(17) lgkmcnt(11)\n"
    "v_mfma_f32_16x16x32_bf16 a[56:59], a[100:103], a[108:111], a[56:59]\n"
    "buffer_load_dwordx4 a[96:99], v1, s[24:27], s53 offen offset:3072 sc1\n"
    "s_waitcnt lgkmcnt(10)\n"
    "v_mfma_f32_16x16x32_bf16 a[60:63], a[216:219], a[108:111], a[60:63]\n"
    "s_waitcnt lgkmcnt(9)\n"
    "v_mfma_f32_16x16x32_bf16 a[64:67], a[220:223], a[108:111], a[64:67]\n"
    "s_waitcnt lgkmcnt(8)\n"
    "v_mfma_f32_16x16x32_bf16 a[68:71], a[224:227], a[108:111], a[68:71]\n"
    "v_mfma_f32_16x16x32_bf16 a[72:75], v[60:63], a[108:111], a[72:75]\n"
    "s_waitcnt vmcnt(17)\n"
    "v_mfma_f32_16x16x32_bf16 a[56:59], v[64:67], a[152:155], a[56:59]\n"
    "buffer_load_dwordx4 a[104:107], v57, s[24:27], s53 offen sc1\n"
    "s_waitcnt vmcnt(10)\n"
    "s_waitcnt lgkmcnt(0)\n"
    "s_barrier\n"
    "s_cmp_lg_u32 s16, 0\n"
    "s_cbranch_scc1 .Lfb_fs_44\n"
    "s_mov_b64 exec, 1\n"
    "v_mov_b32 v229, s96\n"
    "s_add_u32 s97, s12, 0x2060\n"
    "buffer_store_dword v229, v25, s[24:27], s97 offen sc1\n"
    "s_mov_b64 exec, -1\n"
    ".Lfb_fs_44:\n"
    "v_mfma_f32_16x16x32_bf16 a[60:63], v[156:159], a[152:155], a[60:63]\n"
    "v_mfma_f32_16x16x32_bf16 a[64:67], v[160:163], a[152:155], a[64:67]\n"
    "v_mfma_f32_16x16x32_bf16 a[68:71], v[164:167], a[152:155], a[68:71]\n"
    "v_mfma_f32_16x16x32_bf16 a[72:75], v[168:171], a[152:155], a[72:75]\n"
    "v_mfma_f32_16x16x32_bf16 a[56:59], v[172:175], a[156:159], a[56:59]\n"
    "buffer_load_dwordx4 a[108:111], v57, s[24:27], s53 offen offset:1024 sc1\n"
    "v_add_u32 v82, s19, v230\n"
    "v_add_u32 v83, s19, v251\n"
    "v_mfma_f32_16x16x32_bf16 a[60:63], v[176:179], a[156:159], a[60:63]\n"
    "ds_write_b128 v82, v[72:75]\n"
    "ds_write_b128 v82, v[76:79] offset:1024\n"
    "v_mfma_f32_16x16x32_bf16 a[64:67], v[180:183], a[156:159], a[64:67]\n"
    "ds_write_b64 v82, v[80:81] offset:2048\n"
    "ds_read_b128 v[72:75], v83\n"
    "v_mfma_f32_16x16x32_bf16 a[68:71], v[184:187], a[156:159], a[68:71]\n"
    "ds_read_b128 v[76:79], v83 offset:1024\n"
    "ds_read_b64 v[80:81], v83 offset:2048\n"
    "v_mfma_f32_16x16x32_bf16 a[72:75], v[188:191], a[156:159], a[72:75]\n"
    "s_lshr_b32 s1, s16, 1\n"
    "s_mul_i32 s1, s1, 0x1400\n"
    "v_mfma_f32_16x16x32_bf16 a[56:59], v[192:195], a[160:163], a[56:59]\n"
    "buffer_load_dwordx4 a[152:155], v57, s[24:27], s53 offen offset:2048 sc1\n"
    "s_mul_i32 s0, s48, 0x5000\n"
    "s_add_u32 s0, s0, s1\n"
    "v_mfma_f32_16x16x32_bf16 a[60:63], v[196:199], a[160:163], a[60:63]\n"
    "s_add_u32 s0, s0, 0\n"
    "v_add_u32 v31, s0, v230\n"
    "v_mfma_f32_16x16x32_bf16 a[64:67], v[200:203], a[160:163], a[64:67]\n"
    "v_add_u32 v137, 0xa000, v31\n"
    "s_mul_i32 s0, s49, 0x5000\n"
    "v_mfma_f32_16x16x32_bf16 a[68:71], v[204:207], a[160:163], a[68:71]\n"
    "s_add_u32 s0, s0, s1\n"
    "s_add_u32 s0, s0, 0x2800\n"
    "v_mfma_f32_16x16x32_bf16 a[72:75], v[208:211], a[160:163], a[72:75]\n"
    "v_add_u32 v136, s0, v230\n"
    "v_add_u32 v138, 0xa000, v136\n"
    "v_mfma_f32_16x16x32_bf16 a[56:59], v[212:215], a[164:167], a[56:59]\n"
    "buffer_load_dwordx4 a[156:159], v57, s[24:27], s53 offen offset:3072 sc1\n"
    "s_mul_i32 s0, s16, 0x5000\n"
    "v_add_u32 v139, s0, v251\n"
    "v_mfma_f32_16x16x32_bf16 a[60:63], v[216:219], a[164:167], a[60:63]\n"
    "buffer_load_dwordx4 v[124:127], v22, s[40:43], 0 offen\n"
    "buffer_load_dwordx4 v[128:131], v22, s[40:43], 0 offen offset:64\n"
    "v_mfma_f32_16x16x32_bf16 a[64:67], v[220:223], a[164:167], a[64:67]\n"
    "buffer_load_dwordx2 v[132:133], v23, s[40:43], 0 offen\n"
    "v_mfma_f32_16x16x32_bf16 a[68:71], v[224:227], a[164:167], a[68:71]\n"
    "v_mfma_f32_16x16x32_bf16 a[72:75], a[240:243], a[164:167], a[72:75]\n"
    "v_mfma_f32_16x16x32_bf16 a[56:59], a[112:115], a[168:171], a[56:59]\n"
    "v_mfma_f32_16x16x32_bf16 a[60:63], a[116:119], a[168:171], a[60:63]\n"
    "v_mfma_f32_16x16x32_bf16 a[64:67], a[120:123], a[168:171], a[64:67]\n"
    "v_mfma_f32_16x16x32_bf16 a[68:71], a[124:127], a[168:171], a[68:71]\n"
    "v_mfma_f32_16x16x32_bf16 a[72:75], a[132:135], a[168:171], a[72:75]\n"
    "v_mfma_f32_16x16x32_bf16 a[56:59], a[140:143], a[172:175], a[56:59]\n"
    "v_mfma_f32_16x16x32_bf16 a[60:63], a[148:151], a[172:175], a[60:63]\n"
    "v_mfma_f32_16x16x32_bf16 a[64:67], a[4:7], a[172:175], a[64:67]\n"
    "v_mfma_f32_16x16x32_bf16 a[68:71], a[12:15], a[172:175], a[68:71]\n"
    "v_mfma_f32_16x16x32_bf16 a[72:75], a[20:23], a[172:175], a[72:75]\n"
    "v_mfma_f32_16x16x32_bf16 a[56:59], a[28:31], a[176:179], a[56:59]\n"
    "v_mfma_f32_16x16x32_bf16 a[60:63], a[32:35], a[176:179], a[60:63]\n"
    "v_mfma_f32_16x16x32_bf16 a[64:67], a[228:231], a[176:179], a[64:67]\n"
    "v_mfma_f32_16x16x32_bf16 a[68:71], a[232:235], a[176:179], a[68:71]\n"
    "v_mfma_f32_16x16x32_bf16 a[72:75], a[236:239], a[176:179], a[72:75]\n"
    "s_waitcnt vmcnt(15)\n"
    "v_mfma_f32_16x16x32_bf16 a[56:59], a[244:247], a[196:199], a[56:59]\n"
    "v_mfma_f32_16x16x32_bf16 a[60:63], a[248:251], a[196:199], a[60:63]\n"
    "v_mfma_f32_16x16x32_bf16 a[64:67], a[252:255], a[196:199], a[64:67]\n"
    "v_mfma_f32_16x16x32_bf16 a[68:71], v[140:143], a[196:199], a[68:71]\n"
    "v_mfma_f32_16x16x32_bf16 a[72:75], v[144:147], a[196:199], a[72:75]\n"
    "s_waitcnt vmcnt(14)\n"
    "v_mfma_f32_16x16x32_bf16 a[76:79], a[128:131], a[200:203], 0\n"
    "v_mfma_f32_16x16x32_bf16 a[180:183], a[136:139], a[200:203], 0\n"
    "v_mfma_f32_16x16x32_bf16 a[184:187], a[144:147], a[200:203], 0\n"
    "v_mfma_f32_16x16x32_bf16 a[188:191], a[0:3], a[200:203], 0\n"
    "v_mfma_f32_16x16x32_bf16 a[192:195], a[8:11], a[200:203], 0\n"
    "ds_write_b128 v31, a[56:59]\n"
    "s_waitcnt vmcnt(13)\n"
    "v_mfma_f32_16x16x32_bf16 a[76:79], a[16:19], a[204:207], a[76:79]\n"
    "ds_write_b128 v31, a[60:63] offset:1024\n"
    "v_mfma_f32_16x16x32_bf16 a[180:183], a[24:27], a[204:207], a[180:183]\n"
    "ds_write_b128 v31, a[64:67] offset:2048\n"
    "v_mfma_f32_16x16x32_bf16 a[184:187], a[36:39], a[204:207], a[184:187]\n"
    "buffer_load_dword v59, v58, s[24:27], 0 offen offset:32 sc1\n"
    "ds_write_b128 v31, a[68:71] offset:3072\n"
    "v_mfma_f32_16x16x32_bf16 a[188:191], a[40:43], a[204:207], a[188:191]\n"
    "ds_write_b128 v31, a[72:75] offset:4096\n"
    "v_mfma_f32_16x16x32_bf16 a[192:195], a[44:47], a[204:207], a[192:195]\n"
    "s_waitcnt lgkmcnt(7)\n"
    "v_lshlrev_b32 v32, 16, v72\n"
    "s_waitcnt vmcnt(13)\n"
    "v_mfma_f32_16x16x32_bf16 a[76:79], a[48:51], a[208:211], a[76:79]\n"
    "v_and_b32 v33, 0xffff0000, v72\n"
    "v_mfma_f32_16x16x32_bf16 a[180:183], a[52:55], a[208:211], a[180:183]\n"
    "v_lshlrev_b32 v34, 16, v73\n"
    "v_mfma_f32_16x16x32_bf16 a[184:187], a[80:83], a[208:211], a[184:187]\n"
    "v_and_b32 v35, 0xffff0000, v73\n"
    "v_mfma_f32_16x16x32_bf16 a[188:191], a[84:87], a[208:211], a[188:191]\n"
    "v_lshlrev_b32 v36, 16, v74\n"
    "v_mfma_f32_16x16x32_bf16 a[192:195], a[88:91], a[208:211], a[192:195]\n"
    "v_and_b32 v37, 0xffff0000, v74\n"
    "s_waitcnt vmcnt(12)\n"
    "v_mfma_f32_16x16x32_bf16 a[76:79], a[100:103], a[212:215], a[76:79]\n"
    "v_lshlrev_b32 v38, 16, v75\n"
    "v_mfma_f32_16x16x32_bf16 a[180:183], a[216:219], a[212:215], a[180:183]\n"
    "v_and_b32 v39, 0xffff0000, v75\n"
    "v_mfma_f32_16x16x32_bf16 a[184:187], a[220:223], a[212:215], a[184:187]\n"
    "s_waitcnt lgkmcnt(6)\n"
    "v_lshlrev_b32 v40, 16, v76\n"
    "v_mfma_f32_16x16x32_bf16 a[188:191], a[224:227], a[212:215], a[188:191]\n"
    "v_and_b32 v41, 0xffff0000, v76\n"
    "v_mfma_f32_16x16x32_bf16 a[192:195], v[60:63], a[212:215], a[192:195]\n"
    "v_lshlrev_b32 v42, 16, v77\n"
    "s_waitcnt vmcnt(11)\n"
    "v_mfma_f32_16x16x32_bf16 a[76:79], v[64:67], v[108:111], a[76:79]\n"
    "v_and_b32 v43, 0xffff0000, v77\n"
    "v_mfma_f32_16x16x32_bf16 a[180:183], v[156:159], v[108:111], a[180:183]\n"
    "v_lshlrev_b32 v44, 16, v78\n"
    "v_mfma_f32_16x16x32_bf16 a[184:187], v[160:163], v[108:111], a[184:187]\n"
    "v_and_b32 v45, 0xffff0000, v78\n"
    "v_mfma_f32_16x16x32_bf16 a[188:191], v[164:167], v[108:111], a[188:191]\n"
    "v_lshlrev_b32 v46, 16, v79\n"
    "v_mfma_f32_16x16x32_bf16 a[192:195], v[168:171], v[108:111], a[192:195]\n"
    "v_and_b32 v47, 0xffff0000, v79\n"
    "s_waitcnt vmcnt(10)\n"
    "v_mfma_f32_16x16x32_bf16 a[76:79], v[172:175], v[112:115], a[76:79]\n"
    "s_waitcnt lgkmcnt(5)\n"
    "v_lshlrev_b32 v48, 16, v80\n"
    "v_mfma_f32_16x16x32_bf16 a[180:183], v[176:179], v[112:115], a[180:183]\n"
    "v_and_b32 v49, 0xffff0000, v80\n"
    "v_mfma_f32_16x16x32_bf16 a[184:187], v[180:183], v[112:115], a[184:187]\n"
    "v_lshlrev_b32 v50, 16, v81\n"
    "v_mfma_f32_16x16x32_bf16 a[188:191], v[184:187], v[112:115], a[188:191]\n"
    "v_and_b32 v51, 0xffff0000, v81\n"
    "v_mfma_f32_16x16x32_bf16 a[192:195], v[188:191], v[112:115], a[192:195]\n"
    "s_waitcnt vmcnt(3)\n"
    "v_lshlrev_b32 v52, 16, v124\n"
    "v_mfma_f32_16x16x32_bf16 a[76:79], v[192:195], a[92:95], a[76:79]\n"
    "v_and_b32 v53, 0xffff0000, v124\n"
    "v_mfma_f32_16x16x32_bf16 a[180:183], v[196:199], a[92:95], a[180:183]\n"
    "v_lshlrev_b32 v54, 16, v125\n"
    "v_mfma_f32_16x16x32_bf16 a[184:187], v[200:203], a[92:95], a[184:187]\n"
    "s_waitcnt vmcnt(0)\n"
    "v_cmp_ne_u32 vcc, s96, v59\n"
    "s_cmp_eq_u64 vcc, 0\n"
    "s_cbranch_scc1 .Lfb_xd_46\n"
    "s_mov_b32 s97, 0\n"
    ".Lfb_xs_45:\n"
    "s_sleep 2\n"
    "buffer_load_dword v59, v58, s[24:27], 0 offen offset:32 sc1\n"
    "s_waitcnt vmcnt(0)\n"
    "v_cmp_ne_u32 vcc, s96, v59\n"
    "s_cmp_eq_u64 vcc, 0\n"
    "s_cbranch_scc1 .Lfb_xd_46\n"
    "s_add_u32 s97, s97, 1\n"
    "s_cmp_lt_u32 s97, 0x10000\n"
    "s_cbranch_scc1 .Lfb_xs_45\n"
    ".Lfb_xd_46:\n"
    ".Lfb_xe_47:\n"
    "v_and_b32 v55, 0xffff0000, v125\n"
    "v_mfma_f32_16x16x32_bf16 a[188:191], v[204:207], a[92:95], a[188:191]\n"
    "buffer_load_dwordx4 a[160:163], v0, s[24:27], s54 offen sc1\n"
    "v_lshlrev_b32 v68, 16, v126\n"
    "v_mfma_f32_16x16x32_bf16 a[192:195], v[208:211], a[92:95], a[192:195]\n"
    "v_and_b32 v69, 0xffff0000, v126\n"
    "v_mfma_f32_16x16x32_bf16 a[76:79], v[212:215], a[96:99], a[76:79]\n"
    "buffer_load_dwordx4 a[164:167], v0, s[24:27], s54 offen offset:1024 sc1\n"
    "v_lshlrev_b32 v70, 16, v127\n"
    "v_mfma_f32_16x16x32_bf16 a[180:183], v[216:219], a[96:99], a[180:183]\n"
    "v_and_b32 v71, 0xffff0000, v127\n"
    "v_mfma_f32_16x16x32_bf16 a[184:187], v[220:223], a[96:99], a[184:187]\n"
    "v_lshlrev_b32 v84, 16, v128\n"
    "v_mfma_f32_16x16x32_bf16 a[188:191], v[224:227], a[96:99], a[188:191]\n"
    "buffer_load_dwordx4 a[168:171], v0, s[24:27], s54 offen offset:2048 sc1\n"
    "v_and_b32 v85, 0xffff0000, v128\n"
    "v_mfma_f32_16x16x32_bf16 a[192:195], a[240:243], a[96:99], a[192:195]\n"
    "v_lshlrev_b32 v86, 16, v129\n"
    "v_mfma_f32_16x16x32_bf16 a[76:79], a[112:115], a[104:107], a[76:79]\n"
    "buffer_load_dwordx4 a[172:175], v0, s[24:27], s54 offen offset:3072 sc1\n"
    "v_and_b32 v87, 0xffff0000, v129\n"
    "v_mfma_f32_16x16x32_bf16 a[180:183], a[116:119], a[104:107], a[180:183]\n"
    "v_lshlrev_b32 v88, 16, v130\n"
    "v_mfma_f32_16x16x32_bf16 a[184:187], a[120:123], a[104:107], a[184:187]\n"
    "v_and_b32 v89, 0xffff0000, v130\n"
    "v_mfma_f32_16x16x32_bf16 a[188:191], a[124:127], a[104:107], a[188:191]\n"
    "buffer_load_dwordx4 a[176:179], v1, s[24:27], s54 offen sc1\n"
    "v_lshlrev_b32 v90, 16, v131\n"
    "v_mfma_f32_16x16x32_bf16 a[192:195], a[132:135], a[104:107], a[192:195]\n"
    "v_and_b32 v91, 0xffff0000, v131\n"
    "v_mfma_f32_16x16x32_bf16 a[76:79], a[140:143], a[108:111], a[76:79]\n"
    "buffer_load_dwordx4 a[196:199], v1, s[24:27], s54 offen offset:1024 sc1\n"
    "v_lshlrev_b32 v92, 16, v132\n"
    "v_mfma_f32_16x16x32_bf16 a[180:183], a[148:151], a[108:111], a[180:183]\n"
    "v_and_b32 v93, 0xffff0000, v132\n"
    "v_mfma_f32_16x16x32_bf16 a[184:187], a[4:7], a[108:111], a[184:187]\n"
    "v_lshlrev_b32 v94, 16, v133\n"
    "v_mfma_f32_16x16x32_bf16 a[188:191], a[12:15], a[108:111], a[188:191]\n"
    "buffer_load_dwordx4 a[200:203], v1, s[24:27], s54 offen offset:2048 sc1\n"
    "v_and_b32 v95, 0xffff0000, v133\n"
    "v_mfma_f32_16x16x32_bf16 a[192:195], a[20:23], a[108:111], a[192:195]\n"
    "v_add_f32 v32, v32, v52\n"
    "v_mfma_f32_16x16x32_bf16 a[76:79], a[28:31], a[152:155], a[76:79]\n"
    "buffer_load_dwordx4 a[204:207], v1, s[24:27], s54 offen offset:3072 sc1\n"
    "v_add_f32 v33, v33, v53\n"
    "v_mfma_f32_16x16x32_bf16 a[180:183], a[32:35], a[152:155], a[180:183]\n"
    "v_add_f32 v34, v34, v54\n"
    "v_mfma_f32_16x16x32_bf16 a[184:187], a[228:231], a[152:155], a[184:187]\n"
    "v_add_f32 v35, v35, v55\n"
    "v_mfma_f32_16x16x32_bf16 a[188:191], a[232:235], a[152:155], a[188:191]\n"
    "buffer_load_dwordx4 a[208:211], v57, s[24:27], s54 offen sc1\n"
    "v_add_f32 v36, v36, v68\n"
    "v_mfma_f32_16x16x32_bf16 a[192:195], a[236:239], a[152:155], a[192:195]\n"
    "v_add_f32 v37, v37, v69\n"
    "v_mfma_f32_16x16x32_bf16 a[76:79], a[244:247], a[156:159], a[76:79]\n"
    "buffer_load_dwordx4 a[212:215], v57, s[24:27], s54 offen offset:1024 sc1\n"
    "v_add_f32 v38, v38, v70\n"
    "v_mfma_f32_16x16x32_bf16 a[180:183], a[248:251], a[156:159], a[180:183]\n"
    "v_add_f32 v39, v39, v71\n"
    "v_mfma_f32_16x16x32_bf16 a[184:187], a[252:255], a[156:159], a[184:187]\n"
    "v_add_f32 v40, v40, v84\n"
    "v_mfma_f32_16x16x32_bf16 a[188:191], v[140:143], a[156:159], a[188:191]\n"
    "buffer_load_dwordx4 v[108:111], v57, s[24:27], s54 offen offset:2048 sc1\n"
    "v_add_f32 v41, v41, v85\n"
    "v_mfma_f32_16x16x32_bf16 a[192:195], v[144:147], a[156:159], a[192:195]\n"
    "v_add_f32 v42, v42, v86\n"
    "s_waitcnt vmcnt(10)\n"
    "v_mfma_f32_16x16x32_bf16 a[56:59], a[128:131], a[160:163], 0\n"
    "buffer_load_dwordx4 v[112:115], v57, s[24:27], s54 offen offset:3072 sc1\n"
    "v_mfma_f32_16x16x32_bf16 a[60:63], a[136:139], a[160:163], 0\n"
    "v_mfma_f32_16x16x32_bf16 a[64:67], a[144:147], a[160:163], 0\n"
    "v_mfma_f32_16x16x32_bf16 a[68:71], a[0:3], a[160:163], 0\n"
    "buffer_load_dwordx4 a[92:95], v0, s[24:27], s55 offen sc1\n"
    "v_mfma_f32_16x16x32_bf16 a[72:75], a[8:11], a[160:163], 0\n"
    "ds_write_b128 v136, a[76:79]\n"
    "s_waitcnt vmcnt(11)\n"
    "v_mfma_f32_16x16x32_bf16 a[56:59], a[16:19], a[164:167], a[56:59]\n"
    "buffer_load_dwordx4 a[96:99], v0, s[24:27], s55 offen offset:1024 sc1\n"
    "ds_write_b128 v136, a[180:183] offset:1024\n"
    "v_mfma_f32_16x16x32_bf16 a[60:63], a[24:27], a[164:167], a[60:63]\n"
    "ds_write_b128 v136, a[184:187] offset:2048\n"
    "v_mfma_f32_16x16x32_bf16 a[64:67], a[36:39], a[164:167], a[64:67]\n"
    "ds_write_b128 v136, a[188:191] offset:3072\n"
    "v_mfma_f32_16x16x32_bf16 a[68:71], a[40:43], a[164:167], a[68:71]\n"
    "buffer_load_dwordx4 a[104:107], v0, s[24:27], s55 offen offset:2048 sc1\n"
    "ds_write_b128 v136, a[192:195] offset:4096\n"
    "v_mfma_f32_16x16x32_bf16 a[72:75], a[44:47], a[164:167], a[72:75]\n"
    "v_add_f32 v43, v43, v87\n"
    "s_waitcnt vmcnt(12)\n"
    "v_mfma_f32_16x16x32_bf16 a[56:59], a[48:51], a[168:171], a[56:59]\n"
    "buffer_load_dwordx4 a[108:111], v0, s[24:27], s55 offen offset:3072 sc1\n"
    "v_add_f32 v44, v44, v88\n"
    "v_mfma_f32_16x16x32_bf16 a[60:63], a[52:55], a[168:171], a[60:63]\n"
    "v_add_f32 v45, v45, v89\n"
    "v_mfma_f32_16x16x32_bf16 a[64:67], a[80:83], a[168:171], a[64:67]\n"
    "v_add_f32 v46, v46, v90\n"
    "v_mfma_f32_16x16x32_bf16 a[68:71], a[84:87], a[168:171], a[68:71]\n"
    "buffer_load_dwordx4 a[152:155], v1, s[24:27], s55 offen sc1\n"
    "v_add_f32 v47, v47, v91\n"
    "v_mfma_f32_16x16x32_bf16 a[72:75], a[88:91], a[168:171], a[72:75]\n"
    "v_add_f32 v48, v48, v92\n"
    "s_waitcnt vmcnt(13)\n"
    "v_mfma_f32_16x16x32_bf16 a[56:59], a[100:103], a[172:175], a[56:59]\n"
    "buffer_load_dwordx4 a[156:159], v1, s[24:27], s55 offen offset:1024 sc1\n"
    "v_add_f32 v49, v49, v93\n"
    "v_mfma_f32_16x16x32_bf16 a[60:63], a[216:219], a[172:175], a[60:63]\n"
    "v_add_f32 v50, v50, v94\n"
    "v_mfma_f32_16x16x32_bf16 a[64:67], a[220:223], a[172:175], a[64:67]\n"
    "v_add_f32 v51, v51, v95\n"
    "v_mfma_f32_16x16x32_bf16 a[68:71], a[224:227], a[172:175], a[68:71]\n"
    "buffer_load_dwordx4 a[160:163], v1, s[24:27], s55 offen offset:2048 sc1\n"
    "v_mfma_f32_16x16x32_bf16 a[72:75], v[60:63], a[172:175], a[72:75]\n"
    "s_waitcnt vmcnt(14)\n"
    "v_mfma_f32_16x16x32_bf16 a[56:59], v[64:67], a[176:179], a[56:59]\n"
    "buffer_load_dwordx4 a[164:167], v1, s[24:27], s55 offen offset:3072 sc1\n"
    "v_mfma_f32_16x16x32_bf16 a[60:63], v[156:159], a[176:179], a[60:63]\n"
    "v_mfma_f32_16x16x32_bf16 a[64:67], v[160:163], a[176:179], a[64:67]\n"
    "v_mfma_f32_16x16x32_bf16 a[68:71], v[164:167], a[176:179], a[68:71]\n"
    "buffer_load_dwordx4 a[168:171], v57, s[24:27], s55 offen sc1\n"
    "v_mfma_f32_16x16x32_bf16 a[72:75], v[168:171], a[176:179], a[72:75]\n"
    "s_waitcnt vmcnt(15)\n"
    "v_mfma_f32_16x16x32_bf16 a[56:59], v[172:175], a[196:199], a[56:59]\n"
    "buffer_load_dwordx4 a[172:175], v57, s[24:27], s55 offen offset:1024 sc1\n"
    "v_mfma_f32_16x16x32_bf16 a[60:63], v[176:179], a[196:199], a[60:63]\n"
    "v_mfma_f32_16x16x32_bf16 a[64:67], v[180:183], a[196:199], a[64:67]\n"
    "v_mfma_f32_16x16x32_bf16 a[68:71], v[184:187], a[196:199], a[68:71]\n"
    "v_mfma_f32_16x16x32_bf16 a[72:75], v[188:191], a[196:199], a[72:75]\n"
    "s_waitcnt vmcnt(15)\n"
    "v_mfma_f32_16x16x32_bf16 a[56:59], v[192:195], a[200:203], a[56:59]\n"
    "buffer_load_dwordx4 a[176:179], v57, s[24:27], s55 offen offset:2048 sc1\n"
    "v_mfma_f32_16x16x32_bf16 a[60:63], v[196:199], a[200:203], a[60:63]\n"
    "v_mfma_f32_16x16x32_bf16 a[64:67], v[200:203], a[200:203], a[64:67]\n"
    "v_mfma_f32_16x16x32_bf16 a[68:71], v[204:207], a[200:203], a[68:71]\n"
    "v_mfma_f32_16x16x32_bf16 a[72:75], v[208:211], a[200:203], a[72:75]\n"
    "s_waitcnt vmcnt(15)\n"
    "v_mfma_f32_16x16x32_bf16 a[56:59], v[212:215], a[204:207], a[56:59]\n"
    "buffer_load_dwordx4 a[196:199], v57, s[24:27], s55 offen offset:3072 sc1\n"
    "v_mfma_f32_16x16x32_bf16 a[60:63], v[216:219], a[204:207], a[60:63]\n"
    "v_mfma_f32_16x16x32_bf16 a[64:67], v[220:223], a[204:207], a[64:67]\n"
    "v_mfma_f32_16x16x32_bf16 a[68:71], v[224:227], a[204:207], a[68:71]\n"
    "v_mfma_f32_16x16x32_bf16 a[72:75], a[240:243], a[204:207], a[72:75]\n"
    "s_waitcnt vmcnt(15)\n"
    "v_mfma_f32_16x16x32_bf16 a[56:59], a[112:115], a[208:211], a[56:59]\n"
    "v_mfma_f32_16x16x32_bf16 a[60:63], a[116:119], a[208:211], a[60:63]\n"
    "v_mfma_f32_16x16x32_bf16 a[64:67], a[120:123], a[208:211], a[64:67]\n"
    "v_mfma_f32_16x16x32_bf16 a[68:71], a[124:127], a[208:211], a[68:71]\n"
    "v_mfma_f32_16x16x32_bf16 a[72:75], a[132:135], a[208:211], a[72:75]\n"
    "s_waitcnt vmcnt(14)\n"
    "v_mfma_f32_16x16x32_bf16 a[56:59], a[140:143], a[212:215], a[56:59]\n"
    "v_mfma_f32_16x16x32_bf16 a[60:63], a[148:151], a[212:215], a[60:63]\n"
    "v_mfma_f32_16x16x32_bf16 a[64:67], a[4:7], a[212:215], a[64:67]\n"
    "v_mfma_f32_16x16x32_bf16 a[68:71], a[12:15], a[212:215], a[68:71]\n"
    "v_mfma_f32_16x16x32_bf16 a[72:75], a[20:23], a[212:215], a[72:75]\n"
    "s_waitcnt vmcnt(13)\n"
    "v_mfma_f32_16x16x32_bf16 a[56:59], a[28:31], v[108:111], a[56:59]\n"
    "v_mfma_f32_16x16x32_bf16 a[60:63], a[32:35], v[108:111], a[60:63]\n"
    "v_mfma_f32_16x16x32_bf16 a[64:67], a[228:231], v[108:111], a[64:67]\n"
    "v_mfma_f32_16x16x32_bf16 a[68:71], a[232:235], v[108:111], a[68:71]\n"
    "v_mfma_f32_16x16x32_bf16 a[72:75], a[236:239], v[108:111], a[72:75]\n"
    "s_waitcnt vmcnt(12)\n"
    "v_mfma_f32_16x16x32_bf16 a[56:59], a[244:247], v[112:115], a[56:59]\n"
    "v_mfma_f32_16x16x32_bf16 a[60:63], a[248:251], v[112:115], a[60:63]\n"
    "v_mfma_f32_16x16x32_bf16 a[64:67], a[252:255], v[112:115], a[64:67]\n"
    "v_mfma_f32_16x16x32_bf16 a[68:71], v[140:143], v[112:115], a[68:71]\n"
    "v_mfma_f32_16x16x32_bf16 a[72:75], v[144:147], v[112:115], a[72:75]\n"
    "s_waitcnt vmcnt(11)\n"
    "v_mfma_f32_16x16x32_bf16 a[76:79], a[128:131], a[92:95], 0\n"
    "v_mfma_f32_16x16x32_bf16 a[180:183], a[136:139], a[92:95], 0\n"
    "v_mfma_f32_16x16x32_bf16 a[184:187], a[144:147], a[92:95], 0\n"
    "v_mfma_f32_16x16x32_bf16 a[188:191], a[0:3], a[92:95], 0\n"
    "v_mfma_f32_16x16x32_bf16 a[192:195], a[8:11], a[92:95], 0\n"
    "ds_write_b128 v137, a[56:59]\n"
    "s_waitcnt vmcnt(10)\n"
    "v_mfma_f32_16x16x32_bf16 a[76:79], a[16:19], a[96:99], a[76:79]\n"
    "ds_write_b128 v137, a[60:63] offset:1024\n"
    "v_mfma_f32_16x16x32_bf16 a[180:183], a[24:27], a[96:99], a[180:183]\n"
    "ds_write_b128 v137, a[64:67] offset:2048\n"
    "v_mfma_f32_16x16x32_bf16 a[184:187], a[36:39], a[96:99], a[184:187]\n"
    "ds_write_b128 v137, a[68:71] offset:3072\n"
    "v_mfma_f32_16x16x32_bf16 a[188:191], a[40:43], a[96:99], a[188:191]\n"
    "ds_write_b128 v137, a[72:75] offset:4096\n"
    "v_mfma_f32_16x16x32_bf16 a[192:195], a[44:47], a[96:99], a[192:195]\n"
    "s_waitcnt vmcnt(9)\n"
    "v_mfma_f32_16x16x32_bf16 a[76:79], a[48:51], a[104:107], a[76:79]\n"
    "v_mfma_f32_16x16x32_bf16 a[180:183], a[52:55], a[104:107], a[180:183]\n"
    "v_mfma_f32_16x16x32_bf16 a[184:187], a[80:83], a[104:107], a[184:187]\n"
    "v_mfma_f32_16x16x32_bf16 a[188:191], a[84:87], a[104:107], a[188:191]\n"
    "v_mfma_f32_16x16x32_bf16 a[192:195], a[88:91], a[104:107], a[192:195]\n"
    "s_waitcnt lgkmcnt(0)\n"
    "s_barrier\n"
    "s_waitcnt vmcnt(8)\n"
    "v_mfma_f32_16x16x32_bf16 a[76:79], a[100:103], a[108:111], a[76:79]\n"
    "ds_read_b128 v[52:55], v139\n"
    "ds_read_b128 v[96:99], v139 offset:5120\n"
    "v_mfma_f32_16x16x32_bf16 a[180:183], a[216:219], a[108:111], a[180:183]\n"
    "ds_read_b128 v[68:71], v139 offset:1024\n"
    "ds_read_b128 v[100:103], v139 offset:6144\n"
    "v_mfma_f32_16x16x32_bf16 a[184:187], a[220:223], a[108:111], a[184:187]\n"
    "ds_read_b128 v[84:87], v139 offset:2048\n"
    "ds_read_b128 v[104:107], v139 offset:7168\n"
    "v_mfma_f32_16x16x32_bf16 a[188:191], a[224:227], a[108:111], a[188:191]\n"
    "ds_read_b128 v[88:91], v139 offset:3072\n"
    "ds_read_b128 v[116:119], v139 offset:8192\n"
    "v_mfma_f32_16x16x32_bf16 a[192:195], v[60:63], a[108:111], a[192:195]\n"
    "ds_read_b128 v[92:95], v139 offset:4096\n"
    "ds_read_b128 v[120:123], v139 offset:9216\n"
    "s_waitcnt vmcnt(7)\n"
    "v_mfma_f32_16x16x32_bf16 a[76:79], v[64:67], a[152:155], a[76:79]\n"
    "v_mfma_f32_16x16x32_bf16 a[180:183], v[156:159], a[152:155], a[180:183]\n"
    "v_mfma_f32_16x16x32_bf16 a[184:187], v[160:163], a[152:155], a[184:187]\n"
    "s_waitcnt lgkmcnt(9)\n"
    "v_add_f32 v32, v32, v52\n"
    "v_add_f32 v33, v33, v53\n"
    "v_mfma_f32_16x16x32_bf16 a[188:191], v[164:167], a[152:155], a[188:191]\n"
    "v_add_f32 v34, v34, v54\n"
    "v_add_f32 v35, v35, v55\n"
    "v_mfma_f32_16x16x32_bf16 a[192:195], v[168:171], a[152:155], a[192:195]\n"
    "s_waitcnt lgkmcnt(8)\n"
    "v_add_f32 v32, v32, v96\n"
    "v_add_f32 v33, v33, v97\n"
    "s_waitcnt vmcnt(6)\n"
    "v_mfma_f32_16x16x32_bf16 a[76:79], v[172:175], a[156:159], a[76:79]\n"
    "v_add_f32 v34, v34, v98\n"
    "v_add_f32 v35, v35, v99\n"
    "v_mfma_f32_16x16x32_bf16 a[180:183], v[176:179], a[156:159], a[180:183]\n"
    "s_waitcnt lgkmcnt(7)\n"
    "v_add_f32 v36, v36, v68\n"
    "v_add_f32 v37, v37, v69\n"
    "v_mfma_f32_16x16x32_bf16 a[184:187], v[180:183], a[156:159], a[184:187]\n"
    "v_add_f32 v38, v38, v70\n"
    "v_add_f32 v39, v39, v71\n"
    "v_mfma_f32_16x16x32_bf16 a[188:191], v[184:187], a[156:159], a[188:191]\n"
    "s_waitcnt lgkmcnt(6)\n"
    "v_add_f32 v36, v36, v100\n"
    "v_add_f32 v37, v37, v101\n"
    "v_mfma_f32_16x16x32_bf16 a[192:195], v[188:191], a[156:159], a[192:195]\n"
    "v_add_f32 v38, v38, v102\n"
    "v_add_f32 v39, v39, v103\n"
    "s_waitcnt vmcnt(5)\n"
    "v_mfma_f32_16x16x32_bf16 a[76:79], v[192:195], a[160:163], a[76:79]\n"
    "s_waitcnt lgkmcnt(5)\n"
    "v_add_f32 v40, v40, v84\n"
    "v_add_f32 v41, v41, v85\n"
    "v_mfma_f32_16x16x32_bf16 a[180:183], v[196:199], a[160:163], a[180:183]\n"
    "v_add_f32 v42, v42, v86\n"
    "v_add_f32 v43, v43, v87\n"
    "v_mfma_f32_16x16x32_bf16 a[184:187], v[200:203], a[160:163], a[184:187]\n"
    "s_waitcnt lgkmcnt(4)\n"
    "v_add_f32 v40, v40, v104\n"
    "v_add_f32 v41, v41, v105\n"
    "v_mfma_f32_16x16x32_bf16 a[188:191], v[204:207], a[160:163], a[188:191]\n"
    "v_add_f32 v42, v42, v106\n"
    "v_add_f32 v43, v43, v107\n"
    "v_mfma_f32_16x16x32_bf16 a[192:195], v[208:211], a[160:163], a[192:195]\n"
    "s_waitcnt lgkmcnt(3)\n"
    "v_add_f32 v44, v44, v88\n"
    "v_add_f32 v45, v45, v89\n"
    "s_waitcnt vmcnt(4)\n"
    "v_mfma_f32_16x16x32_bf16 a[76:79], v[212:215], a[164:167], a[76:79]\n"
    "v_add_f32 v46, v46, v90\n"
    "v_add_f32 v47, v47, v91\n"
    "v_mfma_f32_16x16x32_bf16 a[180:183], v[216:219], a[164:167], a[180:183]\n"
    "s_waitcnt lgkmcnt(2)\n"
    "v_add_f32 v44, v44, v116\n"
    "v_add_f32 v45, v45, v117\n"
    "v_mfma_f32_16x16x32_bf16 a[184:187], v[220:223], a[164:167], a[184:187]\n"
    "v_add_f32 v46, v46, v118\n"
    "v_add_f32 v47, v47, v119\n"
    "v_mfma_f32_16x16x32_bf16 a[188:191], v[224:227], a[164:167], a[188:191]\n"
    "s_waitcnt lgkmcnt(1)\n"
    "v_add_f32 v48, v48, v92\n"
    "v_add_f32 v49, v49, v93\n"
    "v_mfma_f32_16x16x32_bf16 a[192:195], a[240:243], a[164:167], a[192:195]\n"
    "v_add_f32 v50, v50, v94\n"
    "v_add_f32 v51, v51, v95\n"
    "s_waitcnt vmcnt(3)\n"
    "v_mfma_f32_16x16x32_bf16 a[76:79], a[112:115], a[168:171], a[76:79]\n"
    "s_waitcnt lgkmcnt(0)\n"
    "v_add_f32 v48, v48, v120\n"
    "v_add_f32 v49, v49, v121\n"
    "v_mfma_f32_16x16x32_bf16 a[180:183], a[116:119], a[168:171], a[180:183]\n"
    "v_add_f32 v50, v50, v122\n"
    "v_add_f32 v51, v51, v123\n"
    "v_mfma_f32_16x16x32_bf16 a[184:187], a[120:123], a[168:171], a[184:187]\n"
    "v_mfma_f32_16x16x32_bf16 a[188:191], a[124:127], a[168:171], a[188:191]\n"
    "v_mfma_f32_16x16x32_bf16 a[192:195], a[132:135], a[168:171], a[192:195]\n"
    "s_waitcnt vmcnt(2)\n"
    "v_mfma_f32_16x16x32_bf16 a[76:79], a[140:143], a[172:175], a[76:79]\n"
    "v_mfma_f32_16x16x32_bf16 a[180:183], a[148:151], a[172:175], a[180:183]\n"
    "v_mfma_f32_16x16x32_bf16 a[184:187], a[4:7], a[172:175], a[184:187]\n"
    "v_mfma_f32_16x16x32_bf16 a[188:191], a[12:15], a[172:175], a[188:191]\n"
    "v_mfma_f32_16x16x32_bf16 a[192:195], a[20:23], a[172:175], a[192:195]\n"
    "s_waitcnt vmcnt(1)\n"
    "v_mfma_f32_16x16x32_bf16 a[76:79], a[28:31], a[176:179], a[76:79]\n"
    "v_mfma_f32_16x16x32_bf16 a[180:183], a[32:35], a[176:179], a[180:183]\n"
    "v_mfma_f32_16x16x32_bf16 a[184:187], a[228:231], a[176:179], a[184:187]\n"
    "v_mfma_f32_16x16x32_bf16 a[188:191], a[232:235], a[176:179], a[188:191]\n"
    "v_mfma_f32_16x16x32_bf16 a[192:195], a[236:239], a[176:179], a[192:195]\n"
    "s_waitcnt vmcnt(0)\n"
    "v_mfma_f32_16x16x32_bf16 a[76:79], a[244:247], a[196:199], a[76:79]\n"
    "v_mfma_f32_16x16x32_bf16 a[180:183], a[248:251], a[196:199], a[180:183]\n"
    "v_mfma_f32_16x16x32_bf16 a[184:187], a[252:255], a[196:199], a[184:187]\n"
    "v_mfma_f32_16x16x32_bf16 a[188:191], v[140:143], a[196:199], a[188:191]\n"
    "v_mfma_f32_16x16x32_bf16 a[192:195], v[144:147], a[196:199], a[192:195]\n"
    "s_nop 3\n"
    "ds_write_b128 v138, a[76:79]\n"
    "ds_write_b128 v138, a[180:183] offset:1024\n"
    "ds_write_b128 v138, a[184:187] offset:2048\n"
    "ds_write_b128 v138, a[188:191] offset:3072\n"
    "ds_write_b128 v138, a[192:195] offset:4096\n"
    "s_waitcnt lgkmcnt(0)\n"
    "s_barrier\n"
    "ds_read_b128 v[52:55], v139 offset:10240\n"
    "ds_read_b128 v[96:99], v139 offset:15360\n"
    "ds_read_b128 v[68:71], v139 offset:11264\n"
    "ds_read_b128 v[100:103], v139 offset:16384\n"
    "ds_read_b128 v[84:87], v139 offset:12288\n"
    "ds_read_b128 v[104:107], v139 offset:17408\n"
    "ds_read_b128 v[88:91], v139 offset:13312\n"
    "ds_read_b128 v[116:119], v139 offset:18432\n"
    "ds_read_b128 v[92:95], v139 offset:14336\n"
    "ds_read_b128 v[120:123], v139 offset:19456\n"
    "s_waitcnt lgkmcnt(9)\n"
    "v_pk_add_f32 v[32:33], v[32:33], v[52:53]\n"
    "v_pk_add_f32 v[34:35], v[34:35], v[54:55]\n"
    "s_waitcnt lgkmcnt(8)\n"
    "v_pk_add_f32 v[32:33], v[32:33], v[96:97]\n"
    "v_pk_add_f32 v[34:35], v[34:35], v[98:99]\n"
    "v_cvt_pk_bf16_f32 v72, v32, v33\n"
    "v_cvt_pk_bf16_f32 v73, v34, v35\n"
    "s_waitcnt lgkmcnt(7)\n"
    "v_pk_add_f32 v[36:37], v[36:37], v[68:69]\n"
    "v_pk_add_f32 v[38:39], v[38:39], v[70:71]\n"
    "s_waitcnt lgkmcnt(6)\n"
    "v_pk_add_f32 v[36:37], v[36:37], v[100:101]\n"
    "v_pk_add_f32 v[38:39], v[38:39], v[102:103]\n"
    "v_cvt_pk_bf16_f32 v74, v36, v37\n"
    "v_cvt_pk_bf16_f32 v75, v38, v39\n"
    "buffer_store_dwordx4 v[72:75], v9, s[44:47], 0 offen\n"
    "s_waitcnt lgkmcnt(5)\n"
    "v_pk_add_f32 v[40:41], v[40:41], v[84:85]\n"
    "v_pk_add_f32 v[42:43], v[42:43], v[86:87]\n"
    "s_waitcnt lgkmcnt(4)\n"
    "v_pk_add_f32 v[40:41], v[40:41], v[104:105]\n"
    "v_pk_add_f32 v[42:43], v[42:43], v[106:107]\n"
    "v_cvt_pk_bf16_f32 v76, v40, v41\n"
    "v_cvt_pk_bf16_f32 v77, v42, v43\n"
    "s_waitcnt lgkmcnt(3)\n"
    "v_pk_add_f32 v[44:45], v[44:45], v[88:89]\n"
    "v_pk_add_f32 v[46:47], v[46:47], v[90:91]\n"
    "s_waitcnt lgkmcnt(2)\n"
    "v_pk_add_f32 v[44:45], v[44:45], v[116:117]\n"
    "v_pk_add_f32 v[46:47], v[46:47], v[118:119]\n"
    "v_cvt_pk_bf16_f32 v78, v44, v45\n"
    "v_cvt_pk_bf16_f32 v79, v46, v47\n"
    "buffer_store_dwordx4 v[76:79], v9, s[44:47], 0 offen offset:64\n"
    "s_waitcnt lgkmcnt(1)\n"
    "v_pk_add_f32 v[48:49], v[48:49], v[92:93]\n"
    "v_pk_add_f32 v[50:51], v[50:51], v[94:95]\n"
    "s_waitcnt lgkmcnt(0)\n"
    "v_pk_add_f32 v[48:49], v[48:49], v[120:121]\n"
    "v_pk_add_f32 v[50:51], v[50:51], v[122:123]\n"
    "v_cvt_pk_bf16_f32 v80, v48, v49\n"
    "v_cvt_pk_bf16_f32 v81, v50, v51\n"
    "buffer_store_dwordx2 v[80:81], v10, s[44:47], 0 offen\n"
    ".Lfb_end:\n"
    "s_branch .Lfb_ool_exit\n"
    ".Lfb_ool_in_20:\n"
    "s_waitcnt vmcnt(0) lgkmcnt(0)\n"
    "s_nop 15\n"
    "v_mfma_f32_32x32x16_bf16 v[120:135], a[32:35], v[32:35], 0\n"
    "v_mfma_f32_32x32x16_bf16 v[104:119], a[0:3], v[32:35], 0\n"
    "v_mfma_f32_32x32x16_bf16 v[120:135], a[56:59], v[56:59], v[120:135]\n"
    "v_mfma_f32_32x32x16_bf16 v[104:119], a[40:43], v[56:59], v[104:119]\n"
    "s_cmp_ge_i32 s14, 8\n"
    "s_cbranch_scc1 .Lfb_fv_48\n"
    "s_nop 9\n"
    "v_mov_b32 v104, 0xff61b1e6\n"
    "v_mov_b32 v105, 0xff61b1e6\n"
    "v_mov_b32 v106, 0xff61b1e6\n"
    "v_mov_b32 v107, 0xff61b1e6\n"
    "v_mov_b32 v108, 0xff61b1e6\n"
    "v_mov_b32 v109, 0xff61b1e6\n"
    "v_mov_b32 v110, 0xff61b1e6\n"
    "v_mov_b32 v111, 0xff61b1e6\n"
    "v_mov_b32 v112, 0xff61b1e6\n"
    "v_mov_b32 v113, 0xff61b1e6\n"
    "v_mov_b32 v114, 0xff61b1e6\n"
    "v_mov_b32 v115, 0xff61b1e6\n"
    "v_mov_b32 v116, 0xff61b1e6\n"
    "v_mov_b32 v117, 0xff61b1e6\n"
    "v_mov_b32 v118, 0xff61b1e6\n"
    "v_mov_b32 v119, 0xff61b1e6\n"
    ".Lfb_fv_48:\n"
    "s_nop 9\n"
    "v_cndmask_b32_e64 v120, v120, v104, s[64:65]\n"
    "v_cndmask_b32_e64 v121, v121, v105, s[66:67]\n"
    "v_cndmask_b32_e64 v122, v122, v106, s[68:69]\n"
    "v_cndmask_b32_e64 v123, v123, v107, s[70:71]\n"
    "v_cndmask_b32_e64 v124, v124, v108, s[72:73]\n"
    "v_cndmask_b32_e64 v125, v125, v109, s[74:75]\n"
    "v_cndmask_b32_e64 v126, v126, v110, s[76:77]\n"
    "v_cndmask_b32_e64 v127, v127, v111, s[78:79]\n"
    "v_cndmask_b32_e64 v128, v128, v112, s[80:81]\n"
    "v_cndmask_b32_e64 v129, v129, v113, s[82:83]\n"
    "v_cndmask_b32_e64 v130, v130, v114, s[84:85]\n"
    "v_cndmask_b32_e64 v131, v131, v115, s[86:87]\n"
    "v_cndmask_b32_e64 v132, v132, v116, s[88:89]\n"
    "v_cndmask_b32_e64 v133, v133, v117, s[90:91]\n"
    "v_cndmask_b32_e64 v134, v134, v118, s[92:93]\n"
    "v_cndmask_b32_e64 v135, v135, v119, s[94:95]\n"
    "v_max3_f32 v192, v120, v121, v122\n"
    "v_max3_f32 v192, v192, v122, v123\n"
    "v_max3_f32 v192, v192, v124, v125\n"
    "v_max3_f32 v192, v192, v126, v127\n"
    "v_max3_f32 v192, v192, v128, v129\n"
    "v_max3_f32 v192, v192, v130, v131\n"
    "v_max3_f32 v192, v192, v132, v133\n"
    "v_max3_f32 v192, v192, v134, v135\n"
    "v_mfma_f32_32x32x16_bf16 v[72:87], a[4:7], v[32:35], 0\n"
    "v_mfma_f32_32x32x16_bf16 v[72:87], a[40:43], v[60:63], v[72:87]\n"
    "s_nop 11\n"
    "v_max3_f32 v193, v72, v73, v74\n"
    "v_max3_f32 v193, v193, v74, v75\n"
    "v_max3_f32 v193, v193, v76, v77\n"
    "v_max3_f32 v193, v193, v78, v79\n"
    "v_max3_f32 v193, v193, v80, v81\n"
    "v_max3_f32 v193, v193, v82, v83\n"
    "v_max3_f32 v193, v193, v84, v85\n"
    "v_max3_f32 v193, v193, v86, v87\n"
    "v_max_f32 v193, v193, v192\n"
    "s_cmp_ge_i32 s14, 7\n"
    "s_cselect_b64 vcc, -1, 0\n"
    "v_cndmask_b32 v192, v192, v193, vcc\n"
    "v_mfma_f32_32x32x16_bf16 v[72:87], a[8:11], v[32:35], 0\n"
    "v_mfma_f32_32x32x16_bf16 v[72:87], a[44:47], v[56:59], v[72:87]\n"
    "s_nop 11\n"
    "v_max3_f32 v193, v72, v73, v74\n"
    "v_max3_f32 v193, v193, v74, v75\n"
    "v_max3_f32 v193, v193, v76, v77\n"
    "v_max3_f32 v193, v193, v78, v79\n"
    "v_max3_f32 v193, v193, v80, v81\n"
    "v_max3_f32 v193, v193, v82, v83\n"
    "v_max3_f32 v193, v193, v84, v85\n"
    "v_max3_f32 v193, v193, v86, v87\n"
    "v_max_f32 v193, v193, v192\n"
    "s_cmp_ge_i32 s14, 6\n"
    "s_cselect_b64 vcc, -1, 0\n"
    "v_cndmask_b32 v192, v192, v193, vcc\n"
    "v_mfma_f32_32x32x16_bf16 v[72:87], a[12:15], v[32:35], 0\n"
    "v_mfma_f32_32x32x16_bf16 v[72:87], a[44:47], v[60:63], v[72:87]\n"
    "s_nop 11\n"
    "v_max3_f32 v193, v72, v73, v74\n"
    "v_max3_f32 v193, v193, v74, v75\n"
    "v_max3_f32 v193, v193, v76, v77\n"
    "v_max3_f32 v193, v193, v78, v79\n"
    "v_max3_f32 v193, v193, v80, v81\n"
    "v_max3_f32 v193, v193, v82, v83\n"
    "v_max3_f32 v193, v193, v84, v85\n"
    "v_max3_f32 v193, v193, v86, v87\n"
    "v_max_f32 v193, v193, v192\n"
    "s_cmp_ge_i32 s14, 5\n"
    "s_cselect_b64 vcc, -1, 0\n"
    "v_cndmask_b32 v192, v192, v193, vcc\n"
    "v_mfma_f32_32x32x16_bf16 v[72:87], a[16:19], v[32:35], 0\n"
    "v_mfma_f32_32x32x16_bf16 v[72:87], a[48:51], v[56:59], v[72:87]\n"
    "s_nop 11\n"
    "v_max3_f32 v193, v72, v73, v74\n"
    "v_max3_f32 v193, v193, v74, v75\n"
    "v_max3_f32 v193, v193, v76, v77\n"
    "v_max3_f32 v193, v193, v78, v79\n"
    "v_max3_f32 v193, v193, v80, v81\n"
    "v_max3_f32 v193, v193, v82, v83\n"
    "v_max3_f32 v193, v193, v84, v85\n"
    "v_max3_f32 v193, v193, v86, v87\n"
    "v_max_f32 v193, v193, v192\n"
    "s_cmp_ge_i32 s14, 4\n"
    "s_cselect_b64 vcc, -1, 0\n"
    "v_cndmask_b32 v192, v192, v193, vcc\n"
    "v_mfma_f32_32x32x16_bf16 v[72:87], a[20:23], v[32:35], 0\n"
    "v_mfma_f32_32x32x16_bf16 v[72:87], a[48:51], v[60:63], v[72:87]\n"
    "s_nop 11\n"
    "v_max3_f32 v193, v72, v73, v74\n"
    "v_max3_f32 v193, v193, v74, v75\n"
    "v_max3_f32 v193, v193, v76, v77\n"
    "v_max3_f32 v193, v193, v78, v79\n"
    "v_max3_f32 v193, v193, v80, v81\n"
    "v_max3_f32 v193, v193, v82, v83\n"
    "v_max3_f32 v193, v193, v84, v85\n"
    "v_max3_f32 v193, v193, v86, v87\n"
    "v_max_f32 v193, v193, v192\n"
    "s_cmp_ge_i32 s14, 3\n"
    "s_cselect_b64 vcc, -1, 0\n"
    "v_cndmask_b32 v192, v192, v193, vcc\n"
    "v_mfma_f32_32x32x16_bf16 v[72:87], a[24:27], v[32:35], 0\n"
    "v_mfma_f32_32x32x16_bf16 v[72:87], a[52:55], v[56:59], v[72:87]\n"
    "s_nop 11\n"
    "v_max3_f32 v193, v72, v73, v74\n"
    "v_max3_f32 v193, v193, v74, v75\n"
    "v_max3_f32 v193, v193, v76, v77\n"
    "v_max3_f32 v193, v193, v78, v79\n"
    "v_max3_f32 v193, v193, v80, v81\n"
    "v_max3_f32 v193, v193, v82, v83\n"
    "v_max3_f32 v193, v193, v84, v85\n"
    "v_max3_f32 v193, v193, v86, v87\n"
    "v_max_f32 v193, v193, v192\n"
    "s_cmp_ge_i32 s14, 2\n"
    "s_cselect_b64 vcc, -1, 0\n"
    "v_cndmask_b32 v192, v192, v193, vcc\n"
    "v_mfma_f32_32x32x16_bf16 v[72:87], a[28:31], v[32:35], 0\n"
    "v_mfma_f32_32x32x16_bf16 v[72:87], a[52:55], v[60:63], v[72:87]\n"
    "s_nop 11\n"
    "v_max3_f32 v193, v72, v73, v74\n"
    "v_max3_f32 v193, v193, v74, v75\n"
    "v_max3_f32 v193, v193, v76, v77\n"
    "v_max3_f32 v193, v193, v78, v79\n"
    "v_max3_f32 v193, v193, v80, v81\n"
    "v_max3_f32 v193, v193, v82, v83\n"
    "v_max3_f32 v193, v193, v84, v85\n"
    "v_max3_f32 v193, v193, v86, v87\n"
    "v_max_f32 v193, v193, v192\n"
    "s_cmp_ge_i32 s14, 1\n"
    "s_cselect_b64 vcc, -1, 0\n"
    "v_cndmask_b32 v192, v192, v193, vcc\n"
    "v_mov_b32 v193, v192\n"
    "s_nop 1\n"
    "v_permlane32_swap_b32 v192, v193\n"
    "v_max_f32 v192, v192, v193\n"
    "v_sub_f32 v136, 0, v192\n"
    "v_sub_f32 v137, 0, v192\n"
    "v_sub_f32 v138, 0, v192\n"
    "v_sub_f32 v139, 0, v192\n"
    "v_sub_f32 v140, 0, v192\n"
    "v_sub_f32 v141, 0, v192\n"
    "v_sub_f32 v142, 0, v192\n"
    "v_sub_f32 v143, 0, v192\n"
    "v_sub_f32 v144, 0, v192\n"
    "v_sub_f32 v145, 0, v192\n"
    "v_sub_f32 v146, 0, v192\n"
    "v_sub_f32 v147, 0, v192\n"
    "v_sub_f32 v148, 0, v192\n"
    "v_sub_f32 v149, 0, v192\n"
    "v_sub_f32 v150, 0, v192\n"
    "v_sub_f32 v151, 0, v192\n"
    "v_add_f32 v120, v120, v136\n"
    "v_add_f32 v121, v121, v136\n"
    "v_add_f32 v122, v122, v136\n"
    "v_add_f32 v123, v123, v136\n"
    "v_exp_f32 v196, v120\n"
    "v_exp_f32 v197, v121\n"
    "v_exp_f32 v198, v122\n"
    "v_exp_f32 v199, v123\n"
    "v_cvt_pk_bf16_f32 v196, v196, v197\n"
    "v_cvt_pk_bf16_f32 v198, v198, v199\n"
    "v_and_b32 v168, v40, v196\n"
    "v_xor_b32 v184, v168, v196\n"
    "v_and_b32 v169, v41, v198\n"
    "v_xor_b32 v185, v169, v198\n"
    "v_add_f32 v124, v124, v136\n"
    "v_add_f32 v125, v125, v136\n"
    "v_add_f32 v126, v126, v136\n"
    "v_add_f32 v127, v127, v136\n"
    "v_exp_f32 v196, v124\n"
    "v_exp_f32 v197, v125\n"
    "v_exp_f32 v198, v126\n"
    "v_exp_f32 v199, v127\n"
    "v_cvt_pk_bf16_f32 v196, v196, v197\n"
    "v_cvt_pk_bf16_f32 v198, v198, v199\n"
    "v_and_b32 v170, v42, v196\n"
    "v_xor_b32 v186, v170, v196\n"
    "v_and_b32 v171, v43, v198\n"
    "v_xor_b32 v187, v171, v198\n"
    "v_add_f32 v128, v128, v136\n"
    "v_add_f32 v129, v129, v136\n"
    "v_add_f32 v130, v130, v136\n"
    "v_add_f32 v131, v131, v136\n"
    "v_exp_f32 v196, v128\n"
    "v_exp_f32 v197, v129\n"
    "v_exp_f32 v198, v130\n"
    "v_exp_f32 v199, v131\n"
    "v_cvt_pk_bf16_f32 v196, v196, v197\n"
    "v_cvt_pk_bf16_f32 v198, v198, v199\n"
    "v_and_b32 v172, v44, v196\n"
    "v_xor_b32 v188, v172, v196\n"
    "v_and_b32 v173, v45, v198\n"
    "v_xor_b32 v189, v173, v198\n"
    "v_add_f32 v132, v132, v136\n"
    "v_add_f32 v133, v133, v136\n"
    "v_add_f32 v134, v134, v136\n"
    "v_add_f32 v135, v135, v136\n"
    "v_exp_f32 v196, v132\n"
    "v_exp_f32 v197, v133\n"
    "v_exp_f32 v198, v134\n"
    "v_exp_f32 v199, v135\n"
    "v_cvt_pk_bf16_f32 v196, v196, v197\n"
    "v_cvt_pk_bf16_f32 v198, v198, v199\n"
    "v_and_b32 v174, v46, v196\n"
    "v_xor_b32 v190, v174, v196\n"
    "v_and_b32 v175, v47, v198\n"
    "v_xor_b32 v191, v175, v198\n"
    "v_mfma_f32_32x32x16_bf16 v[152:167], a[60:63], v[168:171], 0\n"
    "v_mfma_f32_32x32x16_bf16 v[152:167], a[64:67], v[172:175], v[152:167]\n"
    "v_mfma_f32_32x32x16_bf16 v[152:167], a[124:127], v[184:187], v[152:167]\n"
    "v_mfma_f32_32x32x16_bf16 v[152:167], a[128:131], v[188:191], v[152:167]\n"
    "v_mfma_f32_32x32x16_bf16 v[72:87], a[4:7], v[32:35], v[136:151]\n"
    "v_mfma_f32_32x32x16_bf16 v[72:87], a[40:43], v[60:63], v[72:87]\n"
    "s_nop 11\n"
    "v_exp_f32_e64 v72, v72 clamp\n"
    "v_exp_f32_e64 v73, v73 clamp\n"
    "v_exp_f32_e64 v74, v74 clamp\n"
    "v_exp_f32_e64 v75, v75 clamp\n"
    "v_exp_f32_e64 v76, v76 clamp\n"
    "v_exp_f32_e64 v77, v77 clamp\n"
    "v_exp_f32_e64 v78, v78 clamp\n"
    "v_exp_f32_e64 v79, v79 clamp\n"
    "v_exp_f32_e64 v80, v80 clamp\n"
    "v_exp_f32_e64 v81, v81 clamp\n"
    "v_exp_f32_e64 v82, v82 clamp\n"
    "v_exp_f32_e64 v83, v83 clamp\n"
    "v_exp_f32_e64 v84, v84 clamp\n"
    "v_exp_f32_e64 v85, v85 clamp\n"
    "v_exp_f32_e64 v86, v86 clamp\n"
    "v_exp_f32_e64 v87, v87 clamp\n"
    "v_cvt_pk_bf16_f32 v176, v72, v73\n"
    "v_cvt_pk_bf16_f32 v177, v74, v75\n"
    "v_cvt_pk_bf16_f32 v178, v76, v77\n"
    "v_cvt_pk_bf16_f32 v179, v78, v79\n"
    "v_cvt_pk_bf16_f32 v180, v80, v81\n"
    "v_cvt_pk_bf16_f32 v181, v82, v83\n"
    "v_cvt_pk_bf16_f32 v182, v84, v85\n"
    "v_cvt_pk_bf16_f32 v183, v86, v87\n"
    "v_mfma_f32_32x32x16_bf16 v[152:167], a[68:71], v[176:179], v[152:167]\n"
    "s_nop 0\n"
    "v_mfma_f32_32x32x16_bf16 v[152:167], a[72:75], v[180:183], v[152:167]\n"
    "v_mfma_f32_32x32x16_bf16 v[72:87], a[8:11], v[32:35], v[136:151]\n"
    "v_mfma_f32_32x32x16_bf16 v[72:87], a[44:47], v[56:59], v[72:87]\n"
    "s_nop 11\n"
    "v_exp_f32_e64 v72, v72 clamp\n"
    "v_exp_f32_e64 v73, v73 clamp\n"
    "v_exp_f32_e64 v74, v74 clamp\n"
    "v_exp_f32_e64 v75, v75 clamp\n"
    "v_exp_f32_e64 v76, v76 clamp\n"
    "v_exp_f32_e64 v77, v77 clamp\n"
    "v_exp_f32_e64 v78, v78 clamp\n"
    "v_exp_f32_e64 v79, v79 clamp\n"
    "v_exp_f32_e64 v80, v80 clamp\n"
    "v_exp_f32_e64 v81, v81 clamp\n"
    "v_exp_f32_e64 v82, v82 clamp\n"
    "v_exp_f32_e64 v83, v83 clamp\n"
    "v_exp_f32_e64 v84, v84 clamp\n"
    "v_exp_f32_e64 v85, v85 clamp\n"
    "v_exp_f32_e64 v86, v86 clamp\n"
    "v_exp_f32_e64 v87, v87 clamp\n"
    "v_cvt_pk_bf16_f32 v176, v72, v73\n"
    "v_cvt_pk_bf16_f32 v177, v74, v75\n"
    "v_cvt_pk_bf16_f32 v178, v76, v77\n"
    "v_cvt_pk_bf16_f32 v179, v78, v79\n"
    "v_cvt_pk_bf16_f32 v180, v80, v81\n"
    "v_cvt_pk_bf16_f32 v181, v82, v83\n"
    "v_cvt_pk_bf16_f32 v182, v84, v85\n"
    "v_cvt_pk_bf16_f32 v183, v86, v87\n"
    "v_mfma_f32_32x32x16_bf16 v[152:167], a[76:79], v[176:179], v[152:167]\n"
    "s_nop 0\n"
    "v_mfma_f32_32x32x16_bf16 v[152:167], a[80:83], v[180:183], v[152:167]\n"
    "v_mfma_f32_32x32x16_bf16 v[72:87], a[12:15], v[32:35], v[136:151]\n"
    "v_mfma_f32_32x32x16_bf16 v[72:87], a[44:47], v[60:63], v[72:87]\n"
    "s_nop 11\n"
    "v_exp_f32_e64 v72, v72 clamp\n"
    "v_exp_f32_e64 v73, v73 clamp\n"
    "v_exp_f32_e64 v74, v74 clamp\n"
    "v_exp_f32_e64 v75, v75 clamp\n"
    "v_exp_f32_e64 v76, v76 clamp\n"
    "v_exp_f32_e64 v77, v77 clamp\n"
    "v_exp_f32_e64 v78, v78 clamp\n"
    "v_exp_f32_e64 v79, v79 clamp\n"
    "v_exp_f32_e64 v80, v80 clamp\n"
    "v_exp_f32_e64 v81, v81 clamp\n"
    "v_exp_f32_e64 v82, v82 clamp\n"
    "v_exp_f32_e64 v83, v83 clamp\n"
    "v_exp_f32_e64 v84, v84 clamp\n"
    "v_exp_f32_e64 v85, v85 clamp\n"
    "v_exp_f32_e64 v86, v86 clamp\n"
    "v_exp_f32_e64 v87, v87 clamp\n"
    "v_cvt_pk_bf16_f32 v176, v72, v73\n"
    "v_cvt_pk_bf16_f32 v177, v74, v75\n"
    "v_cvt_pk_bf16_f32 v178, v76, v77\n"
    "v_cvt_pk_bf16_f32 v179, v78, v79\n"
    "v_cvt_pk_bf16_f32 v180, v80, v81\n"
    "v_cvt_pk_bf16_f32 v181, v82, v83\n"
    "v_cvt_pk_bf16_f32 v182, v84, v85\n"
    "v_cvt_pk_bf16_f32 v183, v86, v87\n"
    "v_mfma_f32_32x32x16_bf16 v[152:167], a[84:87], v[176:179], v[152:167]\n"
    "s_nop 0\n"
    "v_mfma_f32_32x32x16_bf16 v[152:167], a[88:91], v[180:183], v[152:167]\n"
    "v_mfma_f32_32x32x16_bf16 v[72:87], a[16:19], v[32:35], v[136:151]\n"
    "v_mfma_f32_32x32x16_bf16 v[72:87], a[48:51], v[56:59], v[72:87]\n"
    "s_nop 11\n"
    "v_exp_f32_e64 v72, v72 clamp\n"
    "v_exp_f32_e64 v73, v73 clamp\n"
    "v_exp_f32_e64 v74, v74 clamp\n"
    "v_exp_f32_e64 v75, v75 clamp\n"
    "v_exp_f32_e64 v76, v76 clamp\n"
    "v_exp_f32_e64 v77, v77 clamp\n"
    "v_exp_f32_e64 v78, v78 clamp\n"
    "v_exp_f32_e64 v79, v79 clamp\n"
    "v_exp_f32_e64 v80, v80 clamp\n"
    "v_exp_f32_e64 v81, v81 clamp\n"
    "v_exp_f32_e64 v82, v82 clamp\n"
    "v_exp_f32_e64 v83, v83 clamp\n"
    "v_exp_f32_e64 v84, v84 clamp\n"
    "v_exp_f32_e64 v85, v85 clamp\n"
    "v_exp_f32_e64 v86, v86 clamp\n"
    "v_exp_f32_e64 v87, v87 clamp\n"
    "v_cvt_pk_bf16_f32 v176, v72, v73\n"
    "v_cvt_pk_bf16_f32 v177, v74, v75\n"
    "v_cvt_pk_bf16_f32 v178, v76, v77\n"
    "v_cvt_pk_bf16_f32 v179, v78, v79\n"
    "v_cvt_pk_bf16_f32 v180, v80, v81\n"
    "v_cvt_pk_bf16_f32 v181, v82, v83\n"
    "v_cvt_pk_bf16_f32 v182, v84, v85\n"
    "v_cvt_pk_bf16_f32 v183, v86, v87\n"
    "v_mfma_f32_32x32x16_bf16 v[152:167], a[92:95], v[176:179], v[152:167]\n"
    "s_nop 0\n"
    "v_mfma_f32_32x32x16_bf16 v[152:167], a[96:99], v[180:183], v[152:167]\n"
    "v_mfma_f32_32x32x16_bf16 v[72:87], a[20:23], v[32:35], v[136:151]\n"
    "v_mfma_f32_32x32x16_bf16 v[72:87], a[48:51], v[60:63], v[72:87]\n"
    "s_nop 11\n"
    "v_exp_f32_e64 v72, v72 clamp\n"
    "v_exp_f32_e64 v73, v73 clamp\n"
    "v_exp_f32_e64 v74, v74 clamp\n"
    "v_exp_f32_e64 v75, v75 clamp\n"
    "v_exp_f32_e64 v76, v76 clamp\n"
    "v_exp_f32_e64 v77, v77 clamp\n"
    "v_exp_f32_e64 v78, v78 clamp\n"
    "v_exp_f32_e64 v79, v79 clamp\n"
    "v_exp_f32_e64 v80, v80 clamp\n"
    "v_exp_f32_e64 v81, v81 clamp\n"
    "v_exp_f32_e64 v82, v82 clamp\n"
    "v_exp_f32_e64 v83, v83 clamp\n"
    "v_exp_f32_e64 v84, v84 clamp\n"
    "v_exp_f32_e64 v85, v85 clamp\n"
    "v_exp_f32_e64 v86, v86 clamp\n"
    "v_exp_f32_e64 v87, v87 clamp\n"
    "v_cvt_pk_bf16_f32 v176, v72, v73\n"
    "v_cvt_pk_bf16_f32 v177, v74, v75\n"
    "v_cvt_pk_bf16_f32 v178, v76, v77\n"
    "v_cvt_pk_bf16_f32 v179, v78, v79\n"
    "v_cvt_pk_bf16_f32 v180, v80, v81\n"
    "v_cvt_pk_bf16_f32 v181, v82, v83\n"
    "v_cvt_pk_bf16_f32 v182, v84, v85\n"
    "v_cvt_pk_bf16_f32 v183, v86, v87\n"
    "v_mfma_f32_32x32x16_bf16 v[152:167], a[100:103], v[176:179], v[152:167]\n"
    "s_nop 0\n"
    "v_mfma_f32_32x32x16_bf16 v[152:167], a[104:107], v[180:183], v[152:167]\n"
    "v_mfma_f32_32x32x16_bf16 v[72:87], a[24:27], v[32:35], v[136:151]\n"
    "v_mfma_f32_32x32x16_bf16 v[72:87], a[52:55], v[56:59], v[72:87]\n"
    "s_nop 11\n"
    "v_exp_f32_e64 v72, v72 clamp\n"
    "v_exp_f32_e64 v73, v73 clamp\n"
    "v_exp_f32_e64 v74, v74 clamp\n"
    "v_exp_f32_e64 v75, v75 clamp\n"
    "v_exp_f32_e64 v76, v76 clamp\n"
    "v_exp_f32_e64 v77, v77 clamp\n"
    "v_exp_f32_e64 v78, v78 clamp\n"
    "v_exp_f32_e64 v79, v79 clamp\n"
    "v_exp_f32_e64 v80, v80 clamp\n"
    "v_exp_f32_e64 v81, v81 clamp\n"
    "v_exp_f32_e64 v82, v82 clamp\n"
    "v_exp_f32_e64 v83, v83 clamp\n"
    "v_exp_f32_e64 v84, v84 clamp\n"
    "v_exp_f32_e64 v85, v85 clamp\n"
    "v_exp_f32_e64 v86, v86 clamp\n"
    "v_exp_f32_e64 v87, v87 clamp\n"
    "v_cvt_pk_bf16_f32 v176, v72, v73\n"
    "v_cvt_pk_bf16_f32 v177, v74, v75\n"
    "v_cvt_pk_bf16_f32 v178, v76, v77\n"
    "v_cvt_pk_bf16_f32 v179, v78, v79\n"
    "v_cvt_pk_bf16_f32 v180, v80, v81\n"
    "v_cvt_pk_bf16_f32 v181, v82, v83\n"
    "v_cvt_pk_bf16_f32 v182, v84, v85\n"
    "v_cvt_pk_bf16_f32 v183, v86, v87\n"
    "v_mfma_f32_32x32x16_bf16 v[152:167], a[108:111], v[176:179], v[152:167]\n"
    "s_nop 0\n"
    "v_mfma_f32_32x32x16_bf16 v[152:167], a[112:115], v[180:183], v[152:167]\n"
    "v_mfma_f32_32x32x16_bf16 v[72:87], a[28:31], v[32:35], v[136:151]\n"
    "v_mfma_f32_32x32x16_bf16 v[72:87], a[52:55], v[60:63], v[72:87]\n"
    "s_nop 11\n"
    "v_exp_f32_e64 v72, v72 clamp\n"
    "v_exp_f32_e64 v73, v73 clamp\n"
    "v_exp_f32_e64 v74, v74 clamp\n"
    "v_exp_f32_e64 v75, v75 clamp\n"
    "v_exp_f32_e64 v76, v76 clamp\n"
    "v_exp_f32_e64 v77, v77 clamp\n"
    "v_exp_f32_e64 v78, v78 clamp\n"
    "v_exp_f32_e64 v79, v79 clamp\n"
    "v_exp_f32_e64 v80, v80 clamp\n"
    "v_exp_f32_e64 v81, v81 clamp\n"
    "v_exp_f32_e64 v82, v82 clamp\n"
    "v_exp_f32_e64 v83, v83 clamp\n"
    "v_exp_f32_e64 v84, v84 clamp\n"
    "v_exp_f32_e64 v85, v85 clamp\n"
    "v_exp_f32_e64 v86, v86 clamp\n"
    "v_exp_f32_e64 v87, v87 clamp\n"
    "v_cvt_pk_bf16_f32 v176, v72, v73\n"
    "v_cvt_pk_bf16_f32 v177, v74, v75\n"
    "v_cvt_pk_bf16_f32 v178, v76, v77\n"
    "v_cvt_pk_bf16_f32 v179, v78, v79\n"
    "v_cvt_pk_bf16_f32 v180, v80, v81\n"
    "v_cvt_pk_bf16_f32 v181, v82, v83\n"
    "v_cvt_pk_bf16_f32 v182, v84, v85\n"
    "v_cvt_pk_bf16_f32 v183, v86, v87\n"
    "v_mfma_f32_32x32x16_bf16 v[152:167], a[116:119], v[176:179], v[152:167]\n"
    "s_nop 0\n"
    "v_mfma_f32_32x32x16_bf16 v[152:167], a[120:123], v[180:183], v[152:167]\n"
    "v_mfma_f32_32x32x16_bf16 v[120:135], a[36:39], v[36:39], 0\n"
    "v_mfma_f32_32x32x16_bf16 v[104:119], a[4:7], v[36:39], 0\n"
    "v_mfma_f32_32x32x16_bf16 v[120:135], a[56:59], v[68:71], v[120:135]\n"
    "v_mfma_f32_32x32x16_bf16 v[104:119], a[40:43], v[68:71], v[104:119]\n"
    "s_cmp_ge_i32 s14, 8\n"
    "s_cbranch_scc1 .Lfb_fv_49\n"
    "s_nop 9\n"
    "v_mov_b32 v104, 0xff61b1e6\n"
    "v_mov_b32 v105, 0xff61b1e6\n"
    "v_mov_b32 v106, 0xff61b1e6\n"
    "v_mov_b32 v107, 0xff61b1e6\n"
    "v_mov_b32 v108, 0xff61b1e6\n"
    "v_mov_b32 v109, 0xff61b1e6\n"
    "v_mov_b32 v110, 0xff61b1e6\n"
    "v_mov_b32 v111, 0xff61b1e6\n"
    "v_mov_b32 v112, 0xff61b1e6\n"
    "v_mov_b32 v113, 0xff61b1e6\n"
    "v_mov_b32 v114, 0xff61b1e6\n"
    "v_mov_b32 v115, 0xff61b1e6\n"
    "v_mov_b32 v116, 0xff61b1e6\n"
    "v_mov_b32 v117, 0xff61b1e6\n"
    "v_mov_b32 v118, 0xff61b1e6\n"
    "v_mov_b32 v119, 0xff61b1e6\n"
    ".Lfb_fv_49:\n"
    "s_nop 9\n"
    "v_cndmask_b32_e64 v120, v120, v104, s[64:65]\n"
    "v_cndmask_b32_e64 v121, v121, v105, s[66:67]\n"
    "v_cndmask_b32_e64 v122, v122, v106, s[68:69]\n"
    "v_cndmask_b32_e64 v123, v123, v107, s[70:71]\n"
    "v_cndmask_b32_e64 v124, v124, v108, s[72:73]\n"
    "v_cndmask_b32_e64 v125, v125, v109, s[74:75]\n"
    "v_cndmask_b32_e64 v126, v126, v110, s[76:77]\n"
    "v_cndmask_b32_e64 v127, v127, v111, s[78:79]\n"
    "v_cndmask_b32_e64 v128, v128, v112, s[80:81]\n"
    "v_cndmask_b32_e64 v129, v129, v113, s[82:83]\n"
    "v_cndmask_b32_e64 v130, v130, v114, s[84:85]\n"
    "v_cndmask_b32_e64 v131, v131, v115, s[86:87]\n"
    "v_cndmask_b32_e64 v132, v132, v116, s[88:89]\n"
    "v_cndmask_b32_e64 v133, v133, v117, s[90:91]\n"
    "v_cndmask_b32_e64 v134, v134, v118, s[92:93]\n"
    "v_cndmask_b32_e64 v135, v135, v119, s[94:95]\n"
    "v_max3_f32 v192, v120, v121, v122\n"
    "v_max3_f32 v192, v192, v122, v123\n"
    "v_max3_f32 v192, v192, v124, v125\n"
    "v_max3_f32 v192, v192, v126, v127\n"
    "v_max3_f32 v192, v192, v128, v129\n"
    "v_max3_f32 v192, v192, v130, v131\n"
    "v_max3_f32 v192, v192, v132, v133\n"
    "v_max3_f32 v192, v192, v134, v135\n"
    "v_mov_b32 v193, v192\n"
    "s_nop 1\n"
    "v_permlane32_swap_b32 v192, v193\n"
    "v_max_f32 v192, v192, v193\n"
    "v_sub_f32 v136, 0, v192\n"
    "v_sub_f32 v137, 0, v192\n"
    "v_sub_f32 v138, 0, v192\n"
    "v_sub_f32 v139, 0, v192\n"
    "v_sub_f32 v140, 0, v192\n"
    "v_sub_f32 v141, 0, v192\n"
    "v_sub_f32 v142, 0, v192\n"
    "v_sub_f32 v143, 0, v192\n"
    "v_sub_f32 v144, 0, v192\n"
    "v_sub_f32 v145, 0, v192\n"
    "v_sub_f32 v146, 0, v192\n"
    "v_sub_f32 v147, 0, v192\n"
    "v_sub_f32 v148, 0, v192\n"
    "v_sub_f32 v149, 0, v192\n"
    "v_sub_f32 v150, 0, v192\n"
    "v_sub_f32 v151, 0, v192\n"
    "s_nop 1\n"
    "v_mfma_f32_32x32x16_bf16 v[88:103], a[8:11], v[36:39], v[136:151]\n"
    "v_mov_b32 v196, v160\n"
    "v_mov_b32 v197, v160\n"
    "s_nop 1\n"
    "v_permlane32_swap_b32 v196, v197\n"
    "s_nop 15\n"
    "s_branch .Lfb_ool_out_21\n"
    ".Lfb_ool_in_22:\n"
    "s_waitcnt vmcnt(0) lgkmcnt(0)\n"
    "s_nop 15\n"
    "v_mfma_f32_32x32x16_bf16 v[120:135], a[36:39], v[36:39], 0\n"
    "v_mfma_f32_32x32x16_bf16 v[104:119], a[4:7], v[36:39], 0\n"
    "v_mfma_f32_32x32x16_bf16 v[120:135], a[56:59], v[68:71], v[120:135]\n"
    "v_mfma_f32_32x32x16_bf16 v[104:119], a[40:43], v[68:71], v[104:119]\n"
    "s_cmp_ge_i32 s14, 8\n"
    "s_cbranch_scc1 .Lfb_fv_50\n"
    "s_nop 9\n"
    "v_mov_b32 v104, 0xff61b1e6\n"
    "v_mov_b32 v105, 0xff61b1e6\n"
    "v_mov_b32 v106, 0xff61b1e6\n"
    "v_mov_b32 v107, 0xff61b1e6\n"
    "v_mov_b32 v108, 0xff61b1e6\n"
    "v_mov_b32 v109, 0xff61b1e6\n"
    "v_mov_b32 v110, 0xff61b1e6\n"
    "v_mov_b32 v111, 0xff61b1e6\n"
    "v_mov_b32 v112, 0xff61b1e6\n"
    "v_mov_b32 v113, 0xff61b1e6\n"
    "v_mov_b32 v114, 0xff61b1e6\n"
    "v_mov_b32 v115, 0xff61b1e6\n"
    "v_mov_b32 v116, 0xff61b1e6\n"
    "v_mov_b32 v117, 0xff61b1e6\n"
    "v_mov_b32 v118, 0xff61b1e6\n"
    "v_mov_b32 v119, 0xff61b1e6\n"
    ".Lfb_fv_50:\n"
    "s_nop 9\n"
    "v_cndmask_b32_e64 v120, v120, v104, s[64:65]\n"
    "v_cndmask_b32_e64 v121, v121, v105, s[66:67]\n"
    "v_cndmask_b32_e64 v122, v122, v106, s[68:69]\n"
    "v_cndmask_b32_e64 v123, v123, v107, s[70:71]\n"
    "v_cndmask_b32_e64 v124, v124, v108, s[72:73]\n"
    "v_cndmask_b32_e64 v125, v125, v109, s[74:75]\n"
    "v_cndmask_b32_e64 v126, v126, v110, s[76:77]\n"
    "v_cndmask_b32_e64 v127, v127, v111, s[78:79]\n"
    "v_cndmask_b32_e64 v128, v128, v112, s[80:81]\n"
    "v_cndmask_b32_e64 v129, v129, v113, s[82:83]\n"
    "v_cndmask_b32_e64 v130, v130, v114, s[84:85]\n"
    "v_cndmask_b32_e64 v131, v131, v115, s[86:87]\n"
    "v_cndmask_b32_e64 v132, v132, v116, s[88:89]\n"
    "v_cndmask_b32_e64 v133, v133, v117, s[90:91]\n"
    "v_cndmask_b32_e64 v134, v134, v118, s[92:93]\n"
    "v_cndmask_b32_e64 v135, v135, v119, s[94:95]\n"
    "v_max3_f32 v192, v120, v121, v122\n"
    "v_max3_f32 v192, v192, v122, v123\n"
    "v_max3_f32 v192, v192, v124, v125\n"
    "v_max3_f32 v192, v192, v126, v127\n"
    "v_max3_f32 v192, v192, v128, v129\n"
    "v_max3_f32 v192, v192, v130, v131\n"
    "v_max3_f32 v192, v192, v132, v133\n"
    "v_max3_f32 v192, v192, v134, v135\n"
    "v_mfma_f32_32x32x16_bf16 v[72:87], a[8:11], v[36:39], 0\n"
    "v_mfma_f32_32x32x16_bf16 v[72:87], a[44:47], v[64:67], v[72:87]\n"
    "s_nop 11\n"
    "v_max3_f32 v193, v72, v73, v74\n"
    "v_max3_f32 v193, v193, v74, v75\n"
    "v_max3_f32 v193, v193, v76, v77\n"
    "v_max3_f32 v193, v193, v78, v79\n"
    "v_max3_f32 v193, v193, v80, v81\n"
    "v_max3_f32 v193, v193, v82, v83\n"
    "v_max3_f32 v193, v193, v84, v85\n"
    "v_max3_f32 v193, v193, v86, v87\n"
    "v_max_f32 v193, v193, v192\n"
    "s_cmp_ge_i32 s14, 6\n"
    "s_cselect_b64 vcc, -1, 0\n"
    "v_cndmask_b32 v192, v192, v193, vcc\n"
    "v_mfma_f32_32x32x16_bf16 v[72:87], a[12:15], v[36:39], 0\n"
    "v_mfma_f32_32x32x16_bf16 v[72:87], a[44:47], v[68:71], v[72:87]\n"
    "s_nop 11\n"
    "v_max3_f32 v193, v72, v73, v74\n"
    "v_max3_f32 v193, v193, v74, v75\n"
    "v_max3_f32 v193, v193, v76, v77\n"
    "v_max3_f32 v193, v193, v78, v79\n"
    "v_max3_f32 v193, v193, v80, v81\n"
    "v_max3_f32 v193, v193, v82, v83\n"
    "v_max3_f32 v193, v193, v84, v85\n"
    "v_max3_f32 v193, v193, v86, v87\n"
    "v_max_f32 v193, v193, v192\n"
    "s_cmp_ge_i32 s14, 5\n"
    "s_cselect_b64 vcc, -1, 0\n"
    "v_cndmask_b32 v192, v192, v193, vcc\n"
    "v_mfma_f32_32x32x16_bf16 v[72:87], a[16:19], v[36:39], 0\n"
    "v_mfma_f32_32x32x16_bf16 v[72:87], a[48:51], v[64:67], v[72:87]\n"
    "s_nop 11\n"
    "v_max3_f32 v193, v72, v73, v74\n"
    "v_max3_f32 v193, v193, v74, v75\n"
    "v_max3_f32 v193, v193, v76, v77\n"
    "v_max3_f32 v193, v193, v78, v79\n"
    "v_max3_f32 v193, v193, v80, v81\n"
    "v_max3_f32 v193, v193, v82, v83\n"
    "v_max3_f32 v193, v193, v84, v85\n"
    "v_max3_f32 v193, v193, v86, v87\n"
    "v_max_f32 v193, v193, v192\n"
    "s_cmp_ge_i32 s14, 4\n"
    "s_cselect_b64 vcc, -1, 0\n"
    "v_cndmask_b32 v192, v192, v193, vcc\n"
    "v_mfma_f32_32x32x16_bf16 v[72:87], a[20:23], v[36:39], 0\n"
    "v_mfma_f32_32x32x16_bf16 v[72:87], a[48:51], v[68:71], v[72:87]\n"
    "s_nop 11\n"
    "v_max3_f32 v193, v72, v73, v74\n"
    "v_max3_f32 v193, v193, v74, v75\n"
    "v_max3_f32 v193, v193, v76, v77\n"
    "v_max3_f32 v193, v193, v78, v79\n"
    "v_max3_f32 v193, v193, v80, v81\n"
    "v_max3_f32 v193, v193, v82, v83\n"
    "v_max3_f32 v193, v193, v84, v85\n"
    "v_max3_f32 v193, v193, v86, v87\n"
    "v_max_f32 v193, v193, v192\n"
    "s_cmp_ge_i32 s14, 3\n"
    "s_cselect_b64 vcc, -1, 0\n"
    "v_cndmask_b32 v192, v192, v193, vcc\n"
    "v_mfma_f32_32x32x16_bf16 v[72:87], a[24:27], v[36:39], 0\n"
    "v_mfma_f32_32x32x16_bf16 v[72:87], a[52:55], v[64:67], v[72:87]\n"
    "s_nop 11\n"
    "v_max3_f32 v193, v72, v73, v74\n"
    "v_max3_f32 v193, v193, v74, v75\n"
    "v_max3_f32 v193, v193, v76, v77\n"
    "v_max3_f32 v193, v193, v78, v79\n"
    "v_max3_f32 v193, v193, v80, v81\n"
    "v_max3_f32 v193, v193, v82, v83\n"
    "v_max3_f32 v193, v193, v84, v85\n"
    "v_max3_f32 v193, v193, v86, v87\n"
    "v_max_f32 v193, v193, v192\n"
    "s_cmp_ge_i32 s14, 2\n"
    "s_cselect_b64 vcc, -1, 0\n"
    "v_cndmask_b32 v192, v192, v193, vcc\n"
    "v_mfma_f32_32x32x16_bf16 v[72:87], a[28:31], v[36:39], 0\n"
    "v_mfma_f32_32x32x16_bf16 v[72:87], a[52:55], v[68:71], v[72:87]\n"
    "s_nop 11\n"
    "v_max3_f32 v193, v72, v73, v74\n"
    "v_max3_f32 v193, v193, v74, v75\n"
    "v_max3_f32 v193, v193, v76, v77\n"
    "v_max3_f32 v193, v193, v78, v79\n"
    "v_max3_f32 v193, v193, v80, v81\n"
    "v_max3_f32 v193, v193, v82, v83\n"
    "v_max3_f32 v193, v193, v84, v85\n"
    "v_max3_f32 v193, v193, v86, v87\n"
    "v_max_f32 v193, v193, v192\n"
    "s_cmp_ge_i32 s14, 1\n"
    "s_cselect_b64 vcc, -1, 0\n"
    "v_cndmask_b32 v192, v192, v193, vcc\n"
    "v_mfma_f32_32x32x16_bf16 v[72:87], a[32:35], v[36:39], 0\n"
    "v_mfma_f32_32x32x16_bf16 v[72:87], a[56:59], v[64:67], v[72:87]\n"
    "s_nop 11\n"
    "v_max3_f32 v193, v72, v73, v74\n"
    "v_max3_f32 v193, v193, v74, v75\n"
    "v_max3_f32 v193, v193, v76, v77\n"
    "v_max3_f32 v193, v193, v78, v79\n"
    "v_max3_f32 v193, v193, v80, v81\n"
    "v_max3_f32 v193, v193, v82, v83\n"
    "v_max3_f32 v193, v193, v84, v85\n"
    "v_max3_f32 v193, v193, v86, v87\n"
    "v_max_f32 v193, v193, v192\n"
    "s_cmp_ge_i32 s14, 0\n"
    "s_cselect_b64 vcc, -1, 0\n"
    "v_cndmask_b32 v192, v192, v193, vcc\n"
    "v_mov_b32 v193, v192\n"
    "s_nop 1\n"
    "v_permlane32_swap_b32 v192, v193\n"
    "v_max_f32 v192, v192, v193\n"
    "v_sub_f32 v136, 0, v192\n"
    "v_sub_f32 v137, 0, v192\n"
    "v_sub_f32 v138, 0, v192\n"
    "v_sub_f32 v139, 0, v192\n"
    "v_sub_f32 v140, 0, v192\n"
    "v_sub_f32 v141, 0, v192\n"
    "v_sub_f32 v142, 0, v192\n"
    "v_sub_f32 v143, 0, v192\n"
    "v_sub_f32 v144, 0, v192\n"
    "v_sub_f32 v145, 0, v192\n"
    "v_sub_f32 v146, 0, v192\n"
    "v_sub_f32 v147, 0, v192\n"
    "v_sub_f32 v148, 0, v192\n"
    "v_sub_f32 v149, 0, v192\n"
    "v_sub_f32 v150, 0, v192\n"
    "v_sub_f32 v151, 0, v192\n"
    "v_add_f32 v120, v120, v136\n"
    "v_add_f32 v121, v121, v136\n"
    "v_add_f32 v122, v122, v136\n"
    "v_add_f32 v123, v123, v136\n"
    "v_exp_f32 v196, v120\n"
    "v_exp_f32 v197, v121\n"
    "v_exp_f32 v198, v122\n"
    "v_exp_f32 v199, v123\n"
    "v_cvt_pk_bf16_f32 v196, v196, v197\n"
    "v_cvt_pk_bf16_f32 v198, v198, v199\n"
    "v_and_b32 v168, v40, v196\n"
    "v_xor_b32 v184, v168, v196\n"
    "v_and_b32 v169, v41, v198\n"
    "v_xor_b32 v185, v169, v198\n"
    "v_add_f32 v124, v124, v136\n"
    "v_add_f32 v125, v125, v136\n"
    "v_add_f32 v126, v126, v136\n"
    "v_add_f32 v127, v127, v136\n"
    "v_exp_f32 v196, v124\n"
    "v_exp_f32 v197, v125\n"
    "v_exp_f32 v198, v126\n"
    "v_exp_f32 v199, v127\n"
    "v_cvt_pk_bf16_f32 v196, v196, v197\n"
    "v_cvt_pk_bf16_f32 v198, v198, v199\n"
    "v_and_b32 v170, v42, v196\n"
    "v_xor_b32 v186, v170, v196\n"
    "v_and_b32 v171, v43, v198\n"
    "v_xor_b32 v187, v171, v198\n"
    "v_add_f32 v128, v128, v136\n"
    "v_add_f32 v129, v129, v136\n"
    "v_add_f32 v130, v130, v136\n"
    "v_add_f32 v131, v131, v136\n"
    "v_exp_f32 v196, v128\n"
    "v_exp_f32 v197, v129\n"
    "v_exp_f32 v198, v130\n"
    "v_exp_f32 v199, v131\n"
    "v_cvt_pk_bf16_f32 v196, v196, v197\n"
    "v_cvt_pk_bf16_f32 v198, v198, v199\n"
    "v_and_b32 v172, v44, v196\n"
    "v_xor_b32 v188, v172, v196\n"
    "v_and_b32 v173, v45, v198\n"
    "v_xor_b32 v189, v173, v198\n"
    "v_add_f32 v132, v132, v136\n"
    "v_add_f32 v133, v133, v136\n"
    "v_add_f32 v134, v134, v136\n"
    "v_add_f32 v135, v135, v136\n"
    "v_exp_f32 v196, v132\n"
    "v_exp_f32 v197, v133\n"
    "v_exp_f32 v198, v134\n"
    "v_exp_f32 v199, v135\n"
    "v_cvt_pk_bf16_f32 v196, v196, v197\n"
    "v_cvt_pk_bf16_f32 v198, v198, v199\n"
    "v_and_b32 v174, v46, v196\n"
    "v_xor_b32 v190, v174, v196\n"
    "v_and_b32 v175, v47, v198\n"
    "v_xor_b32 v191, v175, v198\n"
    "v_mfma_f32_32x32x16_bf16 v[152:167], a[68:71], v[168:171], 0\n"
    "v_mfma_f32_32x32x16_bf16 v[152:167], a[72:75], v[172:175], v[152:167]\n"
    "v_mfma_f32_32x32x16_bf16 v[152:167], a[132:135], v[184:187], v[152:167]\n"
    "v_mfma_f32_32x32x16_bf16 v[152:167], a[136:139], v[188:191], v[152:167]\n"
    "v_mfma_f32_32x32x16_bf16 v[72:87], a[8:11], v[36:39], v[136:151]\n"
    "v_mfma_f32_32x32x16_bf16 v[72:87], a[44:47], v[64:67], v[72:87]\n"
    "s_nop 11\n"
    "v_exp_f32_e64 v72, v72 clamp\n"
    "v_exp_f32_e64 v73, v73 clamp\n"
    "v_exp_f32_e64 v74, v74 clamp\n"
    "v_exp_f32_e64 v75, v75 clamp\n"
    "v_exp_f32_e64 v76, v76 clamp\n"
    "v_exp_f32_e64 v77, v77 clamp\n"
    "v_exp_f32_e64 v78, v78 clamp\n"
    "v_exp_f32_e64 v79, v79 clamp\n"
    "v_exp_f32_e64 v80, v80 clamp\n"
    "v_exp_f32_e64 v81, v81 clamp\n"
    "v_exp_f32_e64 v82, v82 clamp\n"
    "v_exp_f32_e64 v83, v83 clamp\n"
    "v_exp_f32_e64 v84, v84 clamp\n"
    "v_exp_f32_e64 v85, v85 clamp\n"
    "v_exp_f32_e64 v86, v86 clamp\n"
    "v_exp_f32_e64 v87, v87 clamp\n"
    "v_cvt_pk_bf16_f32 v176, v72, v73\n"
    "v_cvt_pk_bf16_f32 v177, v74, v75\n"
    "v_cvt_pk_bf16_f32 v178, v76, v77\n"
    "v_cvt_pk_bf16_f32 v179, v78, v79\n"
    "v_cvt_pk_bf16_f32 v180, v80, v81\n"
    "v_cvt_pk_bf16_f32 v181, v82, v83\n"
    "v_cvt_pk_bf16_f32 v182, v84, v85\n"
    "v_cvt_pk_bf16_f32 v183, v86, v87\n"
    "v_mfma_f32_32x32x16_bf16 v[152:167], a[76:79], v[176:179], v[152:167]\n"
    "s_nop 0\n"
    "v_mfma_f32_32x32x16_bf16 v[152:167], a[80:83], v[180:183], v[152:167]\n"
    "v_mfma_f32_32x32x16_bf16 v[72:87], a[12:15], v[36:39], v[136:151]\n"
    "v_mfma_f32_32x32x16_bf16 v[72:87], a[44:47], v[68:71], v[72:87]\n"
    "s_nop 11\n"
    "v_exp_f32_e64 v72, v72 clamp\n"
    "v_exp_f32_e64 v73, v73 clamp\n"
    "v_exp_f32_e64 v74, v74 clamp\n"
    "v_exp_f32_e64 v75, v75 clamp\n"
    "v_exp_f32_e64 v76, v76 clamp\n"
    "v_exp_f32_e64 v77, v77 clamp\n"
    "v_exp_f32_e64 v78, v78 clamp\n"
    "v_exp_f32_e64 v79, v79 clamp\n"
    "v_exp_f32_e64 v80, v80 clamp\n"
    "v_exp_f32_e64 v81, v81 clamp\n"
    "v_exp_f32_e64 v82, v82 clamp\n"
    "v_exp_f32_e64 v83, v83 clamp\n"
    "v_exp_f32_e64 v84, v84 clamp\n"
    "v_exp_f32_e64 v85, v85 clamp\n"
    "v_exp_f32_e64 v86, v86 clamp\n"
    "v_exp_f32_e64 v87, v87 clamp\n"
    "v_cvt_pk_bf16_f32 v176, v72, v73\n"
    "v_cvt_pk_bf16_f32 v177, v74, v75\n"
    "v_cvt_pk_bf16_f32 v178, v76, v77\n"
    "v_cvt_pk_bf16_f32 v179, v78, v79\n"
    "v_cvt_pk_bf16_f32 v180, v80, v81\n"
    "v_cvt_pk_bf16_f32 v181, v82, v83\n"
    "v_cvt_pk_bf16_f32 v182, v84, v85\n"
    "v_cvt_pk_bf16_f32 v183, v86, v87\n"
    "v_mfma_f32_32x32x16_bf16 v[152:167], a[84:87], v[176:179], v[152:167]\n"
    "s_nop 0\n"
    "v_mfma_f32_32x32x16_bf16 v[152:167], a[88:91], v[180:183], v[152:167]\n"
    "v_mfma_f32_32x32x16_bf16 v[72:87], a[16:19], v[36:39], v[136:151]\n"
    "v_mfma_f32_32x32x16_bf16 v[72:87], a[48:51], v[64:67], v[72:87]\n"
    "s_nop 11\n"
    "v_exp_f32_e64 v72, v72 clamp\n"
    "v_exp_f32_e64 v73, v73 clamp\n"
    "v_exp_f32_e64 v74, v74 clamp\n"
    "v_exp_f32_e64 v75, v75 clamp\n"
    "v_exp_f32_e64 v76, v76 clamp\n"
    "v_exp_f32_e64 v77, v77 clamp\n"
    "v_exp_f32_e64 v78, v78 clamp\n"
    "v_exp_f32_e64 v79, v79 clamp\n"
    "v_exp_f32_e64 v80, v80 clamp\n"
    "v_exp_f32_e64 v81, v81 clamp\n"
    "v_exp_f32_e64 v82, v82 clamp\n"
    "v_exp_f32_e64 v83, v83 clamp\n"
    "v_exp_f32_e64 v84, v84 clamp\n"
    "v_exp_f32_e64 v85, v85 clamp\n"
    "v_exp_f32_e64 v86, v86 clamp\n"
    "v_exp_f32_e64 v87, v87 clamp\n"
    "v_cvt_pk_bf16_f32 v176, v72, v73\n"
    "v_cvt_pk_bf16_f32 v177, v74, v75\n"
    "v_cvt_pk_bf16_f32 v178, v76, v77\n"
    "v_cvt_pk_bf16_f32 v179, v78, v79\n"
    "v_cvt_pk_bf16_f32 v180, v80, v81\n"
    "v_cvt_pk_bf16_f32 v181, v82, v83\n"
    "v_cvt_pk_bf16_f32 v182, v84, v85\n"
    "v_cvt_pk_bf16_f32 v183, v86, v87\n"
    "v_mfma_f32_32x32x16_bf16 v[152:167], a[92:95], v[176:179], v[152:167]\n"
    "s_nop 0\n"
    "v_mfma_f32_32x32x16_bf16 v[152:167], a[96:99], v[180:183], v[152:167]\n"
    "v_mfma_f32_32x32x16_bf16 v[72:87], a[20:23], v[36:39], v[136:151]\n"
    "v_mfma_f32_32x32x16_bf16 v[72:87], a[48:51], v[68:71], v[72:87]\n"
    "s_nop 11\n"
    "v_exp_f32_e64 v72, v72 clamp\n"
    "v_exp_f32_e64 v73, v73 clamp\n"
    "v_exp_f32_e64 v74, v74 clamp\n"
    "v_exp_f32_e64 v75, v75 clamp\n"
    "v_exp_f32_e64 v76, v76 clamp\n"
    "v_exp_f32_e64 v77, v77 clamp\n"
    "v_exp_f32_e64 v78, v78 clamp\n"
    "v_exp_f32_e64 v79, v79 clamp\n"
    "v_exp_f32_e64 v80, v80 clamp\n"
    "v_exp_f32_e64 v81, v81 clamp\n"
    "v_exp_f32_e64 v82, v82 clamp\n"
    "v_exp_f32_e64 v83, v83 clamp\n"
    "v_exp_f32_e64 v84, v84 clamp\n"
    "v_exp_f32_e64 v85, v85 clamp\n"
    "v_exp_f32_e64 v86, v86 clamp\n"
    "v_exp_f32_e64 v87, v87 clamp\n"
    "v_cvt_pk_bf16_f32 v176, v72, v73\n"
    "v_cvt_pk_bf16_f32 v177, v74, v75\n"
    "v_cvt_pk_bf16_f32 v178, v76, v77\n"
    "v_cvt_pk_bf16_f32 v179, v78, v79\n"
    "v_cvt_pk_bf16_f32 v180, v80, v81\n"
    "v_cvt_pk_bf16_f32 v181, v82, v83\n"
    "v_cvt_pk_bf16_f32 v182, v84, v85\n"
    "v_cvt_pk_bf16_f32 v183, v86, v87\n"
    "v_mfma_f32_32x32x16_bf16 v[152:167], a[100:103], v[176:179], v[152:167]\n"
    "s_nop 0\n"
    "v_mfma_f32_32x32x16_bf16 v[152:167], a[104:107], v[180:183], v[152:167]\n"
    "v_mfma_f32_32x32x16_bf16 v[72:87], a[24:27], v[36:39], v[136:151]\n"
    "v_mfma_f32_32x32x16_bf16 v[72:87], a[52:55], v[64:67], v[72:87]\n"
    "s_nop 11\n"
    "v_exp_f32_e64 v72, v72 clamp\n"
    "v_exp_f32_e64 v73, v73 clamp\n"
    "v_exp_f32_e64 v74, v74 clamp\n"
    "v_exp_f32_e64 v75, v75 clamp\n"
    "v_exp_f32_e64 v76, v76 clamp\n"
    "v_exp_f32_e64 v77, v77 clamp\n"
    "v_exp_f32_e64 v78, v78 clamp\n"
    "v_exp_f32_e64 v79, v79 clamp\n"
    "v_exp_f32_e64 v80, v80 clamp\n"
    "v_exp_f32_e64 v81, v81 clamp\n"
    "v_exp_f32_e64 v82, v82 clamp\n"
    "v_exp_f32_e64 v83, v83 clamp\n"
    "v_exp_f32_e64 v84, v84 clamp\n"
    "v_exp_f32_e64 v85, v85 clamp\n"
    "v_exp_f32_e64 v86, v86 clamp\n"
    "v_exp_f32_e64 v87, v87 clamp\n"
    "v_cvt_pk_bf16_f32 v176, v72, v73\n"
    "v_cvt_pk_bf16_f32 v177, v74, v75\n"
    "v_cvt_pk_bf16_f32 v178, v76, v77\n"
    "v_cvt_pk_bf16_f32 v179, v78, v79\n"
    "v_cvt_pk_bf16_f32 v180, v80, v81\n"
    "v_cvt_pk_bf16_f32 v181, v82, v83\n"
    "v_cvt_pk_bf16_f32 v182, v84, v85\n"
    "v_cvt_pk_bf16_f32 v183, v86, v87\n"
    "v_mfma_f32_32x32x16_bf16 v[152:167], a[108:111], v[176:179], v[152:167]\n"
    "s_nop 0\n"
    "v_mfma_f32_32x32x16_bf16 v[152:167], a[112:115], v[180:183], v[152:167]\n"
    "v_mfma_f32_32x32x16_bf16 v[72:87], a[28:31], v[36:39], v[136:151]\n"
    "v_mfma_f32_32x32x16_bf16 v[72:87], a[52:55], v[68:71], v[72:87]\n"
    "s_nop 11\n"
    "v_exp_f32_e64 v72, v72 clamp\n"
    "v_exp_f32_e64 v73, v73 clamp\n"
    "v_exp_f32_e64 v74, v74 clamp\n"
    "v_exp_f32_e64 v75, v75 clamp\n"
    "v_exp_f32_e64 v76, v76 clamp\n"
    "v_exp_f32_e64 v77, v77 clamp\n"
    "v_exp_f32_e64 v78, v78 clamp\n"
    "v_exp_f32_e64 v79, v79 clamp\n"
    "v_exp_f32_e64 v80, v80 clamp\n"
    "v_exp_f32_e64 v81, v81 clamp\n"
    "v_exp_f32_e64 v82, v82 clamp\n"
    "v_exp_f32_e64 v83, v83 clamp\n"
    "v_exp_f32_e64 v84, v84 clamp\n"
    "v_exp_f32_e64 v85, v85 clamp\n"
    "v_exp_f32_e64 v86, v86 clamp\n"
    "v_exp_f32_e64 v87, v87 clamp\n"
    "v_cvt_pk_bf16_f32 v176, v72, v73\n"
    "v_cvt_pk_bf16_f32 v177, v74, v75\n"
    "v_cvt_pk_bf16_f32 v178, v76, v77\n"
    "v_cvt_pk_bf16_f32 v179, v78, v79\n"
    "v_cvt_pk_bf16_f32 v180, v80, v81\n"
    "v_cvt_pk_bf16_f32 v181, v82, v83\n"
    "v_cvt_pk_bf16_f32 v182, v84, v85\n"
    "v_cvt_pk_bf16_f32 v183, v86, v87\n"
    "v_mfma_f32_32x32x16_bf16 v[152:167], a[116:119], v[176:179], v[152:167]\n"
    "s_nop 0\n"
    "v_mfma_f32_32x32x16_bf16 v[152:167], a[120:123], v[180:183], v[152:167]\n"
    "v_mfma_f32_32x32x16_bf16 v[72:87], a[32:35], v[36:39], v[136:151]\n"
    "v_mfma_f32_32x32x16_bf16 v[72:87], a[56:59], v[64:67], v[72:87]\n"
    "s_nop 11\n"
    "v_exp_f32_e64 v72, v72 clamp\n"
    "v_exp_f32_e64 v73, v73 clamp\n"
    "v_exp_f32_e64 v74, v74 clamp\n"
    "v_exp_f32_e64 v75, v75 clamp\n"
    "v_exp_f32_e64 v76, v76 clamp\n"
    "v_exp_f32_e64 v77, v77 clamp\n"
    "v_exp_f32_e64 v78, v78 clamp\n"
    "v_exp_f32_e64 v79, v79 clamp\n"
    "v_exp_f32_e64 v80, v80 clamp\n"
    "v_exp_f32_e64 v81, v81 clamp\n"
    "v_exp_f32_e64 v82, v82 clamp\n"
    "v_exp_f32_e64 v83, v83 clamp\n"
    "v_exp_f32_e64 v84, v84 clamp\n"
    "v_exp_f32_e64 v85, v85 clamp\n"
    "v_exp_f32_e64 v86, v86 clamp\n"
    "v_exp_f32_e64 v87, v87 clamp\n"
    "v_cvt_pk_bf16_f32 v176, v72, v73\n"
    "v_cvt_pk_bf16_f32 v177, v74, v75\n"
    "v_cvt_pk_bf16_f32 v178, v76, v77\n"
    "v_cvt_pk_bf16_f32 v179, v78, v79\n"
    "v_cvt_pk_bf16_f32 v180, v80, v81\n"
    "v_cvt_pk_bf16_f32 v181, v82, v83\n"
    "v_cvt_pk_bf16_f32 v182, v84, v85\n"
    "v_cvt_pk_bf16_f32 v183, v86, v87\n"
    "v_mfma_f32_32x32x16_bf16 v[152:167], a[124:127], v[176:179], v[152:167]\n"
    "s_nop 0\n"
    "v_mfma_f32_32x32x16_bf16 v[152:167], a[128:131], v[180:183], v[152:167]\n"
    "s_nop 11\n"
    "v_mov_b32 v196, v160\n"
    "v_mov_b32 v197, v160\n"
    "s_nop 1\n"
    "v_permlane32_swap_b32 v196, v197\n"
    "s_nop 15\n"
    "s_branch .Lfb_ool_out_23\n"
    ".Lfb_ool_exit:\n"
    "; ---- end (t~45782)\n"
    :
    : "{s[0:1]}"(__builtin_amdgcn_kernarg_segment_ptr()), "{s2}"(blockIdx.x), "{s[4:5]}"(fb_table), "{v0}"(threadIdx.x)
    : "v1", "v2", "v3", "v4", "v5", "v6", "v7", "v8", "v9", "v10", "v11", "v12", "v13", "v14", "v15", "v16", "v17", "v18", "v19", "v20", "v21", "v22", "v23", "v24", "v25", "v26", "v27", "v28", "v29", "v30", "v31", "v32", "v33", "v34", "v35", "v36", "v37", "v38", "v39", "v40", "v41", "v42", "v43", "v44", "v45", "v46", "v47", "v48", "v49", "v50", "v51", "v52", "v53", "v54", "v55", "v56", "v57", "v58", "v59", "v60", "v61", "v62", "v63", "v64", "v65", "v66", "v67", "v68", "v69", "v70", "v71", "v72", "v73", "v74", "v75", "v76", "v77", "v78", "v79", "v80", "v81", "v82", "v83", "v84", "v85", "v86", "v87", "v88", "v89", "v90", "v91", "v92", "v93", "v94", "v95", "v96", "v97", "v98", "v99", "v100", "v101", "v102", "v103", "v104", "v105", "v106", "v107", "v108", "v109", "v110", "v111", "v112", "v113", "v114", "v115", "v116", "v117", "v118", "v119", "v120", "v121", "v122", "v123", "v124", "v125", "v126", "v127", "v128", "v129", "v130", "v131", "v132", "v133", "v134", "v135", "v136", "v137", "v138", "v139", "v140", "v141", "v142", "v143", "v144", "v145", "v146", "v147", "v148", "v149", "v150", "v151", "v152", "v153", "v154", "v155", "v156", "v157", "v158", "v159", "v160", "v161", "v162", "v163", "v164", "v165", "v166", "v167", "v168", "v169", "v170", "v171", "v172", "v173", "v174", "v175", "v176", "v177", "v178", "v179", "v180", "v181", "v182", "v183", "v184", "v185", "v186", "v187", "v188", "v189", "v190", "v191", "v192", "v193", "v194", "v195", "v196", "v197", "v198", "v199", "v200", "v201", "v202", "v203", "v204", "v205", "v206", "v207", "v208", "v209", "v210", "v211", "v212", "v213", "v214", "v215", "v216", "v217", "v218", "v219", "v220", "v221", "v222", "v223", "v224", "v225", "v226", "v227", "v228", "v229", "v230", "v231", "v232", "v233", "v234", "v235", "v236", "v237", "v238", "v239", "v240", "v241", "v242", "v243", "v244", "v245", "v246", "v247", "v248", "v249", "v250", "v251", "v252", "v253", "v254", "v255", "a0", "a1", "a2", "a3", "a4", "a5", "a6", "a7", "a8", "a9", "a10", "a11", "a12", "a13", "a14", "a15", "a16", "a17", "a18", "a19", "a20", "a21", "a22", "a23", "a24", "a25", "a26", "a27", "a28", "a29", "a30", "a31", "a32", "a33", "a34", "a35", "a36", "a37", "a38", "a39", "a40", "a41", "a42", "a43", "a44", "a45", "a46", "a47", "a48", "a49", "a50", "a51", "a52", "a53", "a54", "a55", "a56", "a57", "a58", "a59", "a60", "a61", "a62", "a63", "a64", "a65", "a66", "a67", "a68", "a69", "a70", "a71", "a72", "a73", "a74", "a75", "a76", "a77", "a78", "a79", "a80", "a81", "a82", "a83", "a84", "a85", "a86", "a87", "a88", "a89", "a90", "a91", "a92", "a93", "a94", "a95", "a96", "a97", "a98", "a99", "a100", "a101", "a102", "a103", "a104", "a105", "a106", "a107", "a108", "a109", "a110", "a111", "a112", "a113", "a114", "a115", "a116", "a117", "a118", "a119", "a120", "a121", "a122", "a123", "a124", "a125", "a126", "a127", "a128", "a129", "a130", "a131", "a132", "a133", "a134", "a135", "a136", "a137", "a138", "a139", "a140", "a141", "a142", "a143", "a144", "a145", "a146", "a147", "a148", "a149", "a150", "a151", "a152", "a153", "a154", "a155", "a156", "a157", "a158", "a159", "a160", "a161", "a162", "a163", "a164", "a165", "a166", "a167", "a168", "a169", "a170", "a171", "a172", "a173", "a174", "a175", "a176", "a177", "a178", "a179", "a180", "a181", "a182", "a183", "a184", "a185", "a186", "a187", "a188", "a189", "a190", "a191", "a192", "a193", "a194", "a195", "a196", "a197", "a198", "a199", "a200", "a201", "a202", "a203", "a204", "a205", "a206", "a207", "a208", "a209", "a210", "a211", "a212", "a213", "a214", "a215", "a216", "a217", "a218", "a219", "a220", "a221", "a222", "a223", "a224", "a225", "a226", "a227", "a228", "a229", "a230", "a231", "a232", "a233", "a234", "a235", "a236", "a237", "a238", "a239", "a240", "a241", "a242", "a243", "a244", "a245", "a246", "a247", "a248", "a249", "a250", "a251", "a252", "a253", "a254", "a255", "s3", "s6", "s7", "s8", "s9", "s10", "s11", "s12", "s13", "s14", "s15", "s16", "s17", "s18", "s19", "s20", "s21", "s22", "s23", "s24", "s25", "s26", "s27", "s28", "s29", "s30", "s31", "s33", "s34", "s35", "s36", "s37", "s38", "s39", "s40", "s41", "s42", "s43", "s44", "s45", "s46", "s47", "s48", "s49", "s50", "s51", "s52", "s53", "s54", "s55", "s56", "s57", "s58", "s59", "s60", "s61", "s62", "s63", "s64", "s65", "s66", "s67", "s68", "s69", "s70", "s71", "s72", "s73", "s74", "s75", "s76", "s77", "s78", "s79", "s80", "s81", "s82", "s83", "s84", "s85", "s86", "s87", "s88", "s89", "s90", "s91", "s92", "s93", "s94", "s95", "s96", "s97", "s98", "s99", "vcc", "scc", "memory");
}

extern "C" void kernel_launch(void* const* d_in, const int* in_sizes, int n_in, void* d_out, int out_size, void* d_ws, size_t ws_size,
                              hipStream_t stream) {
  (void)in_sizes; (void)n_in; (void)out_size; (void)ws_size;
  (void)hipFuncSetAttribute((const void*)fused_block, hipFuncAttributeMaxDynamicSharedMemorySize, 161280);
  fused_block<<<256, 256, 161280, stream>>>((const char*)d_in[0], (const char*)d_in[1], (char*)d_ws, (const char*)d_in[2], (const char*)d_in[4],
                                           (const char*)d_in[6], (const char*)d_in[5], (const char*)d_in[7], (char*)d_out);
}
```
